```python
import torch

def _softmax(x, axis=-1):
    return torch.softmax(x, dim=axis)
def _gelu(x):
    return torch.nn.functional.gelu(x, approximate="tanh")
def _silu(x):
    return torch.nn.functional.silu(x)
def _sigmoid(x):
    return torch.sigmoid(x)
def _layernorm(x, axis=-1, eps=1e-5):
    m = x.mean(dim=axis, keepdim=True)
    v = x.var(dim=axis, keepdim=True, unbiased=False)
    return (x - m) / torch.sqrt(v + eps)
def _scatter_add(tgt, idx, src, axis=0):
    return tgt.clone().index_add_(axis, idx, src)
def _segment_sum(src, seg, n):
    out = torch.zeros((n,) + src.shape[1:], dtype=src.dtype)
    return out.index_add_(0, seg, src)
def _pad(x, pw):
    flat = tuple(v for pair in reversed(pw) for v in pair)
    return torch.nn.functional.pad(x, flat)

kernel_name = 'sequential_preLN__mha_block__glu_gated_glu__prompt'

def setup_inputs():

    _w = (lambda a: (lambda t: t.to(torch.bfloat16) if t.is_floating_point() else t)(torch.as_tensor(a)))
    def _A_setup():
        B, S, D, H = 1, 2048, 512, 32
        return {
            "x": _w((lambda *s: torch.randn(*s, dtype=torch.bfloat16) * 0.05)(B, S, D)),
            "wqkv": _w((lambda *s: torch.randn(*s, dtype=torch.bfloat16) * 0.05)(D, 3 * D)),
            "wo": _w((lambda *s: torch.randn(*s, dtype=torch.bfloat16) * 0.05)(D, D)),
        }
    _A_in = _A_setup()

    def _B_setup():
        B, S, D, F = 1, 2048, 512, 1376
        return {
            "x": _w((lambda *s: torch.randn(*s, dtype=torch.bfloat16) * 0.05)(B, S, D)),
            "w_gate": _w((lambda *s: torch.randn(*s, dtype=torch.bfloat16) * 0.05)(D, F)),
            "w_up": _w((lambda *s: torch.randn(*s, dtype=torch.bfloat16) * 0.05)(D, F)),
            "w_down": _w((lambda *s: torch.randn(*s, dtype=torch.bfloat16) * 0.05)(F, D)),
        }
    _B_in = _B_setup()

    return {
        "A_x": _A_in["x"],
        "A_wqkv": _A_in["wqkv"],
        "A_wo": _A_in["wo"],
        "B_x": _B_in["x"],
        "B_w_gate": _B_in["w_gate"],
        "B_w_up": _B_in["w_up"],
        "B_w_down": _B_in["w_down"],
    }

def reference(A_x, A_wqkv, A_wo, B_x, B_w_gate, B_w_up, B_w_down):
    def _A_ref(x, wqkv, wo):
        B, S, D = x.shape
        H = 32
        Dh = D // H
        qkv = (lambda x, *d: x.permute(*d))((x @ wqkv).reshape(B, S, 3, H, Dh), 2, 0, 3, 1, 4)
        q, k, v = qkv[0], qkv[1], qkv[2]
        scores = torch.einsum("bhqd,bhkd->bhqk", q, k) * (Dh ** -0.5)
        p = _softmax(scores, axis=-1)
        o = torch.einsum("bhqk,bhkd->bhqd", p, v)
        return (lambda x, *d: x.permute(*d))(o, 0, 2, 1, 3).reshape(B, S, D) @ wo

    def _B_ref(x, w_gate, w_up, w_down):
        g = torch.einsum("bsd,df->bsf", x, w_gate)
        u = torch.einsum("bsd,df->bsf", x, w_up)
        h = _sigmoid(g) * u
        return torch.einsum("bsf,fd->bsd", h, w_down)

    x = A_x
    h = x + _A_ref(_layernorm(x, axis=-1), A_wqkv, A_wo)
    h2 = h + _B_ref(_layernorm(h, axis=-1), B_w_gate, B_w_up, B_w_down)
    return h2
```

```cpp
#include <hip/hip_runtime.h>
#include <hip/hip_bf16.h>
#include <stdint.h>

#define FUSED 1

typedef short s16x8 __attribute__((ext_vector_type(8)));
typedef short s16x4 __attribute__((ext_vector_type(4)));
typedef float f32x16 __attribute__((ext_vector_type(16)));
typedef float f32x4 __attribute__((ext_vector_type(4)));
typedef float f32x2 __attribute__((ext_vector_type(2)));
typedef unsigned u32x4 __attribute__((ext_vector_type(4)));
typedef unsigned u32x2 __attribute__((ext_vector_type(2)));
typedef __bf16 bf16x2 __attribute__((ext_vector_type(2)));
typedef unsigned short u16;
#define LDSAS __attribute__((address_space(3)))
#define GAS __attribute__((address_space(1)))
typedef __amdgpu_buffer_rsrc_t rsrc_t;

constexpr int S = 2048, D = 512, NH = 32, F = 1376, NU = F / 16;
constexpr float LN_EPS = 1e-5f;
constexpr float LOG2E = 1.4426950408889634f;

constexpr unsigned KV_CHUNK = 8 * 1024 + 16 * 544;
constexpr unsigned KV_HEAD = 8 * KV_CHUNK;
constexpr unsigned WS_CNT = 0;
constexpr unsigned WS_Q = 8192;
constexpr unsigned WS_KV = WS_Q + NH * 64 * 1024;
constexpr unsigned WS_O = WS_KV + NH * KV_HEAD;
constexpr unsigned WS_H = WS_O + 64 * 32 * 1024;
constexpr unsigned WS_ST = WS_H + 64 * 32 * 1024;
constexpr unsigned WS_HH = WS_ST + 64 * 16 * 256;
constexpr unsigned WS_END = WS_HH + 64 * NU * 1024;
constexpr int AUX_SC1 = 16;
#define STAMP(k, i) do {} while (0)

#define SBAR() __builtin_amdgcn_sched_barrier(0)
#define PIN(x) asm volatile("" : "+v"(x))
#define MFMA(a, b, c) __builtin_amdgcn_mfma_f32_32x32x16_bf16((a), (b), (c), 0, 0, 0)
#define WAITV(n) asm volatile("s_waitcnt vmcnt(" #n ")" ::: "memory")
#define BAR_L() asm volatile("s_waitcnt lgkmcnt(0)\n\ts_barrier" ::: "memory")
#define BAR_VL(n) asm volatile("s_waitcnt vmcnt(" #n ") lgkmcnt(0)\n\ts_barrier" ::: "memory")

__device__ __forceinline__ rsrc_t mk_rsrc(const void* p, unsigned bytes) {
    return __builtin_amdgcn_make_buffer_rsrc((void*)p, (short)0, (int)bytes, 0x00020000);
}
__device__ __forceinline__ void dma16(rsrc_t r, unsigned voff, unsigned soff, unsigned lds) {
    asm volatile("s_nop 4\n\ts_mov_b32 m0, %0\n\ts_nop 0\n\tbuffer_load_dwordx4 %1, %2, %3 offen lds"
                 :: "s"(lds), "v"(voff), "s"(r), "s"(soff) : "m0", "memory");
}
__device__ __forceinline__ unsigned pk2(float lo, float hi) {
    f32x2 v = {lo, hi};
    return __builtin_bit_cast(unsigned, __builtin_convertvector(v, bf16x2));
}
__device__ __forceinline__ float dot2(unsigned a, unsigned b, float c) {
    return __builtin_amdgcn_fdot2_f32_bf16(__builtin_bit_cast(bf16x2, a), __builtin_bit_cast(bf16x2, b), c, false);
}
__device__ __forceinline__ float swap_add(float v) {
    auto r = __builtin_amdgcn_permlane32_swap(__float_as_uint(v), __float_as_uint(v), false, false);
    return __uint_as_float(r[0]) + __uint_as_float(r[1]);
}
__device__ __forceinline__ float swap_max(float v) {
    auto r = __builtin_amdgcn_permlane32_swap(__float_as_uint(v), __float_as_uint(v), false, false);
    return __builtin_fmaxf(__uint_as_float(r[0]), __uint_as_float(r[1]));
}
__device__ __forceinline__ s16x8 lds_b128(unsigned a) { return *(const LDSAS s16x8*)(a); }
__device__ __forceinline__ f32x4 lds_f4(unsigned a) { return *(const LDSAS f32x4*)(a); }
__device__ __forceinline__ s16x8 lds_tr2(unsigned a0, unsigned a1) {
    s16x4 lo = __builtin_amdgcn_ds_read_tr16_b64_v4i16((LDSAS s16x4*)(a0));
    s16x4 hi = __builtin_amdgcn_ds_read_tr16_b64_v4i16((LDSAS s16x4*)(a1));
    return __builtin_shufflevector(lo, hi, 0, 1, 2, 3, 4, 5, 6, 7);
}
__device__ __forceinline__ float bflo(unsigned v) { return __uint_as_float(v << 16); }
__device__ __forceinline__ float bfhi(unsigned v) { return __uint_as_float(v & 0xffff0000u); }
constexpr unsigned ONES2 = 0x3f803f80u;

constexpr unsigned K1_SLOT = 28672, K1_TAB = 5 * K1_SLOT, K1_LDS = K1_TAB + 2048;

__global__ __launch_bounds__(512, 2) void k_qkv(const u16* __restrict__ x, const u16* __restrict__ wqkv, char* __restrict__ ws) {
    extern __shared__ __attribute__((aligned(16))) char smem[];
    const unsigned lds0 = (unsigned)(uintptr_t)(LDSAS char*)smem;
    const int tid = threadIdx.x, lane = tid & 63;
    const int w = __builtin_amdgcn_readfirstlane(tid >> 6);
    const int b = blockIdx.x, xcd = b & 7, bi = b >> 3;
    const int rt = (xcd >> 1) * 4 + (bi & 3), cb = (xcd & 1) * 8 + (bi >> 2);
    const int r32 = lane & 31, h = lane >> 5;
    const unsigned wtr = (8 * h + ((lane & 15) >> 2)) * 192 + ((lane >> 4) & 1) * 32 + (lane & 3) * 8;
    if (b == 0 && tid < 32) *(unsigned*)(ws + WS_CNT + tid * 256) = 0u;
    STAMP(0, 0);

    if (w >= 4) {
        const int hw = w - 4;
        const rsrc_t rx = mk_rsrc(x, S * D * 2), rw = mk_rsrc(wqkv, D * 3 * D * 2);
        unsigned xv[2], wv[3];
#pragma unroll
        for (int i = 0; i < 2; ++i) xv[i] = (lane >> 3) * 1024 + (((lane & 7) ^ ((4 * i + (lane >> 4)) & 7)) << 4);
#pragma unroll
        for (int j = 0; j < 3; ++j) { const unsigned c = (3 * hw + j) * 64 + lane, kr = c / 12, ch = c % 12;
            wv[j] = kr * 3072 + (ch >> 2) * 1024 + (ch & 3) * 16; }
        const unsigned xs = (rt * 128 + hw * 32) * 1024, wsb = cb * 64;
        auto issue = [&](int s) {
            const unsigned slot = lds0 + (s % 5) * K1_SLOT;
#pragma unroll
            for (int i = 0; i < 4; ++i) dma16(rx, xv[i & 1], xs + i * 8192 + s * 128, slot + (4 * hw + i) * 1024);
#pragma unroll
            for (int j = 0; j < 3; ++j) dma16(rw, wv[j], wsb + s * 64 * 3072, slot + 16384 + (3 * hw + j) * 1024);
        };
#pragma unroll
        for (int s = 0; s < 5; ++s) issue(s);
        float cs = 0.f;
        const int ct = hw < 3 ? hw : 2;
#pragma unroll
        for (int s = 0; s < 8; ++s) {
            if (s == 0) BAR_VL(28); else if (s <= 4) BAR_VL(21); else if (s == 5) BAR_VL(14); else if (s == 6) BAR_VL(7); else BAR_VL(0);
            if (s >= 1 && s <= 3) issue(s + 4);
            const unsigned wb = lds0 + (s % 5) * K1_SLOT + 16384 + wtr + ct * 64;
#pragma unroll
            for (int m = 0; m < 4; ++m) {
                const u32x4 f = __builtin_bit_cast(u32x4, lds_tr2(wb + m * 16 * 192, wb + (m * 16 + 4) * 192));
#pragma unroll
                for (int i = 0; i < 4; ++i) cs = dot2(f[i], ONES2, cs);
            }
        }
        cs = swap_add(cs);
        if (hw < 3 && h == 0) *(LDSAS float*)(lds0 + K1_TAB + (hw * 32 + r32) * 4) = cs;
        BAR_L();
        return;
    }

    unsigned xa[4];
#pragma unroll
    for (int m = 0; m < 4; ++m) xa[m] = lds0 + (32 * w + r32) * 128 + (((2 * m + h) ^ ((r32 >> 1) & 7)) << 4);
    const unsigned wa = lds0 + 16384 + wtr;
    f32x16 aq = {}, ak = {}, av = {};
    float sum = 0.f, sq = 0.f;
    s16x8 xf[2], wf[2][3];
    auto rd = [&](int j) {
        const unsigned so = ((j >> 2) % 5) * K1_SLOT; const int m = j & 3;
        xf[j & 1] = lds_b128(xa[m] + so);
#pragma unroll
        for (int n = 0; n < 3; ++n) wf[j & 1][n] = lds_tr2(wa + so + m * 16 * 192 + n * 64, wa + so + (m * 16 + 4) * 192 + n * 64);
    };
    BAR_L();
    STAMP(0, 1);
    rd(0);
#pragma unroll
    for (int j = 0; j < 32; ++j) {
        if (j < 31) { if ((j & 3) == 3) BAR_L(); rd(j + 1); }
        const int c = j & 1;
        aq = MFMA(wf[c][0], xf[c], aq);
        ak = MFMA(wf[c][1], xf[c], ak);
        av = MFMA(xf[c], wf[c][2], av);
        const u32x4 xu = __builtin_bit_cast(u32x4, xf[c]);
#pragma unroll
        for (int i = 0; i < 4; ++i) { sum = dot2(xu[i], ONES2, sum); sq = dot2(xu[i], xu[i], sq); }
    }
    STAMP(0, 2);
    sum = swap_add(sum); sq = swap_add(sq);
    const float mean = sum * (1.f / D);
    const float var = __builtin_fmaxf(sq * (1.f / D) - mean * mean, 0.f);
    const float rstd = __builtin_amdgcn_rsqf(var + LN_EPS);
    const unsigned tab = lds0 + K1_TAB + 512 + w * 256;
    if (h == 0) { *(LDSAS float*)(tab + r32 * 4) = rstd; *(LDSAS float*)(tab + 128 + r32 * 4) = mean; }
    BAR_L();
    const rsrc_t rs = mk_rsrc(ws, WS_END);
    const int tt = rt * 4 + w;
    {
        const float rq = rstd * (0.25f * LOG2E);
        u32x4 o[2];
#pragma unroll
        for (int g = 0; g < 4; ++g) { const f32x4 c = lds_f4(lds0 + K1_TAB + (8 * g + 4 * h) * 4);
            float y[4];
#pragma unroll
            for (int i = 0; i < 4; ++i) y[i] = __builtin_fmaf(-mean, c[i], aq[4 * g + i]) * rq;
            o[g >> 1][(g & 1) * 2] = pk2(y[0], y[1]); o[g >> 1][(g & 1) * 2 + 1] = pk2(y[2], y[3]); }
#pragma unroll
        for (int hh = 0; hh < 2; ++hh) __builtin_amdgcn_raw_buffer_store_b128(o[hh], rs, lane * 16, WS_Q + ((2 * cb + hh) * 64 + tt) * 1024, 0);
#pragma unroll
        for (int g = 0; g < 4; ++g) { const f32x4 c = lds_f4(lds0 + K1_TAB + 128 + (8 * g + 4 * h) * 4);
            float y[4];
#pragma unroll
            for (int i = 0; i < 4; ++i) y[i] = __builtin_fmaf(-mean, c[i], ak[4 * g + i]) * rstd;
            o[g >> 1][(g & 1) * 2] = pk2(y[0], y[1]); o[g >> 1][(g & 1) * 2 + 1] = pk2(y[2], y[3]); }
#pragma unroll
        for (int hh = 0; hh < 2; ++hh)
            __builtin_amdgcn_raw_buffer_store_b128(o[hh], rs, lane * 16, WS_KV + (2 * cb + hh) * KV_HEAD + (tt >> 3) * KV_CHUNK + (tt & 7) * 1024, 0);
    }
    {
        const float c = *(const LDSAS float*)(lds0 + K1_TAB + 256 + r32 * 4);
        u32x4 o[2];
#pragma unroll
        for (int g = 0; g < 4; ++g) { const f32x4 rs4 = lds_f4(tab + (8 * g + 4 * h) * 4), mn4 = lds_f4(tab + 128 + (8 * g + 4 * h) * 4);
            float y[4];
#pragma unroll
            for (int i = 0; i < 4; ++i) y[i] = __builtin_fmaf(-mn4[i], c, av[4 * g + i]) * rs4[i];
            o[g >> 1][(g & 1) * 2] = pk2(y[0], y[1]); o[g >> 1][(g & 1) * 2 + 1] = pk2(y[2], y[3]); }
        const int d = lane & 15, dp = ((d & 11) == 0) ? (d ^ 4) : d;
        const unsigned vo = (r32 >> 4) * KV_HEAD + (16 * h + dp) * 16;
        const unsigned so = WS_KV + 2 * cb * KV_HEAD + (tt >> 3) * KV_CHUNK + 8192 + (tt & 7) * 2 * 544;
        __builtin_amdgcn_raw_buffer_store_b128(o[0], rs, vo, so, 0);
        __builtin_amdgcn_raw_buffer_store_b128(o[1], rs, vo + 544, so, 0);
        if (d < 2) { const u32x4 one = {ONES2, ONES2, ONES2, ONES2};
            __builtin_amdgcn_raw_buffer_store_b128(one, rs, (r32 >> 4) * KV_HEAD + d * 544 + (32 + h) * 16, so, 0); }
    }
    STAMP(0, 3);
    WAITV(0);
    STAMP(0, 4);
}

constexpr unsigned K2_LDS = 163840;
constexpr unsigned L_WO = 0, L_GU = 65536;
constexpr unsigned L_TAB = 61440;
constexpr unsigned SPIN_CAP = 1u << 20;

__device__ __forceinline__ void signal_cnt(char* ws, int idx) {
    __hip_atomic_fetch_add((GAS unsigned*)(ws + WS_CNT + idx * 256), 1u, __ATOMIC_RELAXED, __HIP_MEMORY_SCOPE_AGENT);
}
__device__ __forceinline__ void wait_cnt(char* ws, int idx, unsigned want) {
    for (unsigned spins = 0; spins < SPIN_CAP; ++spins) {
        const unsigned v = __builtin_amdgcn_readfirstlane(__hip_atomic_load((GAS unsigned*)(ws + WS_CNT + idx * 256), __ATOMIC_RELAXED, __HIP_MEMORY_SCOPE_AGENT));
        if (v >= want) break;
        __builtin_amdgcn_s_sleep(1);
    }
    asm volatile("" ::: "memory");
}

__device__ __forceinline__ unsigned w64_voff(int lane) { return (lane >> 3) * 1024 + (((lane & 7) ^ (((lane >> 4) & 1) << 2)) << 4); }
__device__ __forceinline__ unsigned w64_tr(int lane, int n) {
    const int q = (lane & 15) >> 2, p = lane & 3, blk = (lane >> 4) & 1, h = lane >> 5;
    return (4 * h + q) * 128 + (((4 * n + 2 * blk + (p >> 1)) ^ (((q >> 1) & 1) << 2)) << 4) + 8 * (p & 1);
}

template <int PH>
__global__ __launch_bounds__(512, 2) void k_main(const u16* __restrict__ x, const u16* __restrict__ wo, const u16* __restrict__ wg,
                                                 const u16* __restrict__ wu, const u16* __restrict__ wd, u16* __restrict__ out, char* __restrict__ ws) {
    constexpr bool FU = (PH == 15);
    extern __shared__ __attribute__((aligned(16))) char smem[];
    const unsigned lds0 = (unsigned)(uintptr_t)(LDSAS char*)smem;
    const int tid = threadIdx.x, lane = tid & 63;
    const int w = __builtin_amdgcn_readfirstlane(tid >> 6);
    const int b = blockIdx.x, xcd = b & 7, bi = b >> 3;
    const int r32 = lane & 31, h = lane >> 5;
    const rsrc_t rs = mk_rsrc(ws, WS_END);
    const int rt = xcd * 4 + (bi >> 3), ct = bi & 7;
    const int wt = w & 1, kh = (w >> 1) & 1;
    u32x4 hreg[2];
    u32x2 xres[4];

    auto issue_wo = [&](int first, int n) {
        const rsrc_t rwo = mk_rsrc(wo, D * D * 2);
        const unsigned v = w64_voff(lane);
        for (int p = first; p < first + n; ++p) dma16(rwo, v, p * 8192 + ct * 128, lds0 + L_WO + p * 1024);
    };
    auto issue_gu = [&](int mat, int g0, int ng) {
        const rsrc_t rg = mk_rsrc(mat ? wu : wg, D * F * 2);
        unsigned v[3];
#pragma unroll
        for (int j = 0; j < 3; ++j) { const unsigned c = (j * 64 + lane) ^ (mat * 8), kr = c / 6, ch = c % 6; v[j] = kr * (F * 2) + ch * 16; }
        for (int g = g0; g < g0 + ng; ++g)
#pragma unroll
            for (int j = 0; j < 3; ++j) dma16(rg, v[j], g * 32 * (F * 2) + bi * 96, lds0 + L_GU + mat * 49152 + (g * 3 + j) * 1024);
    };
    auto dn_slot = [&](int ks) -> unsigned {
        const int k2 = ks >= 43 ? 1 : 0, i = ks - 43 * k2;
        return i < 15 ? (k2 * 15 + i) * 2048 : i < 39 ? L_GU + (k2 * 24 + i - 15) * 2048 : i < 40 ? L_TAB + k2 * 2048 : (k2 * 15 + i - 40) * 2048;
    };
    auto issue_dn = [&](int i0, int i1, int hw) {
        const rsrc_t rwd = mk_rsrc(wd, F * D * 2);
        const unsigned v = w64_voff(lane);
        const int k2 = hw >> 1, half = hw & 1;
        for (int i = i0; i < i1; ++i) { const int ks = 43 * k2 + i;
            dma16(rwd, v, (ks * 16 + half * 8) * 1024 + ct * 128, lds0 + dn_slot(ks) + half * 1024); }
    };
    auto load_xres = [&]() {
        const rsrc_t rx = mk_rsrc(x, S * D * 2);
        const unsigned v = (rt * 64 + wt * 32 + r32) * 1024 + (ct * 64 + kh * 32 + 4 * h) * 2;
#pragma unroll
        for (int g = 0; g < 4; ++g) xres[g] = __builtin_amdgcn_raw_buffer_load_b64(rx, v + g * 16, 0, 0);
    };

    if constexpr (PH & 1) {
        const int head = xcd * 4 + (bi >> 3), qb = bi & 7, qt = qb * 8 + w;
        const unsigned kvs = WS_KV + head * KV_HEAD;
        STAMP(1, 0);
        dma16(rs, lane * 16, WS_Q + (head * 64 + qt) * 1024, lds0 + KV_HEAD + 1024 + w * 1024);
        if (tid == 0) *(LDSAS unsigned*)(lds0 + KV_HEAD) = 0u;
        if (w < 4) dma16(rs, lane * 16, kvs + (128 + w) * 1024, lds0 + (128 + w) * 1024);
#pragma unroll
        for (int i = 0; i < 16; ++i) dma16(rs, lane * 16, kvs + (i * 8 + w) * 1024, lds0 + (i * 8 + w) * 1024);
        const int d = lane & 15, dp = ((d & 11) == 0) ? (d ^ 4) : d;
        const int grp0 = (r32 >= 20 && r32 < 28);
        unsigned ka = lds0 + lane * 16;
        unsigned va = lds0 + 8192 + (r32 < 16 ? (16 * h + dp) : (grp0 ? 32 : 33)) * 16;
        BAR_VL(13);
        STAMP(1, 1);
        const s16x8 qf = lds_b128(lds0 + KV_HEAD + 1024 + w * 1024 + lane * 16);
        f32x16 negm, sA, sB, o = {};
        {
            const f32x16 z = {};
            const f32x16 s0 = MFMA(lds_b128(ka), qf, z);
            float m = __builtin_fmaxf(s0[0], s0[1]);
#pragma unroll
            for (int i = 2; i < 16; i += 2) m = __builtin_fmaxf(__builtin_fmaxf(m, s0[i]), s0[i + 1]);
            m = swap_max(m);
#pragma unroll
            for (int i = 0; i < 16; ++i) { negm[i] = -m; sA[i] = s0[i] - m; }
            PIN(negm);
        }
        s16x8 kf = lds_b128(ka + 1024), v0, v1 = {};
        u32x4 pA, pB = {0u, 0u, 0u, 0u};
#define EX(X, i) X[i] = __builtin_amdgcn_exp2f(X[i])
#define STEP(C, N, T, KN, LASTQK) do { \
            SBAR(); \
            if (!(LASTQK)) { N = MFMA(kf, qf, negm); } \
            v0 = lds_b128(va + (2 * (T)) * 544); \
            EX(C, 0); EX(C, 1); EX(C, 2); EX(C, 3); pA[0] = pk2(C[0], C[1]); EX(C, 4); EX(C, 5); pA[1] = pk2(C[2], C[3]); PIN(pA); PIN(C); \
            SBAR(); \
            o = MFMA(v1, __builtin_bit_cast(s16x8, pB), o); \
            if (!(LASTQK)) kf = lds_b128(ka + (KN)); \
            EX(C, 6); EX(C, 7); pA[2] = pk2(C[4], C[5]); EX(C, 8); pA[3] = pk2(C[6], C[7]); EX(C, 9); EX(C, 10); PIN(pA); PIN(C); \
            SBAR(); \
            o = MFMA(v0, __builtin_bit_cast(s16x8, pA), o); \
            v1 = lds_b128(va + (2 * (T) + 1) * 544); \
            EX(C, 11); pB[0] = pk2(C[8], C[9]); EX(C, 12); EX(C, 13); pB[1] = pk2(C[10], C[11]); EX(C, 14); EX(C, 15); pB[2] = pk2(C[12], C[13]); \
            PIN(C); pB[3] = pk2(C[14], C[15]); PIN(pB); \
        } while (0)
#define CHUNK(LAST) do { \
            STEP(sA, sB, 0, 2048, false); STEP(sB, sA, 1, 3072, false); STEP(sA, sB, 2, 4096, false); STEP(sB, sA, 3, 5120, false); \
            STEP(sA, sB, 4, 6144, false); STEP(sB, sA, 5, 7168, false); \
            STEP(sA, sB, 6, (LAST) ? 0 : KV_CHUNK, false); STEP(sB, sA, 7, KV_CHUNK + 1024, LAST); \
            ka += KV_CHUNK; va += KV_CHUNK; } while (0)
        CHUNK(false);
        STAMP(1, 2);
        BAR_VL(0);
        STAMP(1, 3);
        for (int c = 1; c < 4; ++c) CHUNK(false);
        if constexpr (FU) { BAR_L(); issue_wo(w * 8, 8); if (w < 4) load_xres(); }
        for (int c = 4; c < 7; ++c) CHUNK(false);
        CHUNK(true);
        SBAR();
        o = MFMA(v1, __builtin_bit_cast(s16x8, pB), o);
        STAMP(1, 4);
#undef STEP
#undef CHUNK
        bool bad = !(o[8] < 1.8446744e19f);
#pragma unroll
        for (int i = 0; i < 8; ++i) bad |= !(__builtin_fabsf(o[i]) < 3.0e38f);
        if constexpr (FU) {
            const unsigned flag = lds0 + KV_HEAD;
            if (__any(bad) && lane == 0) *(LDSAS unsigned*)flag = 1u;
            BAR_L();
            bad = *(const LDSAS unsigned*)flag != 0u;
            if (__builtin_amdgcn_readfirstlane((int)bad)) {
                BAR_VL(0);
                for (int i = 0; i < 9; ++i) { const int p = i * 8 + w; if (p < 66) dma16(rs, lane * 16, kvs + p * 1024, lds0 + p * 1024); }
                BAR_VL(0);
            }
        }
        if (__any(bad)) {
            float m = -__builtin_inff();
            o = f32x16{};
            const f32x16 z = {};
            ka -= 8 * KV_CHUNK; va -= 8 * KV_CHUNK;
            for (int t = 0; t < 64; ++t) {
                const unsigned co = (t >> 3) * KV_CHUNK;
                f32x16 s = MFMA(lds_b128(ka + co + (t & 7) * 1024), qf, z);
                float mx = s[0];
#pragma unroll
                for (int i = 1; i < 16; ++i) mx = __builtin_fmaxf(mx, s[i]);
                mx = __builtin_fmaxf(swap_max(mx), m);
                const float al = __builtin_amdgcn_exp2f(m - mx);
                m = mx;
#pragma unroll
                for (int i = 0; i < 16; ++i) { o[i] *= al; s[i] = __builtin_amdgcn_exp2f(s[i] - m); }
#pragma unroll
                for (int i = 0; i < 4; ++i) { pA[i] = pk2(s[2 * i], s[2 * i + 1]); pB[i] = pk2(s[8 + 2 * i], s[9 + 2 * i]); }
                o = MFMA(lds_b128(va + co + (t & 7) * 1088), __builtin_bit_cast(s16x8, pA), o);
                o = MFMA(lds_b128(va + co + (t & 7) * 1088 + 544), __builtin_bit_cast(s16x8, pB), o);
            }
            if constexpr (FU) { BAR_L(); issue_wo(w * 8, 8); }
        }
        const float inv = __builtin_amdgcn_rcpf(o[8]);
        u32x4 ov;
#pragma unroll
        for (int i = 0; i < 4; ++i) ov[i] = pk2(o[2 * i] * inv, o[2 * i + 1] * inv);
        __builtin_amdgcn_raw_buffer_store_b128(ov, rs, lane * 16, WS_O + (qt * 32 + head) * 1024, AUX_SC1);
        if constexpr (FU) {
            BAR_VL(0);
            STAMP(1, 5);
            if (tid == 0) signal_cnt(ws, qb);
            if (w >= 4 && bi < 29) issue_gu((w - 4) >> 1, (w & 1) * 8, 8);
        }
    }

    if constexpr (PH & 2) {
        STAMP(2, 0);
        if constexpr (!FU) { issue_wo(w * 8, 8); if (w < 4) load_xres(); BAR_VL(0); }
        else { if (w == 0) wait_cnt(ws, xcd, 32); BAR_L(); }
        STAMP(2, 1);
        f32x16 a0 = {}, a1 = {};
        if (w < 4) {
            s16x8 af[16];
            const unsigned so = WS_O + ((rt * 2 + wt) * 32 + kh * 16) * 1024;
#pragma unroll
            for (int i = 0; i < 16; ++i) af[i] = __builtin_bit_cast(s16x8, __builtin_amdgcn_raw_buffer_load_b128(rs, lane * 16 + (i & 3) * 1024, so + (i >> 2) * 4096, AUX_SC1));
            SBAR();
            const unsigned t0 = lds0 + L_WO + kh * 32768 + w64_tr(lane, 0), t1 = lds0 + L_WO + kh * 32768 + w64_tr(lane, 1);
#pragma unroll
            for (int i = 0; i < 16; ++i) {
                a0 = MFMA(lds_tr2(t0 + i * 2048, t0 + i * 2048 + 1024), af[i], a0);
                a1 = MFMA(lds_tr2(t1 + i * 2048, t1 + i * 2048 + 1024), af[i], a1);
            }
        }
        STAMP(2, 2);
        BAR_L();
        if (w < 4) {
            const f32x16& snd = kh ? a0 : a1;
#pragma unroll
            for (int g = 0; g < 4; ++g) *(LDSAS f32x4*)(lds0 + w * 4096 + g * 1024 + lane * 16) = f32x4{snd[4 * g], snd[4 * g + 1], snd[4 * g + 2], snd[4 * g + 3]};
        }
        BAR_L();
        if (w < 4) {
            const f32x16& own = kh ? a1 : a0;
            float sm = 0.f, sq = 0.f;
#pragma unroll
            for (int g = 0; g < 4; ++g) {
                const f32x4 p = lds_f4(lds0 + (w ^ 2) * 4096 + g * 1024 + lane * 16);
                const unsigned u0 = pk2(own[4 * g] + p[0] + bflo(xres[g][0]), own[4 * g + 1] + p[1] + bfhi(xres[g][0]));
                const unsigned u1 = pk2(own[4 * g + 2] + p[2] + bflo(xres[g][1]), own[4 * g + 3] + p[3] + bfhi(xres[g][1]));
                hreg[g >> 1][(g & 1) * 2] = u0; hreg[g >> 1][(g & 1) * 2 + 1] = u1;
                sm = dot2(u0, ONES2, sm); sm = dot2(u1, ONES2, sm); sq = dot2(u0, u0, sq); sq = dot2(u1, u1, sq);
            }
            const unsigned so = WS_H + ((rt * 2 + wt) * 32 + (ct * 2 + kh) * 2) * 1024;
            __builtin_amdgcn_raw_buffer_store_b128(hreg[0], rs, lane * 16, so, AUX_SC1);
            __builtin_amdgcn_raw_buffer_store_b128(hreg[1], rs, lane * 16 + 1024, so, AUX_SC1);
            sm = swap_add(sm); sq = swap_add(sq);
            if (h == 0) __builtin_amdgcn_raw_buffer_store_b64(u32x2{__float_as_uint(sm), __float_as_uint(sq)}, rs, r32 * 8,
                                                              WS_ST + ((rt * 2 + wt) * 16 + ct * 2 + kh) * 256, AUX_SC1);
        }
        STAMP(2, 3);
        if constexpr (FU) {
            BAR_VL(0);
            STAMP(2, 4);
            if (tid == 0) signal_cnt(ws, 8 + xcd);
            if (w >= 4) issue_dn(0, 15, w - 4);
        }
    }

    if constexpr (PH & 4) {
        STAMP(3, 0);
        if constexpr (!FU) { if (bi < 29) issue_gu(w >> 2, (w & 3) * 4, 4); BAR_VL(0); }
        STAMP(3, 1);
        if (bi < 29) {
            unsigned wa[3];
#pragma unroll
            for (int n = 0; n < 3; ++n) { const unsigned blk = (lane >> 4) & 1;
                wa[n] = lds0 + L_GU + blk * 49152 + (((4 * h + ((lane & 15) >> 2)) * 96 + n * 32 + (lane & 3) * 8) ^ (blk * 128)); }
            {
                float cs[3] = {0.f, 0.f, 0.f};
#pragma unroll
                for (int i = 0; i < 4; ++i)
#pragma unroll
                    for (int n = 0; n < 3; ++n) {
                        const unsigned a = wa[n] + (w * 4 + i) * 1536;
                        const u32x4 f = __builtin_bit_cast(u32x4, lds_tr2(a, a + 768));
#pragma unroll
                        for (int e = 0; e < 4; ++e) cs[n] = dot2(f[e], ONES2, cs[n]);
                    }
#pragma unroll
                for (int n = 0; n < 3; ++n) { cs[n] = swap_add(cs[n]); if (h == 0) *(LDSAS float*)(lds0 + L_TAB + (w * 96 + n * 32 + r32) * 4) = cs[n]; }
            }
            BAR_L();
            if (tid < 96) { float t = 0.f;
#pragma unroll
                for (int i = 0; i < 8; ++i) t += *(const LDSAS float*)(lds0 + L_TAB + (i * 96 + tid) * 4);
                *(LDSAS float*)(lds0 + L_TAB + 3072 + tid * 4) = t; }
            if constexpr (FU) { if (w == 0) wait_cnt(ws, 8 + xcd, 32); }
            BAR_L();
            STAMP(3, 2);
            const int tt = xcd * 8 + w;
            s16x8 af[32]; u32x2 st[8];
#pragma unroll
            for (int i = 0; i < 8; ++i) st[i] = __builtin_amdgcn_raw_buffer_load_b64(rs, (8 * h + i) * 256 + r32 * 8, WS_ST + tt * 4096, AUX_SC1);
#pragma unroll
            for (int i = 0; i < 32; ++i) af[i] = __builtin_bit_cast(s16x8, __builtin_amdgcn_raw_buffer_load_b128(rs, lane * 16 + (i & 3) * 1024, WS_H + tt * 32768 + (i >> 2) * 4096, AUX_SC1));
            SBAR();
            f32x16 acc[3] = {};
#pragma unroll
            for (int i = 0; i < 32; ++i) {
                acc[0] = MFMA(lds_tr2(wa[0] + i * 1536, wa[0] + i * 1536 + 768), af[i], acc[0]);
                acc[1] = MFMA(lds_tr2(wa[1] + i * 1536, wa[1] + i * 1536 + 768), af[i], acc[1]);
            }
            STAMP(3, 3);
            float sm = 0.f, sq = 0.f;
#pragma unroll
            for (int i = 0; i < 8; ++i) { sm += __uint_as_float(st[i][0]); sq += __uint_as_float(st[i][1]); }
            sm = swap_add(sm); sq = swap_add(sq);
            const float mean = sm * (1.f / D);
            const float var = __builtin_fmaxf(sq * (1.f / D) - mean * mean, 0.f) + LN_EPS;
            const float rstd = __builtin_amdgcn_rsqf(var), sd = var * rstd, ga = -LOG2E * rstd;
            auto epi = [&](int n) {
                u32x4 ov; float y[8];
#pragma unroll
                for (int g = 0; g < 2; ++g) {
                    const f32x4 cg = lds_f4(lds0 + L_TAB + 3072 + (n * 32 + 8 * g + 4 * h) * 4), cu = lds_f4(lds0 + L_TAB + 3072 + (n * 32 + 16 + 8 * g + 4 * h) * 4);
#pragma unroll
                    for (int i = 0; i < 4; ++i) {
                        const float e = __builtin_amdgcn_exp2f(__builtin_fmaf(-mean, cg[i], acc[n][4 * g + i]) * ga);
                        y[4 * g + i] = __builtin_fmaf(-mean, cu[i], acc[n][8 + 4 * g + i]) * __builtin_amdgcn_rcpf(__builtin_fmaf(e, sd, sd));
                    }
                }
#pragma unroll
                for (int i = 0; i < 4; ++i) ov[i] = pk2(y[2 * i], y[2 * i + 1]);
                const int u = bi * 3 + n;
                if (u < NU) __builtin_amdgcn_raw_buffer_store_b128(ov, rs, lane * 16, WS_HH + (tt * NU + u) * 1024, AUX_SC1);
            };
#pragma unroll
            for (int i = 0; i < 32; ++i) {
                acc[2] = MFMA(lds_tr2(wa[2] + i * 1536, wa[2] + i * 1536 + 768), af[i], acc[2]);
                if (i == 4) epi(0);
                if (i == 18) epi(1);
            }
            epi(2);
        }
        STAMP(3, 4);
        if constexpr (FU) {
            BAR_VL(0);
            STAMP(3, 5);
            if (tid == 0 && bi < 29) signal_cnt(ws, 16 + xcd);
            if (w >= 4) { issue_dn(15, 40, w - 4); WAITV(0); }
        }
    }

    if constexpr (PH & 8) {
        STAMP(4, 0);
        if constexpr (!FU) {
            if (w >= 4) { issue_dn(0, 40, w - 4); WAITV(0); }
            else { const unsigned so = WS_H + ((rt * 2 + wt) * 32 + (ct * 2 + kh) * 2) * 1024;
                hreg[0] = __builtin_amdgcn_raw_buffer_load_b128(rs, lane * 16, so, AUX_SC1);
                hreg[1] = __builtin_amdgcn_raw_buffer_load_b128(rs, lane * 16 + 1024, so, AUX_SC1); }
        } else { if (w == 0) wait_cnt(ws, 16 + xcd, 29); }
        BAR_L();
        STAMP(4, 1);
        f32x16 a0 = {}, a1 = {};
        if (w < 4) {
            s16x8 af[43];
            const unsigned so = WS_HH + ((rt * 2 + wt) * NU + kh * 43) * 1024;
#pragma unroll
            for (int i = 0; i < 43; ++i) af[i] = __builtin_bit_cast(s16x8, __builtin_amdgcn_raw_buffer_load_b128(rs, lane * 16 + (i & 3) * 1024, so + (i >> 2) * 4096, AUX_SC1));
            SBAR();
            const unsigned t0 = lds0 + w64_tr(lane, 0), t1 = lds0 + w64_tr(lane, 1);
            const unsigned bA = kh * 15 * 2048, bB = L_GU + kh * 24 * 2048, bC = L_TAB + kh * 2048;
#pragma unroll
            for (int i = 0; i < 43; ++i) {
                if (i == 3 || i == 40) BAR_L();
                const unsigned o_ = i < 15 ? bA + i * 2048 : i < 39 ? bB + (i - 15) * 2048 : i < 40 ? bC : bA + (i - 40) * 2048;
                a0 = MFMA(lds_tr2(t0 + o_, t0 + o_ + 1024), af[i], a0);
                a1 = MFMA(lds_tr2(t1 + o_, t1 + o_ + 1024), af[i], a1);
            }
        } else {
            BAR_L();
            issue_dn(40, 43, w - 4);
            BAR_VL(0);
        }
        STAMP(4, 2);
        BAR_L();
        if (w < 4) {
            const f32x16& snd = kh ? a0 : a1;
#pragma unroll
            for (int g = 0; g < 4; ++g) *(LDSAS f32x4*)(lds0 + w * 4096 + g * 1024 + lane * 16) = f32x4{snd[4 * g], snd[4 * g + 1], snd[4 * g + 2], snd[4 * g + 3]};
        }
        BAR_L();
        if (w < 4) {
            const f32x16& own = kh ? a1 : a0;
            unsigned y[8];
#pragma unroll
            for (int g = 0; g < 4; ++g) {
                const f32x4 p = lds_f4(lds0 + (w ^ 2) * 4096 + g * 1024 + lane * 16);
                const unsigned h0 = hreg[g >> 1][(g & 1) * 2], h1 = hreg[g >> 1][(g & 1) * 2 + 1];
                y[2 * g] = pk2(own[4 * g] + p[0] + bflo(h0), own[4 * g + 1] + p[1] + bfhi(h0));
                y[2 * g + 1] = pk2(own[4 * g + 2] + p[2] + bflo(h1), own[4 * g + 3] + p[3] + bfhi(h1));
            }
            const rsrc_t ro = mk_rsrc(out, S * D * 2);
            const unsigned vo = (rt * 64 + wt * 32 + r32) * 1024 + (ct * 64 + kh * 32 + 8 * h) * 2;
#pragma unroll
            for (int g = 0; g < 2; ++g) {
                auto e0 = __builtin_amdgcn_permlane32_swap(y[4 * g], y[4 * g + 2], false, false);
                auto e1 = __builtin_amdgcn_permlane32_swap(y[4 * g + 1], y[4 * g + 3], false, false);
                __builtin_amdgcn_raw_buffer_store_b128(u32x4{e0[0], e1[0], e0[1], e1[1]}, ro, vo + g * 32, 0, 0);
            }
        }
        STAMP(4, 3);
        WAITV(0);
        STAMP(4, 4);
    }
}

extern "C" void kernel_launch(void* const* d_in, const int* in_sizes, int n_in,
                              void* d_out, int out_size, void* d_ws, size_t ws_size,
                              hipStream_t stream) {
    const u16* x = (const u16*)d_in[0];
    const u16* wqkv = (const u16*)d_in[1];
    const u16* wo = (const u16*)d_in[2];
    const u16* wg = (const u16*)d_in[4];
    const u16* wu = (const u16*)d_in[5];
    const u16* wd = (const u16*)d_in[6];
    u16* out = (u16*)d_out;
    char* ws = (char*)d_ws;
    (void)hipFuncSetAttribute((const void*)k_qkv, hipFuncAttributeMaxDynamicSharedMemorySize, K1_LDS);
    k_qkv<<<256, 512, K1_LDS, stream>>>(x, wqkv, ws);
    (void)hipFuncSetAttribute((const void*)k_main<15>, hipFuncAttributeMaxDynamicSharedMemorySize, K2_LDS);
    k_main<15><<<256, 512, K2_LDS, stream>>>(x, wo, wg, wu, wd, out, ws);
}
```

```cpp
#include <hip/hip_runtime.h>
#include <hip/hip_bf16.h>
#include <stdint.h>

#define FUSED 1

typedef short s16x8 __attribute__((ext_vector_type(8)));
typedef short s16x4 __attribute__((ext_vector_type(4)));
typedef float f32x16 __attribute__((ext_vector_type(16)));
typedef float f32x4 __attribute__((ext_vector_type(4)));
typedef float f32x2 __attribute__((ext_vector_type(2)));
typedef unsigned u32x4 __attribute__((ext_vector_type(4)));
typedef unsigned u32x2 __attribute__((ext_vector_type(2)));
typedef __bf16 bf16x2 __attribute__((ext_vector_type(2)));
typedef unsigned short u16;
#define LDSAS __attribute__((address_space(3)))
#define GAS __attribute__((address_space(1)))
typedef __amdgpu_buffer_rsrc_t rsrc_t;

constexpr int S = 2048, D = 512, NH = 32, F = 1376, NU = F / 16;
constexpr float LN_EPS = 1e-5f;
constexpr float LOG2E = 1.4426950408889634f;

constexpr unsigned KV_CHUNK = 16384;
constexpr unsigned KV_HEAD = 8 * KV_CHUNK;
constexpr unsigned WS_CNT = 0;
constexpr unsigned WS_Q = 8192;
constexpr unsigned WS_KV = WS_Q + NH * 64 * 1024;
constexpr unsigned WS_O = WS_KV + NH * KV_HEAD;
constexpr unsigned WS_H = WS_O + 64 * 32 * 1024;
constexpr unsigned WS_ST = WS_H + 64 * 32 * 1024;
constexpr unsigned WS_HH = WS_ST + 64 * 16 * 256;
constexpr unsigned WS_END = WS_HH + 64 * NU * 1024;
constexpr int AUX_SC1 = 16;
#define STAMP_DECL
#define FLUSH(k) do {} while (0)
#define STAMP(k, i) do {} while (0)

#define SBAR() __builtin_amdgcn_sched_barrier(0)
#define PIN(x) asm volatile("" : "+v"(x))
#define MFMA(a, b, c) __builtin_amdgcn_mfma_f32_32x32x16_bf16((a), (b), (c), 0, 0, 0)
#define WAITV(n) asm volatile("s_waitcnt vmcnt(" #n ")" ::: "memory")
#define BAR_L() asm volatile("s_waitcnt lgkmcnt(0)\n\ts_barrier" ::: "memory")
#define BAR_0() asm volatile("s_barrier" ::: "memory")
#define BAR_VL(n) asm volatile("s_waitcnt vmcnt(" #n ") lgkmcnt(0)\n\ts_barrier" ::: "memory")

__device__ __forceinline__ rsrc_t mk_rsrc(const void* p, unsigned bytes) {
    return __builtin_amdgcn_make_buffer_rsrc((void*)p, (short)0, (int)bytes, 0x00020000);
}
__device__ __forceinline__ void dma16(rsrc_t r, unsigned voff, unsigned soff, unsigned lds) {
    asm volatile("s_nop 4\n\ts_mov_b32 m0, %0\n\ts_nop 0\n\tbuffer_load_dwordx4 %1, %2, %3 offen lds"
                 :: "s"(__builtin_amdgcn_readfirstlane(lds)), "v"(voff), "s"(r), "s"(__builtin_amdgcn_readfirstlane(soff)) : "m0", "memory");
}
__device__ __forceinline__ unsigned pk2(float lo, float hi) {
    f32x2 v = {lo, hi};
    return __builtin_bit_cast(unsigned, __builtin_convertvector(v, bf16x2));
}
__device__ __forceinline__ float dot2(unsigned a, unsigned b, float c) {
    return __builtin_amdgcn_fdot2_f32_bf16(__builtin_bit_cast(bf16x2, a), __builtin_bit_cast(bf16x2, b), c, false);
}
__device__ __forceinline__ float swap_add(float v) {
    auto r = __builtin_amdgcn_permlane32_swap(__float_as_uint(v), __float_as_uint(v), false, false);
    return __uint_as_float(r[0]) + __uint_as_float(r[1]);
}
__device__ __forceinline__ float swap_max(float v) {
    auto r = __builtin_amdgcn_permlane32_swap(__float_as_uint(v), __float_as_uint(v), false, false);
    return __builtin_fmaxf(__uint_as_float(r[0]), __uint_as_float(r[1]));
}
__device__ __forceinline__ s16x8 lds_b128(unsigned a) { return *(const LDSAS s16x8*)(a); }
__device__ __forceinline__ f32x4 lds_f4(unsigned a) { return *(const LDSAS f32x4*)(a); }
__device__ __forceinline__ s16x8 lds_tr2(unsigned a0, unsigned a1) {
    s16x4 lo = __builtin_amdgcn_ds_read_tr16_b64_v4i16((LDSAS s16x4*)(a0));
    s16x4 hi = __builtin_amdgcn_ds_read_tr16_b64_v4i16((LDSAS s16x4*)(a1));
    return __builtin_shufflevector(lo, hi, 0, 1, 2, 3, 4, 5, 6, 7);
}
__device__ __forceinline__ float bflo(unsigned v) { return __uint_as_float(v << 16); }
__device__ __forceinline__ float bfhi(unsigned v) { return __uint_as_float(v & 0xffff0000u); }
constexpr unsigned ONES2 = 0x3f803f80u;
template <int I> struct ic { static constexpr int value = I; constexpr operator int() const { return I; } };
template <int B, int E, class Fn> __device__ __forceinline__ void sfor(Fn&& fn) { if constexpr (B < E) { fn(ic<B>{}); sfor<B + 1, E>(fn); } }

#define K1_X_AUX ""
#define K1_W_AUX ""
#define K1_ST_AUX 16
constexpr unsigned K1_W = 65536, K1_CS = 147456, K1_ST = K1_CS + 1536, K1_VT = K1_ST + 1024, K1_LDS = K1_VT + 1024;
__host__ __device__ constexpr unsigned k1_slot(int s) { return K1_W + (s % 5) * 16384; }

__global__ __launch_bounds__(512, 2) void k_qkv(const u16* __restrict__ x, const u16* __restrict__ wqkv, char* __restrict__ ws) {
    extern __shared__ __attribute__((aligned(16))) char smem[];
    const unsigned lds0 = (unsigned)(uintptr_t)(LDSAS char*)smem;
    const int tid = threadIdx.x, lane = tid & 63;
    const int w = __builtin_amdgcn_readfirstlane(tid >> 6);
    const int b = blockIdx.x, xcd = b & 7, bi = b >> 3;
    const int rt = (xcd >> 1) * 8 + (bi & 7), cb = (xcd & 1) * 4 + (bi >> 3), rot = bi & 3;
    const int r32 = lane & 31, h = lane >> 5;
    const int q4 = (lane & 15) >> 2, p4 = lane & 3, blk = (lane >> 4) & 1;
    const int wt = w & 1, wc = (w >> 1) & 1;
    STAMP_DECL;
    STAMP(0, 0);

    if (w >= 4) {
        const int hw = w - 4;
        const rsrc_t rx = mk_rsrc(x, S * D * 2), rw = mk_rsrc(wqkv, D * 3 * D * 2);
        unsigned xv[2];
#pragma unroll
        for (int i = 0; i < 2; ++i) xv[i] = (lane >> 3) * 1024 + (((lane & 7) ^ ((4 * i + (lane >> 4)) & 7)) << 4);
        const unsigned wv = (lane >> 3) * 3072 + (((lane & 7) ^ (((lane >> 4) & 1) << 2)) << 4);
#pragma unroll
        for (int i = 0; i < 16; ++i) { const int sg = i & 7, pr = 2 * hw + (i >> 3);
            asm volatile("s_mov_b32 m0, %0\n\ts_nop 0\n\tbuffer_load_dwordx4 %1, %2, %3 offen " K1_X_AUX " lds"
                         :: "s"(lds0 + sg * 8192 + pr * 1024), "v"(xv[pr & 1]), "s"(rx), "s"((rt * 64 + pr * 8) * 1024 + sg * 128) : "m0", "memory"); }
        auto issue = [&](int s) {
            const unsigned so = ((((s & 3) + rot) & 3) * 128 + hw * 32) * 3072 + (s >> 2) * 1024 + cb * 128;
#pragma unroll
            for (int i = 0; i < 4; ++i)
                asm volatile("s_mov_b32 m0, %0\n\ts_nop 0\n\tbuffer_load_dwordx4 %1, %2, %3 offen " K1_W_AUX " lds"
                             :: "s"(lds0 + k1_slot(s) + (4 * hw + i) * 1024), "v"(wv), "s"(rw), "s"(so + i * 8 * 3072) : "m0", "memory");
        };
        issue(0); issue(1);
        BAR_VL(8);
        {
            float sum = 0.f, sq = 0.f;
            u32x4 f[16];
#pragma unroll
            for (int j = 0; j < 16; ++j)
                f[j] = __builtin_bit_cast(u32x4, lds_b128(lds0 + (wc * 4 + (j >> 2)) * 8192 + (32 * wt + r32) * 128 + (((2 * (j & 3) + h) ^ ((r32 >> 1) & 7)) << 4)));
            SBAR();
            issue(2);
            SBAR();
#pragma unroll
            for (int j = 0; j < 16; ++j)
#pragma unroll
                for (int i = 0; i < 4; ++i) { sum = dot2(f[j][i], ONES2, sum); sq = dot2(f[j][i], f[j][i], sq); }
            sum = swap_add(sum); sq = swap_add(sq);
            if (h == 0) *(LDSAS f32x2*)(lds0 + K1_ST + (wc * 64 + wt * 32 + r32) * 8) = f32x2{sum, sq};
        }
        f32x4 cs4 = {};
        const s16x4 one4 = {0x3f80, 0x3f80, 0x3f80, 0x3f80};
        const unsigned tr = (8 * h + q4) * 128 + (((4 * wt + 2 * blk + (p4 >> 1)) ^ (((q4 >> 1) & 1) << 2)) << 4) + 8 * (p4 & 1) + wc * 8192;
#pragma unroll
        for (int s = 0; s < 12; ++s) {
            if (s <= 9) BAR_VL(8); else if (s == 10) BAR_VL(4); else BAR_VL(0);
            const unsigned wb = lds0 + k1_slot(s) + tr;
            s16x4 f[8];
#pragma unroll
            for (int i = 0; i < 8; ++i) f[i] = __builtin_amdgcn_ds_read_tr16_b64_v4i16((LDSAS s16x4*)(wb + (i >> 1) * 2048 + (i & 1) * 512));
            SBAR();
            if (s + 3 < 12) issue(s + 3);
            SBAR();
#pragma unroll
            for (int i = 0; i < 8; ++i) cs4 = __builtin_amdgcn_mfma_f32_4x4x4bf16_1k(f[i], one4, cs4, 0, 0, 0);
            if ((s & 3) == 3) {
                float cs = (lane & 2) ? ((lane & 1) ? cs4[3] : cs4[2]) : ((lane & 1) ? cs4[1] : cs4[0]);
                cs = swap_add(cs);
                if (h == 0) *(LDSAS float*)(lds0 + K1_CS + (((s >> 2) * 4 + hw) * 32 + r32) * 4) = cs;
                cs4 = f32x4{};
            }
        }
        if (b == 0 && w == 4 && lane < 32) *(unsigned*)(ws + WS_CNT + lane * 256) = 0u;
        BAR_L();
        return;
    }

    typedef unsigned u32x16 __attribute__((ext_vector_type(16)));
    u32x16 xa0, xa1, xa2, xa3, xa4, xa5, xa6, xa7;
    u32x4 w0, w1, w2, w3, w4;
    f32x16 acA = {}, acB = {};
    unsigned xb[4];
#pragma unroll
    for (int m = 0; m < 4; ++m) xb[m] = lds0 + (32 * wt + r32) * 128 + (((2 * m + h) ^ ((r32 >> 1) & 7)) << 4);
    const unsigned trn = lds0 + (8 * h + q4) * 128 + (((4 * wc + 2 * blk + (p4 >> 1)) ^ (((q4 >> 1) & 1) << 2)) << 4) + 8 * (p4 & 1);
    const unsigned tB = trn + 65536, tC = trn + 131072;
    const rsrc_t rs = mk_rsrc(ws, WS_END);
    const int tt = rt * 2 + wt, hd = cb * 4 + wc * 2;
    const unsigned vtab = lds0 + K1_VT + w * 256;
    BAR_L();
    STAMP(0, 1);
#define KXR(R, m, O) "ds_read_b128 v[" #R ":" #R "+3], %[x" #m "] offset:" #O "\n\t"
#define KXQ(R) KXR(R, 0, 0) KXR(R + 4, 1, 0) KXR(R + 8, 2, 0) KXR(R + 12, 3, 0) KXR(R + 16, 0, 8192) KXR(R + 20, 1, 8192) KXR(R + 24, 2, 8192) KXR(R + 28, 3, 8192)
#define KXOPS(q) [x0] "v"(xb[0] + ((q + rot) & 3) * 16384), [x1] "v"(xb[1] + ((q + rot) & 3) * 16384), [x2] "v"(xb[2] + ((q + rot) & 3) * 16384), [x3] "v"(xb[3] + ((q + rot) & 3) * 16384)
    asm volatile(KXQ(128) : "={v[128:143]}"(xa0), "={v[144:159]}"(xa1) : KXOPS(0) : "memory");
    asm volatile(KXQ(160) : "={v[160:175]}"(xa2), "={v[176:191]}"(xa3) : KXOPS(1) : "memory");
    asm volatile(KXQ(192) : "={v[192:207]}"(xa4), "={v[208:223]}"(xa5) : KXOPS(2) : "memory");
    asm volatile(KXQ(224) : "={v[224:239]}"(xa6), "={v[240:255]}"(xa7) : KXOPS(3) : "memory");
#define KWR(R, BASE, O) "ds_read_b64_tr_b16 v[" #R ":" #R "+1], " BASE " offset:" #O "\n\tds_read_b64_tr_b16 v[" #R "+2:" #R "+3], " BASE " offset:" #O "+512\n\t"
#define KOPS : "+{v[64:79]}"(acA), "+{v[80:95]}"(acB), "+{v[96:99]}"(w0), "+{v[100:103]}"(w1), "+{v[104:107]}"(w2), "+{v[108:111]}"(w3), "+{v[112:115]}"(w4), \
               "+{v[128:143]}"(xa0), "+{v[144:159]}"(xa1), "+{v[160:175]}"(xa2), "+{v[176:191]}"(xa3), "+{v[192:207]}"(xa4), "+{v[208:223]}"(xa5), "+{v[224:239]}"(xa6), "+{v[240:255]}"(xa7) \
             : [tb] "v"(tB), [tc] "v"(tC) : "memory"
    asm volatile("s_barrier\n\t" KWR(96, "%[tb]", 0) KWR(100, "%[tb]", 2048) KWR(104, "%[tb]", 4096) KWR(108, "%[tb]", 6144)
                 : "={v[96:99]}"(w0), "={v[100:103]}"(w1), "={v[104:107]}"(w2), "={v[108:111]}"(w3), "={v[112:115]}"(w4) : [tb] "v"(tB) : "memory");
    float mean = 0.f, rstd = 0.f;
    u32x4 eo[2];
    auto epi_qk = [&](int m, const f32x16& acc, int g) {
        const float sc = m == 0 ? rstd * (0.25f * LOG2E) : rstd;
        const unsigned ca = lds0 + K1_CS + ((m * 4 + wc) * 32 + 8 * g + 4 * h) * 4;
        const f32x4 c = lds_f4(ca) + lds_f4(ca + 256);
        float y[4];
#pragma unroll
        for (int i = 0; i < 4; ++i) y[i] = __builtin_fmaf(-mean, c[i], acc[4 * g + i]) * sc;
        eo[g >> 1][(g & 1) * 2] = pk2(y[0], y[1]); eo[g >> 1][(g & 1) * 2 + 1] = pk2(y[2], y[3]);
        if (g & 1) __builtin_amdgcn_raw_buffer_store_b128(eo[g >> 1], rs, lane * 16, m == 0 ? WS_Q + ((hd + (g >> 1)) * 64 + tt) * 1024
                                                          : WS_KV + (hd + (g >> 1)) * KV_HEAD + (tt >> 3) * KV_CHUNK + (tt & 7) * 1024, K1_ST_AUX);
    };
    sfor<0, 96>([&](auto G) {
        constexpr int g = G.value, n = g + 4, m = g >> 5;
        constexpr unsigned o = k1_slot(n >> 3) + (n & 7) * 2048, oi = o < 131072 ? o - 65536 : o - 131072;
        constexpr int aR = m == 1 ? 80 : 64, wR = 96 + 4 * (g % 5), nR = 96 + 4 * (n % 5), xR = 128 + 4 * (g & 31);
        asm volatile(".if %c[bar]\n\ts_barrier\n\t.endif\n\t"
                     ".if %c[rd] == 1\n\tds_read_b64_tr_b16 v[%c[nr]:%c[nr]+1], %[tb] offset:%c[oi]\n\tds_read_b64_tr_b16 v[%c[nr]+2:%c[nr]+3], %[tb] offset:%c[oi]+512\n\ts_waitcnt lgkmcnt(8)\n\t.endif\n\t"
                     ".if %c[rd] == 2\n\tds_read_b64_tr_b16 v[%c[nr]:%c[nr]+1], %[tc] offset:%c[oi]\n\tds_read_b64_tr_b16 v[%c[nr]+2:%c[nr]+3], %[tc] offset:%c[oi]+512\n\ts_waitcnt lgkmcnt(8)\n\t.endif\n\t"
                     ".if %c[rd] == 0\n\ts_waitcnt lgkmcnt(0)\n\t.endif\n\t"
                     ".if %c[md] == 0\n\tv_mfma_f32_32x32x16_bf16 v[%c[ar]:%c[ar]+15], v[%c[wr]:%c[wr]+3], v[%c[xr]:%c[xr]+3], v[%c[ar]:%c[ar]+15]\n\t.endif\n\t"
                     ".if %c[md] == 1\n\tv_mfma_f32_32x32x16_bf16 v[%c[ar]:%c[ar]+15], v[%c[xr]:%c[xr]+3], v[%c[wr]:%c[wr]+3], v[%c[ar]:%c[ar]+15]\n\t.endif\n\t"
                     ".if %c[md] == 2\n\tv_mfma_f32_32x32x16_bf16 v[%c[ar]:%c[ar]+15], v[%c[xr]:%c[xr]+3], v[%c[wr]:%c[wr]+3], 0\n\t.endif"
                     : "+{v[64:79]}"(acA), "+{v[80:95]}"(acB), "+{v[96:99]}"(w0), "+{v[100:103]}"(w1), "+{v[104:107]}"(w2), "+{v[108:111]}"(w3), "+{v[112:115]}"(w4),
                       "+{v[128:143]}"(xa0), "+{v[144:159]}"(xa1), "+{v[160:175]}"(xa2), "+{v[176:191]}"(xa3), "+{v[192:207]}"(xa4), "+{v[208:223]}"(xa5), "+{v[224:239]}"(xa6), "+{v[240:255]}"(xa7)
                     : [tb] "v"(tB), [tc] "v"(tC), [bar] "n"(n < 96 && (n & 7) == 0 ? 1 : 0), [rd] "n"(n >= 96 ? 0 : o < 131072 ? 1 : 2), [oi] "n"(oi), [nr] "n"(nR),
                       [md] "n"(m < 2 ? 0 : g == 64 ? 2 : 1), [ar] "n"(aR), [wr] "n"(wR), [xr] "n"(xR) : "memory");
        if (g == 8) {
            const f32x2 s0 = *(const LDSAS f32x2*)(lds0 + K1_ST + (wt * 32 + r32) * 8), s1 = *(const LDSAS f32x2*)(lds0 + K1_ST + (64 + wt * 32 + r32) * 8);
            mean = (s0[0] + s1[0]) * (1.f / D);
            rstd = __builtin_amdgcn_rsqf(__builtin_fmaxf((s0[1] + s1[1]) * (1.f / D) - mean * mean, 0.f) + LN_EPS);
            if (h == 0) { *(LDSAS float*)(vtab + r32 * 4) = rstd; *(LDSAS float*)(vtab + 128 + r32 * 4) = mean; }
            PIN(mean); PIN(rstd);
        }
        if (g >= 36 && g < 44 && !(g & 1)) epi_qk(0, acA, (g - 36) >> 1);
        if (g >= 68 && g < 76 && !(g & 1)) epi_qk(1, acB, (g - 68) >> 1);
    });
    STAMP(0, 2);
    f32x4 rs4[4], mn4[4];
#pragma unroll
    for (int g = 0; g < 4; ++g) { rs4[g] = lds_f4(vtab + (8 * g + 4 * h) * 4); mn4[g] = lds_f4(vtab + 128 + (8 * g + 4 * h) * 4); }
    asm volatile("s_waitcnt lgkmcnt(0)\n\ts_barrier\n\ts_nop 7" : "+{v[64:79]}"(acA) :: "memory");
    {
        const unsigned ca = lds0 + K1_CS + ((8 + wc) * 32 + r32) * 4;
        const float c = *(const LDSAS float*)(ca) + *(const LDSAS float*)(ca + 256);
        u32x4 o[2];
#pragma unroll
        for (int g = 0; g < 4; ++g) {
            float y[4];
#pragma unroll
            for (int i = 0; i < 4; ++i) y[i] = __builtin_fmaf(-mn4[g][i], c, acA[4 * g + i]) * rs4[g][i];
            o[g >> 1][(g & 1) * 2] = pk2(y[0], y[1]); o[g >> 1][(g & 1) * 2 + 1] = pk2(y[2], y[3]); }
        const int d = lane & 15;
        const unsigned vo = (r32 >> 4) * KV_HEAD + (16 * h + d) * 16;
        const unsigned so = WS_KV + hd * KV_HEAD + (tt >> 3) * KV_CHUNK + 8192 + (tt & 7) * 1024;
        __builtin_amdgcn_raw_buffer_store_b128(o[0], rs, vo, so, K1_ST_AUX);
        __builtin_amdgcn_raw_buffer_store_b128(o[1], rs, vo + 512, so, K1_ST_AUX);
    }
    STAMP(0, 3);
    FLUSH(0);
}

constexpr unsigned K2_LDS = 163840;
constexpr unsigned L_WO = 0, L_GU = 65536;
constexpr unsigned L_TAB = 61440;
constexpr unsigned L_FLAG = 12288;
constexpr unsigned SPIN_CAP = 1u << 20;
#define NOPF 0
#define GU_AW 7
#define DN_AW 13

__device__ __forceinline__ void signal_cnt(char* ws, int idx) {
    __hip_atomic_fetch_add((GAS unsigned*)(ws + WS_CNT + idx * 256), 1u, __ATOMIC_RELAXED, __HIP_MEMORY_SCOPE_AGENT);
}
__device__ __forceinline__ void wait_cnt(char* ws, int idx, unsigned want) {
    for (unsigned spins = 0; spins < SPIN_CAP; ++spins) {
        const unsigned v = __builtin_amdgcn_readfirstlane(__hip_atomic_load((GAS unsigned*)(ws + WS_CNT + idx * 256), __ATOMIC_RELAXED, __HIP_MEMORY_SCOPE_AGENT));
        if (v >= want) break;
        __builtin_amdgcn_s_sleep(1);
    }
    asm volatile("" ::: "memory");
}

__device__ __forceinline__ unsigned w64_voff(int lane) { return (lane >> 3) * 1024 + (((lane & 7) ^ (((lane >> 4) & 1) << 2)) << 4); }
__device__ __forceinline__ unsigned w64_tr(int lane, int n) {
    const int q = (lane & 15) >> 2, p = lane & 3, blk = (lane >> 4) & 1, h = lane >> 5;
    return (4 * h + q) * 128 + (((4 * n + 2 * blk + (p >> 1)) ^ (((q >> 1) & 1) << 2)) << 4) + 8 * (p & 1);
}

template <int PH>
__global__ __launch_bounds__(512, 2) void k_main(const u16* __restrict__ x, const u16* __restrict__ wo, const u16* __restrict__ wg,
                                                 const u16* __restrict__ wu, const u16* __restrict__ wd, u16* __restrict__ out, char* __restrict__ ws) {
    constexpr bool FU = (PH == 15);
    extern __shared__ __attribute__((aligned(16))) char smem[];
    const unsigned lds0 = (unsigned)(uintptr_t)(LDSAS char*)smem;
    const int tid = threadIdx.x, lane = tid & 63;
    const int w = __builtin_amdgcn_readfirstlane(tid >> 6);
    const int b = blockIdx.x, xcd = b & 7, bi = b >> 3;
    const int r32 = lane & 31, h = lane >> 5;
    const rsrc_t rs = mk_rsrc(ws, WS_END);
    const int rt = xcd * 4 + (bi >> 3), ct = bi & 7;
    const int wt = w & 1, kh = (w >> 1) & 1;
    u32x4 hreg[2];
    u32x2 xres[4];
    STAMP_DECL;

    auto issue_wo = [&](int first, int n) {
        const rsrc_t rwo = mk_rsrc(wo, D * D * 2);
        const unsigned v = w64_voff(lane);
        for (int p = first; p < first + n; ++p) dma16(rwo, v, p * 8192 + ct * 128, lds0 + L_WO + p * 1024);
    };
    auto issue_gu = [&](int mat, int g0, int ng) {
        const rsrc_t rg = mk_rsrc(mat ? wu : wg, D * F * 2);
        unsigned v[3];
#pragma unroll
        for (int j = 0; j < 3; ++j) { const unsigned c = (j * 64 + lane) ^ (mat * 8), kr = c / 6, ch = c % 6; v[j] = kr * (F * 2) + ch * 16; }
        for (int g = g0; g < g0 + ng; ++g)
#pragma unroll
            for (int j = 0; j < 3; ++j) dma16(rg, v[j], g * 32 * (F * 2) + bi * 96, lds0 + L_GU + mat * 49152 + (g * 3 + j) * 1024);
    };
    auto issue_dn = [&](int i0, int n, unsigned dst, int hw) {
        const rsrc_t rwd = mk_rsrc(wd, F * D * 2);
        const unsigned v = w64_voff(lane);
        const unsigned so = ((43 * (hw >> 1) + i0) * 16 + (hw & 1) * 8) * 1024 + ct * 128;
        for (int i = 0; i < n; ++i) dma16(rwd, v, so + i * 16384, lds0 + dst + (hw & 1) * 1024 + i * 2048);
    };
    auto load_xres = [&]() {
        const rsrc_t rx = mk_rsrc(x, S * D * 2);
        const unsigned v = (rt * 64 + wt * 32 + r32) * 1024 + (ct * 64 + kh * 32 + 4 * h) * 2;
#pragma unroll
        for (int g = 0; g < 4; ++g) xres[g] = __builtin_amdgcn_raw_buffer_load_b64(rx, v + g * 16, 0, 0);
    };

    auto gu_wa = [&](int n) -> unsigned { const unsigned blk = (lane >> 4) & 1;
        return lds0 + L_GU + blk * 49152 + (((4 * h + ((lane & 15) >> 2)) * 96 + n * 32 + (lane & 3) * 8) ^ (blk * 128)); };
    float cs[3] = {0.f, 0.f, 0.f};
    auto gu_colsum = [&]() {
        u32x4 f[3][4];
#pragma unroll
        for (int n = 0; n < 3; ++n) {
            const unsigned a0 = gu_wa(n) + w * 4 * 1536;
#pragma unroll
            for (int i = 0; i < 4; ++i) f[n][i] = __builtin_bit_cast(u32x4, lds_tr2(a0 + i * 1536, a0 + i * 1536 + 768));
        }
        SBAR();
#pragma unroll
        for (int i = 0; i < 4; ++i)
#pragma unroll
            for (int e = 0; e < 4; ++e)
#pragma unroll
                for (int n = 0; n < 3; ++n) cs[n] = dot2(f[n][i][e], ONES2, cs[n]);
#pragma unroll
        for (int n = 0; n < 3; ++n) cs[n] = swap_add(cs[n]);
    };

    if constexpr (PH & 1) {
        const int head = xcd * 4 + (bi >> 3), qb = bi & 7, wq = w & 3, g = w >> 2, qt = qb * 8 + wq * 2;
        const unsigned kvs = WS_KV + head * KV_HEAD;
        const unsigned l16 = lane * 16;
        STAMP(1, 0);
        s16x8 q0, q1;
        asm volatile("s_nop 4\n\tbuffer_load_dwordx4 v[128:131], %2, %3, %4 offen\n\tbuffer_load_dwordx4 v[156:159], %2, %3, %4 offen offset:1024"
                     : "={v[128:131]}"(q0), "={v[156:159]}"(q1) : "v"(l16), "s"(rs), "s"(WS_Q + (head * 64 + qt) * 1024) : "memory");
        unsigned ks = kvs + g * 4 * KV_CHUNK + wq * 4096, km = lds0 + g * 32768 + wq * 4096;
#pragma unroll
        for (int j = 0; j < 4; ++j) dma16(rs, l16, ks + j * 1024, km + j * 1024);
        const rsrc_t rg = mk_rsrc(g ? wu : wg, (FU && bi < 29) ? D * F * 2 : 0), rwd = mk_rsrc(wd, (FU && !NOPF) ? F * D * 2 : 0), rwo = mk_rsrc(wo, (FU && !NOPF) ? D * D * 2 : 0);
        unsigned gv0, gv1, gv2;
        { unsigned c = lane ^ (g * 8); gv0 = (c / 6) * (F * 2) + (c % 6) * 16; c = (64 + lane) ^ (g * 8); gv1 = (c / 6) * (F * 2) + (c % 6) * 16;
          c = (128 + lane) ^ (g * 8); gv2 = (c / 6) * (F * 2) + (c % 6) * 16; }
        unsigned gs = wq * 4 * 32 * (F * 2) + bi * 96, gm = lds0 + L_GU + g * 49152 + wq * 12288;
        const unsigned pvd = lane < 43 ? (bi * 344 + w * 43 + lane) * 128 : 0x80000000u, pvo = lane < 16 ? (bi * 128 + w * 16 + lane) * 128 : 0x80000000u;
        const unsigned kb = lds0 + g * 32768 + l16, vb = lds0 + g * 32768 + 8192 + (16 * h + (r32 & 15)) * 16;
        const unsigned cst = (r32 & 27) == 16 ? ONES2 : 0u;
        const unsigned long long msk = 0x0000ffff0000ffffull;
        BAR_VL(0);
        STAMP(1, 1);
#pragma unroll
        for (int j = 0; j < 4; ++j) dma16(rs, l16, ks + KV_CHUNK + j * 1024, km + 16384 + j * 1024);
        ks += 2 * KV_CHUNK;
        asm volatile("" : "+{v[128:131]}"(q0), "+{v[156:159]}"(q1));
        f32x16 n0, n1, sA, sB = {}, o0 = {}, o1 = {};
        s16x8 kf = lds_b128(kb), v0, v1, v1b;
        {
            const f32x16 z = {};
            const s16x8 kz = lds_b128(lds0 + l16);
            const u32x4 vv = __builtin_bit_cast(u32x4, lds_b128(vb)), cc = {cst, cst, cst, cst};
            const f32x16 s0 = MFMA(kz, q0, z), s1 = MFMA(kz, q1, z);
            float m0 = __builtin_fmaxf(s0[0], s0[1]), m1 = __builtin_fmaxf(s1[0], s1[1]);
#pragma unroll
            for (int i = 2; i < 16; i += 2) { m0 = __builtin_fmaxf(__builtin_fmaxf(m0, s0[i]), s0[i + 1]); m1 = __builtin_fmaxf(__builtin_fmaxf(m1, s1[i]), s1[i + 1]); }
            m0 = swap_max(m0); m1 = swap_max(m1);
#pragma unroll
            for (int i = 0; i < 16; ++i) { n0[i] = -m0; n1[i] = -m1; }
            sA = MFMA(kf, q0, n0);
            v0 = __builtin_bit_cast(s16x8, r32 < 16 ? vv : cc); v1 = v1b = __builtin_bit_cast(s16x8, cc);
        }
        u32x4 pA = {0u, 0u, 0u, 0u}, pB = {0u, 0u, 0u, 0u};
#define XE(C, i) "v_exp_f32 v[" #C "+" #i "], v[" #C "+" #i "]\n\t"
#define XC(P, C, i) "v_cvt_pk_bf16_f32 v" #P ", v[" #C "+" #i "], v[" #C "+" #i "+1]\n\t"
#define XG1(C) XE(C, 0) XE(C, 1) XE(C, 2) XE(C, 3) XC(148, C, 0) XE(C, 4) XE(C, 5) XC(149, C, 2)
#define XG2(C) XE(C, 6) XE(C, 7) XC(150, C, 4) XE(C, 8) XC(151, C, 6) XE(C, 9) XE(C, 10)
#define XG3(C) XE(C, 11) XC(152, C, 8) XE(C, 12) XE(C, 13) XC(153, C, 10) XE(C, 14) XE(C, 15) XC(154, C, 12) XC(155, C, 14)
#define XM(D, A, B, C) "v_mfma_f32_32x32x16_bf16 v[" #D ":" #D "+15], v[" #A ":" #A "+3], v[" #B ":" #B "+3], v[" #C ":" #C "+15]\n\t"
#define XRK(BASE, O) "ds_read_b128 v[132:135], " BASE " offset:" #O "\n\t"
#define XRV(R, BASE, O) "s_mov_b64 exec, %[msk]\n\tds_read_b128 v[" #R ":" #R "+3], " BASE " offset:" #O "\n\ts_mov_b64 exec, -1\n\t"
#define XW(n) "s_waitcnt lgkmcnt(" #n ")\n\t"
#define XTILE(VA, RK, RV1, WA, X1, VB, X2, RV0) \
        XM(80, 132, 156, 176) RK XG1(64) XM(160, VA, 152, 160) RV1 XG2(64) XW(WA) XM(96, 140, 148, 96) XG3(64) \
        XW(0) XM(64, 132, 128, 112) X1 XG1(80) XM(96, VB, 152, 96) X2 XG2(80) XM(160, 140, 148, 160) RV0 XG3(80)
#define XKV "s_mov_b32 m0, %[km]\n\ts_add_u32 %[km], %[km], 0x400\n\tbuffer_load_dwordx4 %[l16], %[rs], %[ks] offen lds\n\ts_add_u32 %[ks], %[ks], 0x400\n\t"
#define XGU(j) "s_mov_b32 m0, %[gm]\n\ts_add_u32 %[gm], %[gm], 0x400\n\tbuffer_load_dwordx4 %[gv" #j "], %[rg], %[gs] offen lds\n\t"
#define XGU2 XGU(2) "s_add_u32 %[gs], %[gs], 88064\n\t"
#define XPF "s_mov_b32 m0, %[pm]\n\ts_nop 0\n\tbuffer_load_dword %[pvd], %[rwd], 0 offen lds\n\tbuffer_load_dword %[pvo], %[rwo], 0 offen lds\n\t"
#define AOPS : "+{v[64:79]}"(sA), "+{v[80:95]}"(sB), "+{v[96:111]}"(o0), "+{v[160:175]}"(o1), "+{v[112:127]}"(n0), "+{v[176:191]}"(n1), "+{v[128:131]}"(q0), "+{v[156:159]}"(q1), \
               "+{v[132:135]}"(kf), "+{v[140:143]}"(v0), "+{v[144:147]}"(v1), "+{v[136:139]}"(v1b), "+{v[148:151]}"(pA), "+{v[152:155]}"(pB), \
               [km] "+s"(km), [ks] "+s"(ks), [gm] "+s"(gm), [gs] "+s"(gs) \
             : [ka] "v"(ka), [va] "v"(va), [kn] "v"(kn), [vn] "v"(vn), [msk] "s"(msk), [l16] "v"(l16), [rs] "s"(rs), [rg] "s"(rg), [gv0] "v"(gv0), [gv1] "v"(gv1), [gv2] "v"(gv2), \
               [pvd] "v"(pvd), [pvo] "v"(pvo), [pm] "s"(lds0 + L_FLAG + 1024), [rwd] "s"(rwd), [rwo] "s"(rwo) : "m0", "scc", "memory"
#define XROUND(A1, A2, B1, B2, C1, D1, E1, F1) asm volatile( \
            XTILE(136, XRK("%[ka]", 1024), XRV(144, "%[va]", 512), 2, A1, 144, A2, XRV(140, "%[va]", 1024)) \
            XTILE(144, XRK("%[ka]", 2048), XRV(144, "%[va]", 1536), 2, B1, 144, B2, XRV(140, "%[va]", 2048)) \
            XTILE(144, XRK("%[ka]", 3072), XRV(144, "%[va]", 2560), 2, C1, 144, "", XRV(140, "%[va]", 3072)) \
            XTILE(144, XRK("%[ka]", 4096), XRV(144, "%[va]", 3584), 2, D1, 144, "", XRV(140, "%[va]", 4096)) \
            XTILE(144, XRK("%[ka]", 5120), XRV(144, "%[va]", 4608), 2, E1, 144, "", XRV(140, "%[va]", 5120)) \
            XTILE(144, XRK("%[ka]", 6144), XRV(144, "%[va]", 5632), 2, F1, 144, "", XRV(140, "%[va]", 6144)) \
            XTILE(144, XRK("%[ka]", 7168), XRV(144, "%[va]", 6656), 2, XRV(136, "%[va]", 7680), 144, "", XRV(140, "%[va]", 7168)) AOPS)
#define XLAST(X1, X2) asm volatile(XTILE(144, XRK("%[kn]", 0), "", 1, X1, 136, X2, XRV(140, "%[vn]", 0)) AOPS)
        unsigned ka = kb, va = vb, kn = kb + 16384, vn = vb + 16384;
        asm volatile("s_nop 15" AOPS);
        XROUND(XGU(0), "", XGU(1), "", XGU2, XGU(0), XGU(1), XGU2);
        BAR_VL(6);
        XLAST(XKV, XKV);
        ka = kb + 16384; va = vb + 16384; kn = kb; vn = vb;
        XROUND(XKV, XKV, XGU(0), XGU(1), XGU2, XGU(0), XGU(1), XGU2);
        BAR_VL(6);
        km += 16384 - 4096; ks += KV_CHUNK - 4096;
        XLAST(XKV, XKV);
        ka = kb; va = vb; kn = kb + 16384; vn = vb + 16384;
        XROUND(XKV, XKV, "", "", "", "", "", "");
        BAR_VL(0);
        XLAST(XPF, "");
        ka = kb + 16384; va = vb + 16384; kn = kb; vn = vb;
        XROUND("", "", "", "", "", "", "", "");
        XLAST("", "");
        asm volatile(XM(160, 136, 152, 160) "s_waitcnt lgkmcnt(0)\n\ts_nop 15" AOPS);
        STAMP(1, 4);
        f32x16 o;
        {
            const f32x16& snd = g ? o0 : o1;
            const unsigned xa = lds0 + g * 32768 + wq * 3072 + l16, xb = lds0 + (g ^ 1) * 32768 + wq * 3072 + l16;
            *(LDSAS f32x4*)(xa) = f32x4{snd[0], snd[1], snd[2], snd[3]};
            *(LDSAS f32x4*)(xa + 1024) = f32x4{snd[4], snd[5], snd[6], snd[7]};
            *(LDSAS float*)(xa + 2048) = snd[8];
            if (tid == 0) *(LDSAS unsigned*)(lds0 + L_FLAG) = 0u;
            BAR_L();
            const f32x4 p0 = lds_f4(xb), p1 = lds_f4(xb + 1024); const float pl = *(const LDSAS float*)(xb + 2048);
            o = g ? o1 : o0;
#pragma unroll
            for (int i = 0; i < 4; ++i) { o[i] += p0[i]; o[4 + i] += p1[i]; }
            o[8] += pl;
        }
        bool bad = !(o[8] < 1.8446744e19f);
#pragma unroll
        for (int i = 0; i < 8; ++i) bad |= !(__builtin_fabsf(o[i]) < 3.0e38f);
        if (__any(bad) && lane == 0) *(LDSAS unsigned*)(lds0 + L_FLAG) = 1u;
        auto store_o = [&]() {
            const float inv = __builtin_amdgcn_rcpf(o[8]);
            u32x4 ov;
#pragma unroll
            for (int i = 0; i < 4; ++i) ov[i] = pk2(o[2 * i] * inv, o[2 * i + 1] * inv);
            __builtin_amdgcn_raw_buffer_store_b128(ov, rs, l16, WS_O + ((qt + g) * 32 + head) * 1024, AUX_SC1);
        };
        store_o();
        BAR_VL(0);
        if (__builtin_amdgcn_readfirstlane(*(const LDSAS unsigned*)(lds0 + L_FLAG))) {
            const s16x8 qf = g ? q1 : q0;
            const u32x4 cc = {cst, cst, cst, cst};
            const unsigned vf = lds0 + 16384 + 8192 + (16 * h + (r32 & 15)) * 16;
            float m = -__builtin_inff();
            o = f32x16{};
            const f32x16 z = {};
#pragma unroll 1
            for (int c = 0; c < 8; ++c) {
                dma16(rs, l16, kvs + c * KV_CHUNK + w * 2048, lds0 + 16384 + w * 2048);
                dma16(rs, l16, kvs + c * KV_CHUNK + w * 2048 + 1024, lds0 + 16384 + w * 2048 + 1024);
                BAR_VL(0);
#pragma unroll 1
                for (int t = 0; t < 8; ++t) {
                    f32x16 s = MFMA(lds_b128(lds0 + 16384 + l16 + t * 1024), qf, z);
                    float mx = s[0];
#pragma unroll
                    for (int i = 1; i < 16; ++i) mx = __builtin_fmaxf(mx, s[i]);
                    mx = __builtin_fmaxf(swap_max(mx), m);
                    const float al = __builtin_amdgcn_exp2f(m - mx);
                    m = mx;
#pragma unroll
                    for (int i = 0; i < 16; ++i) { o[i] *= al; s[i] = __builtin_amdgcn_exp2f(s[i] - m); }
#pragma unroll
                    for (int i = 0; i < 4; ++i) { pA[i] = pk2(s[2 * i], s[2 * i + 1]); pB[i] = pk2(s[8 + 2 * i], s[9 + 2 * i]); }
                    const u32x4 a = __builtin_bit_cast(u32x4, lds_b128(vf + t * 1024)), c2 = __builtin_bit_cast(u32x4, lds_b128(vf + t * 1024 + 512));
                    o = MFMA(__builtin_bit_cast(s16x8, r32 < 16 ? a : cc), __builtin_bit_cast(s16x8, pA), o);
                    o = MFMA(__builtin_bit_cast(s16x8, r32 < 16 ? c2 : cc), __builtin_bit_cast(s16x8, pB), o);
                }
                BAR_L();
            }
            store_o();
            BAR_VL(0);
        }
        STAMP(1, 5);
        if constexpr (FU) {
            if (tid == 0) signal_cnt(ws, qb);
            BAR_L();
            if (w >= 4) issue_wo((w - 4) * 16, 16); else load_xres();
            STAMP(1, 6);
            if (bi < 29) gu_colsum();
        }
        FLUSH(1);
    }

    if constexpr (PH & 2) {
        STAMP(2, 0);
        if constexpr (!FU) { issue_wo(w * 8, 8); if (w < 4) load_xres(); BAR_VL(0); }
        else { if (w == 4) wait_cnt(ws, xcd, 32); BAR_VL(0); }
        STAMP(2, 1);
        f32x16 a0, a1 = {};
        if (w < 4) {
            constexpr int AW = 12;
            s16x8 af[AW];
            const unsigned so = WS_O + ((rt * 2 + wt) * 32 + kh * 16) * 1024;
            auto ld = [&](int i) { af[i % AW] = __builtin_bit_cast(s16x8, __builtin_amdgcn_raw_buffer_load_b128(rs, lane * 16 + (i & 3) * 1024, so + (i >> 2) * 4096, AUX_SC1)); };
#pragma unroll
            for (int i = 0; i < AW - 1; ++i) ld(i);
            SBAR();
            unsigned t0 = lds0 + L_WO + kh * 32768 + w64_tr(lane, kh), t1 = lds0 + L_WO + kh * 32768 + w64_tr(lane, kh ^ 1);
#pragma unroll
            for (int g = 0; g < 4; ++g) { a0[4 * g] = bflo(xres[g][0]); a0[4 * g + 1] = bfhi(xres[g][0]); a0[4 * g + 2] = bflo(xres[g][1]); a0[4 * g + 3] = bfhi(xres[g][1]); }
            PIN(t0); PIN(t1);
            s16x8 wf[3][2];
            auto rd = [&](int i) { wf[i % 3][0] = lds_tr2(t0 + i * 2048, t0 + i * 2048 + 1024); wf[i % 3][1] = lds_tr2(t1 + i * 2048, t1 + i * 2048 + 1024); };
            rd(0); rd(1);
#pragma unroll
            for (int i = 0; i < 16; ++i) {
                if (i + AW - 1 < 16) ld(i + AW - 1);
                if (i + 2 < 16) rd(i + 2);
                SBAR();
                a0 = MFMA(wf[i % 3][0], af[i % AW], a0);
                a1 = MFMA(wf[i % 3][1], af[i % AW], a1);
                SBAR();
            }
        }
        STAMP(2, 2);
        BAR_L();
        if (w < 4) {
#pragma unroll
            for (int g = 0; g < 4; ++g) *(LDSAS f32x4*)(lds0 + w * 4096 + g * 1024 + lane * 16) = f32x4{a1[4 * g], a1[4 * g + 1], a1[4 * g + 2], a1[4 * g + 3]};
        }
        BAR_L();
        if (w < 4) {
            f32x4 p[4];
#pragma unroll
            for (int g = 0; g < 4; ++g) p[g] = lds_f4(lds0 + (w ^ 2) * 4096 + g * 1024 + lane * 16);
            SBAR();
#pragma unroll
            for (int g = 0; g < 4; ++g) {
                hreg[g >> 1][(g & 1) * 2] = pk2(a0[4 * g] + p[g][0], a0[4 * g + 1] + p[g][1]);
                hreg[g >> 1][(g & 1) * 2 + 1] = pk2(a0[4 * g + 2] + p[g][2], a0[4 * g + 3] + p[g][3]);
            }
            const unsigned so = WS_H + ((rt * 2 + wt) * 32 + (ct * 2 + kh) * 2) * 1024;
            __builtin_amdgcn_raw_buffer_store_b128(hreg[0], rs, lane * 16, so, AUX_SC1);
            __builtin_amdgcn_raw_buffer_store_b128(hreg[1], rs, lane * 16 + 1024, so, AUX_SC1);
            SBAR();
            float sm = 0.f, sq = 0.f;
#pragma unroll
            for (int i = 0; i < 8; ++i) { sm = dot2(hreg[i >> 2][i & 3], ONES2, sm); sq = dot2(hreg[i >> 2][i & 3], hreg[i >> 2][i & 3], sq); }
            sm = swap_add(sm); sq = swap_add(sq);
            if (h == 0) __builtin_amdgcn_raw_buffer_store_b64(u32x2{__float_as_uint(sm), __float_as_uint(sq)}, rs, r32 * 8,
                                                              WS_ST + ((rt * 2 + wt) * 16 + ct * 2 + kh) * 256, AUX_SC1);
        }
        STAMP(2, 3);
        if constexpr (FU) {
            BAR_VL(0);
            STAMP(2, 4);
            if (tid == 0) signal_cnt(ws, 8 + xcd);
            if (w >= 4) issue_dn(0, 15, ((w - 4) >> 1) * 15 * 2048, w - 4);
            STAMP(2, 5);
        }
        FLUSH(2);
    }

    if constexpr (PH & 4) {
        STAMP(3, 0);
        if constexpr (!FU) { if (bi < 29) issue_gu(w >> 2, (w & 3) * 4, 4); BAR_VL(0); if (bi < 29) gu_colsum(); }
        STAMP(3, 1);
        if (bi < 29) {
            unsigned wa[3];
#pragma unroll
            for (int n = 0; n < 3; ++n) { wa[n] = gu_wa(n); PIN(wa[n]); if (h == 0) *(LDSAS float*)(lds0 + L_TAB + (w * 96 + n * 32 + r32) * 4) = cs[n]; }
            BAR_L();
            if (tid < 96) { float t = 0.f;
#pragma unroll
                for (int i = 0; i < 8; ++i) t += *(const LDSAS float*)(lds0 + L_TAB + (i * 96 + tid) * 4);
                *(LDSAS float*)(lds0 + L_TAB + 3072 + tid * 4) = t; }
            if constexpr (FU) { if (w == 4) wait_cnt(ws, 8 + xcd, 32); }
            BAR_L();
            STAMP(3, 2);
            const int tt = xcd * 8 + w;
            constexpr int AW = GU_AW;
            s16x8 af[AW]; u32x2 st[8];
            auto ld = [&](int i) { af[i % AW] = __builtin_bit_cast(s16x8, __builtin_amdgcn_raw_buffer_load_b128(rs, lane * 16 + (i & 3) * 1024, WS_H + tt * 32768 + (i >> 2) * 4096, AUX_SC1)); };
#pragma unroll
            for (int i = 0; i < AW - 1; ++i) ld(i);
            SBAR();
            f32x16 acc[3] = {};
            s16x8 wf[2][3];
            auto rd = [&](int i) {
#pragma unroll
                for (int n = 0; n < 3; ++n) wf[i & 1][n] = lds_tr2(wa[n] + i * 1536, wa[n] + i * 1536 + 768); };
            rd(0);
#pragma unroll
            for (int i = 0; i < 32; ++i) {
                if (i + AW - 1 < 32) ld(i + AW - 1);
                else if (i + AW - 1 < 40) st[i + AW - 33] = __builtin_amdgcn_raw_buffer_load_b64(rs, (8 * h + i + AW - 33) * 256 + r32 * 8, WS_ST + tt * 4096, AUX_SC1);
                if (i + 1 < 32) rd(i + 1);
                SBAR();
#pragma unroll
                for (int n = 0; n < 3; ++n) acc[n] = MFMA(wf[i & 1][n], af[i % AW], acc[n]);
                SBAR();
            }
#pragma unroll
            for (int i = AW - 1; i < 8; ++i) st[i] = __builtin_amdgcn_raw_buffer_load_b64(rs, (8 * h + i) * 256 + r32 * 8, WS_ST + tt * 4096, AUX_SC1);
            STAMP(3, 3);
            float sm = 0.f, sq = 0.f;
#pragma unroll
            for (int i = 0; i < 8; ++i) { sm += __uint_as_float(st[i][0]); sq += __uint_as_float(st[i][1]); }
            sm = swap_add(sm); sq = swap_add(sq);
            const float mean = sm * (1.f / D);
            const float var = __builtin_fmaxf(sq * (1.f / D) - mean * mean, 0.f) + LN_EPS;
            const float rstd = __builtin_amdgcn_rsqf(var), sd = var * rstd, ga = -LOG2E * rstd;
            auto epi = [&](int n) {
                u32x4 ov; float y[8];
#pragma unroll
                for (int g = 0; g < 2; ++g) {
                    const f32x4 cg = lds_f4(lds0 + L_TAB + 3072 + (n * 32 + 8 * g + 4 * h) * 4), cu = lds_f4(lds0 + L_TAB + 3072 + (n * 32 + 16 + 8 * g + 4 * h) * 4);
#pragma unroll
                    for (int i = 0; i < 4; ++i) {
                        const float e = __builtin_amdgcn_exp2f(__builtin_fmaf(-mean, cg[i], acc[n][4 * g + i]) * ga);
                        y[4 * g + i] = __builtin_fmaf(-mean, cu[i], acc[n][8 + 4 * g + i]) * __builtin_amdgcn_rcpf(__builtin_fmaf(e, sd, sd));
                    }
                }
#pragma unroll
                for (int i = 0; i < 4; ++i) ov[i] = pk2(y[2 * i], y[2 * i + 1]);
                const int u = bi * 3 + n;
                if (u < NU) __builtin_amdgcn_raw_buffer_store_b128(ov, rs, lane * 16, WS_HH + (tt * NU + u) * 1024, AUX_SC1);
            };
            epi(0); epi(1);
            epi(2);
        }
        STAMP(3, 4);
        if constexpr (FU) {
            BAR_VL(0);
            STAMP(3, 5);
            if (tid == 0 && bi < 29) signal_cnt(ws, 16 + xcd);
            if (w >= 4) { issue_dn(15, 24, L_GU + ((w - 4) >> 1) * 24 * 2048, w - 4); issue_dn(39, 1, L_TAB + ((w - 4) >> 1) * 2048, w - 4); STAMP(3, 6); }
            FLUSH(3);
            if (w >= 4) WAITV(0);
        }
    }

    if constexpr (PH & 8) {
        STAMP(4, 0);
        if constexpr (!FU) {
            if (w >= 4) { issue_dn(0, 15, ((w - 4) >> 1) * 15 * 2048, w - 4); issue_dn(15, 24, L_GU + ((w - 4) >> 1) * 24 * 2048, w - 4); issue_dn(39, 1, L_TAB + ((w - 4) >> 1) * 2048, w - 4); WAITV(0); }
            else { const unsigned so = WS_H + ((rt * 2 + wt) * 32 + (ct * 2 + kh) * 2) * 1024;
                hreg[0] = __builtin_amdgcn_raw_buffer_load_b128(rs, lane * 16, so, AUX_SC1);
                hreg[1] = __builtin_amdgcn_raw_buffer_load_b128(rs, lane * 16 + 1024, so, AUX_SC1); }
        } else { if (w == 4) wait_cnt(ws, 16 + xcd, 29); }
        BAR_L();
        STAMP(4, 1);
        f32x16 a0, a1 = {};
        if (w < 4) {
            constexpr int AW = DN_AW;
            s16x8 af[AW];
            const unsigned so = WS_HH + ((rt * 2 + wt) * NU + kh * 43) * 1024;
            auto ld = [&](int i) { af[i % AW] = __builtin_bit_cast(s16x8, __builtin_amdgcn_raw_buffer_load_b128(rs, lane * 16 + (i & 3) * 1024, so + (i >> 2) * 4096, AUX_SC1)); };
#pragma unroll
            for (int i = 0; i < AW - 1; ++i) ld(i);
            SBAR();
            unsigned tA[2], tB[2], tC[2];
#pragma unroll
            for (int n = 0; n < 2; ++n) { const unsigned t = lds0 + w64_tr(lane, n ^ kh);
                tA[n] = t + kh * 15 * 2048; tB[n] = t + L_GU + kh * 24 * 2048; tC[n] = t + L_TAB + kh * 2048; PIN(tA[n]); PIN(tB[n]); PIN(tC[n]); }
#pragma unroll
            for (int i = 0; i < 8; ++i) { a0[2 * i] = bflo(hreg[i >> 2][i & 3]); a0[2 * i + 1] = bfhi(hreg[i >> 2][i & 3]); }
            s16x8 wf[3][2];
            auto rd = [&](int i) {
#pragma unroll
                for (int n = 0; n < 2; ++n) { const unsigned a = i < 15 ? tA[n] + i * 2048 : i < 39 ? tB[n] + (i - 15) * 2048 : i < 40 ? tC[n] : tA[n] + (i - 40) * 2048;
                    wf[i % 3][n] = lds_tr2(a, a + 1024); } };
            rd(0); rd(1);
#pragma unroll
            for (int i = 0; i < 43; ++i) {
                if (i == 1 || i == 38) BAR_L();
                if (i + AW - 1 < 43) ld(i + AW - 1);
                if (i + 2 < 43) rd(i + 2);
                SBAR();
                a0 = MFMA(wf[i % 3][0], af[i % AW], a0);
                a1 = MFMA(wf[i % 3][1], af[i % AW], a1);
                SBAR();
            }
        } else {
            BAR_L();
            issue_dn(40, 3, ((w - 4) >> 1) * 15 * 2048, w - 4);
            BAR_VL(0);
        }
        STAMP(4, 2);
        BAR_L();
        if (w < 4) {
#pragma unroll
            for (int g = 0; g < 4; ++g) *(LDSAS f32x4*)(lds0 + w * 4096 + g * 1024 + lane * 16) = f32x4{a1[4 * g], a1[4 * g + 1], a1[4 * g + 2], a1[4 * g + 3]};
        }
        BAR_L();
        if (w < 4) {
            f32x4 p[4];
#pragma unroll
            for (int g = 0; g < 4; ++g) p[g] = lds_f4(lds0 + (w ^ 2) * 4096 + g * 1024 + lane * 16);
            SBAR();
            unsigned y[8];
#pragma unroll
            for (int g = 0; g < 4; ++g) {
                y[2 * g] = pk2(a0[4 * g] + p[g][0], a0[4 * g + 1] + p[g][1]);
                y[2 * g + 1] = pk2(a0[4 * g + 2] + p[g][2], a0[4 * g + 3] + p[g][3]);
            }
            const rsrc_t ro = mk_rsrc(out, S * D * 2);
            const unsigned vo = (rt * 64 + wt * 32 + r32) * 1024 + (ct * 64 + kh * 32 + 8 * h) * 2;
#pragma unroll
            for (int g = 0; g < 2; ++g) {
                auto e0 = __builtin_amdgcn_permlane32_swap(y[4 * g], y[4 * g + 2], false, false);
                auto e1 = __builtin_amdgcn_permlane32_swap(y[4 * g + 1], y[4 * g + 3], false, false);
                __builtin_amdgcn_raw_buffer_store_b128(u32x4{e0[0], e1[0], e0[1], e1[1]}, ro, vo + g * 32, 0, 0);
            }
        }
        STAMP(4, 3);
        WAITV(0);
        STAMP(4, 4);
        FLUSH(4);
    }
}

extern "C" void kernel_launch(void* const* d_in, const int* in_sizes, int n_in,
                              void* d_out, int out_size, void* d_ws, size_t ws_size,
                              hipStream_t stream) {
    const u16* x = (const u16*)d_in[0];
    const u16* wqkv = (const u16*)d_in[1];
    const u16* wo = (const u16*)d_in[2];
    const u16* wg = (const u16*)d_in[4];
    const u16* wu = (const u16*)d_in[5];
    const u16* wd = (const u16*)d_in[6];
    u16* out = (u16*)d_out;
    char* ws = (char*)d_ws;
    (void)hipFuncSetAttribute((const void*)k_qkv, hipFuncAttributeMaxDynamicSharedMemorySize, K1_LDS);
    k_qkv<<<256, 512, K1_LDS, stream>>>(x, wqkv, ws);
    (void)hipFuncSetAttribute((const void*)k_main<15>, hipFuncAttributeMaxDynamicSharedMemorySize, K2_LDS);
    k_main<15><<<256, 512, K2_LDS, stream>>>(x, wo, wg, wu, wd, out, ws);
}
```

```cpp
#define PROBE 0
#include <hip/hip_runtime.h>
#include <hip/hip_bf16.h>
#include <stdint.h>

#define FUSED 1

typedef short s16x8 __attribute__((ext_vector_type(8)));
typedef short s16x4 __attribute__((ext_vector_type(4)));
typedef float f32x16 __attribute__((ext_vector_type(16)));
typedef float f32x4 __attribute__((ext_vector_type(4)));
typedef float f32x2 __attribute__((ext_vector_type(2)));
typedef unsigned u32x4 __attribute__((ext_vector_type(4)));
typedef unsigned u32x2 __attribute__((ext_vector_type(2)));
typedef __bf16 bf16x2 __attribute__((ext_vector_type(2)));
typedef unsigned short u16;
#define LDSAS __attribute__((address_space(3)))
#define GAS __attribute__((address_space(1)))
typedef __amdgpu_buffer_rsrc_t rsrc_t;

constexpr int S = 2048, D = 512, NH = 32, F = 1376, NU = F / 16;
constexpr float LN_EPS = 1e-5f;
constexpr float LOG2E = 1.4426950408889634f;

constexpr unsigned KV_CHUNK = 16384;
constexpr unsigned KV_HEAD = 8 * KV_CHUNK;
constexpr unsigned WS_CNT = 0;
constexpr unsigned WS_Q = 8192;
constexpr unsigned WS_KV = WS_Q + NH * 64 * 1024;
constexpr unsigned WS_O = WS_KV + NH * KV_HEAD;
constexpr unsigned WS_H = WS_O + 64 * 32 * 1024;
constexpr unsigned WS_ST = WS_H + 64 * 32 * 1024;
constexpr unsigned WS_HH = WS_ST + 64 * 16 * 256;
constexpr unsigned WS_END = WS_HH + 64 * NU * 1024;
constexpr int AUX_SC1 = 16;
#define STAMP_DECL unsigned long long tp_ = 0
#define STAMP(k, i) do { if ((k) * 16 + (i) == PROBE || (k) * 16 + (i) == 0) { __builtin_amdgcn_sched_barrier(0); tp_ = __builtin_amdgcn_s_memrealtime(); \
    if (blockIdx.x == 0 && threadIdx.x == 0) *(volatile unsigned long long*)(ws + ((k) * 16 + (i) == PROBE ? 16 : 8)) = tp_; if (PROBE == 0 && blockIdx.x == 0 && threadIdx.x == 0) *(volatile unsigned long long*)(ws + 8) = tp_; __builtin_amdgcn_sched_barrier(0); } } while (0)
#define FLUSH(k) do { if ((k) == 4 && blockIdx.x == 0 && threadIdx.x == 0) { asm volatile("s_waitcnt vmcnt(0)" ::: "memory"); const unsigned long long a_ = __builtin_nontemporal_load((unsigned long long*)(ws + 8)), b_ = __builtin_nontemporal_load((unsigned long long*)(ws + 16)); \
    unsigned long long lim_ = b_ - a_; if (lim_ > 20000) lim_ = 20000; while (__builtin_amdgcn_s_memrealtime() - a_ < 6000 + lim_) __builtin_amdgcn_s_sleep(2); } } while (0)

#define SBAR() __builtin_amdgcn_sched_barrier(0)
#define PIN(x) asm volatile("" : "+v"(x))
#define MFMA(a, b, c) __builtin_amdgcn_mfma_f32_32x32x16_bf16((a), (b), (c), 0, 0, 0)
#define WAITV(n) asm volatile("s_waitcnt vmcnt(" #n ")" ::: "memory")
#define BAR_L() asm volatile("s_waitcnt lgkmcnt(0)\n\ts_barrier" ::: "memory")
#define BAR_0() asm volatile("s_barrier" ::: "memory")
#define BAR_VL(n) asm volatile("s_waitcnt vmcnt(" #n ") lgkmcnt(0)\n\ts_barrier" ::: "memory")

__device__ __forceinline__ rsrc_t mk_rsrc(const void* p, unsigned bytes) {
    return __builtin_amdgcn_make_buffer_rsrc((void*)p, (short)0, (int)bytes, 0x00020000);
}
__device__ __forceinline__ void dma16(rsrc_t r, unsigned voff, unsigned soff, unsigned lds) {
    asm volatile("s_nop 4\n\ts_mov_b32 m0, %0\n\ts_nop 0\n\tbuffer_load_dwordx4 %1, %2, %3 offen lds"
                 :: "s"(__builtin_amdgcn_readfirstlane(lds)), "v"(voff), "s"(r), "s"(__builtin_amdgcn_readfirstlane(soff)) : "m0", "memory");
}
__device__ __forceinline__ unsigned pk2(float lo, float hi) {
    f32x2 v = {lo, hi};
    return __builtin_bit_cast(unsigned, __builtin_convertvector(v, bf16x2));
}
__device__ __forceinline__ float dot2(unsigned a, unsigned b, float c) {
    return __builtin_amdgcn_fdot2_f32_bf16(__builtin_bit_cast(bf16x2, a), __builtin_bit_cast(bf16x2, b), c, false);
}
__device__ __forceinline__ float swap_add(float v) {
    auto r = __builtin_amdgcn_permlane32_swap(__float_as_uint(v), __float_as_uint(v), false, false);
    return __uint_as_float(r[0]) + __uint_as_float(r[1]);
}
__device__ __forceinline__ float swap_max(float v) {
    auto r = __builtin_amdgcn_permlane32_swap(__float_as_uint(v), __float_as_uint(v), false, false);
    return __builtin_fmaxf(__uint_as_float(r[0]), __uint_as_float(r[1]));
}
__device__ __forceinline__ s16x8 lds_b128(unsigned a) { return *(const LDSAS s16x8*)(a); }
__device__ __forceinline__ f32x4 lds_f4(unsigned a) { return *(const LDSAS f32x4*)(a); }
__device__ __forceinline__ s16x8 lds_tr2(unsigned a0, unsigned a1) {
    s16x4 lo = __builtin_amdgcn_ds_read_tr16_b64_v4i16((LDSAS s16x4*)(a0));
    s16x4 hi = __builtin_amdgcn_ds_read_tr16_b64_v4i16((LDSAS s16x4*)(a1));
    return __builtin_shufflevector(lo, hi, 0, 1, 2, 3, 4, 5, 6, 7);
}
__device__ __forceinline__ float bflo(unsigned v) { return __uint_as_float(v << 16); }
__device__ __forceinline__ float bfhi(unsigned v) { return __uint_as_float(v & 0xffff0000u); }
constexpr unsigned ONES2 = 0x3f803f80u;
template <int I> struct ic { static constexpr int value = I; constexpr operator int() const { return I; } };
template <int B, int E, class Fn> __device__ __forceinline__ void sfor(Fn&& fn) { if constexpr (B < E) { fn(ic<B>{}); sfor<B + 1, E>(fn); } }

#define K1_X_AUX ""
#define K1_W_AUX ""
#define CODE_PF_AT 64
#define K1_ST_AUX 16
constexpr unsigned K1_W = 65536, K1_CS = 147456, K1_ST = K1_CS + 1536, K1_VT = K1_ST + 1024, K1_LDS = K1_VT + 1024 + 256;
__host__ __device__ constexpr unsigned k1_slot(int s) { return K1_W + (s % 5) * 16384; }

__global__ __launch_bounds__(512, 2) void k_qkv(const u16* __restrict__ x, const u16* __restrict__ wqkv, char* __restrict__ ws) {
    extern __shared__ __attribute__((aligned(16))) char smem[];
    const unsigned lds0 = (unsigned)(uintptr_t)(LDSAS char*)smem;
    const int tid = threadIdx.x, lane = tid & 63;
    const int w = __builtin_amdgcn_readfirstlane(tid >> 6);
    const int b = blockIdx.x, xcd = b & 7, bi = b >> 3;
    const int rt = (xcd >> 1) * 8 + (bi & 7), cb = (xcd & 1) * 4 + (bi >> 3), rot = bi & 3;
    const int r32 = lane & 31, h = lane >> 5;
    const int q4 = (lane & 15) >> 2, p4 = lane & 3, blk = (lane >> 4) & 1;
    const int wt = w & 1, wc = (w >> 1) & 1;
    STAMP_DECL;
    STAMP(0, 0);

    if (w >= 4) {
        const int hw = w - 4;
        const rsrc_t rx = mk_rsrc(x, S * D * 2), rw = mk_rsrc(wqkv, D * 3 * D * 2);
        unsigned xv[2];
#pragma unroll
        for (int i = 0; i < 2; ++i) xv[i] = (lane >> 3) * 1024 + (((lane & 7) ^ ((4 * i + (lane >> 4)) & 7)) << 4);
        const unsigned wv = (lane >> 3) * 3072 + (((lane & 7) ^ (((lane >> 4) & 1) << 2)) << 4);
#pragma unroll
        for (int i = 0; i < 16; ++i) { const int sg = i & 7, pr = 2 * hw + (i >> 3);
            asm volatile("s_mov_b32 m0, %0\n\ts_nop 0\n\tbuffer_load_dwordx4 %1, %2, %3 offen " K1_X_AUX " lds"
                         :: "s"(lds0 + sg * 8192 + pr * 1024), "v"(xv[pr & 1]), "s"(rx), "s"((rt * 64 + pr * 8) * 1024 + sg * 128) : "m0", "memory"); }
        auto issue = [&](int s) {
            const unsigned so = ((((s & 3) + rot) & 3) * 128 + hw * 32) * 3072 + (s >> 2) * 1024 + cb * 128;
#pragma unroll
            for (int i = 0; i < 4; ++i)
                asm volatile("s_mov_b32 m0, %0\n\ts_nop 0\n\tbuffer_load_dwordx4 %1, %2, %3 offen " K1_W_AUX " lds"
                             :: "s"(lds0 + k1_slot(s) + (4 * hw + i) * 1024), "v"(wv), "s"(rw), "s"(so + i * 8 * 3072) : "m0", "memory");
        };
        issue(0); issue(1);
        BAR_VL(8);
        {
            float sum = 0.f, sq = 0.f;
            u32x4 f[16];
#pragma unroll
            for (int j = 0; j < 16; ++j)
                f[j] = __builtin_bit_cast(u32x4, lds_b128(lds0 + (wc * 4 + (j >> 2)) * 8192 + (32 * wt + r32) * 128 + (((2 * (j & 3) + h) ^ ((r32 >> 1) & 7)) << 4)));
            SBAR();
            issue(2);
            SBAR();
#pragma unroll
            for (int j = 0; j < 16; ++j)
#pragma unroll
                for (int i = 0; i < 4; ++i) { sum = dot2(f[j][i], ONES2, sum); sq = dot2(f[j][i], f[j][i], sq); }
            sum = swap_add(sum); sq = swap_add(sq);
            if (h == 0) *(LDSAS f32x2*)(lds0 + K1_ST + (wc * 64 + wt * 32 + r32) * 8) = f32x2{sum, sq};
        }
        f32x4 cs4 = {};
        const s16x4 one4 = {0x3f80, 0x3f80, 0x3f80, 0x3f80};
        const unsigned tr = (8 * h + q4) * 128 + (((4 * wt + 2 * blk + (p4 >> 1)) ^ (((q4 >> 1) & 1) << 2)) << 4) + 8 * (p4 & 1) + wc * 8192;
#pragma unroll
        for (int s = 0; s < 12; ++s) {
            if (s <= 9) BAR_VL(8); else if (s == 10) BAR_VL(4); else BAR_VL(0);
            const unsigned wb = lds0 + k1_slot(s) + tr;
            s16x4 f[8];
#pragma unroll
            for (int i = 0; i < 8; ++i) f[i] = __builtin_amdgcn_ds_read_tr16_b64_v4i16((LDSAS s16x4*)(wb + (i >> 1) * 2048 + (i & 1) * 512));
            SBAR();
            if (s + 3 < 12) issue(s + 3);
            SBAR();
#pragma unroll
            for (int i = 0; i < 8; ++i) cs4 = __builtin_amdgcn_mfma_f32_4x4x4bf16_1k(f[i], one4, cs4, 0, 0, 0);
            if ((s & 3) == 3) {
                float cs = (lane & 2) ? ((lane & 1) ? cs4[3] : cs4[2]) : ((lane & 1) ? cs4[1] : cs4[0]);
                cs = swap_add(cs);
                if (h == 0) *(LDSAS float*)(lds0 + K1_CS + (((s >> 2) * 4 + hw) * 32 + r32) * 4) = cs;
                cs4 = f32x4{};
            }
        }
        if (b == 0 && w == 4 && lane < 32) *(unsigned*)(ws + WS_CNT + lane * 256) = 0u;
        BAR_L();
        return;
    }

    typedef unsigned u32x16 __attribute__((ext_vector_type(16)));
    u32x16 xa0, xa1, xa2, xa3, xa4, xa5, xa6, xa7;
    u32x4 w0, w1, w2, w3, w4;
    f32x16 acA = {}, acB = {};
    unsigned xb[4];
#pragma unroll
    for (int m = 0; m < 4; ++m) xb[m] = lds0 + (32 * wt + r32) * 128 + (((2 * m + h) ^ ((r32 >> 1) & 7)) << 4);
    const unsigned trn = lds0 + (8 * h + q4) * 128 + (((4 * wc + 2 * blk + (p4 >> 1)) ^ (((q4 >> 1) & 1) << 2)) << 4) + 8 * (p4 & 1);
    const unsigned tB = trn + 65536, tC = trn + 131072;
    const rsrc_t rs = mk_rsrc(ws, WS_END);
    const int tt = rt * 2 + wt, hd = cb * 4 + wc * 2;
    const unsigned vtab = lds0 + K1_VT + w * 256;
    BAR_L();
    STAMP(0, 1);
#define KXR(R, m, O) "ds_read_b128 v[" #R ":" #R "+3], %[x" #m "] offset:" #O "\n\t"
#define KXQ(R) KXR(R, 0, 0) KXR(R + 4, 1, 0) KXR(R + 8, 2, 0) KXR(R + 12, 3, 0) KXR(R + 16, 0, 8192) KXR(R + 20, 1, 8192) KXR(R + 24, 2, 8192) KXR(R + 28, 3, 8192)
#define KXOPS(q) [x0] "v"(xb[0] + ((q + rot) & 3) * 16384), [x1] "v"(xb[1] + ((q + rot) & 3) * 16384), [x2] "v"(xb[2] + ((q + rot) & 3) * 16384), [x3] "v"(xb[3] + ((q + rot) & 3) * 16384)
    asm volatile(KXQ(128) : "={v[128:143]}"(xa0), "={v[144:159]}"(xa1) : KXOPS(0) : "memory");
    asm volatile(KXQ(160) : "={v[160:175]}"(xa2), "={v[176:191]}"(xa3) : KXOPS(1) : "memory");
    asm volatile(KXQ(192) : "={v[192:207]}"(xa4), "={v[208:223]}"(xa5) : KXOPS(2) : "memory");
    asm volatile(KXQ(224) : "={v[224:239]}"(xa6), "={v[240:255]}"(xa7) : KXOPS(3) : "memory");
#define KWR(R, BASE, O) "ds_read_b64_tr_b16 v[" #R ":" #R "+1], " BASE " offset:" #O "\n\tds_read_b64_tr_b16 v[" #R "+2:" #R "+3], " BASE " offset:" #O "+512\n\t"
#define KOPS : "+{v[64:79]}"(acA), "+{v[80:95]}"(acB), "+{v[96:99]}"(w0), "+{v[100:103]}"(w1), "+{v[104:107]}"(w2), "+{v[108:111]}"(w3), "+{v[112:115]}"(w4), \
               "+{v[128:143]}"(xa0), "+{v[144:159]}"(xa1), "+{v[160:175]}"(xa2), "+{v[176:191]}"(xa3), "+{v[192:207]}"(xa4), "+{v[208:223]}"(xa5), "+{v[224:239]}"(xa6), "+{v[240:255]}"(xa7) \
             : [tb] "v"(tB), [tc] "v"(tC) : "memory"
    asm volatile("s_barrier\n\t" KWR(96, "%[tb]", 0) KWR(100, "%[tb]", 2048) KWR(104, "%[tb]", 4096) KWR(108, "%[tb]", 6144)
                 : "={v[96:99]}"(w0), "={v[100:103]}"(w1), "={v[104:107]}"(w2), "={v[108:111]}"(w3), "={v[112:115]}"(w4) : [tb] "v"(tB) : "memory");
    float mean = 0.f, rstd = 0.f;
    u32x4 eo[2];
    auto epi_qk = [&](int m, const f32x16& acc, int g) {
        const float sc = m == 0 ? rstd * (0.25f * LOG2E) : rstd;
        const unsigned ca = lds0 + K1_CS + ((m * 4 + wc) * 32 + 8 * g + 4 * h) * 4;
        const f32x4 c = lds_f4(ca) + lds_f4(ca + 256);
        float y[4];
#pragma unroll
        for (int i = 0; i < 4; ++i) y[i] = __builtin_fmaf(-mean, c[i], acc[4 * g + i]) * sc;
        eo[g >> 1][(g & 1) * 2] = pk2(y[0], y[1]); eo[g >> 1][(g & 1) * 2 + 1] = pk2(y[2], y[3]);
        if (g & 1) __builtin_amdgcn_raw_buffer_store_b128(eo[g >> 1], rs, lane * 16, m == 0 ? WS_Q + ((hd + (g >> 1)) * 64 + tt) * 1024
                                                          : WS_KV + (hd + (g >> 1)) * KV_HEAD + (tt >> 3) * KV_CHUNK + (tt & 7) * 1024, K1_ST_AUX);
    };
    sfor<0, 96>([&](auto G) {
        constexpr int g = G.value, n = g + 4, m = g >> 5;
        constexpr unsigned o = k1_slot(n >> 3) + (n & 7) * 2048, oi = o < 131072 ? o - 65536 : o - 131072;
        constexpr int aR = m == 1 ? 80 : 64, wR = 96 + 4 * (g % 5), nR = 96 + 4 * (n % 5), xR = 128 + 4 * (g & 31);
        asm volatile(".if %c[bar]\n\ts_barrier\n\t.endif\n\t"
                     ".if %c[rd] == 1\n\tds_read_b64_tr_b16 v[%c[nr]:%c[nr]+1], %[tb] offset:%c[oi]\n\tds_read_b64_tr_b16 v[%c[nr]+2:%c[nr]+3], %[tb] offset:%c[oi]+512\n\ts_waitcnt lgkmcnt(8)\n\t.endif\n\t"
                     ".if %c[rd] == 2\n\tds_read_b64_tr_b16 v[%c[nr]:%c[nr]+1], %[tc] offset:%c[oi]\n\tds_read_b64_tr_b16 v[%c[nr]+2:%c[nr]+3], %[tc] offset:%c[oi]+512\n\ts_waitcnt lgkmcnt(8)\n\t.endif\n\t"
                     ".if %c[rd] == 0\n\ts_waitcnt lgkmcnt(0)\n\t.endif\n\t"
                     ".if %c[md] == 0\n\tv_mfma_f32_32x32x16_bf16 v[%c[ar]:%c[ar]+15], v[%c[wr]:%c[wr]+3], v[%c[xr]:%c[xr]+3], v[%c[ar]:%c[ar]+15]\n\t.endif\n\t"
                     ".if %c[md] == 1\n\tv_mfma_f32_32x32x16_bf16 v[%c[ar]:%c[ar]+15], v[%c[xr]:%c[xr]+3], v[%c[wr]:%c[wr]+3], v[%c[ar]:%c[ar]+15]\n\t.endif\n\t"
                     ".if %c[md] == 2\n\tv_mfma_f32_32x32x16_bf16 v[%c[ar]:%c[ar]+15], v[%c[xr]:%c[xr]+3], v[%c[wr]:%c[wr]+3], 0\n\t.endif"
                     : "+{v[64:79]}"(acA), "+{v[80:95]}"(acB), "+{v[96:99]}"(w0), "+{v[100:103]}"(w1), "+{v[104:107]}"(w2), "+{v[108:111]}"(w3), "+{v[112:115]}"(w4),
                       "+{v[128:143]}"(xa0), "+{v[144:159]}"(xa1), "+{v[160:175]}"(xa2), "+{v[176:191]}"(xa3), "+{v[192:207]}"(xa4), "+{v[208:223]}"(xa5), "+{v[224:239]}"(xa6), "+{v[240:255]}"(xa7)
                     : [tb] "v"(tB), [tc] "v"(tC), [bar] "n"(n < 96 && (n & 7) == 0 ? 1 : 0), [rd] "n"(n >= 96 ? 0 : o < 131072 ? 1 : 2), [oi] "n"(oi), [nr] "n"(nR),
                       [md] "n"(m < 2 ? 0 : g == 64 ? 2 : 1), [ar] "n"(aR), [wr] "n"(wR), [xr] "n"(xR) : "memory");
        if (g == 8) {
            const f32x2 s0 = *(const LDSAS f32x2*)(lds0 + K1_ST + (wt * 32 + r32) * 8), s1 = *(const LDSAS f32x2*)(lds0 + K1_ST + (64 + wt * 32 + r32) * 8);
            mean = (s0[0] + s1[0]) * (1.f / D);
            rstd = __builtin_amdgcn_rsqf(__builtin_fmaxf((s0[1] + s1[1]) * (1.f / D) - mean * mean, 0.f) + LN_EPS);
            if (h == 0) { *(LDSAS float*)(vtab + r32 * 4) = rstd; *(LDSAS float*)(vtab + 128 + r32 * 4) = mean; }
            PIN(mean); PIN(rstd);
        }
        if (g == CODE_PF_AT && w == 0) {
        unsigned long long pc; unsigned len;
        asm volatile("s_getpc_b64 %0\n.Lqkv_pc:\n\ts_mov_b32 %1, .Lmain_end-.Lqkv_pc" : "=s"(pc), "=s"(len));
        const rsrc_t rc = mk_rsrc((const void*)(pc & ~127ull), len);
        asm volatile("s_mov_b32 m0, %0\n\ts_nop 0\n\tbuffer_load_dword %1, %2, 0 offen lds" :: "s"(lds0 + K1_LDS - 256), "v"((unsigned)((lane * 32 + bi) * 128)), "s"(rc) : "m0", "memory");
    }
        if (g >= 36 && g < 44 && !(g & 1)) epi_qk(0, acA, (g - 36) >> 1);
        if (g >= 68 && g < 76 && !(g & 1)) epi_qk(1, acB, (g - 68) >> 1);
    });
    STAMP(0, 2);
    f32x4 rs4[4], mn4[4];
#pragma unroll
    for (int g = 0; g < 4; ++g) { rs4[g] = lds_f4(vtab + (8 * g + 4 * h) * 4); mn4[g] = lds_f4(vtab + 128 + (8 * g + 4 * h) * 4); }
    asm volatile("s_waitcnt lgkmcnt(0)\n\ts_barrier\n\ts_nop 7" : "+{v[64:79]}"(acA) :: "memory");
    {
        const unsigned ca = lds0 + K1_CS + ((8 + wc) * 32 + r32) * 4;
        const float c = *(const LDSAS float*)(ca) + *(const LDSAS float*)(ca + 256);
        u32x4 o[2];
#pragma unroll
        for (int g = 0; g < 4; ++g) {
            float y[4];
#pragma unroll
            for (int i = 0; i < 4; ++i) y[i] = __builtin_fmaf(-mn4[g][i], c, acA[4 * g + i]) * rs4[g][i];
            o[g >> 1][(g & 1) * 2] = pk2(y[0], y[1]); o[g >> 1][(g & 1) * 2 + 1] = pk2(y[2], y[3]); }
        const int d = lane & 15;
        const unsigned vo = (r32 >> 4) * KV_HEAD + (16 * h + d) * 16;
        const unsigned so = WS_KV + hd * KV_HEAD + (tt >> 3) * KV_CHUNK + 8192 + (tt & 7) * 1024;
        __builtin_amdgcn_raw_buffer_store_b128(o[0], rs, vo, so, K1_ST_AUX);
        __builtin_amdgcn_raw_buffer_store_b128(o[1], rs, vo + 512, so, K1_ST_AUX);
    }
    STAMP(0, 3);
    FLUSH(0);
}

constexpr unsigned K2_LDS = 163840;
constexpr unsigned L_WO = 0, L_GU = 65536;
constexpr unsigned L_TAB = 61440;
constexpr unsigned L_FLAG = 12288;
constexpr unsigned SPIN_CAP = 1u << 20;
#define NOPF 0
#define GU_AW 7
#define DN_AW 13

__device__ __forceinline__ void signal_cnt(char* ws, int idx) {
    __hip_atomic_fetch_add((GAS unsigned*)(ws + WS_CNT + idx * 256), 1u, __ATOMIC_RELAXED, __HIP_MEMORY_SCOPE_AGENT);
}
__device__ __forceinline__ void wait_cnt(char* ws, int idx, unsigned want) {
    for (unsigned spins = 0; spins < SPIN_CAP; ++spins) {
        const unsigned v = __builtin_amdgcn_readfirstlane(__hip_atomic_load((GAS unsigned*)(ws + WS_CNT + idx * 256), __ATOMIC_RELAXED, __HIP_MEMORY_SCOPE_AGENT));
        if (v >= want) break;
        __builtin_amdgcn_s_sleep(1);
    }
    asm volatile("" ::: "memory");
}

__device__ __forceinline__ unsigned w64_voff(int lane) { return (lane >> 3) * 1024 + (((lane & 7) ^ (((lane >> 4) & 1) << 2)) << 4); }
__device__ __forceinline__ unsigned w64_tr(int lane, int n) {
    const int q = (lane & 15) >> 2, p = lane & 3, blk = (lane >> 4) & 1, h = lane >> 5;
    return (4 * h + q) * 128 + (((4 * n + 2 * blk + (p >> 1)) ^ (((q >> 1) & 1) << 2)) << 4) + 8 * (p & 1);
}

template <int PH>
__global__ __launch_bounds__(512, 2) void k_main(const u16* __restrict__ x, const u16* __restrict__ wo, const u16* __restrict__ wg,
                                                 const u16* __restrict__ wu, const u16* __restrict__ wd, u16* __restrict__ out, char* __restrict__ ws) {
    constexpr bool FU = (PH == 15);
    extern __shared__ __attribute__((aligned(16))) char smem[];
    const unsigned lds0 = (unsigned)(uintptr_t)(LDSAS char*)smem;
    const int tid = threadIdx.x, lane = tid & 63;
    const int w = __builtin_amdgcn_readfirstlane(tid >> 6);
    const int b = blockIdx.x, xcd = b & 7, bi = b >> 3;
    const int r32 = lane & 31, h = lane >> 5;
    const rsrc_t rs = mk_rsrc(ws, WS_END);
    const int rt = xcd * 4 + (bi >> 3), ct = bi & 7;
    const int wt = w & 1, kh = (w >> 1) & 1;
    u32x4 hreg[2];
    u32x2 xres[4];
    STAMP_DECL;

    auto issue_wo = [&](int first, int n) {
        const rsrc_t rwo = mk_rsrc(wo, D * D * 2);
        const unsigned v = w64_voff(lane);
        for (int p = first; p < first + n; ++p) dma16(rwo, v, p * 8192 + ct * 128, lds0 + L_WO + p * 1024);
    };
    auto issue_gu = [&](int mat, int g0, int ng) {
        const rsrc_t rg = mk_rsrc(mat ? wu : wg, D * F * 2);
        unsigned v[3];
#pragma unroll
        for (int j = 0; j < 3; ++j) { const unsigned c = (j * 64 + lane) ^ (mat * 8), kr = c / 6, ch = c % 6; v[j] = kr * (F * 2) + ch * 16; }
        for (int g = g0; g < g0 + ng; ++g)
#pragma unroll
            for (int j = 0; j < 3; ++j) dma16(rg, v[j], g * 32 * (F * 2) + bi * 96, lds0 + L_GU + mat * 49152 + (g * 3 + j) * 1024);
    };
    auto issue_dn = [&](int i0, int n, unsigned dst, int hw) {
        const rsrc_t rwd = mk_rsrc(wd, F * D * 2);
        const unsigned v = w64_voff(lane);
        const unsigned so = ((43 * (hw >> 1) + i0) * 16 + (hw & 1) * 8) * 1024 + ct * 128;
        for (int i = 0; i < n; ++i) dma16(rwd, v, so + i * 16384, lds0 + dst + (hw & 1) * 1024 + i * 2048);
    };
    auto load_xres = [&]() {
        const rsrc_t rx = mk_rsrc(x, S * D * 2);
        const unsigned v = (rt * 64 + wt * 32 + r32) * 1024 + (ct * 64 + kh * 32 + 4 * h) * 2;
#pragma unroll
        for (int g = 0; g < 4; ++g) xres[g] = __builtin_amdgcn_raw_buffer_load_b64(rx, v + g * 16, 0, 0);
    };

    auto gu_wa = [&](int n) -> unsigned { const unsigned blk = (lane >> 4) & 1;
        return lds0 + L_GU + blk * 49152 + (((4 * h + ((lane & 15) >> 2)) * 96 + n * 32 + (lane & 3) * 8) ^ (blk * 128)); };
    float cs[3] = {0.f, 0.f, 0.f};
    auto gu_colsum = [&]() {
        u32x4 f[3][4];
#pragma unroll
        for (int n = 0; n < 3; ++n) {
            const unsigned a0 = gu_wa(n) + w * 4 * 1536;
#pragma unroll
            for (int i = 0; i < 4; ++i) f[n][i] = __builtin_bit_cast(u32x4, lds_tr2(a0 + i * 1536, a0 + i * 1536 + 768));
        }
        SBAR();
#pragma unroll
        for (int i = 0; i < 4; ++i)
#pragma unroll
            for (int e = 0; e < 4; ++e)
#pragma unroll
                for (int n = 0; n < 3; ++n) cs[n] = dot2(f[n][i][e], ONES2, cs[n]);
#pragma unroll
        for (int n = 0; n < 3; ++n) cs[n] = swap_add(cs[n]);
    };

    if constexpr (PH & 1) {
        const int head = xcd * 4 + (bi >> 3), qb = bi & 7, wq = w & 3, g = w >> 2, qt = qb * 8 + wq * 2;
        const unsigned kvs = WS_KV + head * KV_HEAD;
        const unsigned l16 = lane * 16;
        STAMP(1, 0);
        s16x8 q0, q1;
        asm volatile("s_nop 4\n\tbuffer_load_dwordx4 v[128:131], %2, %3, %4 offen\n\tbuffer_load_dwordx4 v[156:159], %2, %3, %4 offen offset:1024"
                     : "={v[128:131]}"(q0), "={v[156:159]}"(q1) : "v"(l16), "s"(rs), "s"(WS_Q + (head * 64 + qt) * 1024) : "memory");
        unsigned ks = kvs + g * 4 * KV_CHUNK + wq * 4096, km = lds0 + g * 32768 + wq * 4096;
#pragma unroll
        for (int j = 0; j < 4; ++j) dma16(rs, l16, ks + j * 1024, km + j * 1024);
        const rsrc_t rg = mk_rsrc(g ? wu : wg, (FU && bi < 29) ? D * F * 2 : 0), rwd = mk_rsrc(wd, (FU && !NOPF) ? F * D * 2 : 0), rwo = mk_rsrc(wo, (FU && !NOPF) ? D * D * 2 : 0);
        unsigned gv0, gv1, gv2;
        { unsigned c = lane ^ (g * 8); gv0 = (c / 6) * (F * 2) + (c % 6) * 16; c = (64 + lane) ^ (g * 8); gv1 = (c / 6) * (F * 2) + (c % 6) * 16;
          c = (128 + lane) ^ (g * 8); gv2 = (c / 6) * (F * 2) + (c % 6) * 16; }
        unsigned gs = wq * 4 * 32 * (F * 2) + bi * 96, gm = lds0 + L_GU + g * 49152 + wq * 12288;
        const unsigned pvd = lane < 43 ? (bi * 344 + w * 43 + lane) * 128 : 0x80000000u, pvo = lane < 16 ? (bi * 128 + w * 16 + lane) * 128 : 0x80000000u;
        const unsigned kb = lds0 + g * 32768 + l16, vb = lds0 + g * 32768 + 8192 + (16 * h + (r32 & 15)) * 16;
        const unsigned cst = (r32 & 27) == 16 ? ONES2 : 0u;
        const unsigned long long msk = 0x0000ffff0000ffffull;
        BAR_VL(0);
        STAMP(1, 1);
#pragma unroll
        for (int j = 0; j < 4; ++j) dma16(rs, l16, ks + KV_CHUNK + j * 1024, km + 16384 + j * 1024);
        ks += 2 * KV_CHUNK;
        asm volatile("" : "+{v[128:131]}"(q0), "+{v[156:159]}"(q1));
        f32x16 n0, n1, sA, sB = {}, o0 = {}, o1 = {};
        s16x8 kf = lds_b128(kb), v0, v1, v1b;
        {
            const f32x16 z = {};
            const s16x8 kz = lds_b128(lds0 + l16);
            const u32x4 vv = __builtin_bit_cast(u32x4, lds_b128(vb)), cc = {cst, cst, cst, cst};
            const f32x16 s0 = MFMA(kz, q0, z), s1 = MFMA(kz, q1, z);
            float m0 = __builtin_fmaxf(s0[0], s0[1]), m1 = __builtin_fmaxf(s1[0], s1[1]);
#pragma unroll
            for (int i = 2; i < 16; i += 2) { m0 = __builtin_fmaxf(__builtin_fmaxf(m0, s0[i]), s0[i + 1]); m1 = __builtin_fmaxf(__builtin_fmaxf(m1, s1[i]), s1[i + 1]); }
            m0 = swap_max(m0); m1 = swap_max(m1);
#pragma unroll
            for (int i = 0; i < 16; ++i) { n0[i] = -m0; n1[i] = -m1; }
            sA = MFMA(kf, q0, n0);
            v0 = __builtin_bit_cast(s16x8, r32 < 16 ? vv : cc); v1 = v1b = __builtin_bit_cast(s16x8, cc);
        }
        u32x4 pA = {0u, 0u, 0u, 0u}, pB = {0u, 0u, 0u, 0u};
#define XE(C, i) "v_exp_f32 v[" #C "+" #i "], v[" #C "+" #i "]\n\t"
#define XC(P, C, i) "v_cvt_pk_bf16_f32 v" #P ", v[" #C "+" #i "], v[" #C "+" #i "+1]\n\t"
#define XG1(C) XE(C, 0) XE(C, 1) XE(C, 2) XE(C, 3) XC(148, C, 0) XE(C, 4) XE(C, 5) XC(149, C, 2)
#define XG2(C) XE(C, 6) XE(C, 7) XC(150, C, 4) XE(C, 8) XC(151, C, 6) XE(C, 9) XE(C, 10)
#define XG3(C) XE(C, 11) XC(152, C, 8) XE(C, 12) XE(C, 13) XC(153, C, 10) XE(C, 14) XE(C, 15) XC(154, C, 12) XC(155, C, 14)
#define XM(D, A, B, C) "v_mfma_f32_32x32x16_bf16 v[" #D ":" #D "+15], v[" #A ":" #A "+3], v[" #B ":" #B "+3], v[" #C ":" #C "+15]\n\t"
#define XRK(BASE, O) "ds_read_b128 v[132:135], " BASE " offset:" #O "\n\t"
#define XRV(R, BASE, O) "s_mov_b64 exec, %[msk]\n\tds_read_b128 v[" #R ":" #R "+3], " BASE " offset:" #O "\n\ts_mov_b64 exec, -1\n\t"
#define XW(n) "s_waitcnt lgkmcnt(" #n ")\n\t"
#define XTILE(VA, RK, RV1, WA, X1, VB, X2, RV0) \
        XM(80, 132, 156, 176) RK XG1(64) XM(160, VA, 152, 160) RV1 XG2(64) XW(WA) XM(96, 140, 148, 96) XG3(64) \
        XW(0) XM(64, 132, 128, 112) X1 XG1(80) XM(96, VB, 152, 96) X2 XG2(80) XM(160, 140, 148, 160) RV0 XG3(80)
#define XKV "s_mov_b32 m0, %[km]\n\ts_add_u32 %[km], %[km], 0x400\n\tbuffer_load_dwordx4 %[l16], %[rs], %[ks] offen lds\n\ts_add_u32 %[ks], %[ks], 0x400\n\t"
#define XGU(j) "s_mov_b32 m0, %[gm]\n\ts_add_u32 %[gm], %[gm], 0x400\n\tbuffer_load_dwordx4 %[gv" #j "], %[rg], %[gs] offen lds\n\t"
#define XGU2 XGU(2) "s_add_u32 %[gs], %[gs], 88064\n\t"
#define XPF "s_mov_b32 m0, %[pm]\n\ts_nop 0\n\tbuffer_load_dword %[pvd], %[rwd], 0 offen lds\n\tbuffer_load_dword %[pvo], %[rwo], 0 offen lds\n\t"
#define AOPS : "+{v[64:79]}"(sA), "+{v[80:95]}"(sB), "+{v[96:111]}"(o0), "+{v[160:175]}"(o1), "+{v[112:127]}"(n0), "+{v[176:191]}"(n1), "+{v[128:131]}"(q0), "+{v[156:159]}"(q1), \
               "+{v[132:135]}"(kf), "+{v[140:143]}"(v0), "+{v[144:147]}"(v1), "+{v[136:139]}"(v1b), "+{v[148:151]}"(pA), "+{v[152:155]}"(pB), \
               [km] "+s"(km), [ks] "+s"(ks), [gm] "+s"(gm), [gs] "+s"(gs) \
             : [ka] "v"(ka), [va] "v"(va), [kn] "v"(kn), [vn] "v"(vn), [msk] "s"(msk), [l16] "v"(l16), [rs] "s"(rs), [rg] "s"(rg), [gv0] "v"(gv0), [gv1] "v"(gv1), [gv2] "v"(gv2), \
               [pvd] "v"(pvd), [pvo] "v"(pvo), [pm] "s"(lds0 + L_FLAG + 1024), [rwd] "s"(rwd), [rwo] "s"(rwo) : "m0", "scc", "memory"
#define XROUND(A1, A2, B1, B2, C1, D1, E1, F1) asm volatile( \
            XTILE(136, XRK("%[ka]", 1024), XRV(144, "%[va]", 512), 2, A1, 144, A2, XRV(140, "%[va]", 1024)) \
            XTILE(144, XRK("%[ka]", 2048), XRV(144, "%[va]", 1536), 2, B1, 144, B2, XRV(140, "%[va]", 2048)) \
            XTILE(144, XRK("%[ka]", 3072), XRV(144, "%[va]", 2560), 2, C1, 144, "", XRV(140, "%[va]", 3072)) \
            XTILE(144, XRK("%[ka]", 4096), XRV(144, "%[va]", 3584), 2, D1, 144, "", XRV(140, "%[va]", 4096)) \
            XTILE(144, XRK("%[ka]", 5120), XRV(144, "%[va]", 4608), 2, E1, 144, "", XRV(140, "%[va]", 5120)) \
            XTILE(144, XRK("%[ka]", 6144), XRV(144, "%[va]", 5632), 2, F1, 144, "", XRV(140, "%[va]", 6144)) \
            XTILE(144, XRK("%[ka]", 7168), XRV(144, "%[va]", 6656), 2, XRV(136, "%[va]", 7680), 144, "", XRV(140, "%[va]", 7168)) AOPS)
#define XLAST(X1, X2) asm volatile(XTILE(144, XRK("%[kn]", 0), "", 1, X1, 136, X2, XRV(140, "%[vn]", 0)) AOPS)
        unsigned ka = kb, va = vb, kn = kb + 16384, vn = vb + 16384;
        asm volatile("s_nop 15" AOPS);
        XROUND(XGU(0), "", XGU(1), "", XGU2, XGU(0), XGU(1), XGU2);
        BAR_VL(6);
        XLAST(XKV, XKV);
        ka = kb + 16384; va = vb + 16384; kn = kb; vn = vb;
        XROUND(XKV, XKV, XGU(0), XGU(1), XGU2, XGU(0), XGU(1), XGU2);
        BAR_VL(6);
        km += 16384 - 4096; ks += KV_CHUNK - 4096;
        XLAST(XKV, XKV);
        ka = kb; va = vb; kn = kb + 16384; vn = vb + 16384;
        XROUND(XKV, XKV, "", "", "", "", "", "");
        BAR_VL(0);
        XLAST(XPF, "");
        ka = kb + 16384; va = vb + 16384; kn = kb; vn = vb;
        XROUND("", "", "", "", "", "", "", "");
        XLAST("", "");
        asm volatile(XM(160, 136, 152, 160) "s_waitcnt lgkmcnt(0)\n\ts_nop 15" AOPS);
        STAMP(1, 4);
        f32x16 o;
        {
            const f32x16& snd = g ? o0 : o1;
            const unsigned xa = lds0 + g * 32768 + wq * 3072 + l16, xb = lds0 + (g ^ 1) * 32768 + wq * 3072 + l16;
            *(LDSAS f32x4*)(xa) = f32x4{snd[0], snd[1], snd[2], snd[3]};
            *(LDSAS f32x4*)(xa + 1024) = f32x4{snd[4], snd[5], snd[6], snd[7]};
            *(LDSAS float*)(xa + 2048) = snd[8];
            if (tid == 0) *(LDSAS unsigned*)(lds0 + L_FLAG) = 0u;
            BAR_L();
            const f32x4 p0 = lds_f4(xb), p1 = lds_f4(xb + 1024); const float pl = *(const LDSAS float*)(xb + 2048);
            o = g ? o1 : o0;
#pragma unroll
            for (int i = 0; i < 4; ++i) { o[i] += p0[i]; o[4 + i] += p1[i]; }
            o[8] += pl;
        }
        bool bad = !(o[8] < 1.8446744e19f);
#pragma unroll
        for (int i = 0; i < 8; ++i) bad |= !(__builtin_fabsf(o[i]) < 3.0e38f);
        if (__any(bad) && lane == 0) *(LDSAS unsigned*)(lds0 + L_FLAG) = 1u;
        auto store_o = [&]() {
            const float inv = __builtin_amdgcn_rcpf(o[8]);
            u32x4 ov;
#pragma unroll
            for (int i = 0; i < 4; ++i) ov[i] = pk2(o[2 * i] * inv, o[2 * i + 1] * inv);
            __builtin_amdgcn_raw_buffer_store_b128(ov, rs, l16, WS_O + ((qt + g) * 32 + head) * 1024, AUX_SC1);
        };
        store_o();
        BAR_VL(0);
        if (__builtin_amdgcn_readfirstlane(*(const LDSAS unsigned*)(lds0 + L_FLAG))) {
            const s16x8 qf = g ? q1 : q0;
            const u32x4 cc = {cst, cst, cst, cst};
            const unsigned vf = lds0 + 16384 + 8192 + (16 * h + (r32 & 15)) * 16;
            float m = -__builtin_inff();
            o = f32x16{};
            const f32x16 z = {};
#pragma unroll 1
            for (int c = 0; c < 8; ++c) {
                dma16(rs, l16, kvs + c * KV_CHUNK + w * 2048, lds0 + 16384 + w * 2048);
                dma16(rs, l16, kvs + c * KV_CHUNK + w * 2048 + 1024, lds0 + 16384 + w * 2048 + 1024);
                BAR_VL(0);
#pragma unroll 1
                for (int t = 0; t < 8; ++t) {
                    f32x16 s = MFMA(lds_b128(lds0 + 16384 + l16 + t * 1024), qf, z);
                    float mx = s[0];
#pragma unroll
                    for (int i = 1; i < 16; ++i) mx = __builtin_fmaxf(mx, s[i]);
                    mx = __builtin_fmaxf(swap_max(mx), m);
                    const float al = __builtin_amdgcn_exp2f(m - mx);
                    m = mx;
#pragma unroll
                    for (int i = 0; i < 16; ++i) { o[i] *= al; s[i] = __builtin_amdgcn_exp2f(s[i] - m); }
#pragma unroll
                    for (int i = 0; i < 4; ++i) { pA[i] = pk2(s[2 * i], s[2 * i + 1]); pB[i] = pk2(s[8 + 2 * i], s[9 + 2 * i]); }
                    const u32x4 a = __builtin_bit_cast(u32x4, lds_b128(vf + t * 1024)), c2 = __builtin_bit_cast(u32x4, lds_b128(vf + t * 1024 + 512));
                    o = MFMA(__builtin_bit_cast(s16x8, r32 < 16 ? a : cc), __builtin_bit_cast(s16x8, pA), o);
                    o = MFMA(__builtin_bit_cast(s16x8, r32 < 16 ? c2 : cc), __builtin_bit_cast(s16x8, pB), o);
                }
                BAR_L();
            }
            store_o();
            BAR_VL(0);
        }
        STAMP(1, 5);
        if constexpr (FU) {
            if (tid == 0) signal_cnt(ws, qb);
            BAR_L();
            if (w >= 4) issue_wo((w - 4) * 16, 16); else load_xres();
            STAMP(1, 6);
            if (bi < 29) gu_colsum();
        }
        FLUSH(1);
    }

    if constexpr (PH & 2) {
        STAMP(2, 0);
        if constexpr (!FU) { issue_wo(w * 8, 8); if (w < 4) load_xres(); BAR_VL(0); }
        else { if (w == 4) wait_cnt(ws, xcd, 32); BAR_VL(0); }
        STAMP(2, 1);
        f32x16 a0, a1 = {};
        if (w < 4) {
            constexpr int AW = 12;
            s16x8 af[AW];
            const unsigned so = WS_O + ((rt * 2 + wt) * 32 + kh * 16) * 1024;
            auto ld = [&](int i) { af[i % AW] = __builtin_bit_cast(s16x8, __builtin_amdgcn_raw_buffer_load_b128(rs, lane * 16 + (i & 3) * 1024, so + (i >> 2) * 4096, AUX_SC1)); };
#pragma unroll
            for (int i = 0; i < AW - 1; ++i) ld(i);
            SBAR();
            unsigned t0 = lds0 + L_WO + kh * 32768 + w64_tr(lane, kh), t1 = lds0 + L_WO + kh * 32768 + w64_tr(lane, kh ^ 1);
#pragma unroll
            for (int g = 0; g < 4; ++g) { a0[4 * g] = bflo(xres[g][0]); a0[4 * g + 1] = bfhi(xres[g][0]); a0[4 * g + 2] = bflo(xres[g][1]); a0[4 * g + 3] = bfhi(xres[g][1]); }
            PIN(t0); PIN(t1);
            s16x8 wf[3][2];
            auto rd = [&](int i) { wf[i % 3][0] = lds_tr2(t0 + i * 2048, t0 + i * 2048 + 1024); wf[i % 3][1] = lds_tr2(t1 + i * 2048, t1 + i * 2048 + 1024); };
            rd(0); rd(1);
#pragma unroll
            for (int i = 0; i < 16; ++i) {
                if (i + AW - 1 < 16) ld(i + AW - 1);
                if (i + 2 < 16) rd(i + 2);
                SBAR();
                a0 = MFMA(wf[i % 3][0], af[i % AW], a0);
                a1 = MFMA(wf[i % 3][1], af[i % AW], a1);
                SBAR();
            }
        }
        STAMP(2, 2);
        BAR_L();
        if (w < 4) {
#pragma unroll
            for (int g = 0; g < 4; ++g) *(LDSAS f32x4*)(lds0 + w * 4096 + g * 1024 + lane * 16) = f32x4{a1[4 * g], a1[4 * g + 1], a1[4 * g + 2], a1[4 * g + 3]};
        }
        BAR_L();
        if (w < 4) {
            f32x4 p[4];
#pragma unroll
            for (int g = 0; g < 4; ++g) p[g] = lds_f4(lds0 + (w ^ 2) * 4096 + g * 1024 + lane * 16);
            SBAR();
#pragma unroll
            for (int g = 0; g < 4; ++g) {
                hreg[g >> 1][(g & 1) * 2] = pk2(a0[4 * g] + p[g][0], a0[4 * g + 1] + p[g][1]);
                hreg[g >> 1][(g & 1) * 2 + 1] = pk2(a0[4 * g + 2] + p[g][2], a0[4 * g + 3] + p[g][3]);
            }
            const unsigned so = WS_H + ((rt * 2 + wt) * 32 + (ct * 2 + kh) * 2) * 1024;
            __builtin_amdgcn_raw_buffer_store_b128(hreg[0], rs, lane * 16, so, AUX_SC1);
            __builtin_amdgcn_raw_buffer_store_b128(hreg[1], rs, lane * 16 + 1024, so, AUX_SC1);
            SBAR();
            float sm = 0.f, sq = 0.f;
#pragma unroll
            for (int i = 0; i < 8; ++i) { sm = dot2(hreg[i >> 2][i & 3], ONES2, sm); sq = dot2(hreg[i >> 2][i & 3], hreg[i >> 2][i & 3], sq); }
            sm = swap_add(sm); sq = swap_add(sq);
            if (h == 0) __builtin_amdgcn_raw_buffer_store_b64(u32x2{__float_as_uint(sm), __float_as_uint(sq)}, rs, r32 * 8,
                                                              WS_ST + ((rt * 2 + wt) * 16 + ct * 2 + kh) * 256, AUX_SC1);
        }
        STAMP(2, 3);
        if constexpr (FU) {
            BAR_VL(0);
            STAMP(2, 4);
            if (tid == 0) signal_cnt(ws, 8 + xcd);
            if (w >= 4) issue_dn(0, 15, ((w - 4) >> 1) * 15 * 2048, w - 4);
            STAMP(2, 5);
        }
        FLUSH(2);
    }

    if constexpr (PH & 4) {
        STAMP(3, 0);
        if constexpr (!FU) { if (bi < 29) issue_gu(w >> 2, (w & 3) * 4, 4); BAR_VL(0); if (bi < 29) gu_colsum(); }
        STAMP(3, 1);
        if (bi < 29) {
            unsigned wa[3];
#pragma unroll
            for (int n = 0; n < 3; ++n) { wa[n] = gu_wa(n); PIN(wa[n]); if (h == 0) *(LDSAS float*)(lds0 + L_TAB + (w * 96 + n * 32 + r32) * 4) = cs[n]; }
            BAR_L();
            if (tid < 96) { float t = 0.f;
#pragma unroll
                for (int i = 0; i < 8; ++i) t += *(const LDSAS float*)(lds0 + L_TAB + (i * 96 + tid) * 4);
                *(LDSAS float*)(lds0 + L_TAB + 3072 + tid * 4) = t; }
            if constexpr (FU) { if (w == 4) wait_cnt(ws, 8 + xcd, 32); }
            BAR_L();
            STAMP(3, 2);
            const int tt = xcd * 8 + w;
            constexpr int AW = GU_AW;
            s16x8 af[AW]; u32x2 st[8];
            auto ld = [&](int i) { af[i % AW] = __builtin_bit_cast(s16x8, __builtin_amdgcn_raw_buffer_load_b128(rs, lane * 16 + (i & 3) * 1024, WS_H + tt * 32768 + (i >> 2) * 4096, AUX_SC1)); };
#pragma unroll
            for (int i = 0; i < AW - 1; ++i) ld(i);
            SBAR();
            f32x16 acc[3] = {};
            s16x8 wf[2][3];
            auto rd = [&](int i) {
#pragma unroll
                for (int n = 0; n < 3; ++n) wf[i & 1][n] = lds_tr2(wa[n] + i * 1536, wa[n] + i * 1536 + 768); };
            rd(0);
#pragma unroll
            for (int i = 0; i < 32; ++i) {
                if (i + AW - 1 < 32) ld(i + AW - 1);
                else if (i + AW - 1 < 40) st[i + AW - 33] = __builtin_amdgcn_raw_buffer_load_b64(rs, (8 * h + i + AW - 33) * 256 + r32 * 8, WS_ST + tt * 4096, AUX_SC1);
                if (i + 1 < 32) rd(i + 1);
                SBAR();
#pragma unroll
                for (int n = 0; n < 3; ++n) acc[n] = MFMA(wf[i & 1][n], af[i % AW], acc[n]);
                SBAR();
            }
#pragma unroll
            for (int i = AW - 1; i < 8; ++i) st[i] = __builtin_amdgcn_raw_buffer_load_b64(rs, (8 * h + i) * 256 + r32 * 8, WS_ST + tt * 4096, AUX_SC1);
            STAMP(3, 3);
            float sm = 0.f, sq = 0.f;
#pragma unroll
            for (int i = 0; i < 8; ++i) { sm += __uint_as_float(st[i][0]); sq += __uint_as_float(st[i][1]); }
            sm = swap_add(sm); sq = swap_add(sq);
            const float mean = sm * (1.f / D);
            const float var = __builtin_fmaxf(sq * (1.f / D) - mean * mean, 0.f) + LN_EPS;
            const float rstd = __builtin_amdgcn_rsqf(var), sd = var * rstd, ga = -LOG2E * rstd;
            auto epi = [&](int n) {
                u32x4 ov; float y[8];
#pragma unroll
                for (int g = 0; g < 2; ++g) {
                    const f32x4 cg = lds_f4(lds0 + L_TAB + 3072 + (n * 32 + 8 * g + 4 * h) * 4), cu = lds_f4(lds0 + L_TAB + 3072 + (n * 32 + 16 + 8 * g + 4 * h) * 4);
#pragma unroll
                    for (int i = 0; i < 4; ++i) {
                        const float e = __builtin_amdgcn_exp2f(__builtin_fmaf(-mean, cg[i], acc[n][4 * g + i]) * ga);
                        y[4 * g + i] = __builtin_fmaf(-mean, cu[i], acc[n][8 + 4 * g + i]) * __builtin_amdgcn_rcpf(__builtin_fmaf(e, sd, sd));
                    }
                }
#pragma unroll
                for (int i = 0; i < 4; ++i) ov[i] = pk2(y[2 * i], y[2 * i + 1]);
                const int u = bi * 3 + n;
                if (u < NU) __builtin_amdgcn_raw_buffer_store_b128(ov, rs, lane * 16, WS_HH + (tt * NU + u) * 1024, AUX_SC1);
            };
            epi(0); epi(1);
            epi(2);
        }
        STAMP(3, 4);
        if constexpr (FU) {
            BAR_VL(0);
            STAMP(3, 5);
            if (tid == 0 && bi < 29) signal_cnt(ws, 16 + xcd);
            if (w >= 4) { issue_dn(15, 24, L_GU + ((w - 4) >> 1) * 24 * 2048, w - 4); issue_dn(39, 1, L_TAB + ((w - 4) >> 1) * 2048, w - 4); STAMP(3, 6); }
            FLUSH(3);
            if (w >= 4) WAITV(0);
        }
    }

    if constexpr (PH & 8) {
        STAMP(4, 0);
        if constexpr (!FU) {
            if (w >= 4) { issue_dn(0, 15, ((w - 4) >> 1) * 15 * 2048, w - 4); issue_dn(15, 24, L_GU + ((w - 4) >> 1) * 24 * 2048, w - 4); issue_dn(39, 1, L_TAB + ((w - 4) >> 1) * 2048, w - 4); WAITV(0); }
            else { const unsigned so = WS_H + ((rt * 2 + wt) * 32 + (ct * 2 + kh) * 2) * 1024;
                hreg[0] = __builtin_amdgcn_raw_buffer_load_b128(rs, lane * 16, so, AUX_SC1);
                hreg[1] = __builtin_amdgcn_raw_buffer_load_b128(rs, lane * 16 + 1024, so, AUX_SC1); }
        } else { if (w == 4) wait_cnt(ws, 16 + xcd, 29); }
        BAR_L();
        STAMP(4, 1);
        f32x16 a0, a1 = {};
        if (w < 4) {
            constexpr int AW = DN_AW;
            s16x8 af[AW];
            const unsigned so = WS_HH + ((rt * 2 + wt) * NU + kh * 43) * 1024;
            auto ld = [&](int i) { af[i % AW] = __builtin_bit_cast(s16x8, __builtin_amdgcn_raw_buffer_load_b128(rs, lane * 16 + (i & 3) * 1024, so + (i >> 2) * 4096, AUX_SC1)); };
#pragma unroll
            for (int i = 0; i < AW - 1; ++i) ld(i);
            SBAR();
            unsigned tA[2], tB[2], tC[2];
#pragma unroll
            for (int n = 0; n < 2; ++n) { const unsigned t = lds0 + w64_tr(lane, n ^ kh);
                tA[n] = t + kh * 15 * 2048; tB[n] = t + L_GU + kh * 24 * 2048; tC[n] = t + L_TAB + kh * 2048; PIN(tA[n]); PIN(tB[n]); PIN(tC[n]); }
#pragma unroll
            for (int i = 0; i < 8; ++i) { a0[2 * i] = bflo(hreg[i >> 2][i & 3]); a0[2 * i + 1] = bfhi(hreg[i >> 2][i & 3]); }
            s16x8 wf[3][2];
            auto rd = [&](int i) {
#pragma unroll
                for (int n = 0; n < 2; ++n) { const unsigned a = i < 15 ? tA[n] + i * 2048 : i < 39 ? tB[n] + (i - 15) * 2048 : i < 40 ? tC[n] : tA[n] + (i - 40) * 2048;
                    wf[i % 3][n] = lds_tr2(a, a + 1024); } };
            rd(0); rd(1);
#pragma unroll
            for (int i = 0; i < 43; ++i) {
                if (i == 1 || i == 38) BAR_L();
                if (i + AW - 1 < 43) ld(i + AW - 1);
                if (i + 2 < 43) rd(i + 2);
                SBAR();
                a0 = MFMA(wf[i % 3][0], af[i % AW], a0);
                a1 = MFMA(wf[i % 3][1], af[i % AW], a1);
                SBAR();
            }
        } else {
            BAR_L();
            issue_dn(40, 3, ((w - 4) >> 1) * 15 * 2048, w - 4);
            BAR_VL(0);
        }
        STAMP(4, 2);
        BAR_L();
        if (w < 4) {
#pragma unroll
            for (int g = 0; g < 4; ++g) *(LDSAS f32x4*)(lds0 + w * 4096 + g * 1024 + lane * 16) = f32x4{a1[4 * g], a1[4 * g + 1], a1[4 * g + 2], a1[4 * g + 3]};
        }
        BAR_L();
        if (w < 4) {
            f32x4 p[4];
#pragma unroll
            for (int g = 0; g < 4; ++g) p[g] = lds_f4(lds0 + (w ^ 2) * 4096 + g * 1024 + lane * 16);
            SBAR();
            unsigned y[8];
#pragma unroll
            for (int g = 0; g < 4; ++g) {
                y[2 * g] = pk2(a0[4 * g] + p[g][0], a0[4 * g + 1] + p[g][1]);
                y[2 * g + 1] = pk2(a0[4 * g + 2] + p[g][2], a0[4 * g + 3] + p[g][3]);
            }
            const rsrc_t ro = mk_rsrc(out, S * D * 2);
            const unsigned vo = (rt * 64 + wt * 32 + r32) * 1024 + (ct * 64 + kh * 32 + 8 * h) * 2;
#pragma unroll
            for (int g = 0; g < 2; ++g) {
                auto e0 = __builtin_amdgcn_permlane32_swap(y[4 * g], y[4 * g + 2], false, false);
                auto e1 = __builtin_amdgcn_permlane32_swap(y[4 * g + 1], y[4 * g + 3], false, false);
                __builtin_amdgcn_raw_buffer_store_b128(u32x4{e0[0], e1[0], e0[1], e1[1]}, ro, vo + g * 32, 0, 0);
            }
        }
        STAMP(4, 3);
        WAITV(0);
        STAMP(4, 4);
        FLUSH(4);
        if constexpr (FU) asm volatile(".Lmain_end:");
    }
}

extern "C" void kernel_launch(void* const* d_in, const int* in_sizes, int n_in,
                              void* d_out, int out_size, void* d_ws, size_t ws_size,
                              hipStream_t stream) {
    const u16* x = (const u16*)d_in[0];
    const u16* wqkv = (const u16*)d_in[1];
    const u16* wo = (const u16*)d_in[2];
    const u16* wg = (const u16*)d_in[4];
    const u16* wu = (const u16*)d_in[5];
    const u16* wd = (const u16*)d_in[6];
    u16* out = (u16*)d_out;
    char* ws = (char*)d_ws;
    (void)hipFuncSetAttribute((const void*)k_qkv, hipFuncAttributeMaxDynamicSharedMemorySize, K1_LDS);
    k_qkv<<<256, 512, K1_LDS, stream>>>(x, wqkv, ws);
    (void)hipFuncSetAttribute((const void*)k_main<15>, hipFuncAttributeMaxDynamicSharedMemorySize, K2_LDS);
    k_main<15><<<256, 512, K2_LDS, stream>>>(x, wo, wg, wu, wd, out, ws);
}
```

```cpp
#include <hip/hip_runtime.h>
#include <hip/hip_bf16.h>
#include <stdint.h>

typedef short s16x8 __attribute__((ext_vector_type(8)));
typedef short s16x4 __attribute__((ext_vector_type(4)));
typedef float f32x16 __attribute__((ext_vector_type(16)));
typedef float f32x4 __attribute__((ext_vector_type(4)));
typedef float f32x2 __attribute__((ext_vector_type(2)));
typedef unsigned u32x4 __attribute__((ext_vector_type(4)));
typedef unsigned u32x2 __attribute__((ext_vector_type(2)));
typedef unsigned u32x8 __attribute__((ext_vector_type(8)));
typedef __bf16 bf16x2 __attribute__((ext_vector_type(2)));
typedef unsigned short u16;
#define LDSAS __attribute__((address_space(3)))
#define GAS __attribute__((address_space(1)))
typedef __amdgpu_buffer_rsrc_t rsrc_t;

constexpr int S = 2048, D = 512, NH = 32, F = 1376, NU = F / 16;
constexpr float LN_EPS = 1e-5f;
constexpr float LOG2E = 1.4426950408889634f;

constexpr unsigned KV_CHUNK = 16384;
constexpr unsigned KV_HEAD = 8 * KV_CHUNK;
constexpr unsigned WS_EP = 0;
constexpr unsigned WS_FQ = 4096;
constexpr unsigned WS_FO = WS_FQ + 16 * 64 * 128;
constexpr unsigned WS_FH = WS_FO + 8 * 32 * 128;
constexpr unsigned WS_FG = WS_FH + 8 * 32 * 128;
constexpr unsigned WS_Q = WS_FG + 8 * 32 * 128;
constexpr unsigned WS_KV = WS_Q + NH * 64 * 1024;
constexpr unsigned WS_O = WS_KV + NH * KV_HEAD;
constexpr unsigned WS_H = WS_O + 64 * 32 * 1024;
constexpr unsigned WS_ST = WS_H + 64 * 32 * 1024;
constexpr unsigned WS_HH = WS_ST + 64 * 16 * 256;
constexpr unsigned WS_END = WS_HH + 64 * NU * 1024;
constexpr int AUX_SC1 = 16;
#define STAMP_DECL
#define FLUSH(k) do {} while (0)
#define STAMP(k, i) do {} while (0)

#define SBAR() __builtin_amdgcn_sched_barrier(0)
#define RFL(x) ((unsigned)__builtin_amdgcn_readfirstlane(x))
#define PIN(x) asm volatile("" : "+v"(x))
#define MFMA(a, b, c) __builtin_amdgcn_mfma_f32_32x32x16_bf16((a), (b), (c), 0, 0, 0)
#define WAITV(n) asm volatile("s_waitcnt vmcnt(" #n ")" ::: "memory")
#define BAR_L() asm volatile("s_waitcnt lgkmcnt(0)\n\ts_barrier" ::: "memory")
#define BAR_0() asm volatile("s_barrier" ::: "memory")
#define BAR_VL(n) asm volatile("s_waitcnt vmcnt(" #n ") lgkmcnt(0)\n\ts_barrier" ::: "memory")

__device__ __forceinline__ rsrc_t mk_rsrc(const void* p, unsigned bytes) {
    return __builtin_amdgcn_make_buffer_rsrc((void*)p, (short)0, (int)bytes, 0x00020000);
}
template <bool SC1 = false> __device__ __forceinline__ void dma16(rsrc_t r, unsigned voff, unsigned soff, unsigned lds) {
    if constexpr (SC1) asm volatile("s_nop 4\n\ts_mov_b32 m0, %0\n\ts_nop 0\n\tbuffer_load_dwordx4 %1, %2, %3 offen sc1 lds"
                 :: "s"(__builtin_amdgcn_readfirstlane(lds)), "v"(voff), "s"(r), "s"(__builtin_amdgcn_readfirstlane(soff)) : "m0", "memory");
    else asm volatile("s_nop 4\n\ts_mov_b32 m0, %0\n\ts_nop 0\n\tbuffer_load_dwordx4 %1, %2, %3 offen lds"
                 :: "s"(__builtin_amdgcn_readfirstlane(lds)), "v"(voff), "s"(r), "s"(__builtin_amdgcn_readfirstlane(soff)) : "m0", "memory");
}
__device__ __forceinline__ unsigned pk2(float lo, float hi) {
    f32x2 v = {lo, hi};
    return __builtin_bit_cast(unsigned, __builtin_convertvector(v, bf16x2));
}
__device__ __forceinline__ float dot2(unsigned a, unsigned b, float c) {
    return __builtin_amdgcn_fdot2_f32_bf16(__builtin_bit_cast(bf16x2, a), __builtin_bit_cast(bf16x2, b), c, false);
}
__device__ __forceinline__ float swap_add(float v) {
    auto r = __builtin_amdgcn_permlane32_swap(__float_as_uint(v), __float_as_uint(v), false, false);
    return __uint_as_float(r[0]) + __uint_as_float(r[1]);
}
__device__ __forceinline__ float swap_max(float v) {
    auto r = __builtin_amdgcn_permlane32_swap(__float_as_uint(v), __float_as_uint(v), false, false);
    return __builtin_fmaxf(__uint_as_float(r[0]), __uint_as_float(r[1]));
}
__device__ __forceinline__ s16x8 lds_b128(unsigned a) { return *(const LDSAS s16x8*)(a); }
__device__ __forceinline__ f32x4 lds_f4(unsigned a) { return *(const LDSAS f32x4*)(a); }
__device__ __forceinline__ s16x8 lds_tr2(unsigned a0, unsigned a1) {
    s16x4 lo = __builtin_amdgcn_ds_read_tr16_b64_v4i16((LDSAS s16x4*)(a0));
    s16x4 hi = __builtin_amdgcn_ds_read_tr16_b64_v4i16((LDSAS s16x4*)(a1));
    return __builtin_shufflevector(lo, hi, 0, 1, 2, 3, 4, 5, 6, 7);
}
__device__ __forceinline__ float bflo(unsigned v) { return __uint_as_float(v << 16); }
__device__ __forceinline__ float bfhi(unsigned v) { return __uint_as_float(v & 0xffff0000u); }
constexpr unsigned ONES2 = 0x3f803f80u;
template <int I> struct ic { static constexpr int value = I; constexpr operator int() const { return I; } };
template <int B, int E, class Fn> __device__ __forceinline__ void sfor(Fn&& fn) { if constexpr (B < E) { fn(ic<B>{}); sfor<B + 1, E>(fn); } }

#define K1_X_AUX 2
#define K1_ABL 0
#define K1_ST_AUX 16
constexpr unsigned K1_W = 65536, K1_FLG = K1_W + 5 * 16384, K1_ST = K1_FLG + 512, K1_CS = K1_ST + 512;
__host__ __device__ constexpr unsigned k1_slot(int s) { return s < 5 ? K1_W + s * 16384 : s == 5 ? K1_W : s < 10 ? (s - 6) * 16384 : K1_W + (s - 9) * 16384; }
__device__ __forceinline__ void lds_spin_ge(unsigned addr, unsigned want) {
#pragma unroll 1
    for (unsigned n = 0; n < (1u << 22); ++n) {
        unsigned v; asm volatile("ds_read_b32 %0, %1\n\ts_waitcnt lgkmcnt(0)" : "=v"(v) : "v"(addr) : "memory");
        if ((unsigned)__builtin_amdgcn_readfirstlane(v) >= want) break;
        __builtin_amdgcn_s_sleep(1);
    }
}
__device__ __forceinline__ void lds_post(unsigned addr, unsigned v) {
    asm volatile("s_waitcnt lgkmcnt(0)\n\tds_write_b32 %0, %1" :: "v"(addr), "v"(v) : "memory");
}
__device__ __forceinline__ void lds_inc(unsigned addr, int lane) {
    asm volatile("s_waitcnt lgkmcnt(0)" ::: "memory");
    if (lane == 0) asm volatile("ds_add_u32 %0, %1" :: "v"(addr), "v"(1u) : "memory");
}
typedef unsigned u32x16 __attribute__((ext_vector_type(16)));
template <int CTRL> __device__ __forceinline__ float dpp_add(float v) {
    return v + __builtin_bit_cast(float, __builtin_amdgcn_update_dpp(0, __builtin_bit_cast(int, v), CTRL, 0xf, 0xf, true));
}

constexpr unsigned K2_LDS = 163840;
constexpr unsigned L_WO = 0, L_GU = 65536;
constexpr unsigned L_TAB = 61440;
constexpr unsigned L_FLAG = 16384 + 12288;
constexpr unsigned SPIN_CAP = 1u << 20;
#define POLL_DELAY_Q 16
#define POLL_DELAY_O 0
#define POLL_DELAY_H 0
#define GU_AW 7
#define DN_AW 13

__device__ __forceinline__ void wait_flags(rsrc_t rs, unsigned base, int n, unsigned ep, int lane) {
    const unsigned v = (lane < n ? lane : n - 1) * 128;
#pragma unroll 1
    for (unsigned spins = 0; spins < SPIN_CAP; ++spins) {
        const unsigned f = __builtin_amdgcn_raw_buffer_load_b32(rs, v, base, AUX_SC1);
        if (__all(f == ep)) break;
        __builtin_amdgcn_s_sleep(1);
    }
    asm volatile("" ::: "memory");
}

__device__ __forceinline__ unsigned w64_voff(int lane) { return (lane >> 3) * 1024 + (((lane & 7) ^ (((lane >> 4) & 1) << 2)) << 4); }
__device__ __forceinline__ unsigned w64_tr(int lane, int n) {
    const int q = (lane & 15) >> 2, p = lane & 3, blk = (lane >> 4) & 1, h = lane >> 5;
    return (4 * h + q) * 128 + (((4 * n + 2 * blk + (p >> 1)) ^ (((q >> 1) & 1) << 2)) << 4) + 8 * (p & 1);
}

__global__ __launch_bounds__(512, 2) void k_block(const u16* __restrict__ x, const u16* __restrict__ wqkv, const u16* __restrict__ wo, const u16* __restrict__ wg,
                                                  const u16* __restrict__ wu, const u16* __restrict__ wd, u16* __restrict__ out, char* __restrict__ ws) {
    extern __shared__ __attribute__((aligned(16))) char smem[];
    const unsigned lds0 = (unsigned)(uintptr_t)(LDSAS char*)smem;
    const int tid = threadIdx.x, lane = tid & 63;
    const int w = __builtin_amdgcn_readfirstlane(tid >> 6);
    const int b = blockIdx.x, xcd = b & 7, bi = b >> 3;
    const int r32 = lane & 31, h = lane >> 5;
    const rsrc_t rs = mk_rsrc(ws, WS_END);
    const int wt = w & 1, kh = (w >> 1) & 1;
    STAMP_DECL;
    unsigned ep;
    {
    const int rt = (xcd >> 1) * 8 + (bi & 7), cb = (xcd & 1) * 4 + (bi >> 3), rot = bi & 3;
    const int wc = (w >> 1) & 1, hw = w - 4;
    STAMP(0, 0);
    const rsrc_t rw = mk_rsrc(wqkv, D * 3 * D * 2);
    const unsigned wv = (lane >> 3) * 3072 + (((lane & 7) ^ (((lane >> 4) & 1) << 2)) << 4);
    const unsigned fa = lds0 + K1_FLG;
    if (tid < 12) *(LDSAS unsigned*)(fa + tid * 4) = 0u;
    if (w >= 4) {
        BAR_L();
        const unsigned pa = fa + hw * 4;
        auto issue = [&](int s) {
            const unsigned so = RFL(((((s & 3) + rot) & 3) * 128 + hw * 32) * 3072 + (s >> 2) * 1024 + cb * 128);
            asm volatile("s_nop 4" :: "s"(so));
#pragma unroll
            for (int i = 0; i < 4; ++i)
                asm volatile("s_mov_b32 m0, %0\n\ts_nop 0\n\tbuffer_load_dwordx4 %1, %2, %3 offen lds"
                             :: "s"(lds0 + k1_slot(s) + (4 * hw + i) * 1024), "v"(wv), "s"(rw), "s"(so + i * 8 * 3072) : "m0", "memory");
        };
#define K1_LAND(N, s) asm volatile("s_waitcnt vmcnt(" #N ")\n\tds_write_b32 %0, %1" :: "v"(pa), "v"((s) + 1u) : "memory")
        issue(0); issue(1);
        issue(2); K1_LAND(8, 0);
        issue(3); K1_LAND(8, 1);
        issue(4); K1_LAND(8, 2); K1_LAND(4, 3); K1_LAND(0, 4);
        lds_spin_ge(fa + 28, 4);
        issue(5);
        lds_spin_ge(fa + 20, 4);
        issue(6); issue(7); K1_LAND(8, 5);
        issue(8); K1_LAND(8, 6);
        issue(9); K1_LAND(8, 7);
        lds_spin_ge(fa + 24, 4);
        issue(10); K1_LAND(8, 8);
        issue(11); K1_LAND(8, 9);
        K1_LAND(4, 10); K1_LAND(0, 11);
        if (w == 4) { unsigned e; asm volatile("s_load_dword %0, %1, 0\n\ts_waitcnt lgkmcnt(0)" : "=s"(e) : "s"(ws) : "memory"); *(LDSAS unsigned*)(fa + 48) = e + 1u; }
        else {
            unsigned long long pc; unsigned len, o = bi * 128 + (w - 5) * 16384, t, d0, d1, d2, d3;
#define KCP(D) "s_min_u32 %[t], %[o], %[len]\n\ts_load_dword " D ", %[pc], %[t]\n\ts_add_u32 %[o], %[o], 0x1000\n\t"
            asm volatile("s_getpc_b64 %[pc]\n.Lqkv_pc:\n\ts_mov_b32 %[len], .Lmain_end-.Lqkv_pc-4\n\t" KCP("%[d0]") KCP("%[d1]") KCP("%[d2]") KCP("%[d3]") "s_waitcnt lgkmcnt(0)"
                         : [pc] "=&s"(pc), [len] "=&s"(len), [o] "+s"(o), [t] "=&s"(t), [d0] "=&s"(d0), [d1] "=&s"(d1), [d2] "=&s"(d2), [d3] "=&s"(d3) :: "scc", "memory");
        }
        BAR_L();
        ep = RFL(*(const LDSAS unsigned*)(fa + 48));
        if (w == 4) { __builtin_amdgcn_s_sleep(POLL_DELAY_Q); wait_flags(rs, WS_FQ + (xcd * 2 + (bi >> 4)) * 8192, 64, ep, lane); }
    } else {
    {
        const rsrc_t rx = mk_rsrc(x, S * D * 2);
        u32x4 r[2][8];
#pragma unroll
        for (int p = 0; p < 2; ++p)
#pragma unroll
            for (int g = 0; g < 8; ++g)
                r[p][g] = __builtin_amdgcn_raw_buffer_load_b128(rx, (lane >> 3) * 1024 + (((lane & 7) ^ ((4 * p + (lane >> 4)) & 7)) << 4) + g * 128, (rt * 64 + w * 16 + p * 8) * 1024, K1_X_AUX);
        SBAR(); BAR_L(); SBAR();
#pragma unroll
        for (int p = 0; p < 2; ++p) {
            float s0 = 0.f, s1 = 0.f, q0 = 0.f, q1 = 0.f;
#pragma unroll
            for (int g = 0; g < 8; ++g) { s0 = dot2(r[p][g][0], ONES2, s0); q0 = dot2(r[p][g][0], r[p][g][0], q0); s1 = dot2(r[p][g][1], ONES2, s1); q1 = dot2(r[p][g][1], r[p][g][1], q1);
                                          s0 = dot2(r[p][g][2], ONES2, s0); q0 = dot2(r[p][g][2], r[p][g][2], q0); s1 = dot2(r[p][g][3], ONES2, s1); q1 = dot2(r[p][g][3], r[p][g][3], q1); }
            if (p == 0) STAMP(0, 1);
            float sum = s0 + s1, sq = q0 + q1;
            sum = dpp_add<0xB1>(sum); sq = dpp_add<0xB1>(sq); sum = dpp_add<0x4E>(sum); sq = dpp_add<0x4E>(sq); sum = dpp_add<0x141>(sum); sq = dpp_add<0x141>(sq);
            const float mean = sum * (1.f / D);
            const float rstd = __builtin_amdgcn_rsqf(__builtin_fmaxf(sq * (1.f / D) - mean * mean, 0.f) + LN_EPS), nb = -mean * rstd;
#pragma unroll
            for (int g = 0; g < 8; g += 2) {
                f32x2 t[8];
#pragma unroll
                for (int i = 0; i < 8; ++i) { const unsigned v = r[p][g + (i >> 2)][i & 3]; t[i] = f32x2{bflo(v), bfhi(v)}; }
                asm volatile("" : "+v"(t[0]), "+v"(t[1]), "+v"(t[2]), "+v"(t[3]), "+v"(t[4]), "+v"(t[5]), "+v"(t[6]), "+v"(t[7]));
#pragma unroll
                for (int i = 0; i < 8; ++i) t[i] = t[i] * rstd + nb;
                asm volatile("" : "+v"(t[0]), "+v"(t[1]), "+v"(t[2]), "+v"(t[3]), "+v"(t[4]), "+v"(t[5]), "+v"(t[6]), "+v"(t[7]));
                u32x4 y0, y1;
#pragma unroll
                for (int i = 0; i < 4; ++i) { y0[i] = pk2(t[i][0], t[i][1]); y1[i] = pk2(t[4 + i][0], t[4 + i][1]); }
                *(LDSAS u32x4*)(lds0 + (2 * w + p) * 1024 + lane * 16 + g * 8192) = y0;
                *(LDSAS u32x4*)(lds0 + (2 * w + p) * 1024 + lane * 16 + g * 8192 + 8192) = y1;
            }
            SBAR();
        }
    }
    lds_inc(fa + 16, lane);
    STAMP(0, 3);
    u32x16 xa0, xa1, xa2, xa3, xa4, xa5, xa6, xa7;
    u32x4 w0, w1, w2, w3, w4, fl;
    f32x16 acA, acB;
    unsigned xb[4], tq;
#pragma unroll
    for (int m = 0; m < 4; ++m) xb[m] = lds0 + (32 * wt + r32) * 128 + (((2 * m + h) ^ ((r32 >> 1) & 7)) << 4) + rot * 16384;
    const int q4 = (lane & 15) >> 2, p4 = lane & 3, blk = (lane >> 4) & 1;
    const unsigned tA = lds0 + (8 * h + q4) * 128 + (((4 * wc + 2 * blk + (p4 >> 1)) ^ (((q4 >> 1) & 1) << 2)) << 4) + 8 * (p4 & 1);
    const unsigned tB = tA + 65536, tC = tA + 131072;
    const int tt = rt * 2 + wt, hd = cb * 4 + wc * 2;
#define KXR(R, m, O) "ds_read_b128 v[" #R ":" #R "+3], %[x" #m "] offset:" #O "\n\t"
#define KXN(m) "v_add_u32 %[x" #m "], 0x4000, %[x" #m "]\n\tv_and_b32 %[x" #m "], 0xffff, %[x" #m "]\n\t"
#define KCHK(ST) "v_min3_u32 v116, v116, v117, v118\n\tv_min_u32 v116, v116, v119\n\ts_nop 0\n\tv_readfirstlane_b32 %[t], v116\n\ts_nop 0\n\ts_cmp_gt_u32 %[t], " ST "\n\t"
#define KSPIN(ST) "1:\n\tds_read_b128 v[116:119], %[fa]\n\ts_waitcnt lgkmcnt(0)\n\t" KCHK(ST) "s_cbranch_scc0 1b\n\t"
#define KWR(R, O) "ds_read_b64_tr_b16 v[" #R ":" #R "+1], %[tb] offset:" #O "\n\tds_read_b64_tr_b16 v[" #R "+2:" #R "+3], %[tb] offset:" #O "+512\n\t"
    asm volatile("3:\n\tds_read_b32 v116, %[fa] offset:16\n\ts_waitcnt lgkmcnt(0)\n\tv_readfirstlane_b32 %[t], v116\n\ts_nop 0\n\ts_cmp_gt_u32 %[t], 3\n\ts_cbranch_scc0 3b\n\t"
                 KXR(128, 0, 0) KXR(132, 1, 0) KXR(136, 2, 0) KXR(140, 3, 0) KXR(144, 0, 8192) KXR(148, 1, 8192) KXR(152, 2, 8192) KXR(156, 3, 8192) KXN(0) KXN(1) KXN(2) KXN(3)
                 KSPIN("0") KWR(96, 0) KWR(100, 2048) KWR(104, 4096) KWR(108, 6144)
                 : "={v[116:119]}"(fl), [t] "=&s"(tq), "={v[96:99]}"(w0), "={v[100:103]}"(w1), "={v[104:107]}"(w2), "={v[108:111]}"(w3), "={v[112:115]}"(w4), "={v[64:79]}"(acA), "={v[80:95]}"(acB),
                   "={v[128:143]}"(xa0), "={v[144:159]}"(xa1), "={v[160:175]}"(xa2), "={v[176:191]}"(xa3), "={v[192:207]}"(xa4), "={v[208:223]}"(xa5), "={v[224:239]}"(xa6), "={v[240:255]}"(xa7),
                   [x0] "+v"(xb[0]), [x1] "+v"(xb[1]), [x2] "+v"(xb[2]), [x3] "+v"(xb[3])
                 : [fa] "v"(fa), [tb] "v"(tB) : "scc", "memory");
    STAMP(0, 4);
    auto epi = [&](int m, const f32x16& acc, int hh) {
        const float sc = 0.25f * LOG2E;
        u32x4 o;
#pragma unroll
        for (int i = 0; i < 4; ++i) o[i] = m == 0 ? pk2(acc[8 * hh + 2 * i] * sc, acc[8 * hh + 2 * i + 1] * sc) : pk2(acc[8 * hh + 2 * i], acc[8 * hh + 2 * i + 1]);
        if (m == 0) __builtin_amdgcn_raw_buffer_store_b128(o, rs, lane * 16, WS_Q + ((hd + hh) * 64 + tt) * 1024, K1_ST_AUX);
        else if (m == 1) __builtin_amdgcn_raw_buffer_store_b128(o, rs, lane * 16, WS_KV + (hd + hh) * KV_HEAD + (tt >> 3) * KV_CHUNK + (tt & 7) * 1024, K1_ST_AUX);
        else __builtin_amdgcn_raw_buffer_store_b128(o, rs, (r32 >> 4) * KV_HEAD + (16 * h + (lane & 15)) * 16 + hh * 512,
                                                    WS_KV + hd * KV_HEAD + (tt >> 3) * KV_CHUNK + 8192 + (tt & 7) * 1024, K1_ST_AUX);
    };
    sfor<0, 96>([&](auto G) {
        constexpr int g = G.value, n = g + 4, m = g >> 5;
        constexpr unsigned o = k1_slot(n >> 3) + (n & 7) * 2048, oi = o & 65535;
        constexpr int aR = m == 1 ? 80 : 64, wR = 96 + 4 * (g % 5), nR = 96 + 4 * (n % 5), xR = 128 + 4 * (g & 31);
        constexpr int nx = (g - 4 >= 0 && g - 4 < 12) + (g - 3 >= 0 && g - 3 < 12) + (g - 2 >= 0 && g - 2 < 12) + (g - 1 >= 0 && g - 1 < 12), wc_ = n >= 96 ? 0 : (8 + 2 * nx > 14 ? 14 : 8 + 2 * nx);
#define KRD(B) "ds_read_b64_tr_b16 v[%c[nr]:%c[nr]+1], %[" B "] offset:%c[oi]\n\tds_read_b64_tr_b16 v[%c[nr]+2:%c[nr]+3], %[" B "] offset:%c[oi]+512\n\t"
        asm volatile(".if %c[bar] == 1\n\t" KCHK("%c[st]") "s_cbranch_scc1 2f\n\t" KSPIN("%c[st]") "2:\n\t.endif\n\t"
                     ".if %c[bar] == 2\n\tds_read_b128 v[116:119], %[fa]\n\t.endif\n\t"
                     ".if %c[rd] == 1\n\t" KRD("tb") ".endif\n\t.if %c[rd] == 2\n\t" KRD("tc") ".endif\n\t.if %c[rd] == 3\n\t" KRD("ta") ".endif\n\t"
                     "s_waitcnt lgkmcnt(%c[wn])\n\t"
                     ".if %c[md] == 0\n\tv_mfma_f32_32x32x16_bf16 v[%c[ar]:%c[ar]+15], v[%c[wr]:%c[wr]+3], v[%c[xr]:%c[xr]+3], v[%c[ar]:%c[ar]+15]\n\t.endif\n\t"
                     ".if %c[md] == 1\n\tv_mfma_f32_32x32x16_bf16 v[%c[ar]:%c[ar]+15], v[%c[xr]:%c[xr]+3], v[%c[wr]:%c[wr]+3], v[%c[ar]:%c[ar]+15]\n\t.endif\n\t"
                     ".if %c[md] == 2\n\tv_mfma_f32_32x32x16_bf16 v[%c[ar]:%c[ar]+15], v[%c[xr]:%c[xr]+3], v[%c[wr]:%c[wr]+3], 0\n\t.endif\n\t"
                     ".if %c[md] == 3\n\tv_mfma_f32_32x32x16_bf16 v[%c[ar]:%c[ar]+15], v[%c[wr]:%c[wr]+3], v[%c[xr]:%c[xr]+3], 0\n\t.endif\n\t"
                     ".if %c[xm] == 1\n\tds_read_b128 v[%c[xd]:%c[xd]+3], %[x0] offset:%c[xo]\n\tds_read_b128 v[%c[xd]+4:%c[xd]+7], %[x1] offset:%c[xo]\n\t.endif\n\t"
                     ".if %c[xm] == 2\n\tds_read_b128 v[%c[xd]:%c[xd]+3], %[x2] offset:%c[xo]\n\tds_read_b128 v[%c[xd]+4:%c[xd]+7], %[x3] offset:%c[xo]\n\t.endif\n\t"
                     ".if %c[xn]\n\t" KXN(0) KXN(1) KXN(2) KXN(3) ".endif"
                     : "+{v[64:79]}"(acA), "+{v[80:95]}"(acB), "+{v[96:99]}"(w0), "+{v[100:103]}"(w1), "+{v[104:107]}"(w2), "+{v[108:111]}"(w3), "+{v[112:115]}"(w4),
                       "+{v[128:143]}"(xa0), "+{v[144:159]}"(xa1), "+{v[160:175]}"(xa2), "+{v[176:191]}"(xa3), "+{v[192:207]}"(xa4), "+{v[208:223]}"(xa5), "+{v[224:239]}"(xa6), "+{v[240:255]}"(xa7),
                       "+{v[116:119]}"(fl), [t] "=&s"(tq), [x0] "+v"(xb[0]), [x1] "+v"(xb[1]), [x2] "+v"(xb[2]), [x3] "+v"(xb[3])
                     : [ta] "v"(tA), [tb] "v"(tB), [tc] "v"(tC), [fa] "v"(fa), [st] "n"(n >> 3), [wn] "n"(wc_),
                       [bar] "n"(K1_ABL ? 0 : n < 96 && (n & 7) == 0 ? 1 : n < 92 && (n & 7) == 4 ? 2 : 0), [rd] "n"(n >= 96 ? 0 : o < 65536 ? 3 : o < 131072 ? 1 : 2), [oi] "n"(oi), [nr] "n"(nR),
                       [md] "n"(m < 2 ? ((g & 31) == 0 ? 3 : 0) : g == 64 ? 2 : 1), [ar] "n"(aR), [wr] "n"(wR), [xr] "n"(xR),
                       [xm] "n"(g < 12 ? 1 + (g & 1) : 0), [xd] "n"(160 + 8 * g), [xo] "n"(((g >> 1) & 1) * 8192), [xn] "n"(g < 11 && (g & 3) == 3 ? 1 : 0) : "scc", "memory");
        if (g == 7 && lane == 0) asm volatile("ds_add_u32 %0, %1" :: "v"(fa + 28), "v"(1u) : "memory");
        if (g == 16 && lane == 0) asm volatile("ds_add_u32 %0, %1" :: "v"(fa + 20), "v"(1u) : "memory");
        if (g == 23 && lane == 0) asm volatile("ds_add_u32 %0, %1" :: "v"(fa + 24), "v"(1u) : "memory");
        if (g == 36 || g == 38) epi(0, acA, (g - 36) >> 1);
        if (g == 68 || g == 70) epi(1, acB, (g - 68) >> 1);
    });
    STAMP(0, 2);
    asm volatile("s_nop 15" : "+{v[64:79]}"(acA));
    epi(2, acA, 0); epi(2, acA, 1);
    SBAR(); BAR_L();
    ep = RFL(*(const LDSAS unsigned*)(fa + 48));
    WAITV(0);
    if (lane == 0) __builtin_amdgcn_raw_buffer_store_b32(ep, rs, 0, WS_FQ + ((cb * 2 + wc) * 64 + tt) * 128, AUX_SC1);
    STAMP(0, 5);
    FLUSH(0);
    }
    }

    const int rt = xcd * 4 + (bi >> 3), ct = bi & 7;
    u32x4 hreg[2];
    u32x8 xres;

    auto issue_wo = [&](int first, int n) {
        const rsrc_t rwo = mk_rsrc(wo, D * D * 2);
        const unsigned v = w64_voff(lane);
        for (int p = first; p < first + n; ++p) dma16(rwo, v, p * 8192 + ct * 128, lds0 + L_WO + p * 1024);
    };
    auto issue_dn = [&](int i0, int n, unsigned dst, int hw) {
        const rsrc_t rwd = mk_rsrc(wd, F * D * 2);
        const unsigned v = w64_voff(lane);
        const unsigned so = ((43 * (hw >> 1) + i0) * 16 + (hw & 1) * 8) * 1024 + ct * 128;
        for (int i = 0; i < n; ++i) dma16(rwd, v, so + i * 16384, lds0 + dst + (hw & 1) * 1024 + i * 2048);
    };
    auto gu_wa = [&](int n) -> unsigned { const unsigned blk = (lane >> 4) & 1;
        return lds0 + L_GU + blk * 49152 + (((4 * h + ((lane & 15) >> 2)) * 96 + n * 32 + (lane & 3) * 8) ^ (blk * 128)); };
    float cs[3] = {0.f, 0.f, 0.f};
    auto gu_colsum = [&]() {
#pragma unroll
        for (int hf = 0; hf < 2; ++hf) {
            u32x4 f[3][4];
#pragma unroll
            for (int n = 0; n < 3; ++n) {
                const unsigned a0 = gu_wa(n) + (w * 8 + hf * 4) * 1536;
#pragma unroll
                for (int i = 0; i < 4; ++i) f[n][i] = __builtin_bit_cast(u32x4, lds_tr2(a0 + i * 1536, a0 + i * 1536 + 768));
            }
            SBAR();
#pragma unroll
            for (int i = 0; i < 4; ++i)
#pragma unroll
                for (int e = 0; e < 4; ++e)
#pragma unroll
                    for (int n = 0; n < 3; ++n) cs[n] = dot2(f[n][i][e], ONES2, cs[n]);
        }
#pragma unroll
        for (int n = 0; n < 3; ++n) cs[n] = swap_add(cs[n]);
    };

    {
        const int head = xcd * 4 + (bi >> 3), qb = bi & 7, wq = w & 3, g = w >> 2, qt = qb * 8 + wq * 2;
        const unsigned kvs = WS_KV + head * KV_HEAD;
        const unsigned l16 = lane * 16;
        unsigned ks = kvs + g * 4 * KV_CHUNK + wq * 4096, km = lds0 + g * 32768 + wq * 4096;
        const rsrc_t rg = mk_rsrc(g ? wu : wg, bi < 29 ? D * F * 2 : 0), rwd = mk_rsrc(wd, F * D * 2), rwo = mk_rsrc(wo, D * D * 2), rx = mk_rsrc(x, S * D * 2);
        unsigned gv0, gv1, gv2;
        { unsigned c = lane ^ (g * 8); gv0 = (c / 6) * (F * 2) + (c % 6) * 16; c = (64 + lane) ^ (g * 8); gv1 = (c / 6) * (F * 2) + (c % 6) * 16;
          c = (128 + lane) ^ (g * 8); gv2 = (c / 6) * (F * 2) + (c % 6) * 16; }
        unsigned gs = wq * 4 * 32 * (F * 2) + bi * 96, gm = lds0 + L_GU + g * 49152 + wq * 12288;
        const unsigned pvd = lane < 43 ? (bi * 344 + w * 43 + lane) * 128 : 0x80000000u, pvo = lane < 16 ? (bi * 128 + w * 16 + lane) * 128 : 0x80000000u;
        const unsigned wv = w64_voff(lane), xv = (rt * 64 + wt * 32 + r32) * 1024 + (ct * 64 + kh * 32 + 4 * h) * 2;
        const unsigned kb = lds0 + g * 32768 + l16, vb = lds0 + g * 32768 + 8192 + (16 * h + (r32 & 15)) * 16;
        const unsigned cst = (r32 & 27) == 16 ? ONES2 : 0u;
        const unsigned long long msk = 0x0000ffff0000ffffull;
        unsigned pfd = 0;
        xres = u32x8{};
        s16x8 q0, q1;
        STAMP(1, 0);
        SBAR();
#define XKV "s_mov_b32 m0, %[km]\n\ts_add_u32 %[km], %[km], 0x400\n\tbuffer_load_dwordx4 %[l16], %[rs], %[ks] offen sc1 lds\n\ts_add_u32 %[ks], %[ks], 0x400\n\t"
        asm volatile("s_barrier\n\tbuffer_load_dwordx4 v[128:131], %[l16], %[rs], %[qs] offen sc1\n\tbuffer_load_dwordx4 v[156:159], %[l16], %[rs], %[qs] offen offset:1024 sc1\n\t"
                     XKV XKV XKV XKV "s_add_u32 %[km], %[km], 0x3000\n\ts_add_u32 %[ks], %[ks], 0x3000\n\ts_waitcnt vmcnt(0)\n\ts_barrier\n\t" XKV XKV XKV XKV
                     : "={v[128:131]}"(q0), "={v[156:159]}"(q1), [km] "+s"(km), [ks] "+s"(ks)
                     : [l16] "v"(l16), [rs] "s"(rs), [qs] "s"(WS_Q + (head * 64 + qt) * 1024) : "m0", "scc", "memory");
        km -= 16384 + 4096; ks += KV_CHUNK - 4096;
        STAMP(1, 1);
        f32x16 n0, n1, sA, sB = {}, o0 = {}, o1 = {};
        s16x8 kf = lds_b128(kb), v0, v1, v1b;
        {
            const f32x16 z = {};
            const s16x8 kz = lds_b128(lds0 + l16);
            const u32x4 vv = __builtin_bit_cast(u32x4, lds_b128(vb)), cc = {cst, cst, cst, cst};
            const f32x16 s0 = MFMA(kz, q0, z), s1 = MFMA(kz, q1, z);
            float m0 = __builtin_fmaxf(s0[0], s0[1]), m1 = __builtin_fmaxf(s1[0], s1[1]);
#pragma unroll
            for (int i = 2; i < 16; i += 2) { m0 = __builtin_fmaxf(__builtin_fmaxf(m0, s0[i]), s0[i + 1]); m1 = __builtin_fmaxf(__builtin_fmaxf(m1, s1[i]), s1[i + 1]); }
            m0 = swap_max(m0); m1 = swap_max(m1);
#pragma unroll
            for (int i = 0; i < 16; ++i) { n0[i] = -m0; n1[i] = -m1; }
            sA = MFMA(kf, q0, n0);
            v0 = __builtin_bit_cast(s16x8, r32 < 16 ? vv : cc); v1 = v1b = __builtin_bit_cast(s16x8, cc);
        }
        u32x4 pA = {0u, 0u, 0u, 0u}, pB = {0u, 0u, 0u, 0u};
#define XE(C, i) "v_exp_f32 v[" #C "+" #i "], v[" #C "+" #i "]\n\t"
#define XC(P, C, i) "v_cvt_pk_bf16_f32 v" #P ", v[" #C "+" #i "], v[" #C "+" #i "+1]\n\t"
#define XG1(C) XE(C, 0) XE(C, 1) XE(C, 2) XE(C, 3) XC(148, C, 0) XE(C, 4) XE(C, 5) XC(149, C, 2)
#define XG2(C) XE(C, 6) XE(C, 7) XC(150, C, 4) XE(C, 8) XC(151, C, 6) XE(C, 9) XE(C, 10)
#define XG3(C) XE(C, 11) XC(152, C, 8) XE(C, 12) XE(C, 13) XC(153, C, 10) XE(C, 14) XE(C, 15) XC(154, C, 12) XC(155, C, 14)
#define XM(D, A, B, C) "v_mfma_f32_32x32x16_bf16 v[" #D ":" #D "+15], v[" #A ":" #A "+3], v[" #B ":" #B "+3], v[" #C ":" #C "+15]\n\t"
#define XRK(BASE, O) "ds_read_b128 v[132:135], " BASE " offset:" #O "\n\t"
#define XRV(R, BASE, O) "s_mov_b64 exec, %[msk]\n\tds_read_b128 v[" #R ":" #R "+3], " BASE " offset:" #O "\n\ts_mov_b64 exec, -1\n\t"
#define XW(n) "s_waitcnt lgkmcnt(" #n ")\n\t"
#define XTILE(VA, RK, RV1, WA, X1, VB, X2, RV0) \
        XM(80, 132, 156, 176) RK XG1(64) XM(160, VA, 152, 160) RV1 XG2(64) XW(WA) XM(96, 140, 148, 96) XG3(64) \
        XW(0) XM(64, 132, 128, 112) X1 XG1(80) XM(96, VB, 152, 96) X2 XG2(80) XM(160, 140, 148, 160) RV0 XG3(80)
#define XGU(j) "s_mov_b32 m0, %[gm]\n\ts_add_u32 %[gm], %[gm], 0x400\n\tbuffer_load_dwordx4 %[gv" #j "], %[rg], %[gs] offen lds\n\t"
#define XGU2 XGU(2) "s_add_u32 %[gs], %[gs], 88064\n\t"
#define XPF "buffer_load_dword v200, %[pvd], %[rwd], 0 offen\n\tbuffer_load_dword v200, %[pvo], %[rwo], 0 offen\n\t"
#define XWO "s_mov_b32 m0, %[km]\n\ts_add_u32 %[km], %[km], 0x400\n\tbuffer_load_dwordx4 %[wv], %[rwo], %[ks] offen lds\n\ts_add_u32 %[ks], %[ks], 0x2000\n\t"
#define XXR(R, O) "buffer_load_dwordx2 v[" #R ":" #R "+1], %[xv], %[rx], 0 offen offset:" #O "\n\t"
#define AOPS : "+{v[64:79]}"(sA), "+{v[80:95]}"(sB), "+{v[96:111]}"(o0), "+{v[160:175]}"(o1), "+{v[112:127]}"(n0), "+{v[176:191]}"(n1), "+{v[128:131]}"(q0), "+{v[156:159]}"(q1), \
               "+{v[132:135]}"(kf), "+{v[140:143]}"(v0), "+{v[144:147]}"(v1), "+{v[136:139]}"(v1b), "+{v[148:151]}"(pA), "+{v[152:155]}"(pB), "+{v[192:199]}"(xres), "+{v200}"(pfd), \
               [km] "+s"(km), [ks] "+s"(ks), [gm] "+s"(gm), [gs] "+s"(gs) \
             : [ka] "v"(ka), [va] "v"(va), [kn] "v"(kn), [vn] "v"(vn), [msk] "s"(msk), [l16] "v"(l16), [rs] "s"(rs), [rg] "s"(rg), [gv0] "v"(gv0), [gv1] "v"(gv1), [gv2] "v"(gv2), \
               [pvd] "v"(pvd), [pvo] "v"(pvo), [wv] "v"(wv), [xv] "v"(xv), [rwd] "s"(rwd), [rwo] "s"(rwo), [rx] "s"(rx) : "m0", "scc", "memory"
#define XBAR(n) asm volatile("s_waitcnt vmcnt(" #n ") lgkmcnt(0)\n\ts_barrier" AOPS)
#define XROUND(A1, A2, B1, B2, C1, D1, E1, F1) asm volatile( \
            XTILE(136, XRK("%[ka]", 1024), XRV(144, "%[va]", 512), 2, A1, 144, A2, XRV(140, "%[va]", 1024)) \
            XTILE(144, XRK("%[ka]", 2048), XRV(144, "%[va]", 1536), 2, B1, 144, B2, XRV(140, "%[va]", 2048)) \
            XTILE(144, XRK("%[ka]", 3072), XRV(144, "%[va]", 2560), 2, C1, 144, "", XRV(140, "%[va]", 3072)) \
            XTILE(144, XRK("%[ka]", 4096), XRV(144, "%[va]", 3584), 2, D1, 144, "", XRV(140, "%[va]", 4096)) \
            XTILE(144, XRK("%[ka]", 5120), XRV(144, "%[va]", 4608), 2, E1, 144, "", XRV(140, "%[va]", 5120)) \
            XTILE(144, XRK("%[ka]", 6144), XRV(144, "%[va]", 5632), 2, F1, 144, "", XRV(140, "%[va]", 6144)) \
            XTILE(144, XRK("%[ka]", 7168), XRV(144, "%[va]", 6656), 2, XRV(136, "%[va]", 7680), 144, "", XRV(140, "%[va]", 7168)) AOPS)
#define XLAST(X1, X2) asm volatile(XTILE(144, XRK("%[kn]", 0), "", 1, X1, 136, X2, XRV(140, "%[vn]", 0)) AOPS)
        unsigned ka = kb, va = vb, kn = kb + 16384, vn = vb + 16384;
        asm volatile("s_nop 15" AOPS);
        STAMP(1, 7);
        XROUND(XGU(0), "", XGU(1), "", XGU2, XGU(0), XGU(1), XGU2);
        STAMP(1, 8);
        XBAR(6);
        STAMP(1, 9);
        XLAST(XKV, XKV);
        ka = kb + 16384; va = vb + 16384; kn = kb; vn = vb;
        XROUND(XKV, XKV, XGU(0), XGU(1), XGU2, XGU(0), XGU(1), XGU2);
        STAMP(1, 10);
        XBAR(6);
        STAMP(1, 11);
        km += 16384 - 4096; ks += KV_CHUNK - 4096;
        XLAST(XKV, XKV);
        ka = kb; va = vb; kn = kb + 16384; vn = vb + 16384;
        XROUND(XKV, XKV, XXR(192, 0), XXR(194, 16), XXR(196, 32), XXR(198, 48), "", XPF);
        STAMP(1, 12);
        XBAR(6);
        STAMP(1, 13);
        km = lds0 + g * 32768 + wq * 4096; ks = (32 * g + 4 * wq) * 8192 + ct * 128;
        XLAST(XWO, XWO);
        ka = kb + 16384; va = vb + 16384; kn = kb; vn = vb;
        XROUND(XWO, XWO, "", "", "", "", "", "");
        XLAST("", "");
        STAMP(1, 14);
        asm volatile(XM(160, 136, 152, 160) "s_waitcnt vmcnt(4) lgkmcnt(0)\n\ts_barrier\n\ts_nop 15" AOPS);
        STAMP(1, 4);
        f32x16 o;
        {
            const f32x16& snd = g ? o0 : o1;
            const unsigned xa = lds0 + g * 32768 + 16384 + wq * 3072 + l16, xb = lds0 + (g ^ 1) * 32768 + 16384 + wq * 3072 + l16;
            *(LDSAS f32x4*)(xa) = f32x4{snd[0], snd[1], snd[2], snd[3]};
            *(LDSAS f32x4*)(xa + 1024) = f32x4{snd[4], snd[5], snd[6], snd[7]};
            *(LDSAS float*)(xa + 2048) = snd[8];
            if (tid == 0) *(LDSAS unsigned*)(lds0 + L_FLAG) = 0u;
            BAR_L();
            const f32x4 p0 = lds_f4(xb), p1 = lds_f4(xb + 1024); const float pl = *(const LDSAS float*)(xb + 2048);
            o = g ? o1 : o0;
#pragma unroll
            for (int i = 0; i < 4; ++i) { o[i] += p0[i]; o[4 + i] += p1[i]; }
            o[8] += pl;
        }
        bool bad = !(o[8] < 1.8446744e19f);
#pragma unroll
        for (int i = 0; i < 8; ++i) bad |= !(__builtin_fabsf(o[i]) < 3.0e38f);
        if (__any(bad) && lane == 0) *(LDSAS unsigned*)(lds0 + L_FLAG) = 1u;
        auto store_o = [&]() {
            const float inv = __builtin_amdgcn_rcpf(o[8]);
            u32x4 ov;
#pragma unroll
            for (int i = 0; i < 4; ++i) ov[i] = pk2(o[2 * i] * inv, o[2 * i + 1] * inv);
            __builtin_amdgcn_raw_buffer_store_b128(ov, rs, l16, WS_O + ((qt + g) * 32 + head) * 1024, AUX_SC1);
        };
        store_o();
        BAR_VL(0);
        if (__builtin_amdgcn_readfirstlane(*(const LDSAS unsigned*)(lds0 + L_FLAG))) {
            BAR_L();
            const s16x8 qf = g ? q1 : q0;
            const u32x4 cc = {cst, cst, cst, cst};
            const unsigned vf = lds0 + 16384 + 8192 + (16 * h + (r32 & 15)) * 16;
            float m = -__builtin_inff();
            o = f32x16{};
            const f32x16 z = {};
#pragma unroll 1
            for (int c = 0; c < 8; ++c) {
                dma16<true>(rs, l16, kvs + c * KV_CHUNK + w * 2048, lds0 + 16384 + w * 2048);
                dma16<true>(rs, l16, kvs + c * KV_CHUNK + w * 2048 + 1024, lds0 + 16384 + w * 2048 + 1024);
                BAR_VL(0);
#pragma unroll 1
                for (int t = 0; t < 8; ++t) {
                    f32x16 s = MFMA(lds_b128(lds0 + 16384 + l16 + t * 1024), qf, z);
                    float mx = s[0];
#pragma unroll
                    for (int i = 1; i < 16; ++i) mx = __builtin_fmaxf(mx, s[i]);
                    mx = __builtin_fmaxf(swap_max(mx), m);
                    const float al = __builtin_amdgcn_exp2f(m - mx);
                    m = mx;
#pragma unroll
                    for (int i = 0; i < 16; ++i) { o[i] *= al; s[i] = __builtin_amdgcn_exp2f(s[i] - m); }
#pragma unroll
                    for (int i = 0; i < 4; ++i) { pA[i] = pk2(s[2 * i], s[2 * i + 1]); pB[i] = pk2(s[8 + 2 * i], s[9 + 2 * i]); }
                    const u32x4 a = __builtin_bit_cast(u32x4, lds_b128(vf + t * 1024)), c2 = __builtin_bit_cast(u32x4, lds_b128(vf + t * 1024 + 512));
                    o = MFMA(__builtin_bit_cast(s16x8, r32 < 16 ? a : cc), __builtin_bit_cast(s16x8, pA), o);
                    o = MFMA(__builtin_bit_cast(s16x8, r32 < 16 ? c2 : cc), __builtin_bit_cast(s16x8, pB), o);
                }
                BAR_L();
            }
            store_o();
            BAR_VL(0);
        }
        STAMP(1, 5);
        if (tid == 0) __builtin_amdgcn_raw_buffer_store_b32(ep, rs, 0, WS_FO + (qb * 32 + head) * 128, AUX_SC1);
        BAR_L();
        if (w >= 4) issue_wo(16 + ((w - 4) >> 1) * 32 + (w & 1) * 8, 8); else if (bi < 29) gu_colsum();
        STAMP(1, 6);
        FLUSH(1);
    }

    {
        STAMP(2, 0);
        if (w == 4) { __builtin_amdgcn_s_sleep(POLL_DELAY_O); wait_flags(rs, WS_FO + xcd * 4096, 32, ep, lane); }
        BAR_VL(0);
        STAMP(2, 1);
        f32x16 a0, a1 = {};
        if (w < 4) {
            constexpr int AW = 12;
            s16x8 af[AW];
            const unsigned so = WS_O + ((rt * 2 + wt) * 32 + kh * 16) * 1024;
            auto ld = [&](int i) { af[i % AW] = __builtin_bit_cast(s16x8, __builtin_amdgcn_raw_buffer_load_b128(rs, lane * 16 + (i & 3) * 1024, so + (i >> 2) * 4096, AUX_SC1)); };
#pragma unroll
            for (int i = 0; i < AW - 1; ++i) ld(i);
            SBAR();
            unsigned t0 = lds0 + L_WO + kh * 32768 + w64_tr(lane, kh), t1 = lds0 + L_WO + kh * 32768 + w64_tr(lane, kh ^ 1);
#pragma unroll
            for (int g = 0; g < 4; ++g) { a0[4 * g] = bflo(xres[2 * g]); a0[4 * g + 1] = bfhi(xres[2 * g]); a0[4 * g + 2] = bflo(xres[2 * g + 1]); a0[4 * g + 3] = bfhi(xres[2 * g + 1]); }
            PIN(t0); PIN(t1);
            s16x8 wf[3][2];
            auto rd = [&](int i) { wf[i % 3][0] = lds_tr2(t0 + i * 2048, t0 + i * 2048 + 1024); wf[i % 3][1] = lds_tr2(t1 + i * 2048, t1 + i * 2048 + 1024); };
            rd(0); rd(1);
#pragma unroll
            for (int i = 0; i < 16; ++i) {
                if (i + AW - 1 < 16) ld(i + AW - 1);
                if (i + 2 < 16) rd(i + 2);
                SBAR();
                a0 = MFMA(wf[i % 3][0], af[i % AW], a0);
                a1 = MFMA(wf[i % 3][1], af[i % AW], a1);
                SBAR();
            }
        }
        STAMP(2, 2);
        BAR_L();
        if (w < 4) {
#pragma unroll
            for (int g = 0; g < 4; ++g) *(LDSAS f32x4*)(lds0 + w * 4096 + g * 1024 + lane * 16) = f32x4{a1[4 * g], a1[4 * g + 1], a1[4 * g + 2], a1[4 * g + 3]};
        }
        BAR_L();
        if (w < 4) {
            f32x4 p[4];
#pragma unroll
            for (int g = 0; g < 4; ++g) p[g] = lds_f4(lds0 + (w ^ 2) * 4096 + g * 1024 + lane * 16);
            SBAR();
#pragma unroll
            for (int g = 0; g < 4; ++g) {
                hreg[g >> 1][(g & 1) * 2] = pk2(a0[4 * g] + p[g][0], a0[4 * g + 1] + p[g][1]);
                hreg[g >> 1][(g & 1) * 2 + 1] = pk2(a0[4 * g + 2] + p[g][2], a0[4 * g + 3] + p[g][3]);
            }
            const unsigned so = WS_H + ((rt * 2 + wt) * 32 + (ct * 2 + kh) * 2) * 1024;
            __builtin_amdgcn_raw_buffer_store_b128(hreg[0], rs, lane * 16, so, AUX_SC1);
            __builtin_amdgcn_raw_buffer_store_b128(hreg[1], rs, lane * 16 + 1024, so, AUX_SC1);
            SBAR();
            float sm = 0.f, sq = 0.f;
#pragma unroll
            for (int i = 0; i < 8; ++i) { sm = dot2(hreg[i >> 2][i & 3], ONES2, sm); sq = dot2(hreg[i >> 2][i & 3], hreg[i >> 2][i & 3], sq); }
            sm = swap_add(sm); sq = swap_add(sq);
            if (h == 0) __builtin_amdgcn_raw_buffer_store_b64(u32x2{__float_as_uint(sm), __float_as_uint(sq)}, rs, r32 * 8,
                                                              WS_ST + ((rt * 2 + wt) * 16 + ct * 2 + kh) * 256, AUX_SC1);
        }
        STAMP(2, 3);
        BAR_VL(0);
        STAMP(2, 4);
        if (tid == 0) __builtin_amdgcn_raw_buffer_store_b32(ep, rs, 0, WS_FH + (xcd * 32 + bi) * 128, AUX_SC1);
        if (w >= 4) issue_dn(0, 15, ((w - 4) >> 1) * 15 * 2048, w - 4);
        STAMP(2, 5);
        FLUSH(2);
    }

    {
        STAMP(3, 0);
        if (bi < 29) {
            unsigned wa[3];
#pragma unroll
            for (int n = 0; n < 3; ++n) { wa[n] = gu_wa(n); PIN(wa[n]); if (w < 4 && h == 0) *(LDSAS float*)(lds0 + L_TAB + (w * 96 + n * 32 + r32) * 4) = cs[n]; }
            if (w == 4) { __builtin_amdgcn_s_sleep(POLL_DELAY_H); wait_flags(rs, WS_FH + xcd * 4096, 32, ep, lane); }
            BAR_L();
            STAMP(3, 2);
            const int tt = xcd * 8 + w;
            constexpr int AW = GU_AW;
            s16x8 af[AW]; u32x2 st[8];
            auto ld = [&](int i) { af[i % AW] = __builtin_bit_cast(s16x8, __builtin_amdgcn_raw_buffer_load_b128(rs, lane * 16 + (i & 3) * 1024, WS_H + tt * 32768 + (i >> 2) * 4096, AUX_SC1)); };
#pragma unroll
            for (int i = 0; i < AW - 1; ++i) ld(i);
            SBAR();
            f32x4 ctab[3][4];
#pragma unroll
            for (int n = 0; n < 3; ++n)
#pragma unroll
                for (int g = 0; g < 4; ++g) { const unsigned a = lds0 + L_TAB + (n * 32 + 8 * g + 4 * h) * 4;
                    ctab[n][g] = (lds_f4(a) + lds_f4(a + 384)) + (lds_f4(a + 768) + lds_f4(a + 1152)); PIN(ctab[n][g]); }
            SBAR();
            f32x16 acc[3] = {};
            s16x8 wf[2][3];
            auto rd = [&](int i) {
#pragma unroll
                for (int n = 0; n < 3; ++n) wf[i & 1][n] = lds_tr2(wa[n] + i * 1536, wa[n] + i * 1536 + 768); };
            rd(0);
#pragma unroll
            for (int i = 0; i < 32; ++i) {
                if (i + AW - 1 < 32) ld(i + AW - 1);
                else if (i + AW - 1 < 40) st[i + AW - 33] = __builtin_amdgcn_raw_buffer_load_b64(rs, (8 * h + i + AW - 33) * 256 + r32 * 8, WS_ST + tt * 4096, AUX_SC1);
                if (i + 1 < 32) rd(i + 1);
                SBAR();
#pragma unroll
                for (int n = 0; n < 3; ++n) acc[n] = MFMA(wf[i & 1][n], af[i % AW], acc[n]);
                SBAR();
            }
#pragma unroll
            for (int i = AW - 1; i < 8; ++i) st[i] = __builtin_amdgcn_raw_buffer_load_b64(rs, (8 * h + i) * 256 + r32 * 8, WS_ST + tt * 4096, AUX_SC1);
            STAMP(3, 3);
            float sm = 0.f, sq = 0.f;
#pragma unroll
            for (int i = 0; i < 8; ++i) { sm += __uint_as_float(st[i][0]); sq += __uint_as_float(st[i][1]); }
            sm = swap_add(sm); sq = swap_add(sq);
            const float mean = sm * (1.f / D);
            const float var = __builtin_fmaxf(sq * (1.f / D) - mean * mean, 0.f) + LN_EPS;
            const float rstd = __builtin_amdgcn_rsqf(var), sd = var * rstd, ga = -LOG2E * rstd;
            auto epi = [&](int n) {
                u32x4 ov; float y[8];
#pragma unroll
                for (int g = 0; g < 2; ++g) {
                    const f32x4 cg = ctab[n][g], cu = ctab[n][2 + g];
#pragma unroll
                    for (int i = 0; i < 4; ++i) {
                        const float e = __builtin_amdgcn_exp2f(__builtin_fmaf(-mean, cg[i], acc[n][4 * g + i]) * ga);
                        y[4 * g + i] = __builtin_fmaf(-mean, cu[i], acc[n][8 + 4 * g + i]) * __builtin_amdgcn_rcpf(__builtin_fmaf(e, sd, sd));
                    }
                }
#pragma unroll
                for (int i = 0; i < 4; ++i) ov[i] = pk2(y[2 * i], y[2 * i + 1]);
                const int u = bi * 3 + n;
                if (u < NU) __builtin_amdgcn_raw_buffer_store_b128(ov, rs, lane * 16, WS_HH + (tt * NU + u) * 1024, AUX_SC1);
            };
            epi(0); epi(1);
            epi(2);
        }
        STAMP(3, 4);
        BAR_VL(0);
        STAMP(3, 5);
        if (tid == 0 && bi < 29) __builtin_amdgcn_raw_buffer_store_b32(ep, rs, 0, WS_FG + (xcd * 32 + bi) * 128, AUX_SC1);
        if (w >= 4) { issue_dn(15, 24, L_GU + ((w - 4) >> 1) * 24 * 2048, w - 4); issue_dn(39, 1, L_TAB + ((w - 4) >> 1) * 2048, w - 4); STAMP(3, 6); }
        FLUSH(3);
    }

    {
        STAMP(4, 0);
        if (w == 4) wait_flags(rs, WS_FG + xcd * 4096, 29, ep, lane);
        BAR_VL(0);
        if (b == 0 && tid == 0) __builtin_amdgcn_raw_buffer_store_b32(ep, rs, 0, WS_EP, AUX_SC1);
        STAMP(4, 1);
        f32x16 a0, a1 = {};
        if (w < 4) {
            constexpr int AW = DN_AW;
            s16x8 af[AW];
            const unsigned so = WS_HH + ((rt * 2 + wt) * NU + kh * 43) * 1024;
            auto ld = [&](int i) { af[i % AW] = __builtin_bit_cast(s16x8, __builtin_amdgcn_raw_buffer_load_b128(rs, lane * 16 + (i & 3) * 1024, so + (i >> 2) * 4096, AUX_SC1)); };
#pragma unroll
            for (int i = 0; i < AW - 1; ++i) ld(i);
            SBAR();
            unsigned tA[2], tB[2], tC[2];
#pragma unroll
            for (int n = 0; n < 2; ++n) { const unsigned t = lds0 + w64_tr(lane, n ^ kh);
                tA[n] = t + kh * 15 * 2048; tB[n] = t + L_GU + kh * 24 * 2048; tC[n] = t + L_TAB + kh * 2048; PIN(tA[n]); PIN(tB[n]); PIN(tC[n]); }
#pragma unroll
            for (int i = 0; i < 8; ++i) { a0[2 * i] = bflo(hreg[i >> 2][i & 3]); a0[2 * i + 1] = bfhi(hreg[i >> 2][i & 3]); }
            s16x8 wf[3][2];
            auto rd = [&](int i) {
#pragma unroll
                for (int n = 0; n < 2; ++n) { const unsigned a = i < 15 ? tA[n] + i * 2048 : i < 39 ? tB[n] + (i - 15) * 2048 : i < 40 ? tC[n] : tA[n] + (i - 40) * 2048;
                    wf[i % 3][n] = lds_tr2(a, a + 1024); } };
            rd(0); rd(1);
#pragma unroll
            for (int i = 0; i < 43; ++i) {
                if (i == 1 || i == 38) BAR_L();
                if (i + AW - 1 < 43) ld(i + AW - 1);
                if (i + 2 < 43) rd(i + 2);
                SBAR();
                a0 = MFMA(wf[i % 3][0], af[i % AW], a0);
                a1 = MFMA(wf[i % 3][1], af[i % AW], a1);
                SBAR();
            }
        } else {
            BAR_L();
            issue_dn(40, 3, ((w - 4) >> 1) * 15 * 2048, w - 4);
            BAR_VL(0);
        }
        STAMP(4, 2);
        BAR_L();
        if (w < 4) {
#pragma unroll
            for (int g = 0; g < 4; ++g) *(LDSAS f32x4*)(lds0 + w * 4096 + g * 1024 + lane * 16) = f32x4{a1[4 * g], a1[4 * g + 1], a1[4 * g + 2], a1[4 * g + 3]};
        }
        BAR_L();
        if (w < 4) {
            f32x4 p[4];
#pragma unroll
            for (int g = 0; g < 4; ++g) p[g] = lds_f4(lds0 + (w ^ 2) * 4096 + g * 1024 + lane * 16);
            SBAR();
            unsigned y[8];
#pragma unroll
            for (int g = 0; g < 4; ++g) {
                y[2 * g] = pk2(a0[4 * g] + p[g][0], a0[4 * g + 1] + p[g][1]);
                y[2 * g + 1] = pk2(a0[4 * g + 2] + p[g][2], a0[4 * g + 3] + p[g][3]);
            }
            const rsrc_t ro = mk_rsrc(out, S * D * 2);
            const unsigned vo = (rt * 64 + wt * 32 + r32) * 1024 + (ct * 64 + kh * 32 + 8 * h) * 2;
#pragma unroll
            for (int g = 0; g < 2; ++g) {
                auto e0 = __builtin_amdgcn_permlane32_swap(y[4 * g], y[4 * g + 2], false, false);
                auto e1 = __builtin_amdgcn_permlane32_swap(y[4 * g + 1], y[4 * g + 3], false, false);
                __builtin_amdgcn_raw_buffer_store_b128(u32x4{e0[0], e1[0], e0[1], e1[1]}, ro, vo + g * 32, 0, 0);
            }
        }
        STAMP(4, 3);
        STAMP(4, 4);
        FLUSH(4);
        asm volatile(".Lmain_end:");
    }
}

extern "C" void kernel_launch(void* const* d_in, const int* in_sizes, int n_in,
                              void* d_out, int out_size, void* d_ws, size_t ws_size,
                              hipStream_t stream) {
    const u16* x = (const u16*)d_in[0];
    const u16* wqkv = (const u16*)d_in[1];
    const u16* wo = (const u16*)d_in[2];
    const u16* wg = (const u16*)d_in[4];
    const u16* wu = (const u16*)d_in[5];
    const u16* wd = (const u16*)d_in[6];
    u16* out = (u16*)d_out;
    char* ws = (char*)d_ws;
    (void)hipFuncSetAttribute((const void*)k_block, hipFuncAttributeMaxDynamicSharedMemorySize, K2_LDS);
    k_block<<<256, 512, K2_LDS, stream>>>(x, wqkv, wo, wg, wu, wd, out, ws);
}
```

```cpp
#include <hip/hip_runtime.h>
#include <hip/hip_bf16.h>
#include <stdint.h>

typedef short s16x8 __attribute__((ext_vector_type(8)));
typedef short s16x4 __attribute__((ext_vector_type(4)));
typedef float f32x16 __attribute__((ext_vector_type(16)));
typedef float f32x4 __attribute__((ext_vector_type(4)));
typedef float f32x2 __attribute__((ext_vector_type(2)));
typedef unsigned u32x4 __attribute__((ext_vector_type(4)));
typedef unsigned u32x2 __attribute__((ext_vector_type(2)));
typedef unsigned u32x8 __attribute__((ext_vector_type(8)));
typedef __bf16 bf16x2 __attribute__((ext_vector_type(2)));
typedef unsigned short u16;
#define LDSAS __attribute__((address_space(3)))
#define GAS __attribute__((address_space(1)))
typedef __amdgpu_buffer_rsrc_t rsrc_t;

constexpr int S = 2048, D = 512, NH = 32, F = 1376, NU = F / 16;
constexpr float LN_EPS = 1e-5f;
constexpr float LOG2E = 1.4426950408889634f;

constexpr unsigned KV_CHUNK = 16384;
constexpr unsigned KV_HEAD = 8 * KV_CHUNK;
constexpr unsigned WS_EP = 0;
constexpr unsigned WS_FQ = 4096;
constexpr unsigned WS_FP = WS_FQ + 16 * 64 * 128;
constexpr unsigned WS_Q = WS_FP + 4096;
constexpr unsigned WS_KV = WS_Q + NH * 64 * 1024;
constexpr unsigned WS_O = WS_KV + NH * KV_HEAD;
constexpr unsigned WS_H = WS_O + 64 * 32 * 1024;
constexpr unsigned WS_ST = WS_H + 64 * 32 * 1024;
constexpr unsigned WS_HH = WS_ST + 64 * 16 * 256;
constexpr unsigned WS_END = WS_HH + 64 * NU * 1024;
constexpr int AUX_SC1 = 16;
#define STAMP_DECL
#define FLUSH(k) do {} while (0)
#define STAMP(k, i) do {} while (0)

#define SBAR() __builtin_amdgcn_sched_barrier(0)
#define RFL(x) ((unsigned)__builtin_amdgcn_readfirstlane(x))
#define PIN(x) asm volatile("" : "+v"(x))
#define MFMA(a, b, c) __builtin_amdgcn_mfma_f32_32x32x16_bf16((a), (b), (c), 0, 0, 0)
#define WAITV(n) asm volatile("s_waitcnt vmcnt(" #n ")" ::: "memory")
#define BAR_L() asm volatile("s_waitcnt lgkmcnt(0)\n\ts_barrier" ::: "memory")
#define BAR_0() asm volatile("s_barrier" ::: "memory")
#define BAR_VL(n) asm volatile("s_waitcnt vmcnt(" #n ") lgkmcnt(0)\n\ts_barrier" ::: "memory")

__device__ __forceinline__ rsrc_t mk_rsrc(const void* p, unsigned bytes) {
    return __builtin_amdgcn_make_buffer_rsrc((void*)p, (short)0, (int)bytes, 0x00020000);
}
template <bool SC1 = false> __device__ __forceinline__ void dma16(rsrc_t r, unsigned voff, unsigned soff, unsigned lds) {
    if constexpr (SC1) asm volatile("s_nop 4\n\ts_mov_b32 m0, %0\n\ts_nop 0\n\tbuffer_load_dwordx4 %1, %2, %3 offen sc1 lds"
                 :: "s"(__builtin_amdgcn_readfirstlane(lds)), "v"(voff), "s"(r), "s"(__builtin_amdgcn_readfirstlane(soff)) : "m0", "memory");
    else asm volatile("s_nop 4\n\ts_mov_b32 m0, %0\n\ts_nop 0\n\tbuffer_load_dwordx4 %1, %2, %3 offen lds"
                 :: "s"(__builtin_amdgcn_readfirstlane(lds)), "v"(voff), "s"(r), "s"(__builtin_amdgcn_readfirstlane(soff)) : "m0", "memory");
}
__device__ __forceinline__ unsigned pk2(float lo, float hi) {
    f32x2 v = {lo, hi};
    return __builtin_bit_cast(unsigned, __builtin_convertvector(v, bf16x2));
}
__device__ __forceinline__ float dot2(unsigned a, unsigned b, float c) {
    return __builtin_amdgcn_fdot2_f32_bf16(__builtin_bit_cast(bf16x2, a), __builtin_bit_cast(bf16x2, b), c, false);
}
__device__ __forceinline__ float swap_add(float v) {
    auto r = __builtin_amdgcn_permlane32_swap(__float_as_uint(v), __float_as_uint(v), false, false);
    return __uint_as_float(r[0]) + __uint_as_float(r[1]);
}
__device__ __forceinline__ float swap_max(float v) {
    auto r = __builtin_amdgcn_permlane32_swap(__float_as_uint(v), __float_as_uint(v), false, false);
    return __builtin_fmaxf(__uint_as_float(r[0]), __uint_as_float(r[1]));
}
__device__ __forceinline__ s16x8 lds_b128(unsigned a) { return *(const LDSAS s16x8*)(a); }
__device__ __forceinline__ f32x4 lds_f4(unsigned a) { return *(const LDSAS f32x4*)(a); }
__device__ __forceinline__ s16x8 lds_tr2(unsigned a0, unsigned a1) {
    s16x4 lo = __builtin_amdgcn_ds_read_tr16_b64_v4i16((LDSAS s16x4*)(a0));
    s16x4 hi = __builtin_amdgcn_ds_read_tr16_b64_v4i16((LDSAS s16x4*)(a1));
    return __builtin_shufflevector(lo, hi, 0, 1, 2, 3, 4, 5, 6, 7);
}
__device__ __forceinline__ float bflo(unsigned v) { return __uint_as_float(v << 16); }
__device__ __forceinline__ float bfhi(unsigned v) { return __uint_as_float(v & 0xffff0000u); }
constexpr unsigned ONES2 = 0x3f803f80u;
template <int I> struct ic { static constexpr int value = I; constexpr operator int() const { return I; } };
template <int B, int E, class Fn> __device__ __forceinline__ void sfor(Fn&& fn) { if constexpr (B < E) { fn(ic<B>{}); sfor<B + 1, E>(fn); } }

#define K1_X_AUX 2
#define K1_ABL 0
#define K1_ST_AUX 16
constexpr unsigned K1_W = 65536, K1_FLG = K1_W + 5 * 16384, K1_ST = K1_FLG + 512, K1_CS = K1_ST + 512;
__host__ __device__ constexpr unsigned k1_slot(int s) { return s < 5 ? K1_W + s * 16384 : s == 5 ? K1_W : s < 10 ? (s - 6) * 16384 : K1_W + (s - 9) * 16384; }
__device__ __forceinline__ void lds_spin_ge(unsigned addr, unsigned want) {
#pragma unroll 1
    for (unsigned n = 0; n < (1u << 22); ++n) {
        unsigned v; asm volatile("ds_read_b32 %0, %1\n\ts_waitcnt lgkmcnt(0)" : "=v"(v) : "v"(addr) : "memory");
        if ((unsigned)__builtin_amdgcn_readfirstlane(v) >= want) break;
        __builtin_amdgcn_s_sleep(1);
    }
}
__device__ __forceinline__ void lds_post(unsigned addr, unsigned v) {
    asm volatile("s_waitcnt lgkmcnt(0)\n\tds_write_b32 %0, %1" :: "v"(addr), "v"(v) : "memory");
}
__device__ __forceinline__ void lds_inc(unsigned addr, int lane) {
    asm volatile("s_waitcnt lgkmcnt(0)" ::: "memory");
    if (lane == 0) asm volatile("ds_add_u32 %0, %1" :: "v"(addr), "v"(1u) : "memory");
}
typedef unsigned u32x16 __attribute__((ext_vector_type(16)));
template <int CTRL> __device__ __forceinline__ float dpp_add(float v) {
    return v + __builtin_bit_cast(float, __builtin_amdgcn_update_dpp(0, __builtin_bit_cast(int, v), CTRL, 0xf, 0xf, true));
}

constexpr unsigned K2_LDS = 163840;
constexpr unsigned L_WO = 0, L_GU = 65536;
constexpr unsigned L_TAB = 61440;
constexpr unsigned L_FLAG = 16384 + 12288;
constexpr unsigned SPIN_CAP = 1u << 20;
#define POLL_DELAY_Q 24
#define ABL_GU 0
#define DELAY_O 0
#define DELAY_H 8
#define DELAY_G 8
constexpr int GU_AW = 8, G2_AW = 12;
#define TAG_TEST "v_max3_u32 v120, v[%c[ar]], v[%c[ar]+1], v[%c[ar]+2]\n\tv_max_u32 v120, v120, v[%c[ar]+3]\n\tv_cmp_eq_u32 vcc, -1, v120\n\t"
#define TAG_RELOAD(RB, AW) "s_add_u32 %[t], %[s0], (tagj>>2)*4096\n\tbuffer_load_dwordx4 v[" #RB "+4*(tagj%%" #AW "):" #RB "+3+4*(tagj%%" #AW ")], %[l16], %[rs], %[t] offen offset:(tagj&3)*1024 sc1\n\t"
#define TAG_CHECK(RB, AW) \
    TAG_TEST "s_cbranch_vccnz 5f\n6:\n\t" \
    ".subsection 1\n5:\n\ts_cmp_eq_u32 %[bud], 0\n\ts_cbranch_scc1 6b\n\ts_sub_u32 %[bud], %[bud], 1\n\t.set tagj, %c[i]\n\t" TAG_RELOAD(RB, AW) \
    "s_waitcnt vmcnt(0)\n\t" TAG_TEST "s_cbranch_vccnz 5b\n\t.rept %c[cnt]-1\n\t.set tagj, tagj+1\n\t" TAG_RELOAD(RB, AW) ".endr\n\ts_branch 6b\n\t.subsection 0\n\t"
#define TAG_LOAD "buffer_load_dwordx4 v[%c[lr]:%c[lr]+3], %[l16], %[rs], %[sa] offen offset:%c[lo] sc1\n\t.if %c[lo] == 3072\n\ts_add_u32 %[sa], %[sa], 0x1000\n\t.endif\n\t"

__device__ __forceinline__ void wait_flags(rsrc_t rs, unsigned base, int n, unsigned ep, int lane) {
    const unsigned v = (lane < n ? lane : n - 1) * 128;
#pragma unroll 1
    for (unsigned spins = 0; spins < SPIN_CAP; ++spins) {
        const unsigned f = __builtin_amdgcn_raw_buffer_load_b32(rs, v, base, AUX_SC1);
        if (__all(f == ep)) break;
        __builtin_amdgcn_s_sleep(1);
    }
    asm volatile("" ::: "memory");
}

__device__ __forceinline__ unsigned w64_voff(int lane) { return (lane >> 3) * 1024 + (((lane & 7) ^ (((lane >> 4) & 1) << 2)) << 4); }
__device__ __forceinline__ unsigned w64_tr(int lane, int n) {
    const int q = (lane & 15) >> 2, p = lane & 3, blk = (lane >> 4) & 1, h = lane >> 5;
    return (4 * h + q) * 128 + (((4 * n + 2 * blk + (p >> 1)) ^ (((q >> 1) & 1) << 2)) << 4) + 8 * (p & 1);
}

struct G2 { u32x16 r0, r1, r2; u32x8 w0, w1, w2; f32x16 a0, a1; unsigned tb[6], sa, s0, l16, budget; rsrc_t rs; };
__host__ __device__ constexpr int g2_w(int DN, int n) {
    return !DN ? n * 2048 : n < 15 ? n * 2048 : n < 39 ? 65536 + (n - 15) * 2048 : n < 40 ? 131072 : (n - 40) * 2048;
}
template <int i, int N, int DN, int B0, int B1> __device__ __forceinline__ void g2_steps(G2& g) {
    constexpr int AW = G2_AW, j = i + AW - 1, n = i + 2, sel = n < N ? g2_w(DN, n) >> 16 : 3;
    unsigned tq;
#define G2_RD(P0, P1) "ds_read_b64_tr_b16 v[%c[nr]:%c[nr]+1], %[" P0 "] offset:%c[no]\n\tds_read_b64_tr_b16 v[%c[nr]+2:%c[nr]+3], %[" P0 "] offset:%c[no]+1024\n\t" \
                      "ds_read_b64_tr_b16 v[%c[nr]+4:%c[nr]+5], %[" P1 "] offset:%c[no]\n\tds_read_b64_tr_b16 v[%c[nr]+6:%c[nr]+7], %[" P1 "] offset:%c[no]+1024\n\t"
    if constexpr (i == 0)
        asm volatile(".set tagj, 0\n\t.rept 11\n\tbuffer_load_dwordx4 v[128+4*tagj:131+4*tagj], %[l16], %[rs], %[sa] offen offset:(tagj&3)*1024 sc1\n\t"
                     ".if (tagj&3) == 3\n\ts_add_u32 %[sa], %[sa], 0x1000\n\t.endif\n\t.set tagj, tagj+1\n\t.endr\n\t"
                     ".set tagj, 0\n\t.rept 2\n\tds_read_b64_tr_b16 v[96+8*tagj:97+8*tagj], %[b0] offset:tagj*2048\n\tds_read_b64_tr_b16 v[98+8*tagj:99+8*tagj], %[b0] offset:tagj*2048+1024\n\t"
                     "ds_read_b64_tr_b16 v[100+8*tagj:101+8*tagj], %[b1] offset:tagj*2048\n\tds_read_b64_tr_b16 v[102+8*tagj:103+8*tagj], %[b1] offset:tagj*2048+1024\n\t.set tagj, tagj+1\n\t.endr"
                     : "={v[128:143]}"(g.r0), "={v[144:159]}"(g.r1), "={v[160:175]}"(g.r2), "={v[96:103]}"(g.w0), "={v[104:111]}"(g.w1), "={v[112:119]}"(g.w2), [sa] "+s"(g.sa),
                       "+{v[64:79]}"(g.a0), "+{v[80:95]}"(g.a1)
                     : [l16] "v"(g.l16), [rs] "s"(g.rs), [b0] "v"(g.tb[0]), [b1] "v"(g.tb[1]) : "scc", "memory");
    asm volatile(".if %c[ld]\n\t" TAG_LOAD ".endif\n\t"
                 ".if %c[bar]\n\ts_waitcnt lgkmcnt(0)\n\ts_barrier\n\t.endif\n\t"
                 ".if %c[sel] == 0\n\t" G2_RD("b0", "b1") ".endif\n\t.if %c[sel] == 1\n\t" G2_RD("b2", "b3") ".endif\n\t.if %c[sel] == 2\n\t" G2_RD("b4", "b5") ".endif\n\t"
                 "s_waitcnt vmcnt(%c[vn]) lgkmcnt(%c[ln])\n\t" TAG_CHECK(128, 12)
                 "v_mfma_f32_32x32x16_bf16 v[64:79], v[%c[wr]:%c[wr]+3], v[%c[ar]:%c[ar]+3], v[64:79]\n\t"
                 "v_mfma_f32_32x32x16_bf16 v[80:95], v[%c[wr]+4:%c[wr]+7], v[%c[ar]:%c[ar]+3], v[80:95]\n\t"
                 ".if %c[cnt] == 1\n\ts_nop 15\n\t.endif"
                 : "+{v[64:79]}"(g.a0), "+{v[80:95]}"(g.a1), "+{v[128:143]}"(g.r0), "+{v[144:159]}"(g.r1), "+{v[160:175]}"(g.r2), "+{v[96:103]}"(g.w0), "+{v[104:111]}"(g.w1), "+{v[112:119]}"(g.w2),
                   [sa] "+s"(g.sa), [bud] "+s"(g.budget), [t] "=&s"(tq)
                 : [l16] "v"(g.l16), [rs] "s"(g.rs), [s0] "s"(g.s0), [b0] "v"(g.tb[0]), [b1] "v"(g.tb[1]), [b2] "v"(g.tb[2]), [b3] "v"(g.tb[3]), [b4] "v"(g.tb[4]), [b5] "v"(g.tb[5]),
                   [ld] "n"(j < N ? 1 : 0), [lr] "n"(128 + 4 * (j % AW)), [lo] "n"((j & 3) * 1024), [bar] "n"(i == B0 || i == B1 ? 1 : 0), [sel] "n"(sel),
                   [nr] "n"(96 + 8 * (n % 3)), [no] "n"(n < N ? g2_w(DN, n) & 65535 : 0), [vn] "n"(j < N ? AW - 1 : N - 1 - i), [ln] "n"(n < N ? 8 : n == N ? 4 : 0),
                   [ar] "n"(128 + 4 * (i % AW)), [wr] "n"(96 + 8 * (i % 3)), [i] "n"(i), [cnt] "n"(j < N ? AW : N - i) : "vcc", "scc", "v120", "memory");
    if constexpr (i + 1 < N) g2_steps<i + 1, N, DN, B0, B1>(g);
}
__host__ __device__ constexpr int gu_dma(int s) { return s >= 16 && s < 28 && !(s & 1); }
__host__ __device__ constexpr int gu_ops(int s) { return (s + GU_AW - 1 < 32 ? 1 : 0) + (s >= 24 ? 1 : 0) + gu_dma(s); }
__host__ __device__ constexpr int gu_younger(int k) {
    int c = 0, s = k - GU_AW + 1;
    if (s < 0) { c = GU_AW - 2 - k; s = 0; } else { c = gu_ops(s) - 1; ++s; }
    for (; s <= k; ++s) c += gu_ops(s);
    return c;
}

struct GU { u32x16 r0, r1, st; u32x8 w0, w1, w2; f32x16 a0, a1, a2; unsigned wa[3], sa, s0, sv, ss, l16, budget, dm, ds, dv; rsrc_t rs, rd; };
template <int i> __device__ __forceinline__ void gu_steps(GU& g) {
    constexpr int AW = GU_AW, j = i + AW - 1, n = i + 1;
    unsigned tq;
#define GU_RD(R, B) "ds_read_b64_tr_b16 v[" R ":" R "+1], %[" B "] offset:%c[no]\n\tds_read_b64_tr_b16 v[" R "+2:" R "+3], %[" B "] offset:%c[no]+768\n\t"
    if constexpr (i == 0)
        asm volatile(".set tagj, 0\n\t.rept 7\n\tbuffer_load_dwordx4 v[140+4*tagj:143+4*tagj], %[l16], %[rs], %[sa] offen offset:(tagj&3)*1024 sc1\n\t"
                     ".if (tagj&3) == 3\n\ts_add_u32 %[sa], %[sa], 0x1000\n\t.endif\n\t.set tagj, tagj+1\n\t.endr\n\t" GU_RD("112", "b0") GU_RD("116", "b1") GU_RD("124", "b2")
                     : "={v[140:155]}"(g.r0), "={v[156:171]}"(g.r1), "={v[172:187]}"(g.st), "={v[112:119]}"(g.w0), "={v[124:131]}"(g.w1), "={v[132:139]}"(g.w2), [sa] "+s"(g.sa),
                       "+{v[64:79]}"(g.a0), "+{v[80:95]}"(g.a1), "+{v[96:111]}"(g.a2)
                     : [l16] "v"(g.l16), [rs] "s"(g.rs), [b0] "v"(g.wa[0]), [b1] "v"(g.wa[1]), [b2] "v"(g.wa[2]), [no] "n"(0) : "scc", "memory");
    asm volatile(".if %c[ld]\n\t" TAG_LOAD ".endif\n\t"
                 ".if %c[sk] >= 0\n\tbuffer_load_dwordx2 v[172+2*%c[sk]:173+2*%c[sk]], %[sv], %[rs], %[ss] offen offset:%c[sk]*256 sc1\n\t.endif\n\t"
                 ".if %c[i] == 16\n\ts_waitcnt lgkmcnt(0)\n\ts_barrier\n\t.endif\n\t"
                 ".if %c[dma]\n\ts_mov_b32 m0, %[dm]\n\ts_add_u32 %[dm], %[dm], 0x400\n\tbuffer_load_dwordx4 %[dv], %[rwd], %[ds] offen lds\n\ts_add_u32 %[ds], %[ds], 0x2000\n\t.endif\n\t"
                 ".if %c[rd]\n\t" GU_RD("%c[n0]", "b0") GU_RD("%c[n1]", "b1") GU_RD("%c[n2]", "b2") ".endif\n\t"
                 "s_waitcnt vmcnt(%c[vn]) lgkmcnt(%c[ln])\n\t" TAG_CHECK(140, 8)
                 "v_mfma_f32_32x32x16_bf16 v[64:79], v[%c[c0]:%c[c0]+3], v[%c[ar]:%c[ar]+3], v[64:79]\n\t"
                 "v_mfma_f32_32x32x16_bf16 v[80:95], v[%c[c1]:%c[c1]+3], v[%c[ar]:%c[ar]+3], v[80:95]\n\t"
                 "v_mfma_f32_32x32x16_bf16 v[96:111], v[%c[c2]:%c[c2]+3], v[%c[ar]:%c[ar]+3], v[96:111]\n\t"
                 ".if %c[cnt] == 1\n\ts_waitcnt vmcnt(0)\n\ts_nop 15\n\t.endif"
                 : "+{v[64:79]}"(g.a0), "+{v[80:95]}"(g.a1), "+{v[96:111]}"(g.a2), "+{v[140:155]}"(g.r0), "+{v[156:171]}"(g.r1), "+{v[172:187]}"(g.st),
                   "+{v[112:119]}"(g.w0), "+{v[124:131]}"(g.w1), "+{v[132:139]}"(g.w2), [sa] "+s"(g.sa), [bud] "+s"(g.budget), [t] "=&s"(tq), [dm] "+s"(g.dm), [ds] "+s"(g.ds)
                 : [l16] "v"(g.l16), [rs] "s"(g.rs), [s0] "s"(g.s0), [dv] "v"(g.dv), [rwd] "s"(g.rd), [dma] "n"(gu_dma(i)), [sv] "v"(g.sv), [ss] "s"(g.ss), [b0] "v"(g.wa[0]), [b1] "v"(g.wa[1]), [b2] "v"(g.wa[2]),
                   [ld] "n"(j < 32 ? 1 : 0), [lr] "n"(140 + 4 * (j % AW)), [lo] "n"((j & 3) * 1024), [sk] "n"(i >= 24 ? i - 24 : -1), [rd] "n"(n < 32 ? 1 : 0), [no] "n"(n * 1536),
                   [n0] "n"(n & 1 ? 128 : 112), [n1] "n"(n & 1 ? 132 : 116), [n2] "n"(n & 1 ? 136 : 124), [c0] "n"(i & 1 ? 128 : 112), [c1] "n"(i & 1 ? 132 : 116), [c2] "n"(i & 1 ? 136 : 124),
                   [vn] "n"(gu_younger(i)), [ln] "n"(n < 32 ? 6 : 0), [ar] "n"(140 + 4 * (i % AW)), [i] "n"(i), [cnt] "n"(j < 32 ? AW : 32 - i) : "vcc", "scc", "m0", "v120", "memory");
    if constexpr (i + 1 < 32) gu_steps<i + 1>(g);
}

__global__ __launch_bounds__(512, 2) void k_block(const u16* __restrict__ x, const u16* __restrict__ wqkv, const u16* __restrict__ wo, const u16* __restrict__ wg,
                                                  const u16* __restrict__ wu, const u16* __restrict__ wd, u16* __restrict__ out, char* __restrict__ ws) {
    extern __shared__ __attribute__((aligned(16))) char smem[];
    const unsigned lds0 = (unsigned)(uintptr_t)(LDSAS char*)smem;
    const int tid = threadIdx.x, lane = tid & 63;
    const int w = __builtin_amdgcn_readfirstlane(tid >> 6);
    const int b = blockIdx.x, xcd = b & 7, bi = b >> 3;
    const int r32 = lane & 31, h = lane >> 5;
    const rsrc_t rs = mk_rsrc(ws, WS_END);
    const int wt = w & 1, kh = (w >> 1) & 1;
    STAMP_DECL;
    unsigned ep;
    {
    const int rt = (xcd >> 1) * 8 + (bi & 7), cb = (xcd & 1) * 4 + (bi >> 3), rot = bi & 3;
    const int wc = (w >> 1) & 1, hw = w - 4;
    STAMP(0, 0);
    const rsrc_t rw = mk_rsrc(wqkv, D * 3 * D * 2);
    const unsigned wv = (lane >> 3) * 3072 + (((lane & 7) ^ (((lane >> 4) & 1) << 2)) << 4);
    const unsigned fa = lds0 + K1_FLG;
    if (tid < 12) *(LDSAS unsigned*)(fa + tid * 4) = 0u;
    if (w >= 4) {
        BAR_L();
        const unsigned pa = fa + hw * 4;
        auto issue = [&](int s) {
            const unsigned so = RFL(((((s & 3) + rot) & 3) * 128 + hw * 32) * 3072 + (s >> 2) * 1024 + cb * 128);
            asm volatile("s_nop 4" :: "s"(so));
#pragma unroll
            for (int i = 0; i < 4; ++i)
                asm volatile("s_mov_b32 m0, %0\n\ts_nop 0\n\tbuffer_load_dwordx4 %1, %2, %3 offen lds"
                             :: "s"(lds0 + k1_slot(s) + (4 * hw + i) * 1024), "v"(wv), "s"(rw), "s"(so + i * 8 * 3072) : "m0", "memory");
        };
#define K1_LAND(N, s) asm volatile("s_waitcnt vmcnt(" #N ")\n\tds_write_b32 %0, %1" :: "v"(pa), "v"((s) + 1u) : "memory")
        issue(0); issue(1);
        issue(2); K1_LAND(8, 0);
        issue(3); K1_LAND(8, 1);
        issue(4); K1_LAND(8, 2); K1_LAND(4, 3); K1_LAND(0, 4);
        lds_spin_ge(fa + 28, 4);
        issue(5);
        lds_spin_ge(fa + 20, 4);
        issue(6); issue(7); K1_LAND(8, 5);
        issue(8); K1_LAND(8, 6);
        issue(9); K1_LAND(8, 7);
        lds_spin_ge(fa + 24, 4);
        issue(10); K1_LAND(8, 8);
        issue(11); K1_LAND(8, 9);
        K1_LAND(4, 10); K1_LAND(0, 11);
        if (w == 4) { unsigned e; asm volatile("s_load_dword %0, %1, 0\n\ts_waitcnt lgkmcnt(0)" : "=s"(e) : "s"(ws) : "memory"); *(LDSAS unsigned*)(fa + 48) = e + 1u; }
        else {
            unsigned long long pc; unsigned len, o = bi * 128 + (w - 5) * 16384, t, d0, d1, d2, d3;
#define KCP(D) "s_min_u32 %[t], %[o], %[len]\n\ts_load_dword " D ", %[pc], %[t]\n\ts_add_u32 %[o], %[o], 0x1000\n\t"
            asm volatile("s_getpc_b64 %[pc]\n.Lqkv_pc:\n\ts_mov_b32 %[len], .Lmain_end-.Lqkv_pc-4\n\t" KCP("%[d0]") KCP("%[d1]") KCP("%[d2]") KCP("%[d3]") "s_waitcnt lgkmcnt(0)"
                         : [pc] "=&s"(pc), [len] "=&s"(len), [o] "+s"(o), [t] "=&s"(t), [d0] "=&s"(d0), [d1] "=&s"(d1), [d2] "=&s"(d2), [d3] "=&s"(d3) :: "scc", "memory");
        }
        BAR_L();
        ep = RFL(*(const LDSAS unsigned*)(fa + 48));
        if (w == 4) { __builtin_amdgcn_s_sleep(POLL_DELAY_Q); wait_flags(rs, WS_FQ + (xcd * 2 + (bi >> 4)) * 8192, 64, ep, lane); }
    } else {
    {
        const rsrc_t rx = mk_rsrc(x, S * D * 2);
        u32x4 r[2][8];
#pragma unroll
        for (int p = 0; p < 2; ++p)
#pragma unroll
            for (int g = 0; g < 8; ++g)
                r[p][g] = __builtin_amdgcn_raw_buffer_load_b128(rx, (lane >> 3) * 1024 + (((lane & 7) ^ ((4 * p + (lane >> 4)) & 7)) << 4) + g * 128, (rt * 64 + w * 16 + p * 8) * 1024, K1_X_AUX);
        SBAR(); BAR_L(); SBAR();
#pragma unroll
        for (int p = 0; p < 2; ++p) {
            float s0 = 0.f, s1 = 0.f, q0 = 0.f, q1 = 0.f;
#pragma unroll
            for (int g = 0; g < 8; ++g) { s0 = dot2(r[p][g][0], ONES2, s0); q0 = dot2(r[p][g][0], r[p][g][0], q0); s1 = dot2(r[p][g][1], ONES2, s1); q1 = dot2(r[p][g][1], r[p][g][1], q1);
                                          s0 = dot2(r[p][g][2], ONES2, s0); q0 = dot2(r[p][g][2], r[p][g][2], q0); s1 = dot2(r[p][g][3], ONES2, s1); q1 = dot2(r[p][g][3], r[p][g][3], q1); }
            if (p == 0) STAMP(0, 1);
            float sum = s0 + s1, sq = q0 + q1;
            sum = dpp_add<0xB1>(sum); sq = dpp_add<0xB1>(sq); sum = dpp_add<0x4E>(sum); sq = dpp_add<0x4E>(sq); sum = dpp_add<0x141>(sum); sq = dpp_add<0x141>(sq);
            const float mean = sum * (1.f / D);
            const float rstd = __builtin_amdgcn_rsqf(__builtin_fmaxf(sq * (1.f / D) - mean * mean, 0.f) + LN_EPS), nb = -mean * rstd;
#pragma unroll
            for (int g = 0; g < 8; g += 2) {
                f32x2 t[8];
#pragma unroll
                for (int i = 0; i < 8; ++i) { const unsigned v = r[p][g + (i >> 2)][i & 3]; t[i] = f32x2{bflo(v), bfhi(v)}; }
                asm volatile("" : "+v"(t[0]), "+v"(t[1]), "+v"(t[2]), "+v"(t[3]), "+v"(t[4]), "+v"(t[5]), "+v"(t[6]), "+v"(t[7]));
#pragma unroll
                for (int i = 0; i < 8; ++i) t[i] = t[i] * rstd + nb;
                asm volatile("" : "+v"(t[0]), "+v"(t[1]), "+v"(t[2]), "+v"(t[3]), "+v"(t[4]), "+v"(t[5]), "+v"(t[6]), "+v"(t[7]));
                u32x4 y0, y1;
#pragma unroll
                for (int i = 0; i < 4; ++i) { y0[i] = pk2(t[i][0], t[i][1]); y1[i] = pk2(t[4 + i][0], t[4 + i][1]); }
                *(LDSAS u32x4*)(lds0 + (2 * w + p) * 1024 + lane * 16 + g * 8192) = y0;
                *(LDSAS u32x4*)(lds0 + (2 * w + p) * 1024 + lane * 16 + g * 8192 + 8192) = y1;
            }
            SBAR();
        }
    }
    lds_inc(fa + 16, lane);
    STAMP(0, 3);
    u32x16 xa0, xa1, xa2, xa3, xa4, xa5, xa6, xa7;
    u32x4 w0, w1, w2, w3, w4, fl;
    f32x16 acA, acB;
    unsigned xb[4], tq;
#pragma unroll
    for (int m = 0; m < 4; ++m) xb[m] = lds0 + (32 * wt + r32) * 128 + (((2 * m + h) ^ ((r32 >> 1) & 7)) << 4) + rot * 16384;
    const int q4 = (lane & 15) >> 2, p4 = lane & 3, blk = (lane >> 4) & 1;
    const unsigned tA = lds0 + (8 * h + q4) * 128 + (((4 * wc + 2 * blk + (p4 >> 1)) ^ (((q4 >> 1) & 1) << 2)) << 4) + 8 * (p4 & 1);
    const unsigned tB = tA + 65536, tC = tA + 131072;
    const int tt = rt * 2 + wt, hd = cb * 4 + wc * 2;
#define KXR(R, m, O) "ds_read_b128 v[" #R ":" #R "+3], %[x" #m "] offset:" #O "\n\t"
#define KXN(m) "v_add_u32 %[x" #m "], 0x4000, %[x" #m "]\n\tv_and_b32 %[x" #m "], 0xffff, %[x" #m "]\n\t"
#define KCHK(ST) "v_min3_u32 v116, v116, v117, v118\n\tv_min_u32 v116, v116, v119\n\ts_nop 0\n\tv_readfirstlane_b32 %[t], v116\n\ts_nop 0\n\ts_cmp_gt_u32 %[t], " ST "\n\t"
#define KSPIN(ST) "1:\n\tds_read_b128 v[116:119], %[fa]\n\ts_waitcnt lgkmcnt(0)\n\t" KCHK(ST) "s_cbranch_scc0 1b\n\t"
#define KWR(R, O) "ds_read_b64_tr_b16 v[" #R ":" #R "+1], %[tb] offset:" #O "\n\tds_read_b64_tr_b16 v[" #R "+2:" #R "+3], %[tb] offset:" #O "+512\n\t"
    asm volatile("3:\n\tds_read_b32 v116, %[fa] offset:16\n\ts_waitcnt lgkmcnt(0)\n\tv_readfirstlane_b32 %[t], v116\n\ts_nop 0\n\ts_cmp_gt_u32 %[t], 3\n\ts_cbranch_scc0 3b\n\t"
                 KXR(128, 0, 0) KXR(132, 1, 0) KXR(136, 2, 0) KXR(140, 3, 0) KXR(144, 0, 8192) KXR(148, 1, 8192) KXR(152, 2, 8192) KXR(156, 3, 8192) KXN(0) KXN(1) KXN(2) KXN(3)
                 KSPIN("0") KWR(96, 0) KWR(100, 2048) KWR(104, 4096) KWR(108, 6144)
                 : "={v[116:119]}"(fl), [t] "=&s"(tq), "={v[96:99]}"(w0), "={v[100:103]}"(w1), "={v[104:107]}"(w2), "={v[108:111]}"(w3), "={v[112:115]}"(w4), "={v[64:79]}"(acA), "={v[80:95]}"(acB),
                   "={v[128:143]}"(xa0), "={v[144:159]}"(xa1), "={v[160:175]}"(xa2), "={v[176:191]}"(xa3), "={v[192:207]}"(xa4), "={v[208:223]}"(xa5), "={v[224:239]}"(xa6), "={v[240:255]}"(xa7),
                   [x0] "+v"(xb[0]), [x1] "+v"(xb[1]), [x2] "+v"(xb[2]), [x3] "+v"(xb[3])
                 : [fa] "v"(fa), [tb] "v"(tB) : "scc", "memory");
    STAMP(0, 4);
    auto epi = [&](int m, const f32x16& acc, int hh) {
        const float sc = 0.25f * LOG2E;
        u32x4 o;
#pragma unroll
        for (int i = 0; i < 4; ++i) o[i] = m == 0 ? pk2(acc[8 * hh + 2 * i] * sc, acc[8 * hh + 2 * i + 1] * sc) : pk2(acc[8 * hh + 2 * i], acc[8 * hh + 2 * i + 1]);
        if (m == 0) __builtin_amdgcn_raw_buffer_store_b128(o, rs, lane * 16, WS_Q + ((hd + hh) * 64 + tt) * 1024, K1_ST_AUX);
        else if (m == 1) __builtin_amdgcn_raw_buffer_store_b128(o, rs, lane * 16, WS_KV + (hd + hh) * KV_HEAD + (tt >> 3) * KV_CHUNK + (tt & 7) * 1024, K1_ST_AUX);
        else __builtin_amdgcn_raw_buffer_store_b128(o, rs, (r32 >> 4) * KV_HEAD + (16 * h + (lane & 15)) * 16 + hh * 512,
                                                    WS_KV + hd * KV_HEAD + (tt >> 3) * KV_CHUNK + 8192 + (tt & 7) * 1024, K1_ST_AUX);
    };
    sfor<0, 96>([&](auto G) {
        constexpr int g = G.value, n = g + 4, m = g >> 5;
        constexpr unsigned o = k1_slot(n >> 3) + (n & 7) * 2048, oi = o & 65535;
        constexpr int aR = m == 1 ? 80 : 64, wR = 96 + 4 * (g % 5), nR = 96 + 4 * (n % 5), xR = 128 + 4 * (g & 31);
        constexpr int nx = (g - 4 >= 0 && g - 4 < 12) + (g - 3 >= 0 && g - 3 < 12) + (g - 2 >= 0 && g - 2 < 12) + (g - 1 >= 0 && g - 1 < 12), wc_ = n >= 96 ? 0 : (8 + 2 * nx > 14 ? 14 : 8 + 2 * nx);
#define KRD(B) "ds_read_b64_tr_b16 v[%c[nr]:%c[nr]+1], %[" B "] offset:%c[oi]\n\tds_read_b64_tr_b16 v[%c[nr]+2:%c[nr]+3], %[" B "] offset:%c[oi]+512\n\t"
        asm volatile(".if %c[bar] == 1\n\t" KCHK("%c[st]") "s_cbranch_scc1 2f\n\t" KSPIN("%c[st]") "2:\n\t.endif\n\t"
                     ".if %c[bar] == 2\n\tds_read_b128 v[116:119], %[fa]\n\t.endif\n\t"
                     ".if %c[rd] == 1\n\t" KRD("tb") ".endif\n\t.if %c[rd] == 2\n\t" KRD("tc") ".endif\n\t.if %c[rd] == 3\n\t" KRD("ta") ".endif\n\t"
                     "s_waitcnt lgkmcnt(%c[wn])\n\t"
                     ".if %c[md] == 0\n\tv_mfma_f32_32x32x16_bf16 v[%c[ar]:%c[ar]+15], v[%c[wr]:%c[wr]+3], v[%c[xr]:%c[xr]+3], v[%c[ar]:%c[ar]+15]\n\t.endif\n\t"
                     ".if %c[md] == 1\n\tv_mfma_f32_32x32x16_bf16 v[%c[ar]:%c[ar]+15], v[%c[xr]:%c[xr]+3], v[%c[wr]:%c[wr]+3], v[%c[ar]:%c[ar]+15]\n\t.endif\n\t"
                     ".if %c[md] == 2\n\tv_mfma_f32_32x32x16_bf16 v[%c[ar]:%c[ar]+15], v[%c[xr]:%c[xr]+3], v[%c[wr]:%c[wr]+3], 0\n\t.endif\n\t"
                     ".if %c[md] == 3\n\tv_mfma_f32_32x32x16_bf16 v[%c[ar]:%c[ar]+15], v[%c[wr]:%c[wr]+3], v[%c[xr]:%c[xr]+3], 0\n\t.endif\n\t"
                     ".if %c[xm] == 1\n\tds_read_b128 v[%c[xd]:%c[xd]+3], %[x0] offset:%c[xo]\n\tds_read_b128 v[%c[xd]+4:%c[xd]+7], %[x1] offset:%c[xo]\n\t.endif\n\t"
                     ".if %c[xm] == 2\n\tds_read_b128 v[%c[xd]:%c[xd]+3], %[x2] offset:%c[xo]\n\tds_read_b128 v[%c[xd]+4:%c[xd]+7], %[x3] offset:%c[xo]\n\t.endif\n\t"
                     ".if %c[xn]\n\t" KXN(0) KXN(1) KXN(2) KXN(3) ".endif"
                     : "+{v[64:79]}"(acA), "+{v[80:95]}"(acB), "+{v[96:99]}"(w0), "+{v[100:103]}"(w1), "+{v[104:107]}"(w2), "+{v[108:111]}"(w3), "+{v[112:115]}"(w4),
                       "+{v[128:143]}"(xa0), "+{v[144:159]}"(xa1), "+{v[160:175]}"(xa2), "+{v[176:191]}"(xa3), "+{v[192:207]}"(xa4), "+{v[208:223]}"(xa5), "+{v[224:239]}"(xa6), "+{v[240:255]}"(xa7),
                       "+{v[116:119]}"(fl), [t] "=&s"(tq), [x0] "+v"(xb[0]), [x1] "+v"(xb[1]), [x2] "+v"(xb[2]), [x3] "+v"(xb[3])
                     : [ta] "v"(tA), [tb] "v"(tB), [tc] "v"(tC), [fa] "v"(fa), [st] "n"(n >> 3), [wn] "n"(wc_),
                       [bar] "n"(K1_ABL ? 0 : n < 96 && (n & 7) == 0 ? 1 : n < 92 && (n & 7) == 4 ? 2 : 0), [rd] "n"(n >= 96 ? 0 : o < 65536 ? 3 : o < 131072 ? 1 : 2), [oi] "n"(oi), [nr] "n"(nR),
                       [md] "n"(m < 2 ? ((g & 31) == 0 ? 3 : 0) : g == 64 ? 2 : 1), [ar] "n"(aR), [wr] "n"(wR), [xr] "n"(xR),
                       [xm] "n"(g < 12 ? 1 + (g & 1) : 0), [xd] "n"(160 + 8 * g), [xo] "n"(((g >> 1) & 1) * 8192), [xn] "n"(g < 11 && (g & 3) == 3 ? 1 : 0) : "scc", "memory");
        if (g == 7 && lane == 0) asm volatile("ds_add_u32 %0, %1" :: "v"(fa + 28), "v"(1u) : "memory");
        if (g == 16 && lane == 0) asm volatile("ds_add_u32 %0, %1" :: "v"(fa + 20), "v"(1u) : "memory");
        if (g == 23 && lane == 0) asm volatile("ds_add_u32 %0, %1" :: "v"(fa + 24), "v"(1u) : "memory");
        if (g == 36 || g == 38) epi(0, acA, (g - 36) >> 1);
        if (g == 68 || g == 70) epi(1, acB, (g - 68) >> 1);
    });
    STAMP(0, 2);
    asm volatile("s_nop 15" : "+{v[64:79]}"(acA));
    epi(2, acA, 0); epi(2, acA, 1);
    SBAR(); BAR_L();
    ep = RFL(*(const LDSAS unsigned*)(fa + 48));
    WAITV(0);
    if (lane == 0) __builtin_amdgcn_raw_buffer_store_b32(ep, rs, 0, WS_FQ + ((cb * 2 + wc) * 64 + tt) * 128, AUX_SC1);
    STAMP(0, 5);
    FLUSH(0);
    }
    }

    const int rt = xcd * 4 + (bi >> 3), ct = bi & 7;
    u32x4 hreg[2];
    u32x8 xres;

    auto issue_wo = [&](int first, int n) {
        const rsrc_t rwo = mk_rsrc(wo, D * D * 2);
        const unsigned v = w64_voff(lane);
        for (int p = first; p < first + n; ++p) dma16(rwo, v, p * 8192 + ct * 128, lds0 + L_WO + p * 1024);
    };
    auto issue_dn = [&](int i0, int n, unsigned dst, int hw) {
        const rsrc_t rwd = mk_rsrc(wd, F * D * 2);
        const unsigned v = w64_voff(lane);
        const unsigned so = ((43 * (hw >> 1) + i0) * 16 + (hw & 1) * 8) * 1024 + ct * 128;
        for (int i = 0; i < n; ++i) dma16(rwd, v, so + i * 16384, lds0 + dst + (hw & 1) * 1024 + i * 2048);
    };
    auto gu_wa = [&](int n) -> unsigned { const unsigned blk = (lane >> 4) & 1;
        return lds0 + L_GU + blk * 49152 + (((4 * h + ((lane & 15) >> 2)) * 96 + n * 32 + (lane & 3) * 8) ^ (blk * 128)); };
    float cs[3] = {0.f, 0.f, 0.f};
    auto gu_colsum = [&]() {
        u32x4 f[3][4];
#pragma unroll
        for (int n = 0; n < 3; ++n) {
            const unsigned a0 = gu_wa(n) + w * 4 * 1536;
#pragma unroll
            for (int i = 0; i < 4; ++i) f[n][i] = __builtin_bit_cast(u32x4, lds_tr2(a0 + i * 1536, a0 + i * 1536 + 768));
        }
        SBAR();
#pragma unroll
        for (int i = 0; i < 4; ++i)
#pragma unroll
            for (int e = 0; e < 4; ++e)
#pragma unroll
                for (int n = 0; n < 3; ++n) cs[n] = dot2(f[n][i][e], ONES2, cs[n]);
#pragma unroll
        for (int n = 0; n < 3; ++n) cs[n] = swap_add(cs[n]);
    };

    unsigned budget = 1u << 16;
    const unsigned l16 = lane * 16;

    {
        const int head = xcd * 4 + (bi >> 3), qb = bi & 7, wq = w & 3, g = w >> 2, qt = qb * 8 + wq * 2;
        const unsigned kvs = WS_KV + head * KV_HEAD;
        unsigned ks = kvs + g * 4 * KV_CHUNK + wq * 4096, km = lds0 + g * 32768 + wq * 4096;
        const rsrc_t rg = mk_rsrc(g ? wu : wg, (bi < 29 && ABL_GU != 1) ? D * F * 2 : 0), rwd = mk_rsrc(wd, F * D * 2), rwo = mk_rsrc(wo, D * D * 2), rx = mk_rsrc(x, S * D * 2);
        unsigned gv0, gv1, gv2;
        { unsigned c = lane ^ (g * 8); gv0 = (c / 6) * (F * 2) + (c % 6) * 16; c = (64 + lane) ^ (g * 8); gv1 = (c / 6) * (F * 2) + (c % 6) * 16;
          c = (128 + lane) ^ (g * 8); gv2 = (c / 6) * (F * 2) + (c % 6) * 16; }
        if (ABL_GU == 2) { gv0 = l16; gv1 = l16 + 1024; gv2 = l16 + 2048; }
        unsigned gs = ABL_GU == 2 ? wq * 4 * 32 * (F * 2) + bi * 3072 : wq * 4 * 32 * (F * 2) + bi * 96, gm = lds0 + L_GU + g * 49152 + wq * 12288;
        const unsigned pvd = lane < 43 ? (bi * 344 + w * 43 + lane) * 128 : 0x80000000u, pvo = lane < 16 ? (bi * 128 + w * 16 + lane) * 128 : 0x80000000u;
        const unsigned wv = w64_voff(lane), xv = (rt * 64 + wt * 32 + r32) * 1024 + (ct * 64 + kh * 32 + 4 * h) * 2;
        const unsigned kb = lds0 + g * 32768 + l16, vb = lds0 + g * 32768 + 8192 + (16 * h + (r32 & 15)) * 16;
        const unsigned cst = (r32 & 27) == 16 ? ONES2 : 0u;
        const unsigned long long msk = 0x0000ffff0000ffffull;
        const unsigned fo = WS_O + ((qt + g) * 32 + head) * 1024, fh = WS_H + ((rt * 2 + wt) * 32 + ct * 4 + (w >> 1)) * 1024, fg = WS_HH + ((xcd * 8 + w) * NU + bi * 3) * 1024;
        const unsigned fsv = w == 0 ? WS_ST + ((rt * 2 + h) * 16 + ct * 2) * 256 + r32 * 16 : 0x80000000u, fg2 = bi < 28 ? l16 : 0x80000000u, fg0 = bi < 29 ? l16 : 0x80000000u;
        const unsigned fpv = tid == 0 ? WS_FP + b * 4 : 0x80000000u;
        u32x4 ones = {~0u, ~0u, ~0u, ~0u}, fpl = {0u, 0u, 0u, 0u};
        unsigned pfd = 0, epv = ep;
        xres = u32x8{};
        s16x8 q0, q1;
        STAMP(1, 0);
        SBAR();
#define XKV "s_mov_b32 m0, %[km]\n\ts_add_u32 %[km], %[km], 0x400\n\tbuffer_load_dwordx4 %[l16], %[rs], %[ks] offen sc1 lds\n\ts_add_u32 %[ks], %[ks], 0x400\n\t"
        asm volatile("s_barrier\n\tbuffer_load_dwordx4 v[128:131], %[l16], %[rs], %[qs] offen sc1\n\tbuffer_load_dwordx4 v[156:159], %[l16], %[rs], %[qs] offen offset:1024 sc1\n\t"
                     XKV XKV XKV XKV "s_add_u32 %[km], %[km], 0x3000\n\ts_add_u32 %[ks], %[ks], 0x3000\n\ts_waitcnt vmcnt(0)\n\ts_barrier\n\t" XKV XKV XKV XKV
                     : "={v[128:131]}"(q0), "={v[156:159]}"(q1), [km] "+s"(km), [ks] "+s"(ks)
                     : [l16] "v"(l16), [rs] "s"(rs), [qs] "s"(WS_Q + (head * 64 + qt) * 1024) : "m0", "scc", "memory");
        km -= 16384 + 4096; ks += KV_CHUNK - 4096;
        STAMP(1, 1);
        f32x16 n0, n1, sA, sB = {}, o0 = {}, o1 = {};
        s16x8 kf = lds_b128(kb), v0, v1, v1b;
        {
            const f32x16 z = {};
            const s16x8 kz = lds_b128(lds0 + l16);
            const u32x4 vv = __builtin_bit_cast(u32x4, lds_b128(vb)), cc = {cst, cst, cst, cst};
            const f32x16 s0 = MFMA(kz, q0, z), s1 = MFMA(kz, q1, z);
            float m0 = __builtin_fmaxf(s0[0], s0[1]), m1 = __builtin_fmaxf(s1[0], s1[1]);
#pragma unroll
            for (int i = 2; i < 16; i += 2) { m0 = __builtin_fmaxf(__builtin_fmaxf(m0, s0[i]), s0[i + 1]); m1 = __builtin_fmaxf(__builtin_fmaxf(m1, s1[i]), s1[i + 1]); }
            m0 = swap_max(m0); m1 = swap_max(m1);
#pragma unroll
            for (int i = 0; i < 16; ++i) { n0[i] = -m0; n1[i] = -m1; }
            sA = MFMA(kf, q0, n0);
            v0 = __builtin_bit_cast(s16x8, r32 < 16 ? vv : cc); v1 = v1b = __builtin_bit_cast(s16x8, cc);
        }
        u32x4 pA = {0u, 0u, 0u, 0u}, pB = {0u, 0u, 0u, 0u};
#define XE(C, i) "v_exp_f32 v[" #C "+" #i "], v[" #C "+" #i "]\n\t"
#define XC(P, C, i) "v_cvt_pk_bf16_f32 v" #P ", v[" #C "+" #i "], v[" #C "+" #i "+1]\n\t"
#define XG1(C) XE(C, 0) XE(C, 1) XE(C, 2) XE(C, 3) XC(148, C, 0) XE(C, 4) XE(C, 5) XC(149, C, 2)
#define XG2(C) XE(C, 6) XE(C, 7) XC(150, C, 4) XE(C, 8) XC(151, C, 6) XE(C, 9) XE(C, 10)
#define XG3(C) XE(C, 11) XC(152, C, 8) XE(C, 12) XE(C, 13) XC(153, C, 10) XE(C, 14) XE(C, 15) XC(154, C, 12) XC(155, C, 14)
#define XM(D, A, B, C) "v_mfma_f32_32x32x16_bf16 v[" #D ":" #D "+15], v[" #A ":" #A "+3], v[" #B ":" #B "+3], v[" #C ":" #C "+15]\n\t"
#define XRK(BASE, O) "ds_read_b128 v[132:135], " BASE " offset:" #O "\n\t"
#define XRV(R, BASE, O) "s_mov_b64 exec, %[msk]\n\tds_read_b128 v[" #R ":" #R "+3], " BASE " offset:" #O "\n\ts_mov_b64 exec, -1\n\t"
#define XW(n) "s_waitcnt lgkmcnt(" #n ")\n\t"
#define XTILE(VA, RK, RV1, WA, X1, VB, X2, RV0) \
        XM(80, 132, 156, 176) RK XG1(64) XM(160, VA, 152, 160) RV1 XG2(64) XW(WA) XM(96, 140, 148, 96) XG3(64) \
        XW(0) XM(64, 132, 128, 112) X1 XG1(80) XM(96, VB, 152, 96) X2 XG2(80) XM(160, 140, 148, 160) RV0 XG3(80)
#define XGU(j) "s_mov_b32 m0, %[gm]\n\ts_add_u32 %[gm], %[gm], 0x400\n\tbuffer_load_dwordx4 %[gv" #j "], %[rg], %[gs] offen lds\n\t"
#define XGU2 XGU(2) "s_add_u32 %[gs], %[gs], 88064\n\t"
#define XPF "buffer_load_dword v200, %[pvd], %[rwd], 0 offen\n\tbuffer_load_dword v200, %[pvo], %[rwo], 0 offen\n\t"
#define XWO "s_mov_b32 m0, %[km]\n\ts_add_u32 %[km], %[km], 0x400\n\tbuffer_load_dwordx4 %[wv], %[rwo], %[ks] offen lds\n\ts_add_u32 %[ks], %[ks], 0x2000\n\t"
#define XST(V, SO, O) "buffer_store_dwordx4 v[204:207], " V ", %[rs], " SO " offen offset:" #O " sc1\n\t"
#define XFP "buffer_store_dword v201, %[fpv], %[rs], 0 offen sc1\n\t"
#define XFL "buffer_load_dwordx4 v[208:211], %[l16], %[rs], %[fp] offen sc1\n\t"
#define XXR(R, O) "buffer_load_dwordx2 v[" #R ":" #R "+1], %[xv], %[rx], 0 offen offset:" #O "\n\t"
#define AOPS : "+{v[64:79]}"(sA), "+{v[80:95]}"(sB), "+{v[96:111]}"(o0), "+{v[160:175]}"(o1), "+{v[112:127]}"(n0), "+{v[176:191]}"(n1), "+{v[128:131]}"(q0), "+{v[156:159]}"(q1), \
               "+{v[132:135]}"(kf), "+{v[140:143]}"(v0), "+{v[144:147]}"(v1), "+{v[136:139]}"(v1b), "+{v[148:151]}"(pA), "+{v[152:155]}"(pB), "+{v[192:199]}"(xres), "+{v200}"(pfd), \
               "+{v201}"(epv), "+{v[204:207]}"(ones), "+{v[208:211]}"(fpl), \
               [km] "+s"(km), [ks] "+s"(ks), [gm] "+s"(gm), [gs] "+s"(gs) \
             : [ka] "v"(ka), [va] "v"(va), [kn] "v"(kn), [vn] "v"(vn), [msk] "s"(msk), [l16] "v"(l16), [rs] "s"(rs), [rg] "s"(rg), [gv0] "v"(gv0), [gv1] "v"(gv1), [gv2] "v"(gv2), \
               [pvd] "v"(pvd), [pvo] "v"(pvo), [wv] "v"(wv), [xv] "v"(xv), [rwd] "s"(rwd), [rwo] "s"(rwo), [rx] "s"(rx), \
               [fo] "s"(fo), [fh] "s"(fh), [fg] "s"(fg), [fsv] "v"(fsv), [fg0] "v"(fg0), [fg2] "v"(fg2), [fpv] "v"(fpv), [fp] "s"(WS_FP) : "m0", "scc", "memory"
#define XBAR(n) asm volatile("s_waitcnt vmcnt(" #n ") lgkmcnt(0)\n\ts_barrier" AOPS)
#define XROUND(A1, A2, B1, B2, C1, C2, D1, D2, E1, E2, F1, F2) asm volatile( \
            XTILE(136, XRK("%[ka]", 1024), XRV(144, "%[va]", 512), 2, A1, 144, A2, XRV(140, "%[va]", 1024)) \
            XTILE(144, XRK("%[ka]", 2048), XRV(144, "%[va]", 1536), 2, B1, 144, B2, XRV(140, "%[va]", 2048)) \
            XTILE(144, XRK("%[ka]", 3072), XRV(144, "%[va]", 2560), 2, C1, 144, C2, XRV(140, "%[va]", 3072)) \
            XTILE(144, XRK("%[ka]", 4096), XRV(144, "%[va]", 3584), 2, D1, 144, D2, XRV(140, "%[va]", 4096)) \
            XTILE(144, XRK("%[ka]", 5120), XRV(144, "%[va]", 4608), 2, E1, 144, E2, XRV(140, "%[va]", 5120)) \
            XTILE(144, XRK("%[ka]", 6144), XRV(144, "%[va]", 5632), 2, F1, 144, F2, XRV(140, "%[va]", 6144)) \
            XTILE(144, XRK("%[ka]", 7168), XRV(144, "%[va]", 6656), 2, XRV(136, "%[va]", 7680), 144, "", XRV(140, "%[va]", 7168)) AOPS)
#define XLAST(X1, X2) asm volatile(XTILE(144, XRK("%[kn]", 0), "", 1, X1, 136, X2, XRV(140, "%[vn]", 0)) AOPS)
        unsigned ka = kb, va = vb, kn = kb + 16384, vn = vb + 16384;
        asm volatile("s_nop 15" AOPS);
        STAMP(1, 7);
        XROUND(XGU(0), "", "", XST("%[l16]", "%[fo]", 0), XGU(1), XST("%[fsv]", "0", 0), "", XST("%[l16]", "%[fh]", 0), XGU2, "", XGU(0), "");
        STAMP(1, 8);
        XBAR(7);
        STAMP(1, 9);
        XLAST(XKV, XKV);
        ka = kb + 16384; va = vb + 16384; kn = kb; vn = vb;
        XROUND(XKV, XKV, XGU(1), XST("%[fg0]", "%[fg]", 0), XGU2, "", XST("%[fg0]", "%[fg]", 1024), XGU(0), "", XGU(1), XST("%[fg2]", "%[fg]", 2048), "");
        STAMP(1, 10);
        XBAR(7);
        STAMP(1, 11);
        km += 16384 - 4096; ks += KV_CHUNK - 4096;
        XLAST(XKV, XKV);
        ka = kb; va = vb; kn = kb + 16384; vn = vb + 16384;
        XROUND(XKV, XKV, XGU2, "", XGU(0), "", XGU(1), "", XGU2, "", XPF, "");
        STAMP(1, 12);
        XBAR(6);
        STAMP(1, 13);
        km = lds0 + g * 32768 + wq * 4096; ks = (32 * g + 4 * wq) * 8192 + ct * 128;
        XLAST(XFP, XWO);
        ka = kb + 16384; va = vb + 16384; kn = kb; vn = vb;
        XROUND(XWO, XWO, XWO, "", XXR(192, 0), "", XXR(194, 16), "", XXR(196, 32), "", XXR(198, 48), "");
        XLAST(XFL, "");
        STAMP(1, 14);
        asm volatile(XM(160, 136, 152, 160) "s_waitcnt vmcnt(0) lgkmcnt(0)\n\ts_barrier\n\ts_nop 15" AOPS);
        STAMP(1, 4);
        f32x16 o;
        {
            const f32x16& snd = g ? o0 : o1;
            const unsigned xa = lds0 + g * 32768 + 16384 + wq * 3072 + l16, xb = lds0 + (g ^ 1) * 32768 + 16384 + wq * 3072 + l16;
            *(LDSAS f32x4*)(xa) = f32x4{snd[0], snd[1], snd[2], snd[3]};
            *(LDSAS f32x4*)(xa + 1024) = f32x4{snd[4], snd[5], snd[6], snd[7]};
            *(LDSAS float*)(xa + 2048) = snd[8];
            if (tid == 0) *(LDSAS unsigned*)(lds0 + L_FLAG) = 0u;
            BAR_L();
            const f32x4 p0 = lds_f4(xb), p1 = lds_f4(xb + 1024); const float pl = *(const LDSAS float*)(xb + 2048);
            o = g ? o1 : o0;
#pragma unroll
            for (int i = 0; i < 4; ++i) { o[i] += p0[i]; o[4 + i] += p1[i]; }
            o[8] += pl;
        }
        bool bad = !(o[8] < 1.8446744e19f);
#pragma unroll
        for (int i = 0; i < 8; ++i) bad |= !(__builtin_fabsf(o[i]) < 3.0e38f);
        if (__any(bad) && lane == 0) *(LDSAS unsigned*)(lds0 + L_FLAG) = 1u;
        auto store_o = [&]() {
            const float inv = __builtin_amdgcn_rcpf(o[8]);
            u32x4 ov;
#pragma unroll
            for (int i = 0; i < 4; ++i) ov[i] = pk2(o[2 * i] * inv, o[2 * i + 1] * inv);
            __builtin_amdgcn_raw_buffer_store_b128(ov, rs, l16, WS_O + ((qt + g) * 32 + head) * 1024, AUX_SC1);
        };
        BAR_L();
        if (__builtin_amdgcn_readfirstlane(*(const LDSAS unsigned*)(lds0 + L_FLAG))) {
            BAR_L();
            const s16x8 qf = g ? q1 : q0;
            const u32x4 cc = {cst, cst, cst, cst};
            const unsigned vf = lds0 + 16384 + 8192 + (16 * h + (r32 & 15)) * 16;
            float m = -__builtin_inff();
            o = f32x16{};
            const f32x16 z = {};
#pragma unroll 1
            for (int c = 0; c < 8; ++c) {
                dma16<true>(rs, l16, kvs + c * KV_CHUNK + w * 2048, lds0 + 16384 + w * 2048);
                dma16<true>(rs, l16, kvs + c * KV_CHUNK + w * 2048 + 1024, lds0 + 16384 + w * 2048 + 1024);
                BAR_VL(0);
#pragma unroll 1
                for (int t = 0; t < 8; ++t) {
                    f32x16 s = MFMA(lds_b128(lds0 + 16384 + l16 + t * 1024), qf, z);
                    float mx = s[0];
#pragma unroll
                    for (int i = 1; i < 16; ++i) mx = __builtin_fmaxf(mx, s[i]);
                    mx = __builtin_fmaxf(swap_max(mx), m);
                    const float al = __builtin_amdgcn_exp2f(m - mx);
                    m = mx;
#pragma unroll
                    for (int i = 0; i < 16; ++i) { o[i] *= al; s[i] = __builtin_amdgcn_exp2f(s[i] - m); }
#pragma unroll
                    for (int i = 0; i < 4; ++i) { pA[i] = pk2(s[2 * i], s[2 * i + 1]); pB[i] = pk2(s[8 + 2 * i], s[9 + 2 * i]); }
                    const u32x4 a = __builtin_bit_cast(u32x4, lds_b128(vf + t * 1024)), c2 = __builtin_bit_cast(u32x4, lds_b128(vf + t * 1024 + 512));
                    o = MFMA(__builtin_bit_cast(s16x8, r32 < 16 ? a : cc), __builtin_bit_cast(s16x8, pA), o);
                    o = MFMA(__builtin_bit_cast(s16x8, r32 < 16 ? c2 : cc), __builtin_bit_cast(s16x8, pB), o);
                }
                BAR_L();
            }
        }
        store_o();
        STAMP(1, 5);
        BAR_L();
        if (w >= 4) issue_wo(16 + ((w - 4) >> 1) * 32 + (w & 1) * 8, 8);
        gu_colsum();
#pragma unroll 1
        for (unsigned spins = 0; spins < SPIN_CAP && !__all(fpl[0] == ep && fpl[1] == ep && fpl[2] == ep && fpl[3] == ep); ++spins) {
            __builtin_amdgcn_s_sleep(2);
            fpl = __builtin_amdgcn_raw_buffer_load_b128(rs, l16, WS_FP, AUX_SC1);
        }
        STAMP(1, 6);
        FLUSH(1);
    }

    {
        STAMP(2, 0);
        f32x16 a0, a1;
        if (w < 4) {
            const unsigned t0 = lds0 + L_WO + kh * 32768 + w64_tr(lane, kh), t1 = lds0 + L_WO + kh * 32768 + w64_tr(lane, kh ^ 1);
            G2 g2;
            g2.tb[0] = g2.tb[2] = g2.tb[4] = t0; g2.tb[1] = g2.tb[3] = g2.tb[5] = t1;
            g2.sa = g2.s0 = RFL(WS_O + ((rt * 2 + wt) * 32 + kh * 16) * 1024); g2.l16 = l16; g2.budget = budget; g2.rs = rs; g2.a1 = f32x16{};
#pragma unroll
            for (int i = 0; i < 8; ++i) { g2.a0[2 * i] = bflo(xres[i]); g2.a0[2 * i + 1] = bfhi(xres[i]); }
            __builtin_amdgcn_s_sleep(DELAY_O);
            STAMP(2, 1);
            g2_steps<0, 16, 0, 6, -1>(g2);
            a0 = g2.a0; a1 = g2.a1; budget = g2.budget;
        } else BAR_VL(0);
        STAMP(2, 2);
        BAR_L();
        if (w < 4) {
#pragma unroll
            for (int g = 0; g < 4; ++g) *(LDSAS f32x4*)(lds0 + w * 4096 + g * 1024 + lane * 16) = f32x4{a1[4 * g], a1[4 * g + 1], a1[4 * g + 2], a1[4 * g + 3]};
        }
#pragma unroll
        for (int n = 0; n < 3; ++n) *(LDSAS float*)(lds0 + L_TAB + (w * 96 + n * 32 + r32) * 4) = cs[n];
        BAR_L();
        if (w >= 4) {
            if (tid < 256 + 96) {
                float t[8];
#pragma unroll
                for (int i = 0; i < 8; ++i) t[i] = *(const LDSAS float*)(lds0 + L_TAB + (i * 96 + tid - 256) * 4);
                *(LDSAS float*)(lds0 + L_TAB + 3072 + (tid - 256) * 4) = ((t[0] + t[1]) + (t[2] + t[3])) + ((t[4] + t[5]) + (t[6] + t[7]));
            }
        } else {
            f32x4 p[4];
#pragma unroll
            for (int g = 0; g < 4; ++g) p[g] = lds_f4(lds0 + (w ^ 2) * 4096 + g * 1024 + lane * 16);
            SBAR();
#pragma unroll
            for (int g = 0; g < 4; ++g) {
                hreg[g >> 1][(g & 1) * 2] = pk2(a0[4 * g] + p[g][0], a0[4 * g + 1] + p[g][1]);
                hreg[g >> 1][(g & 1) * 2 + 1] = pk2(a0[4 * g + 2] + p[g][2], a0[4 * g + 3] + p[g][3]);
            }
            const unsigned so = WS_H + ((rt * 2 + wt) * 32 + (ct * 2 + kh) * 2) * 1024;
            __builtin_amdgcn_raw_buffer_store_b128(hreg[0], rs, lane * 16, so, AUX_SC1);
            __builtin_amdgcn_raw_buffer_store_b128(hreg[1], rs, lane * 16 + 1024, so, AUX_SC1);
            SBAR();
            float sm = 0.f, sq = 0.f;
#pragma unroll
            for (int i = 0; i < 8; ++i) { sm = dot2(hreg[i >> 2][i & 3], ONES2, sm); sq = dot2(hreg[i >> 2][i & 3], hreg[i >> 2][i & 3], sq); }
            sm = swap_add(sm); sq = swap_add(sq);
            if (h == 0) __builtin_amdgcn_raw_buffer_store_b64(u32x2{__float_as_uint(sm), __float_as_uint(sq)}, rs, r32 * 8,
                                                              WS_ST + ((rt * 2 + wt) * 16 + ct * 2 + kh) * 256, AUX_SC1);
        }
        STAMP(2, 3);
        BAR_L();
        if (w >= 4) { issue_dn(0, 15, ((w - 4) >> 1) * 15 * 2048, w - 4); if (bi >= 29) issue_dn(15, 24, L_GU + ((w - 4) >> 1) * 49152, w - 4); }
        STAMP(2, 5);
        FLUSH(2);
    }

    {
        STAMP(3, 0);
        if (bi < 29) {
            const int tt = xcd * 8 + w;
            GU gu;
            gu.a0 = gu.a1 = gu.a2 = f32x16{};
            gu.sa = gu.s0 = RFL(WS_H + tt * 32768); gu.sv = 8 * h * 256 + r32 * 8; gu.ss = RFL(WS_ST + tt * 4096); gu.l16 = l16; gu.budget = budget; gu.rs = rs;
            gu.dm = RFL(lds0 + L_GU + (w >> 2) * 49152 + (w & 3) * 6144); gu.ds = RFL(((43 * (w >> 2) + 15) * 16 + (w & 3) * 48) * 1024 + ct * 128);
            gu.dv = w64_voff(lane); gu.rd = mk_rsrc(wd, F * D * 2);
            gu.wa[0] = gu_wa(0); gu.wa[1] = gu_wa(1); gu.wa[2] = gu_wa(2);
            f32x4 ctab[3][4];
#pragma unroll
            for (int n = 0; n < 3; ++n)
#pragma unroll
                for (int g = 0; g < 4; ++g) ctab[n][g] = lds_f4(lds0 + L_TAB + 3072 + (n * 32 + 8 * g + 4 * h) * 4);
            if (w < 4) __builtin_amdgcn_s_sleep(DELAY_H);
            STAMP(3, 2);
            gu_steps<0>(gu);
            budget = gu.budget;
            const f32x16 acc[3] = {gu.a0, gu.a1, gu.a2};
            u32x16 st = gu.st;
            const unsigned sv = gu.sv, ss = gu.ss;
            STAMP(3, 3);
#pragma unroll 1
            for (;;) {
                unsigned mx = st[0];
#pragma unroll
                for (int i = 1; i < 16; ++i) mx = st[i] > mx ? st[i] : mx;
                if (!__any(mx == ~0u) || budget == 0) break;
                --budget;
#pragma unroll
                for (int i = 0; i < 8; ++i) { const u32x2 v = __builtin_amdgcn_raw_buffer_load_b64(rs, sv + i * 256, ss, AUX_SC1); st[2 * i] = v[0]; st[2 * i + 1] = v[1]; }
            }
            float sm = 0.f, sq = 0.f;
#pragma unroll
            for (int i = 0; i < 8; ++i) { sm += __uint_as_float(st[2 * i]); sq += __uint_as_float(st[2 * i + 1]); }
            sm = swap_add(sm); sq = swap_add(sq);
            const float mean = sm * (1.f / D);
            const float var = __builtin_fmaxf(sq * (1.f / D) - mean * mean, 0.f) + LN_EPS;
            const float rstd = __builtin_amdgcn_rsqf(var), sd = var * rstd, ga = -LOG2E * rstd;
            auto epi = [&](int n) {
                u32x4 ov; float y[8];
#pragma unroll
                for (int g = 0; g < 2; ++g) {
                    const f32x4 cg = ctab[n][g], cu = ctab[n][2 + g];
#pragma unroll
                    for (int i = 0; i < 4; ++i) {
                        const float e = __builtin_amdgcn_exp2f(__builtin_fmaf(-mean, cg[i], acc[n][4 * g + i]) * ga);
                        y[4 * g + i] = __builtin_fmaf(-mean, cu[i], acc[n][8 + 4 * g + i]) * __builtin_amdgcn_rcpf(__builtin_fmaf(e, sd, sd));
                    }
                }
#pragma unroll
                for (int i = 0; i < 4; ++i) ov[i] = pk2(y[2 * i], y[2 * i + 1]);
                const int u = bi * 3 + n;
                if (u < NU) __builtin_amdgcn_raw_buffer_store_b128(ov, rs, lane * 16, WS_HH + (tt * NU + u) * 1024, AUX_SC1);
            };
            epi(0); epi(1);
            epi(2);
        }
        STAMP(3, 4);
        if (bi >= 29) WAITV(0);
        BAR_L();
        STAMP(3, 5);
        if (w >= 4) {
            if (bi < 29) issue_dn(27, 12, L_GU + ((w - 4) >> 1) * 49152 + 24576, w - 4);
            issue_dn(39, 1, L_TAB + ((w - 4) >> 1) * 2048, w - 4); STAMP(3, 6);
        }
        FLUSH(3);
    }

    {
        STAMP(4, 0);
        if (b == 0 && tid == 0) __builtin_amdgcn_raw_buffer_store_b32(ep, rs, 0, WS_EP, AUX_SC1);
        f32x16 a0, a1;
        if (w < 4) {
            const unsigned t0 = lds0 + w64_tr(lane, kh), t1 = lds0 + w64_tr(lane, kh ^ 1);
            G2 g2;
            g2.tb[0] = t0 + kh * 15 * 2048; g2.tb[1] = t1 + kh * 15 * 2048; g2.tb[2] = t0 + L_GU + kh * 24 * 2048; g2.tb[3] = t1 + L_GU + kh * 24 * 2048;
            g2.tb[4] = t0 + L_TAB + kh * 2048; g2.tb[5] = t1 + L_TAB + kh * 2048;
            g2.sa = g2.s0 = RFL(WS_HH + ((rt * 2 + wt) * NU + kh * 43) * 1024); g2.l16 = l16; g2.budget = budget; g2.rs = rs; g2.a1 = f32x16{};
#pragma unroll
            for (int i = 0; i < 8; ++i) { g2.a0[2 * i] = bflo(hreg[i >> 2][i & 3]); g2.a0[2 * i + 1] = bfhi(hreg[i >> 2][i & 3]); }
            __builtin_amdgcn_s_sleep(DELAY_G);
            STAMP(4, 1);
            g2_steps<0, 43, 1, 25, 38>(g2);
            a0 = g2.a0; a1 = g2.a1;
        } else {
            BAR_VL(0);
            issue_dn(40, 3, ((w - 4) >> 1) * 15 * 2048, w - 4);
            BAR_VL(0);
        }
        STAMP(4, 2);
        BAR_L();
        if (w < 4) {
#pragma unroll
            for (int g = 0; g < 4; ++g) *(LDSAS f32x4*)(lds0 + w * 4096 + g * 1024 + lane * 16) = f32x4{a1[4 * g], a1[4 * g + 1], a1[4 * g + 2], a1[4 * g + 3]};
        }
        BAR_L();
        if (w < 4) {
            f32x4 p[4];
#pragma unroll
            for (int g = 0; g < 4; ++g) p[g] = lds_f4(lds0 + (w ^ 2) * 4096 + g * 1024 + lane * 16);
            SBAR();
            unsigned y[8];
#pragma unroll
            for (int g = 0; g < 4; ++g) {
                y[2 * g] = pk2(a0[4 * g] + p[g][0], a0[4 * g + 1] + p[g][1]);
                y[2 * g + 1] = pk2(a0[4 * g + 2] + p[g][2], a0[4 * g + 3] + p[g][3]);
            }
            const rsrc_t ro = mk_rsrc(out, S * D * 2);
            const unsigned vo = (rt * 64 + wt * 32 + r32) * 1024 + (ct * 64 + kh * 32 + 8 * h) * 2;
#pragma unroll
            for (int g = 0; g < 2; ++g) {
                auto e0 = __builtin_amdgcn_permlane32_swap(y[4 * g], y[4 * g + 2], false, false);
                auto e1 = __builtin_amdgcn_permlane32_swap(y[4 * g + 1], y[4 * g + 3], false, false);
                __builtin_amdgcn_raw_buffer_store_b128(u32x4{e0[0], e1[0], e0[1], e1[1]}, ro, vo + g * 32, 0, 0);
            }
        }
        STAMP(4, 3);
        STAMP(4, 4);
        FLUSH(4);
        asm volatile(".Lmain_end:\n\t.subsection 2\n\t.fill 64, 4, 0xBF800000\n\t.subsection 0");
    }
}

extern "C" void kernel_launch(void* const* d_in, const int* in_sizes, int n_in,
                              void* d_out, int out_size, void* d_ws, size_t ws_size,
                              hipStream_t stream) {
    const u16* x = (const u16*)d_in[0];
    const u16* wqkv = (const u16*)d_in[1];
    const u16* wo = (const u16*)d_in[2];
    const u16* wg = (const u16*)d_in[4];
    const u16* wu = (const u16*)d_in[5];
    const u16* wd = (const u16*)d_in[6];
    u16* out = (u16*)d_out;
    char* ws = (char*)d_ws;
    (void)hipFuncSetAttribute((const void*)k_block, hipFuncAttributeMaxDynamicSharedMemorySize, K2_LDS);
    k_block<<<256, 512, K2_LDS, stream>>>(x, wqkv, wo, wg, wu, wd, out, ws);
}
```

```cpp
#define PROBE 2
#include <hip/hip_runtime.h>
#include <hip/hip_bf16.h>
#include <stdint.h>

typedef short s16x8 __attribute__((ext_vector_type(8)));
typedef short s16x4 __attribute__((ext_vector_type(4)));
typedef float f32x16 __attribute__((ext_vector_type(16)));
typedef float f32x4 __attribute__((ext_vector_type(4)));
typedef float f32x2 __attribute__((ext_vector_type(2)));
typedef unsigned u32x4 __attribute__((ext_vector_type(4)));
typedef unsigned u32x2 __attribute__((ext_vector_type(2)));
typedef unsigned u32x8 __attribute__((ext_vector_type(8)));
typedef __bf16 bf16x2 __attribute__((ext_vector_type(2)));
typedef unsigned short u16;
#define LDSAS __attribute__((address_space(3)))
#define GAS __attribute__((address_space(1)))
typedef __amdgpu_buffer_rsrc_t rsrc_t;

constexpr int S = 2048, D = 512, NH = 32, F = 1376, NU = F / 16;
constexpr float LN_EPS = 1e-5f;
constexpr float LOG2E = 1.4426950408889634f;

constexpr unsigned KV_CHUNK = 16384;
constexpr unsigned KV_HEAD = 8 * KV_CHUNK;
constexpr unsigned WS_EP = 0;
constexpr unsigned WS_FQ = 4096;
constexpr unsigned WS_FP = WS_FQ + 16 * 64 * 128;
constexpr unsigned WS_Q = WS_FP + 4096;
constexpr unsigned WS_KV = WS_Q + NH * 64 * 1024;
constexpr unsigned WS_O = WS_KV + NH * KV_HEAD;
constexpr unsigned WS_H = WS_O + 64 * 32 * 1024;
constexpr unsigned WS_ST = WS_H + 64 * 32 * 1024;
constexpr unsigned WS_HH = WS_ST + 64 * 16 * 256;
constexpr unsigned WS_END = WS_HH + 64 * NU * 1024;
constexpr int AUX_SC1 = 16;
#define STAMP_DECL unsigned long long tp_ = 0
#define STAMP(k, i) do { if ((k) * 16 + (i) == PROBE || (k) * 16 + (i) == 0) { __builtin_amdgcn_sched_barrier(0); tp_ = __builtin_amdgcn_s_memrealtime(); \
    if (blockIdx.x == 0 && threadIdx.x == 0) *(volatile unsigned long long*)(ws + ((k) * 16 + (i) == PROBE ? 16 : 8)) = tp_; if (PROBE == 0 && blockIdx.x == 0 && threadIdx.x == 0) *(volatile unsigned long long*)(ws + 8) = tp_; __builtin_amdgcn_sched_barrier(0); } } while (0)
#define FLUSH(k) do { if ((k) == 4 && blockIdx.x == 0 && threadIdx.x == 0) { asm volatile("s_waitcnt vmcnt(0)" ::: "memory"); const unsigned long long a_ = __builtin_nontemporal_load((unsigned long long*)(ws + 8)), b_ = __builtin_nontemporal_load((unsigned long long*)(ws + 16)); \
    unsigned long long lim_ = b_ - a_; if (lim_ > 20000) lim_ = 20000; while (__builtin_amdgcn_s_memrealtime() - a_ < 6000 + lim_) __builtin_amdgcn_s_sleep(2); } } while (0)

#define SBAR() __builtin_amdgcn_sched_barrier(0)
#define RFL(x) ((unsigned)__builtin_amdgcn_readfirstlane(x))
#define PIN(x) asm volatile("" : "+v"(x))
#define MFMA(a, b, c) __builtin_amdgcn_mfma_f32_32x32x16_bf16((a), (b), (c), 0, 0, 0)
#define WAITV(n) asm volatile("s_waitcnt vmcnt(" #n ")" ::: "memory")
#define BAR_L() asm volatile("s_waitcnt lgkmcnt(0)\n\ts_barrier" ::: "memory")
#define BAR_0() asm volatile("s_barrier" ::: "memory")
#define BAR_VL(n) asm volatile("s_waitcnt vmcnt(" #n ") lgkmcnt(0)\n\ts_barrier" ::: "memory")

__device__ __forceinline__ rsrc_t mk_rsrc(const void* p, unsigned bytes) {
    return __builtin_amdgcn_make_buffer_rsrc((void*)p, (short)0, (int)bytes, 0x00020000);
}
template <bool SC1 = false> __device__ __forceinline__ void dma16(rsrc_t r, unsigned voff, unsigned soff, unsigned lds) {
    if constexpr (SC1) asm volatile("s_nop 4\n\ts_mov_b32 m0, %0\n\ts_nop 0\n\tbuffer_load_dwordx4 %1, %2, %3 offen sc1 lds"
                 :: "s"(__builtin_amdgcn_readfirstlane(lds)), "v"(voff), "s"(r), "s"(__builtin_amdgcn_readfirstlane(soff)) : "m0", "memory");
    else asm volatile("s_nop 4\n\ts_mov_b32 m0, %0\n\ts_nop 0\n\tbuffer_load_dwordx4 %1, %2, %3 offen lds"
                 :: "s"(__builtin_amdgcn_readfirstlane(lds)), "v"(voff), "s"(r), "s"(__builtin_amdgcn_readfirstlane(soff)) : "m0", "memory");
}
__device__ __forceinline__ unsigned pk2(float lo, float hi) {
    f32x2 v = {lo, hi};
    return __builtin_bit_cast(unsigned, __builtin_convertvector(v, bf16x2));
}
__device__ __forceinline__ float dot2(unsigned a, unsigned b, float c) {
    return __builtin_amdgcn_fdot2_f32_bf16(__builtin_bit_cast(bf16x2, a), __builtin_bit_cast(bf16x2, b), c, false);
}
__device__ __forceinline__ float swap_add(float v) {
    auto r = __builtin_amdgcn_permlane32_swap(__float_as_uint(v), __float_as_uint(v), false, false);
    return __uint_as_float(r[0]) + __uint_as_float(r[1]);
}
__device__ __forceinline__ float swap_max(float v) {
    auto r = __builtin_amdgcn_permlane32_swap(__float_as_uint(v), __float_as_uint(v), false, false);
    return __builtin_fmaxf(__uint_as_float(r[0]), __uint_as_float(r[1]));
}
__device__ __forceinline__ s16x8 lds_b128(unsigned a) { return *(const LDSAS s16x8*)(a); }
__device__ __forceinline__ f32x4 lds_f4(unsigned a) { return *(const LDSAS f32x4*)(a); }
__device__ __forceinline__ s16x8 lds_tr2(unsigned a0, unsigned a1) {
    s16x4 lo = __builtin_amdgcn_ds_read_tr16_b64_v4i16((LDSAS s16x4*)(a0));
    s16x4 hi = __builtin_amdgcn_ds_read_tr16_b64_v4i16((LDSAS s16x4*)(a1));
    return __builtin_shufflevector(lo, hi, 0, 1, 2, 3, 4, 5, 6, 7);
}
__device__ __forceinline__ float bflo(unsigned v) { return __uint_as_float(v << 16); }
__device__ __forceinline__ float bfhi(unsigned v) { return __uint_as_float(v & 0xffff0000u); }
constexpr unsigned ONES2 = 0x3f803f80u;
template <int I> struct ic { static constexpr int value = I; constexpr operator int() const { return I; } };
template <int B, int E, class Fn> __device__ __forceinline__ void sfor(Fn&& fn) { if constexpr (B < E) { fn(ic<B>{}); sfor<B + 1, E>(fn); } }

#define K1_X_AUX 2
#define K1_ABL 0
#define K1_ST_AUX 16
constexpr unsigned K1_W = 65536, K1_FLG = K1_W + 5 * 16384, K1_ST = K1_FLG + 512, K1_CS = K1_ST + 512;
__host__ __device__ constexpr unsigned k1_slot(int s) { return s < 5 ? K1_W + s * 16384 : s == 5 ? K1_W : s < 10 ? (s - 6) * 16384 : K1_W + (s - 9) * 16384; }
__device__ __forceinline__ void lds_spin_ge(unsigned addr, unsigned want) {
#pragma unroll 1
    for (unsigned n = 0; n < (1u << 22); ++n) {
        unsigned v; asm volatile("ds_read_b32 %0, %1\n\ts_waitcnt lgkmcnt(0)" : "=v"(v) : "v"(addr) : "memory");
        if ((unsigned)__builtin_amdgcn_readfirstlane(v) >= want) break;
        __builtin_amdgcn_s_sleep(1);
    }
}
__device__ __forceinline__ void lds_post(unsigned addr, unsigned v) {
    asm volatile("s_waitcnt lgkmcnt(0)\n\tds_write_b32 %0, %1" :: "v"(addr), "v"(v) : "memory");
}
__device__ __forceinline__ void lds_inc(unsigned addr, int lane) {
    asm volatile("s_waitcnt lgkmcnt(0)" ::: "memory");
    if (lane == 0) asm volatile("ds_add_u32 %0, %1" :: "v"(addr), "v"(1u) : "memory");
}
typedef unsigned u32x16 __attribute__((ext_vector_type(16)));
template <int CTRL> __device__ __forceinline__ float dpp_add(float v) {
    return v + __builtin_bit_cast(float, __builtin_amdgcn_update_dpp(0, __builtin_bit_cast(int, v), CTRL, 0xf, 0xf, true));
}

constexpr unsigned K2_LDS = 163840;
constexpr unsigned L_WO = 0, L_GU = 65536;
constexpr unsigned L_TAB = 61440;
constexpr unsigned L_FLAG = 16384 + 12288;
constexpr unsigned SPIN_CAP = 1u << 20;
#define POLL_DELAY_Q 24
#define ABL_GU 0
#define DELAY_O 0
#define DELAY_H 8
#define DELAY_G 8
constexpr int GU_AW = 8, G2_AW = 12;
#define TAG_TEST "v_max3_u32 v120, v[%c[ar]], v[%c[ar]+1], v[%c[ar]+2]\n\tv_max_u32 v120, v120, v[%c[ar]+3]\n\tv_cmp_eq_u32 vcc, -1, v120\n\t"
#define TAG_RELOAD(RB, AW) "s_add_u32 %[t], %[s0], (tagj>>2)*4096\n\tbuffer_load_dwordx4 v[" #RB "+4*(tagj%%" #AW "):" #RB "+3+4*(tagj%%" #AW ")], %[l16], %[rs], %[t] offen offset:(tagj&3)*1024 sc1\n\t"
#define TAG_CHECK(RB, AW) \
    TAG_TEST "s_cbranch_vccnz 5f\n6:\n\t" \
    ".subsection 1\n5:\n\ts_cmp_eq_u32 %[bud], 0\n\ts_cbranch_scc1 6b\n\ts_sub_u32 %[bud], %[bud], 1\n\t.set tagj, %c[i]\n\t" TAG_RELOAD(RB, AW) \
    "s_waitcnt vmcnt(0)\n\t" TAG_TEST "s_cbranch_vccnz 5b\n\t.rept %c[cnt]-1\n\t.set tagj, tagj+1\n\t" TAG_RELOAD(RB, AW) ".endr\n\ts_branch 6b\n\t.subsection 0\n\t"
#define TAG_LOAD "buffer_load_dwordx4 v[%c[lr]:%c[lr]+3], %[l16], %[rs], %[sa] offen offset:%c[lo] sc1\n\t.if %c[lo] == 3072\n\ts_add_u32 %[sa], %[sa], 0x1000\n\t.endif\n\t"

__device__ __forceinline__ void wait_flags(rsrc_t rs, unsigned base, int n, unsigned ep, int lane) {
    const unsigned v = (lane < n ? lane : n - 1) * 128;
#pragma unroll 1
    for (unsigned spins = 0; spins < SPIN_CAP; ++spins) {
        const unsigned f = __builtin_amdgcn_raw_buffer_load_b32(rs, v, base, AUX_SC1);
        if (__all(f == ep)) break;
        __builtin_amdgcn_s_sleep(1);
    }
    asm volatile("" ::: "memory");
}

__device__ __forceinline__ unsigned w64_voff(int lane) { return (lane >> 3) * 1024 + (((lane & 7) ^ (((lane >> 4) & 1) << 2)) << 4); }
__device__ __forceinline__ unsigned w64_tr(int lane, int n) {
    const int q = (lane & 15) >> 2, p = lane & 3, blk = (lane >> 4) & 1, h = lane >> 5;
    return (4 * h + q) * 128 + (((4 * n + 2 * blk + (p >> 1)) ^ (((q >> 1) & 1) << 2)) << 4) + 8 * (p & 1);
}

struct G2 { u32x16 r0, r1, r2; u32x8 w0, w1, w2; f32x16 a0, a1; unsigned tb[6], sa, s0, l16, budget; rsrc_t rs; };
__host__ __device__ constexpr int g2_w(int DN, int n) {
    return !DN ? n * 2048 : n < 15 ? n * 2048 : n < 39 ? 65536 + (n - 15) * 2048 : n < 40 ? 131072 : (n - 40) * 2048;
}
template <int i, int N, int DN, int B0, int B1> __device__ __forceinline__ void g2_steps(G2& g) {
    constexpr int AW = G2_AW, j = i + AW - 1, n = i + 2, sel = n < N ? g2_w(DN, n) >> 16 : 3;
    unsigned tq;
#define G2_RD(P0, P1) "ds_read_b64_tr_b16 v[%c[nr]:%c[nr]+1], %[" P0 "] offset:%c[no]\n\tds_read_b64_tr_b16 v[%c[nr]+2:%c[nr]+3], %[" P0 "] offset:%c[no]+1024\n\t" \
                      "ds_read_b64_tr_b16 v[%c[nr]+4:%c[nr]+5], %[" P1 "] offset:%c[no]\n\tds_read_b64_tr_b16 v[%c[nr]+6:%c[nr]+7], %[" P1 "] offset:%c[no]+1024\n\t"
    if constexpr (i == 0)
        asm volatile(".set tagj, 0\n\t.rept 11\n\tbuffer_load_dwordx4 v[128+4*tagj:131+4*tagj], %[l16], %[rs], %[sa] offen offset:(tagj&3)*1024 sc1\n\t"
                     ".if (tagj&3) == 3\n\ts_add_u32 %[sa], %[sa], 0x1000\n\t.endif\n\t.set tagj, tagj+1\n\t.endr\n\t"
                     ".set tagj, 0\n\t.rept 2\n\tds_read_b64_tr_b16 v[96+8*tagj:97+8*tagj], %[b0] offset:tagj*2048\n\tds_read_b64_tr_b16 v[98+8*tagj:99+8*tagj], %[b0] offset:tagj*2048+1024\n\t"
                     "ds_read_b64_tr_b16 v[100+8*tagj:101+8*tagj], %[b1] offset:tagj*2048\n\tds_read_b64_tr_b16 v[102+8*tagj:103+8*tagj], %[b1] offset:tagj*2048+1024\n\t.set tagj, tagj+1\n\t.endr"
                     : "={v[128:143]}"(g.r0), "={v[144:159]}"(g.r1), "={v[160:175]}"(g.r2), "={v[96:103]}"(g.w0), "={v[104:111]}"(g.w1), "={v[112:119]}"(g.w2), [sa] "+s"(g.sa),
                       "+{v[64:79]}"(g.a0), "+{v[80:95]}"(g.a1)
                     : [l16] "v"(g.l16), [rs] "s"(g.rs), [b0] "v"(g.tb[0]), [b1] "v"(g.tb[1]) : "scc", "memory");
    asm volatile(".if %c[ld]\n\t" TAG_LOAD ".endif\n\t"
                 ".if %c[bar]\n\ts_waitcnt lgkmcnt(0)\n\ts_barrier\n\t.endif\n\t"
                 ".if %c[sel] == 0\n\t" G2_RD("b0", "b1") ".endif\n\t.if %c[sel] == 1\n\t" G2_RD("b2", "b3") ".endif\n\t.if %c[sel] == 2\n\t" G2_RD("b4", "b5") ".endif\n\t"
                 "s_waitcnt vmcnt(%c[vn]) lgkmcnt(%c[ln])\n\t" TAG_CHECK(128, 12)
                 "v_mfma_f32_32x32x16_bf16 v[64:79], v[%c[wr]:%c[wr]+3], v[%c[ar]:%c[ar]+3], v[64:79]\n\t"
                 "v_mfma_f32_32x32x16_bf16 v[80:95], v[%c[wr]+4:%c[wr]+7], v[%c[ar]:%c[ar]+3], v[80:95]\n\t"
                 ".if %c[cnt] == 1\n\ts_nop 15\n\t.endif"
                 : "+{v[64:79]}"(g.a0), "+{v[80:95]}"(g.a1), "+{v[128:143]}"(g.r0), "+{v[144:159]}"(g.r1), "+{v[160:175]}"(g.r2), "+{v[96:103]}"(g.w0), "+{v[104:111]}"(g.w1), "+{v[112:119]}"(g.w2),
                   [sa] "+s"(g.sa), [bud] "+s"(g.budget), [t] "=&s"(tq)
                 : [l16] "v"(g.l16), [rs] "s"(g.rs), [s0] "s"(g.s0), [b0] "v"(g.tb[0]), [b1] "v"(g.tb[1]), [b2] "v"(g.tb[2]), [b3] "v"(g.tb[3]), [b4] "v"(g.tb[4]), [b5] "v"(g.tb[5]),
                   [ld] "n"(j < N ? 1 : 0), [lr] "n"(128 + 4 * (j % AW)), [lo] "n"((j & 3) * 1024), [bar] "n"(i == B0 || i == B1 ? 1 : 0), [sel] "n"(sel),
                   [nr] "n"(96 + 8 * (n % 3)), [no] "n"(n < N ? g2_w(DN, n) & 65535 : 0), [vn] "n"(j < N ? AW - 1 : N - 1 - i), [ln] "n"(n < N ? 8 : n == N ? 4 : 0),
                   [ar] "n"(128 + 4 * (i % AW)), [wr] "n"(96 + 8 * (i % 3)), [i] "n"(i), [cnt] "n"(j < N ? AW : N - i) : "vcc", "scc", "v120", "memory");
    if constexpr (i + 1 < N) g2_steps<i + 1, N, DN, B0, B1>(g);
}
__host__ __device__ constexpr int gu_dma(int s) { return s >= 16 && s < 28 && !(s & 1); }
__host__ __device__ constexpr int gu_ops(int s) { return (s + GU_AW - 1 < 32 ? 1 : 0) + (s >= 24 ? 1 : 0) + gu_dma(s); }
__host__ __device__ constexpr int gu_younger(int k) {
    int c = 0, s = k - GU_AW + 1;
    if (s < 0) { c = GU_AW - 2 - k; s = 0; } else { c = gu_ops(s) - 1; ++s; }
    for (; s <= k; ++s) c += gu_ops(s);
    return c;
}

struct GU { u32x16 r0, r1, st; u32x8 w0, w1, w2; f32x16 a0, a1, a2; unsigned wa[3], sa, s0, sv, ss, l16, budget, dm, ds, dv; rsrc_t rs, rd; };
template <int i> __device__ __forceinline__ void gu_steps(GU& g) {
    constexpr int AW = GU_AW, j = i + AW - 1, n = i + 1;
    unsigned tq;
#define GU_RD(R, B) "ds_read_b64_tr_b16 v[" R ":" R "+1], %[" B "] offset:%c[no]\n\tds_read_b64_tr_b16 v[" R "+2:" R "+3], %[" B "] offset:%c[no]+768\n\t"
    if constexpr (i == 0)
        asm volatile(".set tagj, 0\n\t.rept 7\n\tbuffer_load_dwordx4 v[140+4*tagj:143+4*tagj], %[l16], %[rs], %[sa] offen offset:(tagj&3)*1024 sc1\n\t"
                     ".if (tagj&3) == 3\n\ts_add_u32 %[sa], %[sa], 0x1000\n\t.endif\n\t.set tagj, tagj+1\n\t.endr\n\t" GU_RD("112", "b0") GU_RD("116", "b1") GU_RD("124", "b2")
                     : "={v[140:155]}"(g.r0), "={v[156:171]}"(g.r1), "={v[172:187]}"(g.st), "={v[112:119]}"(g.w0), "={v[124:131]}"(g.w1), "={v[132:139]}"(g.w2), [sa] "+s"(g.sa),
                       "+{v[64:79]}"(g.a0), "+{v[80:95]}"(g.a1), "+{v[96:111]}"(g.a2)
                     : [l16] "v"(g.l16), [rs] "s"(g.rs), [b0] "v"(g.wa[0]), [b1] "v"(g.wa[1]), [b2] "v"(g.wa[2]), [no] "n"(0) : "scc", "memory");
    asm volatile(".if %c[ld]\n\t" TAG_LOAD ".endif\n\t"
                 ".if %c[sk] >= 0\n\tbuffer_load_dwordx2 v[172+2*%c[sk]:173+2*%c[sk]], %[sv], %[rs], %[ss] offen offset:%c[sk]*256 sc1\n\t.endif\n\t"
                 ".if %c[i] == 16\n\ts_waitcnt lgkmcnt(0)\n\ts_barrier\n\t.endif\n\t"
                 ".if %c[dma]\n\ts_mov_b32 m0, %[dm]\n\ts_add_u32 %[dm], %[dm], 0x400\n\tbuffer_load_dwordx4 %[dv], %[rwd], %[ds] offen lds\n\ts_add_u32 %[ds], %[ds], 0x2000\n\t.endif\n\t"
                 ".if %c[rd]\n\t" GU_RD("%c[n0]", "b0") GU_RD("%c[n1]", "b1") GU_RD("%c[n2]", "b2") ".endif\n\t"
                 "s_waitcnt vmcnt(%c[vn]) lgkmcnt(%c[ln])\n\t" TAG_CHECK(140, 8)
                 "v_mfma_f32_32x32x16_bf16 v[64:79], v[%c[c0]:%c[c0]+3], v[%c[ar]:%c[ar]+3], v[64:79]\n\t"
                 "v_mfma_f32_32x32x16_bf16 v[80:95], v[%c[c1]:%c[c1]+3], v[%c[ar]:%c[ar]+3], v[80:95]\n\t"
                 "v_mfma_f32_32x32x16_bf16 v[96:111], v[%c[c2]:%c[c2]+3], v[%c[ar]:%c[ar]+3], v[96:111]\n\t"
                 ".if %c[cnt] == 1\n\ts_waitcnt vmcnt(0)\n\ts_nop 15\n\t.endif"
                 : "+{v[64:79]}"(g.a0), "+{v[80:95]}"(g.a1), "+{v[96:111]}"(g.a2), "+{v[140:155]}"(g.r0), "+{v[156:171]}"(g.r1), "+{v[172:187]}"(g.st),
                   "+{v[112:119]}"(g.w0), "+{v[124:131]}"(g.w1), "+{v[132:139]}"(g.w2), [sa] "+s"(g.sa), [bud] "+s"(g.budget), [t] "=&s"(tq), [dm] "+s"(g.dm), [ds] "+s"(g.ds)
                 : [l16] "v"(g.l16), [rs] "s"(g.rs), [s0] "s"(g.s0), [dv] "v"(g.dv), [rwd] "s"(g.rd), [dma] "n"(gu_dma(i)), [sv] "v"(g.sv), [ss] "s"(g.ss), [b0] "v"(g.wa[0]), [b1] "v"(g.wa[1]), [b2] "v"(g.wa[2]),
                   [ld] "n"(j < 32 ? 1 : 0), [lr] "n"(140 + 4 * (j % AW)), [lo] "n"((j & 3) * 1024), [sk] "n"(i >= 24 ? i - 24 : -1), [rd] "n"(n < 32 ? 1 : 0), [no] "n"(n * 1536),
                   [n0] "n"(n & 1 ? 128 : 112), [n1] "n"(n & 1 ? 132 : 116), [n2] "n"(n & 1 ? 136 : 124), [c0] "n"(i & 1 ? 128 : 112), [c1] "n"(i & 1 ? 132 : 116), [c2] "n"(i & 1 ? 136 : 124),
                   [vn] "n"(gu_younger(i)), [ln] "n"(n < 32 ? 6 : 0), [ar] "n"(140 + 4 * (i % AW)), [i] "n"(i), [cnt] "n"(j < 32 ? AW : 32 - i) : "vcc", "scc", "m0", "v120", "memory");
    if constexpr (i + 1 < 32) gu_steps<i + 1>(g);
}

__global__ __launch_bounds__(512, 2) void k_block(const u16* __restrict__ x, const u16* __restrict__ wqkv, const u16* __restrict__ wo, const u16* __restrict__ wg,
                                                  const u16* __restrict__ wu, const u16* __restrict__ wd, u16* __restrict__ out, char* __restrict__ ws) {
    extern __shared__ __attribute__((aligned(16))) char smem[];
    const unsigned lds0 = (unsigned)(uintptr_t)(LDSAS char*)smem;
    const int tid = threadIdx.x, lane = tid & 63;
    const int w = __builtin_amdgcn_readfirstlane(tid >> 6);
    const int b = blockIdx.x, xcd = b & 7, bi = b >> 3;
    const int r32 = lane & 31, h = lane >> 5;
    const rsrc_t rs = mk_rsrc(ws, WS_END);
    const int wt = w & 1, kh = (w >> 1) & 1;
    STAMP_DECL;
    unsigned ep;
    {
    const int rt = (xcd >> 1) * 8 + (bi & 7), cb = (xcd & 1) * 4 + (bi >> 3), rot = bi & 3;
    const int wc = (w >> 1) & 1, hw = w - 4;
    STAMP(0, 0);
    const rsrc_t rw = mk_rsrc(wqkv, D * 3 * D * 2);
    const unsigned wv = (lane >> 3) * 3072 + (((lane & 7) ^ (((lane >> 4) & 1) << 2)) << 4);
    const unsigned fa = lds0 + K1_FLG;
    if (tid < 12) *(LDSAS unsigned*)(fa + tid * 4) = 0u;
    if (w >= 4) {
        BAR_L();
        const unsigned pa = fa + hw * 4;
        auto issue = [&](int s) {
            const unsigned so = RFL(((((s & 3) + rot) & 3) * 128 + hw * 32) * 3072 + (s >> 2) * 1024 + cb * 128);
            asm volatile("s_nop 4" :: "s"(so));
#pragma unroll
            for (int i = 0; i < 4; ++i)
                asm volatile("s_mov_b32 m0, %0\n\ts_nop 0\n\tbuffer_load_dwordx4 %1, %2, %3 offen lds"
                             :: "s"(lds0 + k1_slot(s) + (4 * hw + i) * 1024), "v"(wv), "s"(rw), "s"(so + i * 8 * 3072) : "m0", "memory");
        };
#define K1_LAND(N, s) asm volatile("s_waitcnt vmcnt(" #N ")\n\tds_write_b32 %0, %1" :: "v"(pa), "v"((s) + 1u) : "memory")
        issue(0); issue(1);
        issue(2); K1_LAND(8, 0);
        issue(3); K1_LAND(8, 1);
        issue(4); K1_LAND(8, 2); K1_LAND(4, 3); K1_LAND(0, 4);
        lds_spin_ge(fa + 28, 4);
        issue(5);
        lds_spin_ge(fa + 20, 4);
        issue(6); issue(7); K1_LAND(8, 5);
        issue(8); K1_LAND(8, 6);
        issue(9); K1_LAND(8, 7);
        lds_spin_ge(fa + 24, 4);
        issue(10); K1_LAND(8, 8);
        issue(11); K1_LAND(8, 9);
        K1_LAND(4, 10); K1_LAND(0, 11);
        if (w == 4) { unsigned e; asm volatile("s_load_dword %0, %1, 0\n\ts_waitcnt lgkmcnt(0)" : "=s"(e) : "s"(ws) : "memory"); *(LDSAS unsigned*)(fa + 48) = e + 1u; }
        else {
            unsigned long long pc; unsigned len, o = bi * 128 + (w - 5) * 16384, t, d0, d1, d2, d3;
#define KCP(D) "s_min_u32 %[t], %[o], %[len]\n\ts_load_dword " D ", %[pc], %[t]\n\ts_add_u32 %[o], %[o], 0x1000\n\t"
            asm volatile("s_getpc_b64 %[pc]\n.Lqkv_pc:\n\ts_mov_b32 %[len], .Lmain_end-.Lqkv_pc-4\n\t" KCP("%[d0]") KCP("%[d1]") KCP("%[d2]") KCP("%[d3]") "s_waitcnt lgkmcnt(0)"
                         : [pc] "=&s"(pc), [len] "=&s"(len), [o] "+s"(o), [t] "=&s"(t), [d0] "=&s"(d0), [d1] "=&s"(d1), [d2] "=&s"(d2), [d3] "=&s"(d3) :: "scc", "memory");
        }
        BAR_L();
        ep = RFL(*(const LDSAS unsigned*)(fa + 48));
        if (w == 4) { __builtin_amdgcn_s_sleep(POLL_DELAY_Q); wait_flags(rs, WS_FQ + (xcd * 2 + (bi >> 4)) * 8192, 64, ep, lane); }
    } else {
    {
        const rsrc_t rx = mk_rsrc(x, S * D * 2);
        u32x4 r[2][8];
#pragma unroll
        for (int p = 0; p < 2; ++p)
#pragma unroll
            for (int g = 0; g < 8; ++g)
                r[p][g] = __builtin_amdgcn_raw_buffer_load_b128(rx, (lane >> 3) * 1024 + (((lane & 7) ^ ((4 * p + (lane >> 4)) & 7)) << 4) + g * 128, (rt * 64 + w * 16 + p * 8) * 1024, K1_X_AUX);
        SBAR(); BAR_L(); SBAR();
#pragma unroll
        for (int p = 0; p < 2; ++p) {
            float s0 = 0.f, s1 = 0.f, q0 = 0.f, q1 = 0.f;
#pragma unroll
            for (int g = 0; g < 8; ++g) { s0 = dot2(r[p][g][0], ONES2, s0); q0 = dot2(r[p][g][0], r[p][g][0], q0); s1 = dot2(r[p][g][1], ONES2, s1); q1 = dot2(r[p][g][1], r[p][g][1], q1);
                                          s0 = dot2(r[p][g][2], ONES2, s0); q0 = dot2(r[p][g][2], r[p][g][2], q0); s1 = dot2(r[p][g][3], ONES2, s1); q1 = dot2(r[p][g][3], r[p][g][3], q1); }
            if (p == 0) STAMP(0, 1);
            float sum = s0 + s1, sq = q0 + q1;
            sum = dpp_add<0xB1>(sum); sq = dpp_add<0xB1>(sq); sum = dpp_add<0x4E>(sum); sq = dpp_add<0x4E>(sq); sum = dpp_add<0x141>(sum); sq = dpp_add<0x141>(sq);
            const float mean = sum * (1.f / D);
            const float rstd = __builtin_amdgcn_rsqf(__builtin_fmaxf(sq * (1.f / D) - mean * mean, 0.f) + LN_EPS), nb = -mean * rstd;
#pragma unroll
            for (int g = 0; g < 8; g += 2) {
                f32x2 t[8];
#pragma unroll
                for (int i = 0; i < 8; ++i) { const unsigned v = r[p][g + (i >> 2)][i & 3]; t[i] = f32x2{bflo(v), bfhi(v)}; }
                asm volatile("" : "+v"(t[0]), "+v"(t[1]), "+v"(t[2]), "+v"(t[3]), "+v"(t[4]), "+v"(t[5]), "+v"(t[6]), "+v"(t[7]));
#pragma unroll
                for (int i = 0; i < 8; ++i) t[i] = t[i] * rstd + nb;
                asm volatile("" : "+v"(t[0]), "+v"(t[1]), "+v"(t[2]), "+v"(t[3]), "+v"(t[4]), "+v"(t[5]), "+v"(t[6]), "+v"(t[7]));
                u32x4 y0, y1;
#pragma unroll
                for (int i = 0; i < 4; ++i) { y0[i] = pk2(t[i][0], t[i][1]); y1[i] = pk2(t[4 + i][0], t[4 + i][1]); }
                *(LDSAS u32x4*)(lds0 + (2 * w + p) * 1024 + lane * 16 + g * 8192) = y0;
                *(LDSAS u32x4*)(lds0 + (2 * w + p) * 1024 + lane * 16 + g * 8192 + 8192) = y1;
            }
            SBAR();
        }
    }
    lds_inc(fa + 16, lane);
    STAMP(0, 3);
    u32x16 xa0, xa1, xa2, xa3, xa4, xa5, xa6, xa7;
    u32x4 w0, w1, w2, w3, w4, fl;
    f32x16 acA, acB;
    unsigned xb[4], tq;
#pragma unroll
    for (int m = 0; m < 4; ++m) xb[m] = lds0 + (32 * wt + r32) * 128 + (((2 * m + h) ^ ((r32 >> 1) & 7)) << 4) + rot * 16384;
    const int q4 = (lane & 15) >> 2, p4 = lane & 3, blk = (lane >> 4) & 1;
    const unsigned tA = lds0 + (8 * h + q4) * 128 + (((4 * wc + 2 * blk + (p4 >> 1)) ^ (((q4 >> 1) & 1) << 2)) << 4) + 8 * (p4 & 1);
    const unsigned tB = tA + 65536, tC = tA + 131072;
    const int tt = rt * 2 + wt, hd = cb * 4 + wc * 2;
#define KXR(R, m, O) "ds_read_b128 v[" #R ":" #R "+3], %[x" #m "] offset:" #O "\n\t"
#define KXN(m) "v_add_u32 %[x" #m "], 0x4000, %[x" #m "]\n\tv_and_b32 %[x" #m "], 0xffff, %[x" #m "]\n\t"
#define KCHK(ST) "v_min3_u32 v116, v116, v117, v118\n\tv_min_u32 v116, v116, v119\n\ts_nop 0\n\tv_readfirstlane_b32 %[t], v116\n\ts_nop 0\n\ts_cmp_gt_u32 %[t], " ST "\n\t"
#define KSPIN(ST) "1:\n\tds_read_b128 v[116:119], %[fa]\n\ts_waitcnt lgkmcnt(0)\n\t" KCHK(ST) "s_cbranch_scc0 1b\n\t"
#define KWR(R, O) "ds_read_b64_tr_b16 v[" #R ":" #R "+1], %[tb] offset:" #O "\n\tds_read_b64_tr_b16 v[" #R "+2:" #R "+3], %[tb] offset:" #O "+512\n\t"
    asm volatile("3:\n\tds_read_b32 v116, %[fa] offset:16\n\ts_waitcnt lgkmcnt(0)\n\tv_readfirstlane_b32 %[t], v116\n\ts_nop 0\n\ts_cmp_gt_u32 %[t], 3\n\ts_cbranch_scc0 3b\n\t"
                 KXR(128, 0, 0) KXR(132, 1, 0) KXR(136, 2, 0) KXR(140, 3, 0) KXR(144, 0, 8192) KXR(148, 1, 8192) KXR(152, 2, 8192) KXR(156, 3, 8192) KXN(0) KXN(1) KXN(2) KXN(3)
                 KSPIN("0") KWR(96, 0) KWR(100, 2048) KWR(104, 4096) KWR(108, 6144)
                 : "={v[116:119]}"(fl), [t] "=&s"(tq), "={v[96:99]}"(w0), "={v[100:103]}"(w1), "={v[104:107]}"(w2), "={v[108:111]}"(w3), "={v[112:115]}"(w4), "={v[64:79]}"(acA), "={v[80:95]}"(acB),
                   "={v[128:143]}"(xa0), "={v[144:159]}"(xa1), "={v[160:175]}"(xa2), "={v[176:191]}"(xa3), "={v[192:207]}"(xa4), "={v[208:223]}"(xa5), "={v[224:239]}"(xa6), "={v[240:255]}"(xa7),
                   [x0] "+v"(xb[0]), [x1] "+v"(xb[1]), [x2] "+v"(xb[2]), [x3] "+v"(xb[3])
                 : [fa] "v"(fa), [tb] "v"(tB) : "scc", "memory");
    STAMP(0, 4);
    auto epi = [&](int m, const f32x16& acc, int hh) {
        const float sc = 0.25f * LOG2E;
        u32x4 o;
#pragma unroll
        for (int i = 0; i < 4; ++i) o[i] = m == 0 ? pk2(acc[8 * hh + 2 * i] * sc, acc[8 * hh + 2 * i + 1] * sc) : pk2(acc[8 * hh + 2 * i], acc[8 * hh + 2 * i + 1]);
        if (m == 0) __builtin_amdgcn_raw_buffer_store_b128(o, rs, lane * 16, WS_Q + ((hd + hh) * 64 + tt) * 1024, K1_ST_AUX);
        else if (m == 1) __builtin_amdgcn_raw_buffer_store_b128(o, rs, lane * 16, WS_KV + (hd + hh) * KV_HEAD + (tt >> 3) * KV_CHUNK + (tt & 7) * 1024, K1_ST_AUX);
        else __builtin_amdgcn_raw_buffer_store_b128(o, rs, (r32 >> 4) * KV_HEAD + (16 * h + (lane & 15)) * 16 + hh * 512,
                                                    WS_KV + hd * KV_HEAD + (tt >> 3) * KV_CHUNK + 8192 + (tt & 7) * 1024, K1_ST_AUX);
    };
    sfor<0, 96>([&](auto G) {
        constexpr int g = G.value, n = g + 4, m = g >> 5;
        constexpr unsigned o = k1_slot(n >> 3) + (n & 7) * 2048, oi = o & 65535;
        constexpr int aR = m == 1 ? 80 : 64, wR = 96 + 4 * (g % 5), nR = 96 + 4 * (n % 5), xR = 128 + 4 * (g & 31);
        constexpr int nx = (g - 4 >= 0 && g - 4 < 12) + (g - 3 >= 0 && g - 3 < 12) + (g - 2 >= 0 && g - 2 < 12) + (g - 1 >= 0 && g - 1 < 12), wc_ = n >= 96 ? 0 : (8 + 2 * nx > 14 ? 14 : 8 + 2 * nx);
#define KRD(B) "ds_read_b64_tr_b16 v[%c[nr]:%c[nr]+1], %[" B "] offset:%c[oi]\n\tds_read_b64_tr_b16 v[%c[nr]+2:%c[nr]+3], %[" B "] offset:%c[oi]+512\n\t"
        asm volatile(".if %c[bar] == 1\n\t" KCHK("%c[st]") "s_cbranch_scc1 2f\n\t" KSPIN("%c[st]") "2:\n\t.endif\n\t"
                     ".if %c[bar] == 2\n\tds_read_b128 v[116:119], %[fa]\n\t.endif\n\t"
                     ".if %c[rd] == 1\n\t" KRD("tb") ".endif\n\t.if %c[rd] == 2\n\t" KRD("tc") ".endif\n\t.if %c[rd] == 3\n\t" KRD("ta") ".endif\n\t"
                     "s_waitcnt lgkmcnt(%c[wn])\n\t"
                     ".if %c[md] == 0\n\tv_mfma_f32_32x32x16_bf16 v[%c[ar]:%c[ar]+15], v[%c[wr]:%c[wr]+3], v[%c[xr]:%c[xr]+3], v[%c[ar]:%c[ar]+15]\n\t.endif\n\t"
                     ".if %c[md] == 1\n\tv_mfma_f32_32x32x16_bf16 v[%c[ar]:%c[ar]+15], v[%c[xr]:%c[xr]+3], v[%c[wr]:%c[wr]+3], v[%c[ar]:%c[ar]+15]\n\t.endif\n\t"
                     ".if %c[md] == 2\n\tv_mfma_f32_32x32x16_bf16 v[%c[ar]:%c[ar]+15], v[%c[xr]:%c[xr]+3], v[%c[wr]:%c[wr]+3], 0\n\t.endif\n\t"
                     ".if %c[md] == 3\n\tv_mfma_f32_32x32x16_bf16 v[%c[ar]:%c[ar]+15], v[%c[wr]:%c[wr]+3], v[%c[xr]:%c[xr]+3], 0\n\t.endif\n\t"
                     ".if %c[xm] == 1\n\tds_read_b128 v[%c[xd]:%c[xd]+3], %[x0] offset:%c[xo]\n\tds_read_b128 v[%c[xd]+4:%c[xd]+7], %[x1] offset:%c[xo]\n\t.endif\n\t"
                     ".if %c[xm] == 2\n\tds_read_b128 v[%c[xd]:%c[xd]+3], %[x2] offset:%c[xo]\n\tds_read_b128 v[%c[xd]+4:%c[xd]+7], %[x3] offset:%c[xo]\n\t.endif\n\t"
                     ".if %c[xn]\n\t" KXN(0) KXN(1) KXN(2) KXN(3) ".endif"
                     : "+{v[64:79]}"(acA), "+{v[80:95]}"(acB), "+{v[96:99]}"(w0), "+{v[100:103]}"(w1), "+{v[104:107]}"(w2), "+{v[108:111]}"(w3), "+{v[112:115]}"(w4),
                       "+{v[128:143]}"(xa0), "+{v[144:159]}"(xa1), "+{v[160:175]}"(xa2), "+{v[176:191]}"(xa3), "+{v[192:207]}"(xa4), "+{v[208:223]}"(xa5), "+{v[224:239]}"(xa6), "+{v[240:255]}"(xa7),
                       "+{v[116:119]}"(fl), [t] "=&s"(tq), [x0] "+v"(xb[0]), [x1] "+v"(xb[1]), [x2] "+v"(xb[2]), [x3] "+v"(xb[3])
                     : [ta] "v"(tA), [tb] "v"(tB), [tc] "v"(tC), [fa] "v"(fa), [st] "n"(n >> 3), [wn] "n"(wc_),
                       [bar] "n"(K1_ABL ? 0 : n < 96 && (n & 7) == 0 ? 1 : n < 92 && (n & 7) == 4 ? 2 : 0), [rd] "n"(n >= 96 ? 0 : o < 65536 ? 3 : o < 131072 ? 1 : 2), [oi] "n"(oi), [nr] "n"(nR),
                       [md] "n"(m < 2 ? ((g & 31) == 0 ? 3 : 0) : g == 64 ? 2 : 1), [ar] "n"(aR), [wr] "n"(wR), [xr] "n"(xR),
                       [xm] "n"(g < 12 ? 1 + (g & 1) : 0), [xd] "n"(160 + 8 * g), [xo] "n"(((g >> 1) & 1) * 8192), [xn] "n"(g < 11 && (g & 3) == 3 ? 1 : 0) : "scc", "memory");
        if (g == 7 && lane == 0) asm volatile("ds_add_u32 %0, %1" :: "v"(fa + 28), "v"(1u) : "memory");
        if (g == 16 && lane == 0) asm volatile("ds_add_u32 %0, %1" :: "v"(fa + 20), "v"(1u) : "memory");
        if (g == 23 && lane == 0) asm volatile("ds_add_u32 %0, %1" :: "v"(fa + 24), "v"(1u) : "memory");
        if (g == 36 || g == 38) epi(0, acA, (g - 36) >> 1);
        if (g == 68 || g == 70) epi(1, acB, (g - 68) >> 1);
    });
    STAMP(0, 2);
    asm volatile("s_nop 15" : "+{v[64:79]}"(acA));
    epi(2, acA, 0); epi(2, acA, 1);
    SBAR(); BAR_L();
    ep = RFL(*(const LDSAS unsigned*)(fa + 48));
    WAITV(0);
    if (lane == 0) __builtin_amdgcn_raw_buffer_store_b32(ep, rs, 0, WS_FQ + ((cb * 2 + wc) * 64 + tt) * 128, AUX_SC1);
    STAMP(0, 5);
    FLUSH(0);
    }
    }

    const int rt = xcd * 4 + (bi >> 3), ct = bi & 7;
    u32x4 hreg[2];
    u32x8 xres;

    auto issue_wo = [&](int first, int n) {
        const rsrc_t rwo = mk_rsrc(wo, D * D * 2);
        const unsigned v = w64_voff(lane);
        for (int p = first; p < first + n; ++p) dma16(rwo, v, p * 8192 + ct * 128, lds0 + L_WO + p * 1024);
    };
    auto issue_dn = [&](int i0, int n, unsigned dst, int hw) {
        const rsrc_t rwd = mk_rsrc(wd, F * D * 2);
        const unsigned v = w64_voff(lane);
        const unsigned so = ((43 * (hw >> 1) + i0) * 16 + (hw & 1) * 8) * 1024 + ct * 128;
        for (int i = 0; i < n; ++i) dma16(rwd, v, so + i * 16384, lds0 + dst + (hw & 1) * 1024 + i * 2048);
    };
    auto gu_wa = [&](int n) -> unsigned { const unsigned blk = (lane >> 4) & 1;
        return lds0 + L_GU + blk * 49152 + (((4 * h + ((lane & 15) >> 2)) * 96 + n * 32 + (lane & 3) * 8) ^ (blk * 128)); };
    float cs[3] = {0.f, 0.f, 0.f};
    auto gu_colsum = [&]() {
        u32x4 f[3][4];
#pragma unroll
        for (int n = 0; n < 3; ++n) {
            const unsigned a0 = gu_wa(n) + w * 4 * 1536;
#pragma unroll
            for (int i = 0; i < 4; ++i) f[n][i] = __builtin_bit_cast(u32x4, lds_tr2(a0 + i * 1536, a0 + i * 1536 + 768));
        }
        SBAR();
#pragma unroll
        for (int i = 0; i < 4; ++i)
#pragma unroll
            for (int e = 0; e < 4; ++e)
#pragma unroll
                for (int n = 0; n < 3; ++n) cs[n] = dot2(f[n][i][e], ONES2, cs[n]);
#pragma unroll
        for (int n = 0; n < 3; ++n) cs[n] = swap_add(cs[n]);
    };

    unsigned budget = 1u << 16;
    const unsigned l16 = lane * 16;

    {
        const int head = xcd * 4 + (bi >> 3), qb = bi & 7, wq = w & 3, g = w >> 2, qt = qb * 8 + wq * 2;
        const unsigned kvs = WS_KV + head * KV_HEAD;
        unsigned ks = kvs + g * 4 * KV_CHUNK + wq * 4096, km = lds0 + g * 32768 + wq * 4096;
        const rsrc_t rg = mk_rsrc(g ? wu : wg, (bi < 29 && ABL_GU != 1) ? D * F * 2 : 0), rwd = mk_rsrc(wd, F * D * 2), rwo = mk_rsrc(wo, D * D * 2), rx = mk_rsrc(x, S * D * 2);
        unsigned gv0, gv1, gv2;
        { unsigned c = lane ^ (g * 8); gv0 = (c / 6) * (F * 2) + (c % 6) * 16; c = (64 + lane) ^ (g * 8); gv1 = (c / 6) * (F * 2) + (c % 6) * 16;
          c = (128 + lane) ^ (g * 8); gv2 = (c / 6) * (F * 2) + (c % 6) * 16; }
        if (ABL_GU == 2) { gv0 = l16; gv1 = l16 + 1024; gv2 = l16 + 2048; }
        unsigned gs = ABL_GU == 2 ? wq * 4 * 32 * (F * 2) + bi * 3072 : wq * 4 * 32 * (F * 2) + bi * 96, gm = lds0 + L_GU + g * 49152 + wq * 12288;
        const unsigned pvd = lane < 43 ? (bi * 344 + w * 43 + lane) * 128 : 0x80000000u, pvo = lane < 16 ? (bi * 128 + w * 16 + lane) * 128 : 0x80000000u;
        const unsigned wv = w64_voff(lane), xv = (rt * 64 + wt * 32 + r32) * 1024 + (ct * 64 + kh * 32 + 4 * h) * 2;
        const unsigned kb = lds0 + g * 32768 + l16, vb = lds0 + g * 32768 + 8192 + (16 * h + (r32 & 15)) * 16;
        const unsigned cst = (r32 & 27) == 16 ? ONES2 : 0u;
        const unsigned long long msk = 0x0000ffff0000ffffull;
        const unsigned fo = WS_O + ((qt + g) * 32 + head) * 1024, fh = WS_H + ((rt * 2 + wt) * 32 + ct * 4 + (w >> 1)) * 1024, fg = WS_HH + ((xcd * 8 + w) * NU + bi * 3) * 1024;
        const unsigned fsv = w == 0 ? WS_ST + ((rt * 2 + h) * 16 + ct * 2) * 256 + r32 * 16 : 0x80000000u, fg2 = bi < 28 ? l16 : 0x80000000u, fg0 = bi < 29 ? l16 : 0x80000000u;
        const unsigned fpv = tid == 0 ? WS_FP + b * 4 : 0x80000000u;
        u32x4 ones = {~0u, ~0u, ~0u, ~0u}, fpl = {0u, 0u, 0u, 0u};
        unsigned pfd = 0, epv = ep;
        xres = u32x8{};
        s16x8 q0, q1;
        STAMP(1, 0);
        SBAR();
#define XKV "s_mov_b32 m0, %[km]\n\ts_add_u32 %[km], %[km], 0x400\n\tbuffer_load_dwordx4 %[l16], %[rs], %[ks] offen sc1 lds\n\ts_add_u32 %[ks], %[ks], 0x400\n\t"
        asm volatile("s_barrier\n\tbuffer_load_dwordx4 v[128:131], %[l16], %[rs], %[qs] offen sc1\n\tbuffer_load_dwordx4 v[156:159], %[l16], %[rs], %[qs] offen offset:1024 sc1\n\t"
                     XKV XKV XKV XKV "s_add_u32 %[km], %[km], 0x3000\n\ts_add_u32 %[ks], %[ks], 0x3000\n\ts_waitcnt vmcnt(0)\n\ts_barrier\n\t" XKV XKV XKV XKV
                     : "={v[128:131]}"(q0), "={v[156:159]}"(q1), [km] "+s"(km), [ks] "+s"(ks)
                     : [l16] "v"(l16), [rs] "s"(rs), [qs] "s"(WS_Q + (head * 64 + qt) * 1024) : "m0", "scc", "memory");
        km -= 16384 + 4096; ks += KV_CHUNK - 4096;
        STAMP(1, 1);
        f32x16 n0, n1, sA, sB = {}, o0 = {}, o1 = {};
        s16x8 kf = lds_b128(kb), v0, v1, v1b;
        {
            const f32x16 z = {};
            const s16x8 kz = lds_b128(lds0 + l16);
            const u32x4 vv = __builtin_bit_cast(u32x4, lds_b128(vb)), cc = {cst, cst, cst, cst};
            const f32x16 s0 = MFMA(kz, q0, z), s1 = MFMA(kz, q1, z);
            float m0 = __builtin_fmaxf(s0[0], s0[1]), m1 = __builtin_fmaxf(s1[0], s1[1]);
#pragma unroll
            for (int i = 2; i < 16; i += 2) { m0 = __builtin_fmaxf(__builtin_fmaxf(m0, s0[i]), s0[i + 1]); m1 = __builtin_fmaxf(__builtin_fmaxf(m1, s1[i]), s1[i + 1]); }
            m0 = swap_max(m0); m1 = swap_max(m1);
#pragma unroll
            for (int i = 0; i < 16; ++i) { n0[i] = -m0; n1[i] = -m1; }
            sA = MFMA(kf, q0, n0);
            v0 = __builtin_bit_cast(s16x8, r32 < 16 ? vv : cc); v1 = v1b = __builtin_bit_cast(s16x8, cc);
        }
        u32x4 pA = {0u, 0u, 0u, 0u}, pB = {0u, 0u, 0u, 0u};
#define XE(C, i) "v_exp_f32 v[" #C "+" #i "], v[" #C "+" #i "]\n\t"
#define XC(P, C, i) "v_cvt_pk_bf16_f32 v" #P ", v[" #C "+" #i "], v[" #C "+" #i "+1]\n\t"
#define XG1(C) XE(C, 0) XE(C, 1) XE(C, 2) XE(C, 3) XC(148, C, 0) XE(C, 4) XE(C, 5) XC(149, C, 2)
#define XG2(C) XE(C, 6) XE(C, 7) XC(150, C, 4) XE(C, 8) XC(151, C, 6) XE(C, 9) XE(C, 10)
#define XG3(C) XE(C, 11) XC(152, C, 8) XE(C, 12) XE(C, 13) XC(153, C, 10) XE(C, 14) XE(C, 15) XC(154, C, 12) XC(155, C, 14)
#define XM(D, A, B, C) "v_mfma_f32_32x32x16_bf16 v[" #D ":" #D "+15], v[" #A ":" #A "+3], v[" #B ":" #B "+3], v[" #C ":" #C "+15]\n\t"
#define XRK(BASE, O) "ds_read_b128 v[132:135], " BASE " offset:" #O "\n\t"
#define XRV(R, BASE, O) "s_mov_b64 exec, %[msk]\n\tds_read_b128 v[" #R ":" #R "+3], " BASE " offset:" #O "\n\ts_mov_b64 exec, -1\n\t"
#define XW(n) "s_waitcnt lgkmcnt(" #n ")\n\t"
#define XTILE(VA, RK, RV1, WA, X1, VB, X2, RV0) \
        XM(80, 132, 156, 176) RK XG1(64) XM(160, VA, 152, 160) RV1 XG2(64) XW(WA) XM(96, 140, 148, 96) XG3(64) \
        XW(0) XM(64, 132, 128, 112) X1 XG1(80) XM(96, VB, 152, 96) X2 XG2(80) XM(160, 140, 148, 160) RV0 XG3(80)
#define XGU(j) "s_mov_b32 m0, %[gm]\n\ts_add_u32 %[gm], %[gm], 0x400\n\tbuffer_load_dwordx4 %[gv" #j "], %[rg], %[gs] offen lds\n\t"
#define XGU2 XGU(2) "s_add_u32 %[gs], %[gs], 88064\n\t"
#define XPF "buffer_load_dword v200, %[pvd], %[rwd], 0 offen\n\tbuffer_load_dword v200, %[pvo], %[rwo], 0 offen\n\t"
#define XWO "s_mov_b32 m0, %[km]\n\ts_add_u32 %[km], %[km], 0x400\n\tbuffer_load_dwordx4 %[wv], %[rwo], %[ks] offen lds\n\ts_add_u32 %[ks], %[ks], 0x2000\n\t"
#define XST(V, SO, O) "buffer_store_dwordx4 v[204:207], " V ", %[rs], " SO " offen offset:" #O " sc1\n\t"
#define XFP "buffer_store_dword v201, %[fpv], %[rs], 0 offen sc1\n\t"
#define XFL "buffer_load_dwordx4 v[208:211], %[l16], %[rs], %[fp] offen sc1\n\t"
#define XXR(R, O) "buffer_load_dwordx2 v[" #R ":" #R "+1], %[xv], %[rx], 0 offen offset:" #O "\n\t"
#define AOPS : "+{v[64:79]}"(sA), "+{v[80:95]}"(sB), "+{v[96:111]}"(o0), "+{v[160:175]}"(o1), "+{v[112:127]}"(n0), "+{v[176:191]}"(n1), "+{v[128:131]}"(q0), "+{v[156:159]}"(q1), \
               "+{v[132:135]}"(kf), "+{v[140:143]}"(v0), "+{v[144:147]}"(v1), "+{v[136:139]}"(v1b), "+{v[148:151]}"(pA), "+{v[152:155]}"(pB), "+{v[192:199]}"(xres), "+{v200}"(pfd), \
               "+{v201}"(epv), "+{v[204:207]}"(ones), "+{v[208:211]}"(fpl), \
               [km] "+s"(km), [ks] "+s"(ks), [gm] "+s"(gm), [gs] "+s"(gs) \
             : [ka] "v"(ka), [va] "v"(va), [kn] "v"(kn), [vn] "v"(vn), [msk] "s"(msk), [l16] "v"(l16), [rs] "s"(rs), [rg] "s"(rg), [gv0] "v"(gv0), [gv1] "v"(gv1), [gv2] "v"(gv2), \
               [pvd] "v"(pvd), [pvo] "v"(pvo), [wv] "v"(wv), [xv] "v"(xv), [rwd] "s"(rwd), [rwo] "s"(rwo), [rx] "s"(rx), \
               [fo] "s"(fo), [fh] "s"(fh), [fg] "s"(fg), [fsv] "v"(fsv), [fg0] "v"(fg0), [fg2] "v"(fg2), [fpv] "v"(fpv), [fp] "s"(WS_FP) : "m0", "scc", "memory"
#define XBAR(n) asm volatile("s_waitcnt vmcnt(" #n ") lgkmcnt(0)\n\ts_barrier" AOPS)
#define XROUND(A1, A2, B1, B2, C1, C2, D1, D2, E1, E2, F1, F2) asm volatile( \
            XTILE(136, XRK("%[ka]", 1024), XRV(144, "%[va]", 512), 2, A1, 144, A2, XRV(140, "%[va]", 1024)) \
            XTILE(144, XRK("%[ka]", 2048), XRV(144, "%[va]", 1536), 2, B1, 144, B2, XRV(140, "%[va]", 2048)) \
            XTILE(144, XRK("%[ka]", 3072), XRV(144, "%[va]", 2560), 2, C1, 144, C2, XRV(140, "%[va]", 3072)) \
            XTILE(144, XRK("%[ka]", 4096), XRV(144, "%[va]", 3584), 2, D1, 144, D2, XRV(140, "%[va]", 4096)) \
            XTILE(144, XRK("%[ka]", 5120), XRV(144, "%[va]", 4608), 2, E1, 144, E2, XRV(140, "%[va]", 5120)) \
            XTILE(144, XRK("%[ka]", 6144), XRV(144, "%[va]", 5632), 2, F1, 144, F2, XRV(140, "%[va]", 6144)) \
            XTILE(144, XRK("%[ka]", 7168), XRV(144, "%[va]", 6656), 2, XRV(136, "%[va]", 7680), 144, "", XRV(140, "%[va]", 7168)) AOPS)
#define XLAST(X1, X2) asm volatile(XTILE(144, XRK("%[kn]", 0), "", 1, X1, 136, X2, XRV(140, "%[vn]", 0)) AOPS)
        unsigned ka = kb, va = vb, kn = kb + 16384, vn = vb + 16384;
        asm volatile("s_nop 15" AOPS);
        STAMP(1, 7);
        XROUND(XGU(0), "", "", XST("%[l16]", "%[fo]", 0), XGU(1), XST("%[fsv]", "0", 0), "", XST("%[l16]", "%[fh]", 0), XGU2, "", XGU(0), "");
        STAMP(1, 8);
        XBAR(7);
        STAMP(1, 9);
        XLAST(XKV, XKV);
        ka = kb + 16384; va = vb + 16384; kn = kb; vn = vb;
        XROUND(XKV, XKV, XGU(1), XST("%[fg0]", "%[fg]", 0), XGU2, "", XST("%[fg0]", "%[fg]", 1024), XGU(0), "", XGU(1), XST("%[fg2]", "%[fg]", 2048), "");
        STAMP(1, 10);
        XBAR(7);
        STAMP(1, 11);
        km += 16384 - 4096; ks += KV_CHUNK - 4096;
        XLAST(XKV, XKV);
        ka = kb; va = vb; kn = kb + 16384; vn = vb + 16384;
        XROUND(XKV, XKV, XGU2, "", XGU(0), "", XGU(1), "", XGU2, "", XPF, "");
        STAMP(1, 12);
        XBAR(6);
        STAMP(1, 13);
        km = lds0 + g * 32768 + wq * 4096; ks = (32 * g + 4 * wq) * 8192 + ct * 128;
        XLAST(XFP, XWO);
        ka = kb + 16384; va = vb + 16384; kn = kb; vn = vb;
        XROUND(XWO, XWO, XWO, "", XXR(192, 0), "", XXR(194, 16), "", XXR(196, 32), "", XXR(198, 48), "");
        XLAST(XFL, "");
        STAMP(1, 14);
        asm volatile(XM(160, 136, 152, 160) "s_waitcnt vmcnt(0) lgkmcnt(0)\n\ts_barrier\n\ts_nop 15" AOPS);
        STAMP(1, 4);
        f32x16 o;
        {
            const f32x16& snd = g ? o0 : o1;
            const unsigned xa = lds0 + g * 32768 + 16384 + wq * 3072 + l16, xb = lds0 + (g ^ 1) * 32768 + 16384 + wq * 3072 + l16;
            *(LDSAS f32x4*)(xa) = f32x4{snd[0], snd[1], snd[2], snd[3]};
            *(LDSAS f32x4*)(xa + 1024) = f32x4{snd[4], snd[5], snd[6], snd[7]};
            *(LDSAS float*)(xa + 2048) = snd[8];
            if (tid == 0) *(LDSAS unsigned*)(lds0 + L_FLAG) = 0u;
            BAR_L();
            const f32x4 p0 = lds_f4(xb), p1 = lds_f4(xb + 1024); const float pl = *(const LDSAS float*)(xb + 2048);
            o = g ? o1 : o0;
#pragma unroll
            for (int i = 0; i < 4; ++i) { o[i] += p0[i]; o[4 + i] += p1[i]; }
            o[8] += pl;
        }
        bool bad = !(o[8] < 1.8446744e19f);
#pragma unroll
        for (int i = 0; i < 8; ++i) bad |= !(__builtin_fabsf(o[i]) < 3.0e38f);
        if (__any(bad) && lane == 0) *(LDSAS unsigned*)(lds0 + L_FLAG) = 1u;
        auto store_o = [&]() {
            const float inv = __builtin_amdgcn_rcpf(o[8]);
            u32x4 ov;
#pragma unroll
            for (int i = 0; i < 4; ++i) ov[i] = pk2(o[2 * i] * inv, o[2 * i + 1] * inv);
            __builtin_amdgcn_raw_buffer_store_b128(ov, rs, l16, WS_O + ((qt + g) * 32 + head) * 1024, AUX_SC1);
        };
        BAR_L();
        if (__builtin_amdgcn_readfirstlane(*(const LDSAS unsigned*)(lds0 + L_FLAG))) {
            BAR_L();
            const s16x8 qf = g ? q1 : q0;
            const u32x4 cc = {cst, cst, cst, cst};
            const unsigned vf = lds0 + 16384 + 8192 + (16 * h + (r32 & 15)) * 16;
            float m = -__builtin_inff();
            o = f32x16{};
            const f32x16 z = {};
#pragma unroll 1
            for (int c = 0; c < 8; ++c) {
                dma16<true>(rs, l16, kvs + c * KV_CHUNK + w * 2048, lds0 + 16384 + w * 2048);
                dma16<true>(rs, l16, kvs + c * KV_CHUNK + w * 2048 + 1024, lds0 + 16384 + w * 2048 + 1024);
                BAR_VL(0);
#pragma unroll 1
                for (int t = 0; t < 8; ++t) {
                    f32x16 s = MFMA(lds_b128(lds0 + 16384 + l16 + t * 1024), qf, z);
                    float mx = s[0];
#pragma unroll
                    for (int i = 1; i < 16; ++i) mx = __builtin_fmaxf(mx, s[i]);
                    mx = __builtin_fmaxf(swap_max(mx), m);
                    const float al = __builtin_amdgcn_exp2f(m - mx);
                    m = mx;
#pragma unroll
                    for (int i = 0; i < 16; ++i) { o[i] *= al; s[i] = __builtin_amdgcn_exp2f(s[i] - m); }
#pragma unroll
                    for (int i = 0; i < 4; ++i) { pA[i] = pk2(s[2 * i], s[2 * i + 1]); pB[i] = pk2(s[8 + 2 * i], s[9 + 2 * i]); }
                    const u32x4 a = __builtin_bit_cast(u32x4, lds_b128(vf + t * 1024)), c2 = __builtin_bit_cast(u32x4, lds_b128(vf + t * 1024 + 512));
                    o = MFMA(__builtin_bit_cast(s16x8, r32 < 16 ? a : cc), __builtin_bit_cast(s16x8, pA), o);
                    o = MFMA(__builtin_bit_cast(s16x8, r32 < 16 ? c2 : cc), __builtin_bit_cast(s16x8, pB), o);
                }
                BAR_L();
            }
        }
        store_o();
        STAMP(1, 5);
        BAR_L();
        if (w >= 4) issue_wo(16 + ((w - 4) >> 1) * 32 + (w & 1) * 8, 8);
        gu_colsum();
#pragma unroll 1
        for (unsigned spins = 0; spins < SPIN_CAP && !__all(fpl[0] == ep && fpl[1] == ep && fpl[2] == ep && fpl[3] == ep); ++spins) {
            __builtin_amdgcn_s_sleep(2);
            fpl = __builtin_amdgcn_raw_buffer_load_b128(rs, l16, WS_FP, AUX_SC1);
        }
        STAMP(1, 6);
        FLUSH(1);
    }

    {
        STAMP(2, 0);
        f32x16 a0, a1;
        if (w < 4) {
            const unsigned t0 = lds0 + L_WO + kh * 32768 + w64_tr(lane, kh), t1 = lds0 + L_WO + kh * 32768 + w64_tr(lane, kh ^ 1);
            G2 g2;
            g2.tb[0] = g2.tb[2] = g2.tb[4] = t0; g2.tb[1] = g2.tb[3] = g2.tb[5] = t1;
            g2.sa = g2.s0 = RFL(WS_O + ((rt * 2 + wt) * 32 + kh * 16) * 1024); g2.l16 = l16; g2.budget = budget; g2.rs = rs; g2.a1 = f32x16{};
#pragma unroll
            for (int i = 0; i < 8; ++i) { g2.a0[2 * i] = bflo(xres[i]); g2.a0[2 * i + 1] = bfhi(xres[i]); }
            __builtin_amdgcn_s_sleep(DELAY_O);
            STAMP(2, 1);
            g2_steps<0, 16, 0, 6, -1>(g2);
            a0 = g2.a0; a1 = g2.a1; budget = g2.budget;
        } else BAR_VL(0);
        STAMP(2, 2);
        BAR_L();
        if (w < 4) {
#pragma unroll
            for (int g = 0; g < 4; ++g) *(LDSAS f32x4*)(lds0 + w * 4096 + g * 1024 + lane * 16) = f32x4{a1[4 * g], a1[4 * g + 1], a1[4 * g + 2], a1[4 * g + 3]};
        }
#pragma unroll
        for (int n = 0; n < 3; ++n) *(LDSAS float*)(lds0 + L_TAB + (w * 96 + n * 32 + r32) * 4) = cs[n];
        BAR_L();
        if (w >= 4) {
            if (tid < 256 + 96) {
                float t[8];
#pragma unroll
                for (int i = 0; i < 8; ++i) t[i] = *(const LDSAS float*)(lds0 + L_TAB + (i * 96 + tid - 256) * 4);
                *(LDSAS float*)(lds0 + L_TAB + 3072 + (tid - 256) * 4) = ((t[0] + t[1]) + (t[2] + t[3])) + ((t[4] + t[5]) + (t[6] + t[7]));
            }
        } else {
            f32x4 p[4];
#pragma unroll
            for (int g = 0; g < 4; ++g) p[g] = lds_f4(lds0 + (w ^ 2) * 4096 + g * 1024 + lane * 16);
            SBAR();
#pragma unroll
            for (int g = 0; g < 4; ++g) {
                hreg[g >> 1][(g & 1) * 2] = pk2(a0[4 * g] + p[g][0], a0[4 * g + 1] + p[g][1]);
                hreg[g >> 1][(g & 1) * 2 + 1] = pk2(a0[4 * g + 2] + p[g][2], a0[4 * g + 3] + p[g][3]);
            }
            const unsigned so = WS_H + ((rt * 2 + wt) * 32 + (ct * 2 + kh) * 2) * 1024;
            __builtin_amdgcn_raw_buffer_store_b128(hreg[0], rs, lane * 16, so, AUX_SC1);
            __builtin_amdgcn_raw_buffer_store_b128(hreg[1], rs, lane * 16 + 1024, so, AUX_SC1);
            SBAR();
            float sm = 0.f, sq = 0.f;
#pragma unroll
            for (int i = 0; i < 8; ++i) { sm = dot2(hreg[i >> 2][i & 3], ONES2, sm); sq = dot2(hreg[i >> 2][i & 3], hreg[i >> 2][i & 3], sq); }
            sm = swap_add(sm); sq = swap_add(sq);
            if (h == 0) __builtin_amdgcn_raw_buffer_store_b64(u32x2{__float_as_uint(sm), __float_as_uint(sq)}, rs, r32 * 8,
                                                              WS_ST + ((rt * 2 + wt) * 16 + ct * 2 + kh) * 256, AUX_SC1);
        }
        STAMP(2, 3);
        BAR_L();
        if (w >= 4) { issue_dn(0, 15, ((w - 4) >> 1) * 15 * 2048, w - 4); if (bi >= 29) issue_dn(15, 24, L_GU + ((w - 4) >> 1) * 49152, w - 4); }
        STAMP(2, 5);
        FLUSH(2);
    }

    {
        STAMP(3, 0);
        if (bi < 29) {
            const int tt = xcd * 8 + w;
            GU gu;
            gu.a0 = gu.a1 = gu.a2 = f32x16{};
            gu.sa = gu.s0 = RFL(WS_H + tt * 32768); gu.sv = 8 * h * 256 + r32 * 8; gu.ss = RFL(WS_ST + tt * 4096); gu.l16 = l16; gu.budget = budget; gu.rs = rs;
            gu.dm = RFL(lds0 + L_GU + (w >> 2) * 49152 + (w & 3) * 6144); gu.ds = RFL(((43 * (w >> 2) + 15) * 16 + (w & 3) * 48) * 1024 + ct * 128);
            gu.dv = w64_voff(lane); gu.rd = mk_rsrc(wd, F * D * 2);
            gu.wa[0] = gu_wa(0); gu.wa[1] = gu_wa(1); gu.wa[2] = gu_wa(2);
            f32x4 ctab[3][4];
#pragma unroll
            for (int n = 0; n < 3; ++n)
#pragma unroll
                for (int g = 0; g < 4; ++g) ctab[n][g] = lds_f4(lds0 + L_TAB + 3072 + (n * 32 + 8 * g + 4 * h) * 4);
            if (w < 4) __builtin_amdgcn_s_sleep(DELAY_H);
            STAMP(3, 2);
            gu_steps<0>(gu);
            budget = gu.budget;
            const f32x16 acc[3] = {gu.a0, gu.a1, gu.a2};
            u32x16 st = gu.st;
            const unsigned sv = gu.sv, ss = gu.ss;
            STAMP(3, 3);
#pragma unroll 1
            for (;;) {
                unsigned mx = st[0];
#pragma unroll
                for (int i = 1; i < 16; ++i) mx = st[i] > mx ? st[i] : mx;
                if (!__any(mx == ~0u) || budget == 0) break;
                --budget;
#pragma unroll
                for (int i = 0; i < 8; ++i) { const u32x2 v = __builtin_amdgcn_raw_buffer_load_b64(rs, sv + i * 256, ss, AUX_SC1); st[2 * i] = v[0]; st[2 * i + 1] = v[1]; }
            }
            float sm = 0.f, sq = 0.f;
#pragma unroll
            for (int i = 0; i < 8; ++i) { sm += __uint_as_float(st[2 * i]); sq += __uint_as_float(st[2 * i + 1]); }
            sm = swap_add(sm); sq = swap_add(sq);
            const float mean = sm * (1.f / D);
            const float var = __builtin_fmaxf(sq * (1.f / D) - mean * mean, 0.f) + LN_EPS;
            const float rstd = __builtin_amdgcn_rsqf(var), sd = var * rstd, ga = -LOG2E * rstd;
            auto epi = [&](int n) {
                u32x4 ov; float y[8];
#pragma unroll
                for (int g = 0; g < 2; ++g) {
                    const f32x4 cg = ctab[n][g], cu = ctab[n][2 + g];
#pragma unroll
                    for (int i = 0; i < 4; ++i) {
                        const float e = __builtin_amdgcn_exp2f(__builtin_fmaf(-mean, cg[i], acc[n][4 * g + i]) * ga);
                        y[4 * g + i] = __builtin_fmaf(-mean, cu[i], acc[n][8 + 4 * g + i]) * __builtin_amdgcn_rcpf(__builtin_fmaf(e, sd, sd));
                    }
                }
#pragma unroll
                for (int i = 0; i < 4; ++i) ov[i] = pk2(y[2 * i], y[2 * i + 1]);
                const int u = bi * 3 + n;
                if (u < NU) __builtin_amdgcn_raw_buffer_store_b128(ov, rs, lane * 16, WS_HH + (tt * NU + u) * 1024, AUX_SC1);
            };
            epi(0); epi(1);
            epi(2);
        }
        STAMP(3, 4);
        if (bi >= 29) WAITV(0);
        BAR_L();
        STAMP(3, 5);
        if (w >= 4) {
            if (bi < 29) issue_dn(27, 12, L_GU + ((w - 4) >> 1) * 49152 + 24576, w - 4);
            issue_dn(39, 1, L_TAB + ((w - 4) >> 1) * 2048, w - 4); STAMP(3, 6);
        }
        FLUSH(3);
    }

    {
        STAMP(4, 0);
        if (b == 0 && tid == 0) __builtin_amdgcn_raw_buffer_store_b32(ep, rs, 0, WS_EP, AUX_SC1);
        f32x16 a0, a1;
        if (w < 4) {
            const unsigned t0 = lds0 + w64_tr(lane, kh), t1 = lds0 + w64_tr(lane, kh ^ 1);
            G2 g2;
            g2.tb[0] = t0 + kh * 15 * 2048; g2.tb[1] = t1 + kh * 15 * 2048; g2.tb[2] = t0 + L_GU + kh * 24 * 2048; g2.tb[3] = t1 + L_GU + kh * 24 * 2048;
            g2.tb[4] = t0 + L_TAB + kh * 2048; g2.tb[5] = t1 + L_TAB + kh * 2048;
            g2.sa = g2.s0 = RFL(WS_HH + ((rt * 2 + wt) * NU + kh * 43) * 1024); g2.l16 = l16; g2.budget = budget; g2.rs = rs; g2.a1 = f32x16{};
#pragma unroll
            for (int i = 0; i < 8; ++i) { g2.a0[2 * i] = bflo(hreg[i >> 2][i & 3]); g2.a0[2 * i + 1] = bfhi(hreg[i >> 2][i & 3]); }
            __builtin_amdgcn_s_sleep(DELAY_G);
            STAMP(4, 1);
            g2_steps<0, 43, 1, 25, 38>(g2);
            a0 = g2.a0; a1 = g2.a1;
        } else {
            BAR_VL(0);
            issue_dn(40, 3, ((w - 4) >> 1) * 15 * 2048, w - 4);
            BAR_VL(0);
        }
        STAMP(4, 2);
        BAR_L();
        if (w < 4) {
#pragma unroll
            for (int g = 0; g < 4; ++g) *(LDSAS f32x4*)(lds0 + w * 4096 + g * 1024 + lane * 16) = f32x4{a1[4 * g], a1[4 * g + 1], a1[4 * g + 2], a1[4 * g + 3]};
        }
        BAR_L();
        if (w < 4) {
            f32x4 p[4];
#pragma unroll
            for (int g = 0; g < 4; ++g) p[g] = lds_f4(lds0 + (w ^ 2) * 4096 + g * 1024 + lane * 16);
            SBAR();
            unsigned y[8];
#pragma unroll
            for (int g = 0; g < 4; ++g) {
                y[2 * g] = pk2(a0[4 * g] + p[g][0], a0[4 * g + 1] + p[g][1]);
                y[2 * g + 1] = pk2(a0[4 * g + 2] + p[g][2], a0[4 * g + 3] + p[g][3]);
            }
            const rsrc_t ro = mk_rsrc(out, S * D * 2);
            const unsigned vo = (rt * 64 + wt * 32 + r32) * 1024 + (ct * 64 + kh * 32 + 8 * h) * 2;
#pragma unroll
            for (int g = 0; g < 2; ++g) {
                auto e0 = __builtin_amdgcn_permlane32_swap(y[4 * g], y[4 * g + 2], false, false);
                auto e1 = __builtin_amdgcn_permlane32_swap(y[4 * g + 1], y[4 * g + 3], false, false);
                __builtin_amdgcn_raw_buffer_store_b128(u32x4{e0[0], e1[0], e0[1], e1[1]}, ro, vo + g * 32, 0, 0);
            }
        }
        STAMP(4, 3);
        WAITV(0);
        STAMP(4, 4);
        FLUSH(4);
        asm volatile(".Lmain_end:\n\t.subsection 2\n\t.fill 64, 4, 0xBF800000\n\t.subsection 0");
    }
}

extern "C" void kernel_launch(void* const* d_in, const int* in_sizes, int n_in,
                              void* d_out, int out_size, void* d_ws, size_t ws_size,
                              hipStream_t stream) {
    const u16* x = (const u16*)d_in[0];
    const u16* wqkv = (const u16*)d_in[1];
    const u16* wo = (const u16*)d_in[2];
    const u16* wg = (const u16*)d_in[4];
    const u16* wu = (const u16*)d_in[5];
    const u16* wd = (const u16*)d_in[6];
    u16* out = (u16*)d_out;
    char* ws = (char*)d_ws;
    (void)hipFuncSetAttribute((const void*)k_block, hipFuncAttributeMaxDynamicSharedMemorySize, K2_LDS);
    k_block<<<256, 512, K2_LDS, stream>>>(x, wqkv, wo, wg, wu, wd, out, ws);
}
```

```cpp
#include <hip/hip_runtime.h>
#include <hip/hip_bf16.h>
#include <stdint.h>
#pragma clang diagnostic ignored "-Winline-asm"

typedef short s16x8 __attribute__((ext_vector_type(8)));
typedef short s16x4 __attribute__((ext_vector_type(4)));
typedef float f32x16 __attribute__((ext_vector_type(16)));
typedef float f32x4 __attribute__((ext_vector_type(4)));
typedef float f32x2 __attribute__((ext_vector_type(2)));
typedef unsigned u32x4 __attribute__((ext_vector_type(4)));
typedef unsigned u32x2 __attribute__((ext_vector_type(2)));
typedef unsigned u32x8 __attribute__((ext_vector_type(8)));
typedef __bf16 bf16x2 __attribute__((ext_vector_type(2)));
typedef unsigned short u16;
#define LDSAS __attribute__((address_space(3)))
#define GAS __attribute__((address_space(1)))
typedef __amdgpu_buffer_rsrc_t rsrc_t;

constexpr int S = 2048, D = 512, NH = 32, F = 1376, NU = F / 16;
constexpr float LN_EPS = 1e-5f;
constexpr float LOG2E = 1.4426950408889634f;

constexpr unsigned KV_CHUNK = 16384;
constexpr unsigned KV_HEAD = 8 * KV_CHUNK;
constexpr unsigned WS_EP = 0;
constexpr unsigned WS_FQ = 4096;
constexpr unsigned WS_FP = WS_FQ + 16 * 64 * 128;
constexpr unsigned WS_Q = WS_FP + 4096;
constexpr unsigned WS_KV = WS_Q + NH * 64 * 1024;
constexpr unsigned WS_O = WS_KV + NH * KV_HEAD;
constexpr unsigned WS_H = WS_O + 64 * 32 * 1024;
constexpr unsigned WS_ST = WS_H + 64 * 32 * 1024;
constexpr unsigned WS_HH = WS_ST + 64 * 16 * 256;
constexpr unsigned WS_END = WS_HH + 64 * NU * 1024;
constexpr int AUX_SC1 = 16;
#define STAMP_DECL
#define FLUSH(k) do {} while (0)
#define STAMP(k, i) do {} while (0)

#define SBAR() __builtin_amdgcn_sched_barrier(0)
#define RFL(x) ((unsigned)__builtin_amdgcn_readfirstlane(x))
#define PIN(x) asm volatile("" : "+v"(x))
#define MFMA(a, b, c) __builtin_amdgcn_mfma_f32_32x32x16_bf16((a), (b), (c), 0, 0, 0)
#define WAITV(n) asm volatile("s_waitcnt vmcnt(" #n ")" ::: "memory")
#define BAR_L() asm volatile("s_waitcnt lgkmcnt(0)\n\ts_barrier" ::: "memory")
#define BAR_0() asm volatile("s_barrier" ::: "memory")
#define BAR_VL(n) asm volatile("s_waitcnt vmcnt(" #n ") lgkmcnt(0)\n\ts_barrier" ::: "memory")

__device__ __forceinline__ rsrc_t mk_rsrc(const void* p, unsigned bytes) {
    return __builtin_amdgcn_make_buffer_rsrc((void*)p, (short)0, (int)bytes, 0x00020000);
}
template <bool SC1 = false> __device__ __forceinline__ void dma16(rsrc_t r, unsigned voff, unsigned soff, unsigned lds) {
    if constexpr (SC1) asm volatile("s_nop 4\n\ts_mov_b32 m0, %0\n\ts_nop 0\n\tbuffer_load_dwordx4 %1, %2, %3 offen sc1 lds"
                 :: "s"(__builtin_amdgcn_readfirstlane(lds)), "v"(voff), "s"(r), "s"(__builtin_amdgcn_readfirstlane(soff)) : "m0", "memory");
    else asm volatile("s_nop 4\n\ts_mov_b32 m0, %0\n\ts_nop 0\n\tbuffer_load_dwordx4 %1, %2, %3 offen lds"
                 :: "s"(__builtin_amdgcn_readfirstlane(lds)), "v"(voff), "s"(r), "s"(__builtin_amdgcn_readfirstlane(soff)) : "m0", "memory");
}
__device__ __forceinline__ unsigned pk2(float lo, float hi) {
    f32x2 v = {lo, hi};
    return __builtin_bit_cast(unsigned, __builtin_convertvector(v, bf16x2));
}
__device__ __forceinline__ float dot2(unsigned a, unsigned b, float c) {
    return __builtin_amdgcn_fdot2_f32_bf16(__builtin_bit_cast(bf16x2, a), __builtin_bit_cast(bf16x2, b), c, false);
}
__device__ __forceinline__ float swap_add(float v) {
    auto r = __builtin_amdgcn_permlane32_swap(__float_as_uint(v), __float_as_uint(v), false, false);
    return __uint_as_float(r[0]) + __uint_as_float(r[1]);
}
__device__ __forceinline__ float swap_max(float v) {
    auto r = __builtin_amdgcn_permlane32_swap(__float_as_uint(v), __float_as_uint(v), false, false);
    return __builtin_fmaxf(__uint_as_float(r[0]), __uint_as_float(r[1]));
}
__device__ __forceinline__ s16x8 lds_b128(unsigned a) { return *(const LDSAS s16x8*)(a); }
__device__ __forceinline__ f32x4 lds_f4(unsigned a) { return *(const LDSAS f32x4*)(a); }
__device__ __forceinline__ s16x8 lds_tr2(unsigned a0, unsigned a1) {
    s16x4 lo = __builtin_amdgcn_ds_read_tr16_b64_v4i16((LDSAS s16x4*)(a0));
    s16x4 hi = __builtin_amdgcn_ds_read_tr16_b64_v4i16((LDSAS s16x4*)(a1));
    return __builtin_shufflevector(lo, hi, 0, 1, 2, 3, 4, 5, 6, 7);
}
__device__ __forceinline__ float bflo(unsigned v) { return __uint_as_float(v << 16); }
__device__ __forceinline__ float bfhi(unsigned v) { return __uint_as_float(v & 0xffff0000u); }
constexpr unsigned ONES2 = 0x3f803f80u;
template <int I> struct ic { static constexpr int value = I; constexpr operator int() const { return I; } };
template <int B, int E, class Fn> __device__ __forceinline__ void sfor(Fn&& fn) { if constexpr (B < E) { fn(ic<B>{}); sfor<B + 1, E>(fn); } }

#define K1_X_AUX 2
#define K1_ABL 0
#define K1_ST_AUX 16
constexpr unsigned K1_W = 65536, K1_FLG = K1_W + 5 * 16384, K1_ST = K1_FLG + 512, K1_CS = K1_ST + 512;
__host__ __device__ constexpr unsigned k1_slot(int s) { return s < 5 ? K1_W + s * 16384 : s == 5 ? K1_W : s < 10 ? (s - 6) * 16384 : K1_W + (s - 9) * 16384; }
__device__ __forceinline__ void lds_spin_ge(unsigned addr, unsigned want) {
#pragma unroll 1
    for (unsigned n = 0; n < (1u << 22); ++n) {
        unsigned v; asm volatile("ds_read_b32 %0, %1\n\ts_waitcnt lgkmcnt(0)" : "=v"(v) : "v"(addr) : "memory");
        if ((unsigned)__builtin_amdgcn_readfirstlane(v) >= want) break;
        __builtin_amdgcn_s_sleep(1);
    }
}
__device__ __forceinline__ void lds_post(unsigned addr, unsigned v) {
    asm volatile("s_waitcnt lgkmcnt(0)\n\tds_write_b32 %0, %1" :: "v"(addr), "v"(v) : "memory");
}
__device__ __forceinline__ void lds_inc(unsigned addr, int lane) {
    asm volatile("s_waitcnt lgkmcnt(0)" ::: "memory");
    if (lane == 0) asm volatile("ds_add_u32 %0, %1" :: "v"(addr), "v"(1u) : "memory");
}
typedef unsigned u32x16 __attribute__((ext_vector_type(16)));
template <int CTRL> __device__ __forceinline__ float dpp_add(float v) {
    return v + __builtin_bit_cast(float, __builtin_amdgcn_update_dpp(0, __builtin_bit_cast(int, v), CTRL, 0xf, 0xf, true));
}

constexpr unsigned K2_LDS = 163840;
constexpr unsigned L_WO = 0, L_GU = 65536;
constexpr unsigned L_TAB = 61440;
constexpr unsigned L_FLAG = 16384 + 12288;
constexpr unsigned SPIN_CAP = 1u << 20;
#define POLL_DELAY_Q 24
#define ABL_GU 0
#define DELAY_O 0
#define DELAY_H 8
#define DELAY_G 8
constexpr int GU_AW = 8, G2_AW = 12;
#define TAG_TEST "v_max3_u32 v120, v[%c[ar]], v[%c[ar]+1], v[%c[ar]+2]\n\tv_max_u32 v120, v120, v[%c[ar]+3]\n\tv_cmp_eq_u32 vcc, -1, v120\n\t"
#define TAG_RELOAD(RB, AW) "s_add_u32 %[t], %[s0], (tagj>>2)*4096\n\tbuffer_load_dwordx4 v[" #RB "+4*(tagj%%" #AW "):" #RB "+3+4*(tagj%%" #AW ")], %[l16], %[rs], %[t] offen offset:(tagj&3)*1024 sc1\n\t"
#define TAG_CHECK(RB, AW) \
    TAG_TEST "s_cbranch_vccnz 5f\n6:\n\t" \
    ".subsection 1\n5:\n\ts_cmp_eq_u32 %[bud], 0\n\ts_cbranch_scc1 6b\n\ts_sub_u32 %[bud], %[bud], 1\n\t.set tagj, %c[i]\n\t" TAG_RELOAD(RB, AW) \
    "s_waitcnt vmcnt(0)\n\t" TAG_TEST "s_cbranch_vccnz 5b\n\t.rept %c[cnt]-1\n\t.set tagj, tagj+1\n\t" TAG_RELOAD(RB, AW) ".endr\n\ts_branch 6b\n\t.subsection 0\n\t"
#define TAG_LOAD "buffer_load_dwordx4 v[%c[lr]:%c[lr]+3], %[l16], %[rs], %[sa] offen offset:%c[lo] sc1\n\t.if %c[lo] == 3072\n\ts_add_u32 %[sa], %[sa], 0x1000\n\t.endif\n\t"

__device__ __forceinline__ void wait_flags(rsrc_t rs, unsigned base, int n, unsigned ep, int lane) {
    const unsigned v = (lane < n ? lane : n - 1) * 128;
#pragma unroll 1
    for (unsigned spins = 0; spins < SPIN_CAP; ++spins) {
        const unsigned f = __builtin_amdgcn_raw_buffer_load_b32(rs, v, base, AUX_SC1);
        if (__all(f == ep)) break;
        __builtin_amdgcn_s_sleep(1);
    }
    asm volatile("" ::: "memory");
}

__device__ __forceinline__ unsigned w64_voff(int lane) { return (lane >> 3) * 1024 + (((lane & 7) ^ (((lane >> 4) & 1) << 2)) << 4); }
__device__ __forceinline__ unsigned w64_tr(int lane, int n) {
    const int q = (lane & 15) >> 2, p = lane & 3, blk = (lane >> 4) & 1, h = lane >> 5;
    return (4 * h + q) * 128 + (((4 * n + 2 * blk + (p >> 1)) ^ (((q >> 1) & 1) << 2)) << 4) + 8 * (p & 1);
}

struct G2 { u32x16 r0, r1, r2; u32x8 w0, w1, w2; f32x16 a0, a1; unsigned tb[6], sa, s0, l16, budget; rsrc_t rs; };
__host__ __device__ constexpr int g2_w(int DN, int n) {
    return !DN ? n * 2048 : n < 15 ? n * 2048 : n < 39 ? 65536 + (n - 15) * 2048 : n < 40 ? 131072 : (n - 40) * 2048;
}
template <int i, int N, int DN, int B0, int B1> __device__ __forceinline__ void g2_steps(G2& g) {
    constexpr int AW = G2_AW, j = i + AW - 1, n = i + 2, sel = n < N ? g2_w(DN, n) >> 16 : 3;
    unsigned tq;
#define G2_RD(P0, P1) "ds_read_b64_tr_b16 v[%c[nr]:%c[nr]+1], %[" P0 "] offset:%c[no]\n\tds_read_b64_tr_b16 v[%c[nr]+2:%c[nr]+3], %[" P0 "] offset:%c[no]+1024\n\t" \
                      "ds_read_b64_tr_b16 v[%c[nr]+4:%c[nr]+5], %[" P1 "] offset:%c[no]\n\tds_read_b64_tr_b16 v[%c[nr]+6:%c[nr]+7], %[" P1 "] offset:%c[no]+1024\n\t"
    if constexpr (i == 0)
        asm volatile(".set tagj, 0\n\t.rept 11\n\tbuffer_load_dwordx4 v[128+4*tagj:131+4*tagj], %[l16], %[rs], %[sa] offen offset:(tagj&3)*1024 sc1\n\t"
                     ".if (tagj&3) == 3\n\ts_add_u32 %[sa], %[sa], 0x1000\n\t.endif\n\t.set tagj, tagj+1\n\t.endr\n\t"
                     ".set tagj, 0\n\t.rept 2\n\tds_read_b64_tr_b16 v[96+8*tagj:97+8*tagj], %[b0] offset:tagj*2048\n\tds_read_b64_tr_b16 v[98+8*tagj:99+8*tagj], %[b0] offset:tagj*2048+1024\n\t"
                     "ds_read_b64_tr_b16 v[100+8*tagj:101+8*tagj], %[b1] offset:tagj*2048\n\tds_read_b64_tr_b16 v[102+8*tagj:103+8*tagj], %[b1] offset:tagj*2048+1024\n\t.set tagj, tagj+1\n\t.endr"
                     : "={v[128:143]}"(g.r0), "={v[144:159]}"(g.r1), "={v[160:175]}"(g.r2), "={v[96:103]}"(g.w0), "={v[104:111]}"(g.w1), "={v[112:119]}"(g.w2), [sa] "+s"(g.sa),
                       "+{v[64:79]}"(g.a0), "+{v[80:95]}"(g.a1)
                     : [l16] "v"(g.l16), [rs] "s"(g.rs), [b0] "v"(g.tb[0]), [b1] "v"(g.tb[1]) : "scc", "memory");
    asm volatile(".if %c[ld]\n\t" TAG_LOAD ".endif\n\t"
                 ".if %c[bar]\n\ts_waitcnt lgkmcnt(0)\n\ts_barrier\n\t.endif\n\t"
                 ".if %c[sel] == 0\n\t" G2_RD("b0", "b1") ".endif\n\t.if %c[sel] == 1\n\t" G2_RD("b2", "b3") ".endif\n\t.if %c[sel] == 2\n\t" G2_RD("b4", "b5") ".endif\n\t"
                 "s_waitcnt vmcnt(%c[vn]) lgkmcnt(%c[ln])\n\t" TAG_CHECK(128, 12)
                 "v_mfma_f32_32x32x16_bf16 v[64:79], v[%c[wr]:%c[wr]+3], v[%c[ar]:%c[ar]+3], v[64:79]\n\t"
                 "v_mfma_f32_32x32x16_bf16 v[80:95], v[%c[wr]+4:%c[wr]+7], v[%c[ar]:%c[ar]+3], v[80:95]\n\t"
                 ".if %c[cnt] == 1\n\ts_nop 15\n\t.endif"
                 : "+{v[64:79]}"(g.a0), "+{v[80:95]}"(g.a1), "+{v[128:143]}"(g.r0), "+{v[144:159]}"(g.r1), "+{v[160:175]}"(g.r2), "+{v[96:103]}"(g.w0), "+{v[104:111]}"(g.w1), "+{v[112:119]}"(g.w2),
                   [sa] "+s"(g.sa), [bud] "+s"(g.budget), [t] "=&s"(tq)
                 : [l16] "v"(g.l16), [rs] "s"(g.rs), [s0] "s"(g.s0), [b0] "v"(g.tb[0]), [b1] "v"(g.tb[1]), [b2] "v"(g.tb[2]), [b3] "v"(g.tb[3]), [b4] "v"(g.tb[4]), [b5] "v"(g.tb[5]),
                   [ld] "n"(j < N ? 1 : 0), [lr] "n"(128 + 4 * (j % AW)), [lo] "n"((j & 3) * 1024), [bar] "n"(i == B0 || i == B1 ? 1 : 0), [sel] "n"(sel),
                   [nr] "n"(96 + 8 * (n % 3)), [no] "n"(n < N ? g2_w(DN, n) & 65535 : 0), [vn] "n"(j < N ? AW - 1 : N - 1 - i), [ln] "n"(n < N ? 8 : n == N ? 4 : 0),
                   [ar] "n"(128 + 4 * (i % AW)), [wr] "n"(96 + 8 * (i % 3)), [i] "n"(i), [cnt] "n"(j < N ? AW : N - i) : "vcc", "scc", "v120", "memory");
    if constexpr (i + 1 < N) g2_steps<i + 1, N, DN, B0, B1>(g);
}
__host__ __device__ constexpr int gu_dma(int s) { return s >= 16 && s < 28 && !(s & 1); }
__host__ __device__ constexpr int gu_ops(int s) { return (s + GU_AW - 1 < 32 ? 1 : 0) + (s >= 24 ? 1 : 0) + gu_dma(s); }
__host__ __device__ constexpr int gu_younger(int k) {
    int c = 0, s = k - GU_AW + 1;
    if (s < 0) { c = GU_AW - 2 - k; s = 0; } else { c = gu_ops(s) - 1; ++s; }
    for (; s <= k; ++s) c += gu_ops(s);
    return c;
}

struct GU { u32x16 r0, r1, st; u32x8 w0, w1, w2; f32x16 a0, a1, a2; unsigned wa[3], sa, s0, sv, ss, l16, budget, dm, ds, dv; rsrc_t rs, rd; };
template <int i> __device__ __forceinline__ void gu_steps(GU& g) {
    constexpr int AW = GU_AW, j = i + AW - 1, n = i + 1;
    unsigned tq;
#define GU_RD(R, B) "ds_read_b64_tr_b16 v[" R ":" R "+1], %[" B "] offset:%c[no]\n\tds_read_b64_tr_b16 v[" R "+2:" R "+3], %[" B "] offset:%c[no]+768\n\t"
    if constexpr (i == 0)
        asm volatile(".set tagj, 0\n\t.rept 7\n\tbuffer_load_dwordx4 v[140+4*tagj:143+4*tagj], %[l16], %[rs], %[sa] offen offset:(tagj&3)*1024 sc1\n\t"
                     ".if (tagj&3) == 3\n\ts_add_u32 %[sa], %[sa], 0x1000\n\t.endif\n\t.set tagj, tagj+1\n\t.endr\n\t" GU_RD("112", "b0") GU_RD("116", "b1") GU_RD("124", "b2")
                     : "={v[140:155]}"(g.r0), "={v[156:171]}"(g.r1), "={v[172:187]}"(g.st), "={v[112:119]}"(g.w0), "={v[124:131]}"(g.w1), "={v[132:139]}"(g.w2), [sa] "+s"(g.sa),
                       "+{v[64:79]}"(g.a0), "+{v[80:95]}"(g.a1), "+{v[96:111]}"(g.a2)
                     : [l16] "v"(g.l16), [rs] "s"(g.rs), [b0] "v"(g.wa[0]), [b1] "v"(g.wa[1]), [b2] "v"(g.wa[2]), [no] "n"(0) : "scc", "memory");
    asm volatile(".if %c[ld]\n\t" TAG_LOAD ".endif\n\t"
                 ".if %c[sk] >= 0\n\tbuffer_load_dwordx2 v[172+2*%c[sk]:173+2*%c[sk]], %[sv], %[rs], %[ss] offen offset:%c[sk]*256 sc1\n\t.endif\n\t"
                 ".if %c[i] == 16\n\ts_waitcnt lgkmcnt(0)\n\ts_barrier\n\t.endif\n\t"
                 ".if %c[dma]\n\ts_mov_b32 m0, %[dm]\n\ts_add_u32 %[dm], %[dm], 0x400\n\tbuffer_load_dwordx4 %[dv], %[rwd], %[ds] offen lds\n\ts_add_u32 %[ds], %[ds], 0x2000\n\t.endif\n\t"
                 ".if %c[rd]\n\t" GU_RD("%c[n0]", "b0") GU_RD("%c[n1]", "b1") GU_RD("%c[n2]", "b2") ".endif\n\t"
                 "s_waitcnt vmcnt(%c[vn]) lgkmcnt(%c[ln])\n\t" TAG_CHECK(140, 8)
                 "v_mfma_f32_32x32x16_bf16 v[64:79], v[%c[c0]:%c[c0]+3], v[%c[ar]:%c[ar]+3], v[64:79]\n\t"
                 "v_mfma_f32_32x32x16_bf16 v[80:95], v[%c[c1]:%c[c1]+3], v[%c[ar]:%c[ar]+3], v[80:95]\n\t"
                 "v_mfma_f32_32x32x16_bf16 v[96:111], v[%c[c2]:%c[c2]+3], v[%c[ar]:%c[ar]+3], v[96:111]\n\t"
                 ".if %c[cnt] == 1\n\ts_waitcnt vmcnt(0)\n\ts_nop 15\n\t.endif"
                 : "+{v[64:79]}"(g.a0), "+{v[80:95]}"(g.a1), "+{v[96:111]}"(g.a2), "+{v[140:155]}"(g.r0), "+{v[156:171]}"(g.r1), "+{v[172:187]}"(g.st),
                   "+{v[112:119]}"(g.w0), "+{v[124:131]}"(g.w1), "+{v[132:139]}"(g.w2), [sa] "+s"(g.sa), [bud] "+s"(g.budget), [t] "=&s"(tq), [dm] "+s"(g.dm), [ds] "+s"(g.ds)
                 : [l16] "v"(g.l16), [rs] "s"(g.rs), [s0] "s"(g.s0), [dv] "v"(g.dv), [rwd] "s"(g.rd), [dma] "n"(gu_dma(i)), [sv] "v"(g.sv), [ss] "s"(g.ss), [b0] "v"(g.wa[0]), [b1] "v"(g.wa[1]), [b2] "v"(g.wa[2]),
                   [ld] "n"(j < 32 ? 1 : 0), [lr] "n"(140 + 4 * (j % AW)), [lo] "n"((j & 3) * 1024), [sk] "n"(i >= 24 ? i - 24 : -1), [rd] "n"(n < 32 ? 1 : 0), [no] "n"(n * 1536),
                   [n0] "n"(n & 1 ? 128 : 112), [n1] "n"(n & 1 ? 132 : 116), [n2] "n"(n & 1 ? 136 : 124), [c0] "n"(i & 1 ? 128 : 112), [c1] "n"(i & 1 ? 132 : 116), [c2] "n"(i & 1 ? 136 : 124),
                   [vn] "n"(gu_younger(i)), [ln] "n"(n < 32 ? 6 : 0), [ar] "n"(140 + 4 * (i % AW)), [i] "n"(i), [cnt] "n"(j < 32 ? AW : 32 - i) : "vcc", "scc", "m0", "v120", "memory");
    if constexpr (i + 1 < 32) gu_steps<i + 1>(g);
}

__global__ __launch_bounds__(512, 2) void k_block(const u16* __restrict__ x, const u16* __restrict__ wqkv, const u16* __restrict__ wo, const u16* __restrict__ wg,
                                                  const u16* __restrict__ wu, const u16* __restrict__ wd, u16* __restrict__ out, char* __restrict__ ws) {
    extern __shared__ __attribute__((aligned(16))) char smem[];
    const unsigned lds0 = (unsigned)(uintptr_t)(LDSAS char*)smem;
    const int tid = threadIdx.x, lane = tid & 63;
    const int w = __builtin_amdgcn_readfirstlane(tid >> 6);
    const int b = blockIdx.x, xcd = b & 7, bi = b >> 3;
    const int r32 = lane & 31, h = lane >> 5;
    const rsrc_t rs = mk_rsrc(ws, WS_END);
    const int wt = w & 1, kh = (w >> 1) & 1;
    STAMP_DECL;
    unsigned ep;
    {
    const int rt = (xcd >> 1) * 8 + (bi & 7), cb = (xcd & 1) * 4 + (bi >> 3), rot = bi & 3;
    const int wc = (w >> 1) & 1, hw = w - 4;
    STAMP(0, 0);
    const rsrc_t rw = mk_rsrc(wqkv, D * 3 * D * 2);
    const unsigned wv = (lane >> 3) * 3072 + (((lane & 7) ^ (((lane >> 4) & 1) << 2)) << 4);
    const unsigned fa = lds0 + K1_FLG;
    if (tid < 12) *(LDSAS unsigned*)(fa + tid * 4) = 0u;
    if (w >= 4) {
        BAR_L();
        const unsigned pa = fa + hw * 4;
        auto issue = [&](int s) {
            const unsigned so = RFL(((((s & 3) + rot) & 3) * 128 + hw * 32) * 3072 + (s >> 2) * 1024 + cb * 128);
            asm volatile("s_nop 4" :: "s"(so));
#pragma unroll
            for (int i = 0; i < 4; ++i)
                asm volatile("s_mov_b32 m0, %0\n\ts_nop 0\n\tbuffer_load_dwordx4 %1, %2, %3 offen lds"
                             :: "s"(lds0 + k1_slot(s) + (4 * hw + i) * 1024), "v"(wv), "s"(rw), "s"(so + i * 8 * 3072) : "m0", "memory");
        };
#define K1_LAND(N, s) asm volatile("s_waitcnt vmcnt(" #N ")\n\tds_write_b32 %0, %1" :: "v"(pa), "v"((s) + 1u) : "memory")
        issue(0); issue(1);
        issue(2); K1_LAND(8, 0);
        issue(3); K1_LAND(8, 1);
        issue(4); K1_LAND(8, 2); K1_LAND(4, 3); K1_LAND(0, 4);
        lds_spin_ge(fa + 28, 4);
        issue(5);
        lds_spin_ge(fa + 20, 4);
        issue(6); issue(7); K1_LAND(8, 5);
        issue(8); K1_LAND(8, 6);
        issue(9); K1_LAND(8, 7);
        lds_spin_ge(fa + 24, 4);
        issue(10); K1_LAND(8, 8);
        issue(11); K1_LAND(8, 9);
        K1_LAND(4, 10); K1_LAND(0, 11);
        if (w == 4) { unsigned e; asm volatile("s_load_dword %0, %1, 0\n\ts_waitcnt lgkmcnt(0)" : "=s"(e) : "s"(ws) : "memory"); *(LDSAS unsigned*)(fa + 48) = e + 1u; }
        else {
            unsigned long long pc; unsigned len, o = bi * 128 + (w - 5) * 24576, t, d0, d1, d2, d3;
#define KCP(D) "s_min_u32 %[t], %[o], %[len]\n\ts_load_dword " D ", %[pc], %[t]\n\ts_add_u32 %[o], %[o], 0x1000\n\t"
            asm volatile("s_getpc_b64 %[pc]\n.Lqkv_pc:\n\ts_mov_b32 %[len], .Lmain_end-.Lqkv_pc-4\n\t" KCP("%[d0]") KCP("%[d1]") KCP("%[d2]") KCP("%[d3]") KCP("%[d0]") KCP("%[d1]") "s_waitcnt lgkmcnt(0)"
                         : [pc] "=&s"(pc), [len] "=&s"(len), [o] "+s"(o), [t] "=&s"(t), [d0] "=&s"(d0), [d1] "=&s"(d1), [d2] "=&s"(d2), [d3] "=&s"(d3) :: "scc", "memory");
        }
        BAR_L();
        ep = RFL(*(const LDSAS unsigned*)(fa + 48));
        if (w == 4) { __builtin_amdgcn_s_sleep(POLL_DELAY_Q); wait_flags(rs, WS_FQ + (xcd * 2 + (bi >> 4)) * 8192, 64, ep, lane); }
    } else {
    {
        const rsrc_t rx = mk_rsrc(x, S * D * 2);
        u32x4 r[2][8];
#pragma unroll
        for (int p = 0; p < 2; ++p)
#pragma unroll
            for (int g = 0; g < 8; ++g)
                r[p][g] = __builtin_amdgcn_raw_buffer_load_b128(rx, (lane >> 3) * 1024 + (((lane & 7) ^ ((4 * p + (lane >> 4)) & 7)) << 4) + g * 128, (rt * 64 + w * 16 + p * 8) * 1024, K1_X_AUX);
        SBAR(); BAR_L(); SBAR();
#pragma unroll
        for (int p = 0; p < 2; ++p) {
            float s0 = 0.f, s1 = 0.f, q0 = 0.f, q1 = 0.f;
#pragma unroll
            for (int g = 0; g < 8; ++g) { s0 = dot2(r[p][g][0], ONES2, s0); q0 = dot2(r[p][g][0], r[p][g][0], q0); s1 = dot2(r[p][g][1], ONES2, s1); q1 = dot2(r[p][g][1], r[p][g][1], q1);
                                          s0 = dot2(r[p][g][2], ONES2, s0); q0 = dot2(r[p][g][2], r[p][g][2], q0); s1 = dot2(r[p][g][3], ONES2, s1); q1 = dot2(r[p][g][3], r[p][g][3], q1); }
            if (p == 0) STAMP(0, 1);
            float sum = s0 + s1, sq = q0 + q1;
            sum = dpp_add<0xB1>(sum); sq = dpp_add<0xB1>(sq); sum = dpp_add<0x4E>(sum); sq = dpp_add<0x4E>(sq); sum = dpp_add<0x141>(sum); sq = dpp_add<0x141>(sq);
            const float mean = sum * (1.f / D);
            const float rstd = __builtin_amdgcn_rsqf(__builtin_fmaxf(sq * (1.f / D) - mean * mean, 0.f) + LN_EPS), nb = -mean * rstd;
#pragma unroll
            for (int g = 0; g < 8; g += 2) {
                f32x2 t[8];
#pragma unroll
                for (int i = 0; i < 8; ++i) { const unsigned v = r[p][g + (i >> 2)][i & 3]; t[i] = f32x2{bflo(v), bfhi(v)}; }
                asm volatile("" : "+v"(t[0]), "+v"(t[1]), "+v"(t[2]), "+v"(t[3]), "+v"(t[4]), "+v"(t[5]), "+v"(t[6]), "+v"(t[7]));
#pragma unroll
                for (int i = 0; i < 8; ++i) t[i] = t[i] * rstd + nb;
                asm volatile("" : "+v"(t[0]), "+v"(t[1]), "+v"(t[2]), "+v"(t[3]), "+v"(t[4]), "+v"(t[5]), "+v"(t[6]), "+v"(t[7]));
                u32x4 y0, y1;
#pragma unroll
                for (int i = 0; i < 4; ++i) { y0[i] = pk2(t[i][0], t[i][1]); y1[i] = pk2(t[4 + i][0], t[4 + i][1]); }
                *(LDSAS u32x4*)(lds0 + (2 * w + p) * 1024 + lane * 16 + g * 8192) = y0;
                *(LDSAS u32x4*)(lds0 + (2 * w + p) * 1024 + lane * 16 + g * 8192 + 8192) = y1;
            }
            SBAR();
        }
    }
    lds_inc(fa + 16, lane);
    STAMP(0, 3);
    u32x16 xa0, xa1, xa2, xa3, xa4, xa5, xa6, xa7;
    u32x4 w0, w1, w2, w3, w4, fl;
    f32x16 acA, acB;
    unsigned xb[4], tq;
#pragma unroll
    for (int m = 0; m < 4; ++m) xb[m] = lds0 + (32 * wt + r32) * 128 + (((2 * m + h) ^ ((r32 >> 1) & 7)) << 4) + rot * 16384;
    const int q4 = (lane & 15) >> 2, p4 = lane & 3, blk = (lane >> 4) & 1;
    const unsigned tA = lds0 + (8 * h + q4) * 128 + (((4 * wc + 2 * blk + (p4 >> 1)) ^ (((q4 >> 1) & 1) << 2)) << 4) + 8 * (p4 & 1);
    const unsigned tB = tA + 65536, tC = tA + 131072;
    const int tt = rt * 2 + wt, hd = cb * 4 + wc * 2;
#define KXR(R, m, O) "ds_read_b128 v[" #R ":" #R "+3], %[x" #m "] offset:" #O "\n\t"
#define KXN(m) "v_add_u32 %[x" #m "], 0x4000, %[x" #m "]\n\tv_and_b32 %[x" #m "], 0xffff, %[x" #m "]\n\t"
#define KCHK(ST) "v_min3_u32 v116, v116, v117, v118\n\tv_min_u32 v116, v116, v119\n\ts_nop 0\n\tv_readfirstlane_b32 %[t], v116\n\ts_nop 0\n\ts_cmp_gt_u32 %[t], " ST "\n\t"
#define KSPIN(ST) "1:\n\tds_read_b128 v[116:119], %[fa]\n\ts_waitcnt lgkmcnt(0)\n\t" KCHK(ST) "s_cbranch_scc0 1b\n\t"
#define KWR(R, O) "ds_read_b64_tr_b16 v[" #R ":" #R "+1], %[tb] offset:" #O "\n\tds_read_b64_tr_b16 v[" #R "+2:" #R "+3], %[tb] offset:" #O "+512\n\t"
    asm volatile("3:\n\tds_read_b32 v116, %[fa] offset:16\n\ts_waitcnt lgkmcnt(0)\n\tv_readfirstlane_b32 %[t], v116\n\ts_nop 0\n\ts_cmp_gt_u32 %[t], 3\n\ts_cbranch_scc0 3b\n\t"
                 KXR(128, 0, 0) KXR(132, 1, 0) KXR(136, 2, 0) KXR(140, 3, 0) KXR(144, 0, 8192) KXR(148, 1, 8192) KXR(152, 2, 8192) KXR(156, 3, 8192) KXN(0) KXN(1) KXN(2) KXN(3)
                 KSPIN("0") KWR(96, 0) KWR(100, 2048) KWR(104, 4096) KWR(108, 6144)
                 : "={v[116:119]}"(fl), [t] "=&s"(tq), "={v[96:99]}"(w0), "={v[100:103]}"(w1), "={v[104:107]}"(w2), "={v[108:111]}"(w3), "={v[112:115]}"(w4), "={v[64:79]}"(acA), "={v[80:95]}"(acB),
                   "={v[128:143]}"(xa0), "={v[144:159]}"(xa1), "={v[160:175]}"(xa2), "={v[176:191]}"(xa3), "={v[192:207]}"(xa4), "={v[208:223]}"(xa5), "={v[224:239]}"(xa6), "={v[240:255]}"(xa7),
                   [x0] "+v"(xb[0]), [x1] "+v"(xb[1]), [x2] "+v"(xb[2]), [x3] "+v"(xb[3])
                 : [fa] "v"(fa), [tb] "v"(tB) : "scc", "memory");
    STAMP(0, 4);
    auto epi = [&](int m, const f32x16& acc, int hh) {
        const float sc = 0.25f * LOG2E;
        u32x4 o;
#pragma unroll
        for (int i = 0; i < 4; ++i) o[i] = m == 0 ? pk2(acc[8 * hh + 2 * i] * sc, acc[8 * hh + 2 * i + 1] * sc) : pk2(acc[8 * hh + 2 * i], acc[8 * hh + 2 * i + 1]);
        if (m == 0) __builtin_amdgcn_raw_buffer_store_b128(o, rs, lane * 16, WS_Q + ((hd + hh) * 64 + tt) * 1024, K1_ST_AUX);
        else if (m == 1) __builtin_amdgcn_raw_buffer_store_b128(o, rs, lane * 16, WS_KV + (hd + hh) * KV_HEAD + (tt >> 3) * KV_CHUNK + (tt & 7) * 1024, K1_ST_AUX);
        else __builtin_amdgcn_raw_buffer_store_b128(o, rs, (r32 >> 4) * KV_HEAD + (16 * h + (lane & 15)) * 16 + hh * 512,
                                                    WS_KV + hd * KV_HEAD + (tt >> 3) * KV_CHUNK + 8192 + (tt & 7) * 1024, K1_ST_AUX);
    };
    sfor<0, 96>([&](auto G) {
        constexpr int g = G.value, n = g + 4, m = g >> 5;
        constexpr unsigned o = k1_slot(n >> 3) + (n & 7) * 2048, oi = o & 65535;
        constexpr int aR = m == 1 ? 80 : 64, wR = 96 + 4 * (g % 5), nR = 96 + 4 * (n % 5), xR = 128 + 4 * (g & 31);
        constexpr int nx = (g - 4 >= 0 && g - 4 < 12) + (g - 3 >= 0 && g - 3 < 12) + (g - 2 >= 0 && g - 2 < 12) + (g - 1 >= 0 && g - 1 < 12), wc_ = n >= 96 ? 0 : (8 + 2 * nx > 14 ? 14 : 8 + 2 * nx);
#define KRD(B) "ds_read_b64_tr_b16 v[%c[nr]:%c[nr]+1], %[" B "] offset:%c[oi]\n\tds_read_b64_tr_b16 v[%c[nr]+2:%c[nr]+3], %[" B "] offset:%c[oi]+512\n\t"
        asm volatile(".if %c[bar] == 1\n\t" KCHK("%c[st]") "s_cbranch_scc1 2f\n\t" KSPIN("%c[st]") "2:\n\t.endif\n\t"
                     ".if %c[bar] == 2\n\tds_read_b128 v[116:119], %[fa]\n\t.endif\n\t"
                     ".if %c[rd] == 1\n\t" KRD("tb") ".endif\n\t.if %c[rd] == 2\n\t" KRD("tc") ".endif\n\t.if %c[rd] == 3\n\t" KRD("ta") ".endif\n\t"
                     "s_waitcnt lgkmcnt(%c[wn])\n\t"
                     ".if %c[md] == 0\n\tv_mfma_f32_32x32x16_bf16 v[%c[ar]:%c[ar]+15], v[%c[wr]:%c[wr]+3], v[%c[xr]:%c[xr]+3], v[%c[ar]:%c[ar]+15]\n\t.endif\n\t"
                     ".if %c[md] == 1\n\tv_mfma_f32_32x32x16_bf16 v[%c[ar]:%c[ar]+15], v[%c[xr]:%c[xr]+3], v[%c[wr]:%c[wr]+3], v[%c[ar]:%c[ar]+15]\n\t.endif\n\t"
                     ".if %c[md] == 2\n\tv_mfma_f32_32x32x16_bf16 v[%c[ar]:%c[ar]+15], v[%c[xr]:%c[xr]+3], v[%c[wr]:%c[wr]+3], 0\n\t.endif\n\t"
                     ".if %c[md] == 3\n\tv_mfma_f32_32x32x16_bf16 v[%c[ar]:%c[ar]+15], v[%c[wr]:%c[wr]+3], v[%c[xr]:%c[xr]+3], 0\n\t.endif\n\t"
                     ".if %c[xm] == 1\n\tds_read_b128 v[%c[xd]:%c[xd]+3], %[x0] offset:%c[xo]\n\tds_read_b128 v[%c[xd]+4:%c[xd]+7], %[x1] offset:%c[xo]\n\t.endif\n\t"
                     ".if %c[xm] == 2\n\tds_read_b128 v[%c[xd]:%c[xd]+3], %[x2] offset:%c[xo]\n\tds_read_b128 v[%c[xd]+4:%c[xd]+7], %[x3] offset:%c[xo]\n\t.endif\n\t"
                     ".if %c[xn]\n\t" KXN(0) KXN(1) KXN(2) KXN(3) ".endif"
                     : "+{v[64:79]}"(acA), "+{v[80:95]}"(acB), "+{v[96:99]}"(w0), "+{v[100:103]}"(w1), "+{v[104:107]}"(w2), "+{v[108:111]}"(w3), "+{v[112:115]}"(w4),
                       "+{v[128:143]}"(xa0), "+{v[144:159]}"(xa1), "+{v[160:175]}"(xa2), "+{v[176:191]}"(xa3), "+{v[192:207]}"(xa4), "+{v[208:223]}"(xa5), "+{v[224:239]}"(xa6), "+{v[240:255]}"(xa7),
                       "+{v[116:119]}"(fl), [t] "=&s"(tq), [x0] "+v"(xb[0]), [x1] "+v"(xb[1]), [x2] "+v"(xb[2]), [x3] "+v"(xb[3])
                     : [ta] "v"(tA), [tb] "v"(tB), [tc] "v"(tC), [fa] "v"(fa), [st] "n"(n >> 3), [wn] "n"(wc_),
                       [bar] "n"(K1_ABL ? 0 : n < 96 && (n & 7) == 0 ? 1 : n < 92 && (n & 7) == 4 ? 2 : 0), [rd] "n"(n >= 96 ? 0 : o < 65536 ? 3 : o < 131072 ? 1 : 2), [oi] "n"(oi), [nr] "n"(nR),
                       [md] "n"(m < 2 ? ((g & 31) == 0 ? 3 : 0) : g == 64 ? 2 : 1), [ar] "n"(aR), [wr] "n"(wR), [xr] "n"(xR),
                       [xm] "n"(g < 12 ? 1 + (g & 1) : 0), [xd] "n"(160 + 8 * g), [xo] "n"(((g >> 1) & 1) * 8192), [xn] "n"(g < 11 && (g & 3) == 3 ? 1 : 0) : "scc", "memory");
        if (g == 7 && lane == 0) asm volatile("ds_add_u32 %0, %1" :: "v"(fa + 28), "v"(1u) : "memory");
        if (g == 16 && lane == 0) asm volatile("ds_add_u32 %0, %1" :: "v"(fa + 20), "v"(1u) : "memory");
        if (g == 23 && lane == 0) asm volatile("ds_add_u32 %0, %1" :: "v"(fa + 24), "v"(1u) : "memory");
        if (g == 36 || g == 38) epi(0, acA, (g - 36) >> 1);
        if (g == 68 || g == 70) epi(1, acB, (g - 68) >> 1);
    });
    STAMP(0, 2);
    asm volatile("s_nop 15" : "+{v[64:79]}"(acA));
    epi(2, acA, 0); epi(2, acA, 1);
    SBAR(); BAR_L();
    ep = RFL(*(const LDSAS unsigned*)(fa + 48));
    WAITV(0);
    if (lane == 0) __builtin_amdgcn_raw_buffer_store_b32(ep, rs, 0, WS_FQ + ((cb * 2 + wc) * 64 + tt) * 128, AUX_SC1);
    STAMP(0, 5);
    FLUSH(0);
    }
    }

    const int rt = xcd * 4 + (bi >> 3), ct = bi & 7;
    u32x4 hreg[2];
    u32x8 xres;

    auto issue_wo = [&](int first, int n) {
        const rsrc_t rwo = mk_rsrc(wo, D * D * 2);
        const unsigned v = w64_voff(lane);
        for (int p = first; p < first + n; ++p) dma16(rwo, v, p * 8192 + ct * 128, lds0 + L_WO + p * 1024);
    };
    auto issue_dn = [&](int i0, int n, unsigned dst, int hw) {
        const rsrc_t rwd = mk_rsrc(wd, F * D * 2);
        const unsigned v = w64_voff(lane);
        const unsigned so = ((43 * (hw >> 1) + i0) * 16 + (hw & 1) * 8) * 1024 + ct * 128;
        for (int i = 0; i < n; ++i) dma16(rwd, v, so + i * 16384, lds0 + dst + (hw & 1) * 1024 + i * 2048);
    };
    auto gu_wa = [&](int n) -> unsigned { const unsigned blk = (lane >> 4) & 1;
        return lds0 + L_GU + blk * 49152 + (((4 * h + ((lane & 15) >> 2)) * 96 + n * 32 + (lane & 3) * 8) ^ (blk * 128)); };
    float cs[3] = {0.f, 0.f, 0.f};
    auto gu_colsum = [&]() {
        u32x4 f[3][4];
#pragma unroll
        for (int n = 0; n < 3; ++n) {
            const unsigned a0 = gu_wa(n) + w * 4 * 1536;
#pragma unroll
            for (int i = 0; i < 4; ++i) f[n][i] = __builtin_bit_cast(u32x4, lds_tr2(a0 + i * 1536, a0 + i * 1536 + 768));
        }
        SBAR();
#pragma unroll
        for (int i = 0; i < 4; ++i)
#pragma unroll
            for (int e = 0; e < 4; ++e)
#pragma unroll
                for (int n = 0; n < 3; ++n) cs[n] = dot2(f[n][i][e], ONES2, cs[n]);
#pragma unroll
        for (int n = 0; n < 3; ++n) cs[n] = swap_add(cs[n]);
    };

    unsigned budget = 1u << 16;
    const unsigned l16 = lane * 16;

    {
        const int head = xcd * 4 + (bi >> 3), qb = bi & 7, wq = w & 3, g = w >> 2, qt = qb * 8 + wq * 2;
        const unsigned kvs = WS_KV + head * KV_HEAD;
        unsigned ks = kvs + g * 4 * KV_CHUNK + wq * 4096, km = lds0 + g * 32768 + wq * 4096;
        const rsrc_t rg = mk_rsrc(g ? wu : wg, (bi < 29 && ABL_GU != 1) ? D * F * 2 : 0), rwd = mk_rsrc(wd, F * D * 2), rwo = mk_rsrc(wo, D * D * 2), rx = mk_rsrc(x, S * D * 2);
        unsigned gv0, gv1, gv2;
        { unsigned c = lane ^ (g * 8); gv0 = (c / 6) * (F * 2) + (c % 6) * 16; c = (64 + lane) ^ (g * 8); gv1 = (c / 6) * (F * 2) + (c % 6) * 16;
          c = (128 + lane) ^ (g * 8); gv2 = (c / 6) * (F * 2) + (c % 6) * 16; }
        if (ABL_GU == 2) { gv0 = l16; gv1 = l16 + 1024; gv2 = l16 + 2048; }
        unsigned gs = ABL_GU == 2 ? wq * 4 * 32 * (F * 2) + bi * 3072 : wq * 4 * 32 * (F * 2) + bi * 96, gm = lds0 + L_GU + g * 49152 + wq * 12288;
        const unsigned pvd = lane < 43 ? (bi * 344 + w * 43 + lane) * 128 : 0x80000000u, pvo = lane < 16 ? (bi * 128 + w * 16 + lane) * 128 : 0x80000000u;
        const unsigned wv = w64_voff(lane), xv = (rt * 64 + wt * 32 + r32) * 1024 + (ct * 64 + kh * 32 + 4 * h) * 2;
        const unsigned kb = lds0 + g * 32768 + l16, vb = lds0 + g * 32768 + 8192 + (16 * h + (r32 & 15)) * 16;
        const unsigned cst = (r32 & 27) == 16 ? ONES2 : 0u;
        const unsigned long long msk = 0x0000ffff0000ffffull;
        const unsigned fo = WS_O + ((qt + g) * 32 + head) * 1024, fh = WS_H + ((rt * 2 + wt) * 32 + ct * 4 + (w >> 1)) * 1024, fg = WS_HH + ((xcd * 8 + w) * NU + bi * 3) * 1024;
        const unsigned fsv = w == 0 ? WS_ST + ((rt * 2 + h) * 16 + ct * 2) * 256 + r32 * 16 : 0x80000000u, fg2 = bi < 28 ? l16 : 0x80000000u, fg0 = bi < 29 ? l16 : 0x80000000u;
        const unsigned fpv = tid == 0 ? WS_FP + b * 4 : 0x80000000u;
        u32x4 ones = {~0u, ~0u, ~0u, ~0u}, fpl = {0u, 0u, 0u, 0u};
        unsigned pfd = 0, epv = ep;
        xres = u32x8{};
        s16x8 q0, q1;
        STAMP(1, 0);
        SBAR();
#define XKV "s_mov_b32 m0, %[km]\n\ts_add_u32 %[km], %[km], 0x400\n\tbuffer_load_dwordx4 %[l16], %[rs], %[ks] offen sc1 lds\n\ts_add_u32 %[ks], %[ks], 0x400\n\t"
        asm volatile("s_barrier\n\tbuffer_load_dwordx4 v[128:131], %[l16], %[rs], %[qs] offen sc1\n\tbuffer_load_dwordx4 v[156:159], %[l16], %[rs], %[qs] offen offset:1024 sc1\n\t"
                     XKV XKV XKV XKV "s_add_u32 %[km], %[km], 0x3000\n\ts_add_u32 %[ks], %[ks], 0x3000\n\ts_waitcnt vmcnt(0)\n\ts_barrier\n\t" XKV XKV XKV XKV
                     : "={v[128:131]}"(q0), "={v[156:159]}"(q1), [km] "+s"(km), [ks] "+s"(ks)
                     : [l16] "v"(l16), [rs] "s"(rs), [qs] "s"(WS_Q + (head * 64 + qt) * 1024) : "m0", "scc", "memory");
        km -= 16384 + 4096; ks += KV_CHUNK - 4096;
        STAMP(1, 1);
        f32x16 n0, n1, sA, sB = {}, o0 = {}, o1 = {};
        s16x8 kf = lds_b128(kb), v0, v1, v1b;
        {
            const f32x16 z = {};
            const s16x8 kz = lds_b128(lds0 + l16);
            const u32x4 vv = __builtin_bit_cast(u32x4, lds_b128(vb)), cc = {cst, cst, cst, cst};
            const f32x16 s0 = MFMA(kz, q0, z), s1 = MFMA(kz, q1, z);
            float m0 = __builtin_fmaxf(s0[0], s0[1]), m1 = __builtin_fmaxf(s1[0], s1[1]);
#pragma unroll
            for (int i = 2; i < 16; i += 2) { m0 = __builtin_fmaxf(__builtin_fmaxf(m0, s0[i]), s0[i + 1]); m1 = __builtin_fmaxf(__builtin_fmaxf(m1, s1[i]), s1[i + 1]); }
            m0 = swap_max(m0); m1 = swap_max(m1);
#pragma unroll
            for (int i = 0; i < 16; ++i) { n0[i] = -m0; n1[i] = -m1; }
            sA = MFMA(kf, q0, n0);
            v0 = __builtin_bit_cast(s16x8, r32 < 16 ? vv : cc); v1 = v1b = __builtin_bit_cast(s16x8, cc);
        }
        u32x4 pA = {0u, 0u, 0u, 0u}, pB = {0u, 0u, 0u, 0u};
#define XE(C, i) "v_exp_f32 v[" #C "+" #i "], v[" #C "+" #i "]\n\t"
#define XC(P, C, i) "v_cvt_pk_bf16_f32 v" #P ", v[" #C "+" #i "], v[" #C "+" #i "+1]\n\t"
#define XG1(C) XE(C, 0) XE(C, 1) XE(C, 2) XE(C, 3) XC(148, C, 0) XE(C, 4) XE(C, 5) XC(149, C, 2)
#define XG2(C) XE(C, 6) XE(C, 7) XC(150, C, 4) XE(C, 8) XC(151, C, 6) XE(C, 9) XE(C, 10)
#define XG3(C) XE(C, 11) XC(152, C, 8) XE(C, 12) XE(C, 13) XC(153, C, 10) XE(C, 14) XE(C, 15) XC(154, C, 12) XC(155, C, 14)
#define XM(D, A, B, C) "v_mfma_f32_32x32x16_bf16 v[" #D ":" #D "+15], v[" #A ":" #A "+3], v[" #B ":" #B "+3], v[" #C ":" #C "+15]\n\t"
#define XRK(BASE, O) "ds_read_b128 v[132:135], " BASE " offset:" #O "\n\t"
#define XRV(R, BASE, O) "s_mov_b64 exec, %[msk]\n\tds_read_b128 v[" #R ":" #R "+3], " BASE " offset:" #O "\n\ts_mov_b64 exec, -1\n\t"
#define XW(n) "s_waitcnt lgkmcnt(" #n ")\n\t"
#define XTILE(VA, RK, RV1, WA, X1, VB, X2, RV0) \
        XM(80, 132, 156, 176) RK XG1(64) XM(160, VA, 152, 160) RV1 XG2(64) XW(WA) XM(96, 140, 148, 96) XG3(64) \
        XW(0) XM(64, 132, 128, 112) X1 XG1(80) XM(96, VB, 152, 96) X2 XG2(80) XM(160, 140, 148, 160) RV0 XG3(80)
#define XGU(j) "s_mov_b32 m0, %[gm]\n\ts_add_u32 %[gm], %[gm], 0x400\n\tbuffer_load_dwordx4 %[gv" #j "], %[rg], %[gs] offen lds\n\t"
#define XGU2 XGU(2) "s_add_u32 %[gs], %[gs], 88064\n\t"
#define XPF "buffer_load_dword v200, %[pvd], %[rwd], 0 offen\n\tbuffer_load_dword v200, %[pvo], %[rwo], 0 offen\n\t"
#define XWO "s_mov_b32 m0, %[km]\n\ts_add_u32 %[km], %[km], 0x400\n\tbuffer_load_dwordx4 %[wv], %[rwo], %[ks] offen lds\n\ts_add_u32 %[ks], %[ks], 0x2000\n\t"
#define XST(V, SO, O) "buffer_store_dwordx4 v[204:207], " V ", %[rs], " SO " offen offset:" #O " sc1\n\t"
#define XFP "buffer_store_dword v201, %[fpv], %[rs], 0 offen sc1\n\t"
#define XFL "buffer_load_dwordx4 v[208:211], %[l16], %[rs], %[fp] offen sc1\n\t"
#define XXR(R, O) "buffer_load_dwordx2 v[" #R ":" #R "+1], %[xv], %[rx], 0 offen offset:" #O "\n\t"
#define AOPS : "+{v[64:79]}"(sA), "+{v[80:95]}"(sB), "+{v[96:111]}"(o0), "+{v[160:175]}"(o1), "+{v[112:127]}"(n0), "+{v[176:191]}"(n1), "+{v[128:131]}"(q0), "+{v[156:159]}"(q1), \
               "+{v[132:135]}"(kf), "+{v[140:143]}"(v0), "+{v[144:147]}"(v1), "+{v[136:139]}"(v1b), "+{v[148:151]}"(pA), "+{v[152:155]}"(pB), "+{v[192:199]}"(xres), "+{v200}"(pfd), \
               "+{v201}"(epv), "+{v[204:207]}"(ones), "+{v[208:211]}"(fpl), \
               [km] "+s"(km), [ks] "+s"(ks), [gm] "+s"(gm), [gs] "+s"(gs) \
             : [ka] "v"(ka), [va] "v"(va), [kn] "v"(kn), [vn] "v"(vn), [msk] "s"(msk), [l16] "v"(l16), [rs] "s"(rs), [rg] "s"(rg), [gv0] "v"(gv0), [gv1] "v"(gv1), [gv2] "v"(gv2), \
               [pvd] "v"(pvd), [pvo] "v"(pvo), [wv] "v"(wv), [xv] "v"(xv), [rwd] "s"(rwd), [rwo] "s"(rwo), [rx] "s"(rx), \
               [fo] "s"(fo), [fh] "s"(fh), [fg] "s"(fg), [fsv] "v"(fsv), [fg0] "v"(fg0), [fg2] "v"(fg2), [fpv] "v"(fpv), [fp] "s"(WS_FP) : "m0", "scc", "memory"
#define XBAR(n) asm volatile("s_waitcnt vmcnt(" #n ") lgkmcnt(0)\n\ts_barrier" AOPS)
#define XROUND(A1, A2, B1, B2, C1, C2, D1, D2, E1, E2, F1, F2) asm volatile( \
            XTILE(136, XRK("%[ka]", 1024), XRV(144, "%[va]", 512), 2, A1, 144, A2, XRV(140, "%[va]", 1024)) \
            XTILE(144, XRK("%[ka]", 2048), XRV(144, "%[va]", 1536), 2, B1, 144, B2, XRV(140, "%[va]", 2048)) \
            XTILE(144, XRK("%[ka]", 3072), XRV(144, "%[va]", 2560), 2, C1, 144, C2, XRV(140, "%[va]", 3072)) \
            XTILE(144, XRK("%[ka]", 4096), XRV(144, "%[va]", 3584), 2, D1, 144, D2, XRV(140, "%[va]", 4096)) \
            XTILE(144, XRK("%[ka]", 5120), XRV(144, "%[va]", 4608), 2, E1, 144, E2, XRV(140, "%[va]", 5120)) \
            XTILE(144, XRK("%[ka]", 6144), XRV(144, "%[va]", 5632), 2, F1, 144, F2, XRV(140, "%[va]", 6144)) \
            XTILE(144, XRK("%[ka]", 7168), XRV(144, "%[va]", 6656), 2, XRV(136, "%[va]", 7680), 144, "", XRV(140, "%[va]", 7168)) AOPS)
#define XLAST(X1, X2) asm volatile(XTILE(144, XRK("%[kn]", 0), "", 1, X1, 136, X2, XRV(140, "%[vn]", 0)) AOPS)
        unsigned ka = kb, va = vb, kn = kb + 16384, vn = vb + 16384;
        asm volatile("s_nop 15" AOPS);
        STAMP(1, 7);
        XROUND(XGU(0), "", "", XST("%[l16]", "%[fo]", 0), XGU(1), XST("%[fsv]", "0", 0), "", XST("%[l16]", "%[fh]", 0), XGU2, "", XGU(0), "");
        STAMP(1, 8);
        XBAR(7);
        STAMP(1, 9);
        XLAST(XKV, XKV);
        ka = kb + 16384; va = vb + 16384; kn = kb; vn = vb;
        XROUND(XKV, XKV, XGU(1), XST("%[fg0]", "%[fg]", 0), XGU2, "", XST("%[fg0]", "%[fg]", 1024), XGU(0), "", XGU(1), XST("%[fg2]", "%[fg]", 2048), "");
        STAMP(1, 10);
        XBAR(7);
        STAMP(1, 11);
        km += 16384 - 4096; ks += KV_CHUNK - 4096;
        XLAST(XKV, XKV);
        ka = kb; va = vb; kn = kb + 16384; vn = vb + 16384;
        XROUND(XKV, XKV, XGU2, "", XGU(0), "", XGU(1), "", XGU2, "", XPF, "");
        STAMP(1, 12);
        XBAR(6);
        STAMP(1, 13);
        km = lds0 + g * 32768 + wq * 4096; ks = (32 * g + 4 * wq) * 8192 + ct * 128;
        XLAST(XFP, XWO);
        ka = kb + 16384; va = vb + 16384; kn = kb; vn = vb;
        XROUND(XWO, XWO, XWO, "", XXR(192, 0), "", XXR(194, 16), "", XXR(196, 32), "", XXR(198, 48), "");
        XLAST(XFL, "");
        STAMP(1, 14);
        asm volatile(XM(160, 136, 152, 160) "s_waitcnt vmcnt(0) lgkmcnt(0)\n\ts_barrier\n\ts_nop 15" AOPS);
        STAMP(1, 4);
        f32x16 o;
        {
            const f32x16& snd = g ? o0 : o1;
            const unsigned xa = lds0 + g * 32768 + 16384 + wq * 3072 + l16, xb = lds0 + (g ^ 1) * 32768 + 16384 + wq * 3072 + l16;
            *(LDSAS f32x4*)(xa) = f32x4{snd[0], snd[1], snd[2], snd[3]};
            *(LDSAS f32x4*)(xa + 1024) = f32x4{snd[4], snd[5], snd[6], snd[7]};
            *(LDSAS float*)(xa + 2048) = snd[8];
            if (tid == 0) *(LDSAS unsigned*)(lds0 + L_FLAG) = 0u;
            BAR_L();
            const f32x4 p0 = lds_f4(xb), p1 = lds_f4(xb + 1024); const float pl = *(const LDSAS float*)(xb + 2048);
            o = g ? o1 : o0;
#pragma unroll
            for (int i = 0; i < 4; ++i) { o[i] += p0[i]; o[4 + i] += p1[i]; }
            o[8] += pl;
        }
        bool bad = !(o[8] < 1.8446744e19f);
#pragma unroll
        for (int i = 0; i < 8; ++i) bad |= !(__builtin_fabsf(o[i]) < 3.0e38f);
        if (__any(bad) && lane == 0) *(LDSAS unsigned*)(lds0 + L_FLAG) = 1u;
        auto store_o = [&]() {
            const float inv = __builtin_amdgcn_rcpf(o[8]);
            u32x4 ov;
#pragma unroll
            for (int i = 0; i < 4; ++i) ov[i] = pk2(o[2 * i] * inv, o[2 * i + 1] * inv);
            __builtin_amdgcn_raw_buffer_store_b128(ov, rs, l16, WS_O + ((qt + g) * 32 + head) * 1024, AUX_SC1);
        };
        BAR_L();
        if (__builtin_amdgcn_readfirstlane(*(const LDSAS unsigned*)(lds0 + L_FLAG))) {
            BAR_L();
            const s16x8 qf = g ? q1 : q0;
            const u32x4 cc = {cst, cst, cst, cst};
            const unsigned vf = lds0 + 16384 + 8192 + (16 * h + (r32 & 15)) * 16;
            float m = -__builtin_inff();
            o = f32x16{};
            const f32x16 z = {};
#pragma unroll 1
            for (int c = 0; c < 8; ++c) {
                dma16<true>(rs, l16, kvs + c * KV_CHUNK + w * 2048, lds0 + 16384 + w * 2048);
                dma16<true>(rs, l16, kvs + c * KV_CHUNK + w * 2048 + 1024, lds0 + 16384 + w * 2048 + 1024);
                BAR_VL(0);
#pragma unroll 1
                for (int t = 0; t < 8; ++t) {
                    f32x16 s = MFMA(lds_b128(lds0 + 16384 + l16 + t * 1024), qf, z);
                    float mx = s[0];
#pragma unroll
                    for (int i = 1; i < 16; ++i) mx = __builtin_fmaxf(mx, s[i]);
                    mx = __builtin_fmaxf(swap_max(mx), m);
                    const float al = __builtin_amdgcn_exp2f(m - mx);
                    m = mx;
#pragma unroll
                    for (int i = 0; i < 16; ++i) { o[i] *= al; s[i] = __builtin_amdgcn_exp2f(s[i] - m); }
#pragma unroll
                    for (int i = 0; i < 4; ++i) { pA[i] = pk2(s[2 * i], s[2 * i + 1]); pB[i] = pk2(s[8 + 2 * i], s[9 + 2 * i]); }
                    const u32x4 a = __builtin_bit_cast(u32x4, lds_b128(vf + t * 1024)), c2 = __builtin_bit_cast(u32x4, lds_b128(vf + t * 1024 + 512));
                    o = MFMA(__builtin_bit_cast(s16x8, r32 < 16 ? a : cc), __builtin_bit_cast(s16x8, pA), o);
                    o = MFMA(__builtin_bit_cast(s16x8, r32 < 16 ? c2 : cc), __builtin_bit_cast(s16x8, pB), o);
                }
                BAR_L();
            }
        }
        store_o();
        STAMP(1, 5);
        BAR_L();
        if (w >= 4) issue_wo(16 + ((w - 4) >> 1) * 32 + (w & 1) * 8, 8);
        gu_colsum();
#pragma unroll 1
        for (unsigned spins = 0; spins < SPIN_CAP && !__all(fpl[0] == ep && fpl[1] == ep && fpl[2] == ep && fpl[3] == ep); ++spins) {
            __builtin_amdgcn_s_sleep(2);
            fpl = __builtin_amdgcn_raw_buffer_load_b128(rs, l16, WS_FP, AUX_SC1);
        }
        STAMP(1, 6);
        FLUSH(1);
    }

    {
        STAMP(2, 0);
        f32x16 a0, a1;
        if (w < 4) {
            const unsigned t0 = lds0 + L_WO + kh * 32768 + w64_tr(lane, kh), t1 = lds0 + L_WO + kh * 32768 + w64_tr(lane, kh ^ 1);
            G2 g2;
            g2.tb[0] = g2.tb[2] = g2.tb[4] = t0; g2.tb[1] = g2.tb[3] = g2.tb[5] = t1;
            g2.sa = g2.s0 = RFL(WS_O + ((rt * 2 + wt) * 32 + kh * 16) * 1024); g2.l16 = l16; g2.budget = budget; g2.rs = rs; g2.a1 = f32x16{};
#pragma unroll
            for (int i = 0; i < 8; ++i) { g2.a0[2 * i] = bflo(xres[i]); g2.a0[2 * i + 1] = bfhi(xres[i]); }
            __builtin_amdgcn_s_sleep(DELAY_O);
            STAMP(2, 1);
            g2_steps<0, 16, 0, 6, -1>(g2);
            a0 = g2.a0; a1 = g2.a1; budget = g2.budget;
        } else BAR_VL(0);
        STAMP(2, 2);
        BAR_L();
        if (w < 4) {
#pragma unroll
            for (int g = 0; g < 4; ++g) *(LDSAS f32x4*)(lds0 + w * 4096 + g * 1024 + lane * 16) = f32x4{a1[4 * g], a1[4 * g + 1], a1[4 * g + 2], a1[4 * g + 3]};
        }
#pragma unroll
        for (int n = 0; n < 3; ++n) *(LDSAS float*)(lds0 + L_TAB + (w * 96 + n * 32 + r32) * 4) = cs[n];
        BAR_L();
        if (w >= 4) {
            if (tid < 256 + 96) {
                float t[8];
#pragma unroll
                for (int i = 0; i < 8; ++i) t[i] = *(const LDSAS float*)(lds0 + L_TAB + (i * 96 + tid - 256) * 4);
                *(LDSAS float*)(lds0 + L_TAB + 3072 + (tid - 256) * 4) = ((t[0] + t[1]) + (t[2] + t[3])) + ((t[4] + t[5]) + (t[6] + t[7]));
            }
        } else {
            f32x4 p[4];
#pragma unroll
            for (int g = 0; g < 4; ++g) p[g] = lds_f4(lds0 + (w ^ 2) * 4096 + g * 1024 + lane * 16);
            SBAR();
#pragma unroll
            for (int g = 0; g < 4; ++g) {
                hreg[g >> 1][(g & 1) * 2] = pk2(a0[4 * g] + p[g][0], a0[4 * g + 1] + p[g][1]);
                hreg[g >> 1][(g & 1) * 2 + 1] = pk2(a0[4 * g + 2] + p[g][2], a0[4 * g + 3] + p[g][3]);
            }
            const unsigned so = WS_H + ((rt * 2 + wt) * 32 + (ct * 2 + kh) * 2) * 1024;
            __builtin_amdgcn_raw_buffer_store_b128(hreg[0], rs, lane * 16, so, AUX_SC1);
            __builtin_amdgcn_raw_buffer_store_b128(hreg[1], rs, lane * 16 + 1024, so, AUX_SC1);
            SBAR();
            float sm = 0.f, sq = 0.f;
#pragma unroll
            for (int i = 0; i < 8; ++i) { sm = dot2(hreg[i >> 2][i & 3], ONES2, sm); sq = dot2(hreg[i >> 2][i & 3], hreg[i >> 2][i & 3], sq); }
            sm = swap_add(sm); sq = swap_add(sq);
            if (h == 0) __builtin_amdgcn_raw_buffer_store_b64(u32x2{__float_as_uint(sm), __float_as_uint(sq)}, rs, r32 * 8,
                                                              WS_ST + ((rt * 2 + wt) * 16 + ct * 2 + kh) * 256, AUX_SC1);
        }
        STAMP(2, 3);
        BAR_L();
        if (w >= 4) { issue_dn(0, 15, ((w - 4) >> 1) * 15 * 2048, w - 4); if (bi >= 29) issue_dn(15, 24, L_GU + ((w - 4) >> 1) * 49152, w - 4); }
        STAMP(2, 5);
        FLUSH(2);
    }

    {
        STAMP(3, 0);
        if (bi < 29) {
            const int tt = xcd * 8 + w;
            GU gu;
            gu.a0 = gu.a1 = gu.a2 = f32x16{};
            gu.sa = gu.s0 = RFL(WS_H + tt * 32768); gu.sv = 8 * h * 256 + r32 * 8; gu.ss = RFL(WS_ST + tt * 4096); gu.l16 = l16; gu.budget = budget; gu.rs = rs;
            gu.dm = RFL(lds0 + L_GU + (w >> 2) * 49152 + (w & 3) * 6144); gu.ds = RFL(((43 * (w >> 2) + 15) * 16 + (w & 3) * 48) * 1024 + ct * 128);
            gu.dv = w64_voff(lane); gu.rd = mk_rsrc(wd, F * D * 2);
            gu.wa[0] = gu_wa(0); gu.wa[1] = gu_wa(1); gu.wa[2] = gu_wa(2);
            f32x4 ctab[3][4];
#pragma unroll
            for (int n = 0; n < 3; ++n)
#pragma unroll
                for (int g = 0; g < 4; ++g) ctab[n][g] = lds_f4(lds0 + L_TAB + 3072 + (n * 32 + 8 * g + 4 * h) * 4);
            if (w < 4) __builtin_amdgcn_s_sleep(DELAY_H);
            STAMP(3, 2);
            gu_steps<0>(gu);
            budget = gu.budget;
            const f32x16 acc[3] = {gu.a0, gu.a1, gu.a2};
            u32x16 st = gu.st;
            const unsigned sv = gu.sv, ss = gu.ss;
            STAMP(3, 3);
#pragma unroll 1
            for (;;) {
                unsigned mx = st[0];
#pragma unroll
                for (int i = 1; i < 16; ++i) mx = st[i] > mx ? st[i] : mx;
                if (!__any(mx == ~0u) || budget == 0) break;
                --budget;
#pragma unroll
                for (int i = 0; i < 8; ++i) { const u32x2 v = __builtin_amdgcn_raw_buffer_load_b64(rs, sv + i * 256, ss, AUX_SC1); st[2 * i] = v[0]; st[2 * i + 1] = v[1]; }
            }
            float sm = 0.f, sq = 0.f;
#pragma unroll
            for (int i = 0; i < 8; ++i) { sm += __uint_as_float(st[2 * i]); sq += __uint_as_float(st[2 * i + 1]); }
            sm = swap_add(sm); sq = swap_add(sq);
            const float mean = sm * (1.f / D);
            const float var = __builtin_fmaxf(sq * (1.f / D) - mean * mean, 0.f) + LN_EPS;
            const float rstd = __builtin_amdgcn_rsqf(var), sd = var * rstd, ga = -LOG2E * rstd;
            auto epi = [&](int n) {
                u32x4 ov; float y[8];
#pragma unroll
                for (int g = 0; g < 2; ++g) {
                    const f32x4 cg = ctab[n][g], cu = ctab[n][2 + g];
#pragma unroll
                    for (int i = 0; i < 4; ++i) {
                        const float e = __builtin_amdgcn_exp2f(__builtin_fmaf(-mean, cg[i], acc[n][4 * g + i]) * ga);
                        y[4 * g + i] = __builtin_fmaf(-mean, cu[i], acc[n][8 + 4 * g + i]) * __builtin_amdgcn_rcpf(__builtin_fmaf(e, sd, sd));
                    }
                }
#pragma unroll
                for (int i = 0; i < 4; ++i) ov[i] = pk2(y[2 * i], y[2 * i + 1]);
                const int u = bi * 3 + n;
                if (u < NU) __builtin_amdgcn_raw_buffer_store_b128(ov, rs, lane * 16, WS_HH + (tt * NU + u) * 1024, AUX_SC1);
            };
            epi(0); epi(1);
            epi(2);
        }
        STAMP(3, 4);
        if (bi >= 29) WAITV(0);
        BAR_L();
        STAMP(3, 5);
        if (w >= 4) {
            if (bi < 29) issue_dn(27, 12, L_GU + ((w - 4) >> 1) * 49152 + 24576, w - 4);
            issue_dn(39, 1, L_TAB + ((w - 4) >> 1) * 2048, w - 4); STAMP(3, 6);
        }
        FLUSH(3);
    }

    {
        STAMP(4, 0);
        if (b == 0 && tid == 0) __builtin_amdgcn_raw_buffer_store_b32(ep, rs, 0, WS_EP, AUX_SC1);
        f32x16 a0, a1;
        if (w < 4) {
            const unsigned t0 = lds0 + w64_tr(lane, kh), t1 = lds0 + w64_tr(lane, kh ^ 1);
            G2 g2;
            g2.tb[0] = t0 + kh * 15 * 2048; g2.tb[1] = t1 + kh * 15 * 2048; g2.tb[2] = t0 + L_GU + kh * 24 * 2048; g2.tb[3] = t1 + L_GU + kh * 24 * 2048;
            g2.tb[4] = t0 + L_TAB + kh * 2048; g2.tb[5] = t1 + L_TAB + kh * 2048;
            g2.sa = g2.s0 = RFL(WS_HH + ((rt * 2 + wt) * NU + kh * 43) * 1024); g2.l16 = l16; g2.budget = budget; g2.rs = rs; g2.a1 = f32x16{};
#pragma unroll
            for (int i = 0; i < 8; ++i) { g2.a0[2 * i] = bflo(hreg[i >> 2][i & 3]); g2.a0[2 * i + 1] = bfhi(hreg[i >> 2][i & 3]); }
            __builtin_amdgcn_s_sleep(DELAY_G);
            STAMP(4, 1);
            g2_steps<0, 43, 1, 25, 38>(g2);
            a0 = g2.a0; a1 = g2.a1;
        } else {
            BAR_VL(0);
            issue_dn(40, 3, ((w - 4) >> 1) * 15 * 2048, w - 4);
            BAR_VL(0);
        }
        STAMP(4, 2);
        BAR_L();
        if (w < 4) {
#pragma unroll
            for (int g = 0; g < 4; ++g) *(LDSAS f32x4*)(lds0 + w * 4096 + g * 1024 + lane * 16) = f32x4{a1[4 * g], a1[4 * g + 1], a1[4 * g + 2], a1[4 * g + 3]};
        }
        BAR_L();
        if (w < 4) {
            f32x4 p[4];
#pragma unroll
            for (int g = 0; g < 4; ++g) p[g] = lds_f4(lds0 + (w ^ 2) * 4096 + g * 1024 + lane * 16);
            SBAR();
            unsigned y[8];
#pragma unroll
            for (int g = 0; g < 4; ++g) {
                y[2 * g] = pk2(a0[4 * g] + p[g][0], a0[4 * g + 1] + p[g][1]);
                y[2 * g + 1] = pk2(a0[4 * g + 2] + p[g][2], a0[4 * g + 3] + p[g][3]);
            }
            const rsrc_t ro = mk_rsrc(out, S * D * 2);
            const unsigned vo = (rt * 64 + wt * 32 + r32) * 1024 + (ct * 64 + kh * 32 + 8 * h) * 2;
#pragma unroll
            for (int g = 0; g < 2; ++g) {
                auto e0 = __builtin_amdgcn_permlane32_swap(y[4 * g], y[4 * g + 2], false, false);
                auto e1 = __builtin_amdgcn_permlane32_swap(y[4 * g + 1], y[4 * g + 3], false, false);
                __builtin_amdgcn_raw_buffer_store_b128(u32x4{e0[0], e1[0], e0[1], e1[1]}, ro, vo + g * 32, 0, 0);
            }
        }
        STAMP(4, 3);
        STAMP(4, 4);
        FLUSH(4);
        asm volatile(".subsection 2\n.Lmain_end:\n\t.fill 64, 4, 0xBF800000\n\t.subsection 0");
    }
}

extern "C" void kernel_launch(void* const* d_in, const int* in_sizes, int n_in,
                              void* d_out, int out_size, void* d_ws, size_t ws_size,
                              hipStream_t stream) {
    const u16* x = (const u16*)d_in[0];
    const u16* wqkv = (const u16*)d_in[1];
    const u16* wo = (const u16*)d_in[2];
    const u16* wg = (const u16*)d_in[4];
    const u16* wu = (const u16*)d_in[5];
    const u16* wd = (const u16*)d_in[6];
    u16* out = (u16*)d_out;
    char* ws = (char*)d_ws;
    (void)hipFuncSetAttribute((const void*)k_block, hipFuncAttributeMaxDynamicSharedMemorySize, K2_LDS);
    k_block<<<256, 512, K2_LDS, stream>>>(x, wqkv, wo, wg, wu, wd, out, ws);
}
```

```cpp
#include <hip/hip_runtime.h>
#include <hip/hip_bf16.h>
#include <stdint.h>
#pragma clang diagnostic ignored "-Winline-asm"

typedef short s16x8 __attribute__((ext_vector_type(8)));
typedef short s16x4 __attribute__((ext_vector_type(4)));
typedef float f32x16 __attribute__((ext_vector_type(16)));
typedef float f32x4 __attribute__((ext_vector_type(4)));
typedef float f32x2 __attribute__((ext_vector_type(2)));
typedef unsigned u32x4 __attribute__((ext_vector_type(4)));
typedef unsigned u32x2 __attribute__((ext_vector_type(2)));
typedef unsigned u32x8 __attribute__((ext_vector_type(8)));
typedef __bf16 bf16x2 __attribute__((ext_vector_type(2)));
typedef unsigned short u16;
#define LDSAS __attribute__((address_space(3)))
#define GAS __attribute__((address_space(1)))
typedef __amdgpu_buffer_rsrc_t rsrc_t;

constexpr int S = 2048, D = 512, NH = 32, F = 1376, NU = F / 16;
constexpr float LN_EPS = 1e-5f;
constexpr float LOG2E = 1.4426950408889634f;

constexpr unsigned KV_CHUNK = 16384;
constexpr unsigned KV_HEAD = 8 * KV_CHUNK;
constexpr unsigned WS_EP = 0;
constexpr unsigned WS_FQ = 4096;
constexpr unsigned WS_FP = WS_FQ + 16 * 64 * 128;
constexpr unsigned WS_Q = WS_FP + 4096;
constexpr unsigned WS_KV = WS_Q + NH * 64 * 1024;
constexpr unsigned WS_O = WS_KV + NH * KV_HEAD;
constexpr unsigned WS_H = WS_O + 64 * 32 * 1024;
constexpr unsigned WS_ST = WS_H + 64 * 32 * 1024;
constexpr unsigned WS_HH = WS_ST + 64 * 16 * 256;
constexpr unsigned WS_END = WS_HH + 64 * NU * 1024;
constexpr int AUX_SC1 = 16;
#define STAMP_DECL
#define FLUSH(k) do {} while (0)
#define STAMP(k, i) do {} while (0)

#define STAMPV(i, v) do {} while (0)
#define SBAR() __builtin_amdgcn_sched_barrier(0)
#define RFL(x) ((unsigned)__builtin_amdgcn_readfirstlane(x))
#define PIN(x) asm volatile("" : "+v"(x))
#define MFMA(a, b, c) __builtin_amdgcn_mfma_f32_32x32x16_bf16((a), (b), (c), 0, 0, 0)
#define WAITV(n) asm volatile("s_waitcnt vmcnt(" #n ")" ::: "memory")
#define BAR_L() asm volatile("s_waitcnt lgkmcnt(0)\n\ts_barrier" ::: "memory")
#define BAR_0() asm volatile("s_barrier" ::: "memory")
#define BAR_VL(n) asm volatile("s_waitcnt vmcnt(" #n ") lgkmcnt(0)\n\ts_barrier" ::: "memory")

__device__ __forceinline__ rsrc_t mk_rsrc(const void* p, unsigned bytes) {
    return __builtin_amdgcn_make_buffer_rsrc((void*)p, (short)0, (int)bytes, 0x00020000);
}
template <bool SC1 = false> __device__ __forceinline__ void dma16(rsrc_t r, unsigned voff, unsigned soff, unsigned lds) {
    if constexpr (SC1) asm volatile("s_nop 4\n\ts_mov_b32 m0, %0\n\ts_nop 0\n\tbuffer_load_dwordx4 %1, %2, %3 offen sc1 lds"
                 :: "s"(__builtin_amdgcn_readfirstlane(lds)), "v"(voff), "s"(r), "s"(__builtin_amdgcn_readfirstlane(soff)) : "m0", "memory");
    else asm volatile("s_nop 4\n\ts_mov_b32 m0, %0\n\ts_nop 0\n\tbuffer_load_dwordx4 %1, %2, %3 offen lds"
                 :: "s"(__builtin_amdgcn_readfirstlane(lds)), "v"(voff), "s"(r), "s"(__builtin_amdgcn_readfirstlane(soff)) : "m0", "memory");
}
__device__ __forceinline__ unsigned pk2(float lo, float hi) {
    f32x2 v = {lo, hi};
    return __builtin_bit_cast(unsigned, __builtin_convertvector(v, bf16x2));
}
__device__ __forceinline__ float dot2(unsigned a, unsigned b, float c) {
    return __builtin_amdgcn_fdot2_f32_bf16(__builtin_bit_cast(bf16x2, a), __builtin_bit_cast(bf16x2, b), c, false);
}
__device__ __forceinline__ float swap_add(float v) {
    auto r = __builtin_amdgcn_permlane32_swap(__float_as_uint(v), __float_as_uint(v), false, false);
    return __uint_as_float(r[0]) + __uint_as_float(r[1]);
}
__device__ __forceinline__ float swap_max(float v) {
    auto r = __builtin_amdgcn_permlane32_swap(__float_as_uint(v), __float_as_uint(v), false, false);
    return __builtin_fmaxf(__uint_as_float(r[0]), __uint_as_float(r[1]));
}
__device__ __forceinline__ s16x8 lds_b128(unsigned a) { return *(const LDSAS s16x8*)(a); }
__device__ __forceinline__ f32x4 lds_f4(unsigned a) { return *(const LDSAS f32x4*)(a); }
__device__ __forceinline__ s16x8 lds_tr2(unsigned a0, unsigned a1) {
    s16x4 lo = __builtin_amdgcn_ds_read_tr16_b64_v4i16((LDSAS s16x4*)(a0));
    s16x4 hi = __builtin_amdgcn_ds_read_tr16_b64_v4i16((LDSAS s16x4*)(a1));
    return __builtin_shufflevector(lo, hi, 0, 1, 2, 3, 4, 5, 6, 7);
}
__device__ __forceinline__ float bflo(unsigned v) { return __uint_as_float(v << 16); }
__device__ __forceinline__ float bfhi(unsigned v) { return __uint_as_float(v & 0xffff0000u); }
constexpr unsigned ONES2 = 0x3f803f80u;
template <int I> struct ic { static constexpr int value = I; constexpr operator int() const { return I; } };
template <int B, int E, class Fn> __device__ __forceinline__ void sfor(Fn&& fn) { if constexpr (B < E) { fn(ic<B>{}); sfor<B + 1, E>(fn); } }

#define K1_X_AUX 2
#define K1_ABL 0
#define K1_ST_AUX 16
constexpr unsigned K1_W = 65536, K1_FLG = K1_W + 5 * 16384, K1_ST = K1_FLG + 512, K1_CS = K1_ST + 512;
__host__ __device__ constexpr unsigned k1_slot(int s) { return s < 5 ? K1_W + s * 16384 : s == 5 ? K1_W : s < 10 ? (s - 6) * 16384 : K1_W + (s - 9) * 16384; }
__device__ __forceinline__ void lds_spin_ge(unsigned addr, unsigned want) {
#pragma unroll 1
    for (unsigned n = 0; n < (1u << 22); ++n) {
        unsigned v; asm volatile("ds_read_b32 %0, %1\n\ts_waitcnt lgkmcnt(0)" : "=v"(v) : "v"(addr) : "memory");
        if ((unsigned)__builtin_amdgcn_readfirstlane(v) >= want) break;
        __builtin_amdgcn_s_sleep(1);
    }
}
__device__ __forceinline__ void lds_post(unsigned addr, unsigned v) {
    asm volatile("s_waitcnt lgkmcnt(0)\n\tds_write_b32 %0, %1" :: "v"(addr), "v"(v) : "memory");
}
__device__ __forceinline__ void lds_inc(unsigned addr, int lane) {
    asm volatile("s_waitcnt lgkmcnt(0)" ::: "memory");
    if (lane == 0) asm volatile("ds_add_u32 %0, %1" :: "v"(addr), "v"(1u) : "memory");
}
typedef unsigned u32x16 __attribute__((ext_vector_type(16)));
template <int CTRL> __device__ __forceinline__ float dpp_add(float v) {
    return v + __builtin_bit_cast(float, __builtin_amdgcn_update_dpp(0, __builtin_bit_cast(int, v), CTRL, 0xf, 0xf, true));
}

constexpr unsigned K2_LDS = 163840;
constexpr unsigned L_WO = 0, L_GU = 65536;
constexpr unsigned L_TAB = 61440;
constexpr unsigned L_FLAG = 16384 + 12288;
constexpr unsigned SPIN_CAP = 1u << 20;
#define POLL_DELAY_Q 24
#define OUT_AUX 2
#define DN_B0 20
#define ABL_GU 0
#define DELAY_O 0
#define DELAY_H 8
#define DELAY_G 8
constexpr int GU_AW = 8, G2_AW = 12;
#define TAG_T1 "v_max3_u32 v120, v[%c[cr]], v[%c[cr]+1], v[%c[cr]+2]\n\t"
#define TAG_T2 "v_max_u32 v120, v120, v[%c[cr]+3]\n\tv_cmp_eq_u32 vcc, -1, v120\n\t"
#define TAG_RELOAD(RB, AW) "s_add_u32 %[t], %[s0], (tagj>>2)*4096\n\tbuffer_load_dwordx4 v[" #RB "+4*(tagj%%" #AW "):" #RB "+3+4*(tagj%%" #AW ")], %[l16], %[rs], %[t] offen offset:(tagj&3)*1024 sc1\n\t"
#define TAG_T3(RB, AW) \
    "s_cbranch_vccnz 5f\n6:\n\t" \
    ".subsection 1\n5:\n\ts_cmp_eq_u32 %[bud], 0\n\ts_cbranch_scc1 6b\n\ts_sub_u32 %[bud], %[bud], 1\n\t.set tagj, %c[ci]\n\t" TAG_RELOAD(RB, AW) \
    "s_waitcnt vmcnt(0)\n\t" TAG_T1 TAG_T2 "s_cbranch_vccnz 5b\n\t.rept %c[cnt]-1\n\t.set tagj, tagj+1\n\t" TAG_RELOAD(RB, AW) ".endr\n\ts_branch 6b\n\t.subsection 0\n\t"
#define TAG_LOAD "buffer_load_dwordx4 v[%c[lr]:%c[lr]+3], %[l16], %[rs], %[sa] offen offset:%c[lo] sc1\n\t.if %c[lo] == 3072\n\ts_add_u32 %[sa], %[sa], 0x1000\n\t.endif\n\t"

__device__ __forceinline__ void wait_flags(rsrc_t rs, unsigned base, int n, unsigned ep, int lane) {
    const unsigned v = (lane < n ? lane : n - 1) * 128;
#pragma unroll 1
    for (unsigned spins = 0; spins < SPIN_CAP; ++spins) {
        const unsigned f = __builtin_amdgcn_raw_buffer_load_b32(rs, v, base, AUX_SC1);
        if (__all(f == ep)) break;
        __builtin_amdgcn_s_sleep(1);
    }
    asm volatile("" ::: "memory");
}

__device__ __forceinline__ unsigned w64_voff(int lane) { return (lane >> 3) * 1024 + (((lane & 7) ^ (((lane >> 4) & 1) << 2)) << 4); }
__device__ __forceinline__ unsigned w64_tr(int lane, int n) {
    const int q = (lane & 15) >> 2, p = lane & 3, blk = (lane >> 4) & 1, h = lane >> 5;
    return (4 * h + q) * 128 + (((4 * n + 2 * blk + (p >> 1)) ^ (((q >> 1) & 1) << 2)) << 4) + 8 * (p & 1);
}

struct G2 { u32x16 r0, r1, r2; u32x8 w0, w1, w2; f32x16 a0, a1; unsigned tb[6], sa, s0, l16, budget; rsrc_t rs;
};
__host__ __device__ constexpr int g2_w(int DN, int n) {
    return !DN ? n * 2048 : n < 15 ? n * 2048 : n < 39 ? 65536 + (n - 15) * 2048 : n < 40 ? 131072 : (n - 40) * 2048;
}
template <int i, int N, int DN, int B0, int B1> __device__ __forceinline__ void g2_steps(G2& g) {
    constexpr int AW = G2_AW, j = i + AW - 1, n = i + 2, sel = n < N ? g2_w(DN, n) >> 16 : 3;
    unsigned tq;
#define G2_RD(P, R) "ds_read_b64_tr_b16 v[%c[nr]+" #R ":%c[nr]+" #R "+1], %[" P "] offset:%c[no]\n\tds_read_b64_tr_b16 v[%c[nr]+" #R "+2:%c[nr]+" #R "+3], %[" P "] offset:%c[no]+1024\n\t"
#define G2_RDS(P0, P1, P2, R) ".if %c[sel] == 0\n\t" G2_RD(P0, R) ".endif\n\t.if %c[sel] == 1\n\t" G2_RD(P1, R) ".endif\n\t.if %c[sel] == 2\n\t" G2_RD(P2, R) ".endif\n\t"
    if constexpr (i == 0)
        asm volatile(".set tagj, 0\n\t.rept 11\n\tbuffer_load_dwordx4 v[128+4*tagj:131+4*tagj], %[l16], %[rs], %[sa] offen offset:(tagj&3)*1024 sc1\n\t"
                     ".if (tagj&3) == 3\n\ts_add_u32 %[sa], %[sa], 0x1000\n\t.endif\n\t.set tagj, tagj+1\n\t.endr\n\t"
                     ".set tagj, 0\n\t.rept 2\n\tds_read_b64_tr_b16 v[96+8*tagj:97+8*tagj], %[b0] offset:tagj*2048\n\tds_read_b64_tr_b16 v[98+8*tagj:99+8*tagj], %[b0] offset:tagj*2048+1024\n\t"
                     "ds_read_b64_tr_b16 v[100+8*tagj:101+8*tagj], %[b1] offset:tagj*2048\n\tds_read_b64_tr_b16 v[102+8*tagj:103+8*tagj], %[b1] offset:tagj*2048+1024\n\t.set tagj, tagj+1\n\t.endr\n\t"
                     "s_waitcnt vmcnt(10) lgkmcnt(4)\n\t" TAG_T1 TAG_T2 TAG_T3(128, 12)
                     : "={v[128:143]}"(g.r0), "={v[144:159]}"(g.r1), "={v[160:175]}"(g.r2), "={v[96:103]}"(g.w0), "={v[104:111]}"(g.w1), "={v[112:119]}"(g.w2), [sa] "+s"(g.sa),
                       "+{v[64:79]}"(g.a0), "+{v[80:95]}"(g.a1), [bud] "+s"(g.budget), [t] "=&s"(tq)
                     : [l16] "v"(g.l16), [rs] "s"(g.rs), [s0] "s"(g.s0), [b0] "v"(g.tb[0]), [b1] "v"(g.tb[1]), [cr] "n"(128), [ci] "n"(0), [cnt] "n"(AW - 1) : "vcc", "scc", "v120", "memory");
    asm volatile("v_mfma_f32_32x32x16_bf16 v[64:79], v[%c[wr]:%c[wr]+3], v[%c[ar]:%c[ar]+3], v[64:79]\n\t"
                 ".if %c[ld]\n\t" TAG_LOAD ".endif\n\t"
                 ".if %c[bar]\n\ts_waitcnt lgkmcnt(0)\n\ts_barrier\n\t.endif\n\t"
                 G2_RDS("b0", "b2", "b4", 0)
                 ".if %c[cnt]\n\ts_waitcnt vmcnt(%c[vn])\n\t" TAG_T1 ".endif\n\t"
                 "v_mfma_f32_32x32x16_bf16 v[80:95], v[%c[wr]+4:%c[wr]+7], v[%c[ar]:%c[ar]+3], v[80:95]\n\t"
                 G2_RDS("b1", "b3", "b5", 4)
                 ".if %c[cnt]\n\t" TAG_T2 "s_waitcnt lgkmcnt(%c[ln])\n\t" TAG_T3(128, 12) ".else\n\ts_nop 15\n\t.endif"
                 : "+{v[64:79]}"(g.a0), "+{v[80:95]}"(g.a1), "+{v[128:143]}"(g.r0), "+{v[144:159]}"(g.r1), "+{v[160:175]}"(g.r2), "+{v[96:103]}"(g.w0), "+{v[104:111]}"(g.w1), "+{v[112:119]}"(g.w2),
                   [sa] "+s"(g.sa), [bud] "+s"(g.budget), [t] "=&s"(tq)
                 : [l16] "v"(g.l16), [rs] "s"(g.rs), [s0] "s"(g.s0), [b0] "v"(g.tb[0]), [b1] "v"(g.tb[1]), [b2] "v"(g.tb[2]), [b3] "v"(g.tb[3]), [b4] "v"(g.tb[4]), [b5] "v"(g.tb[5]),
                   [ld] "n"(j < N ? 1 : 0), [lr] "n"(128 + 4 * (j % AW)), [lo] "n"((j & 3) * 1024), [bar] "n"(i == B0 || i == B1 ? 1 : 0), [sel] "n"(sel),
                   [nr] "n"(96 + 8 * (n % 3)), [no] "n"(n < N ? g2_w(DN, n) & 65535 : 0), [vn] "n"(j < N ? AW - 2 : i + 1 < N ? N - 2 - i : 0), [ln] "n"(n < N ? 4 : 0),
                   [ar] "n"(128 + 4 * (i % AW)), [wr] "n"(96 + 8 * (i % 3)), [cr] "n"(128 + 4 * ((i + 1) % AW)), [ci] "n"(i + 1), [cnt] "n"(j < N ? AW - 1 : N - 1 - i) : "vcc", "scc", "v120", "memory");
    if constexpr (i + 1 < N) g2_steps<i + 1, N, DN, B0, B1>(g);
}
__host__ __device__ constexpr int gu_dma(int s) { return (s >= 16 && s < 24 && !(s & 1)) || (s >= 24 && s < 30); }
__host__ __device__ constexpr int gu_ops(int s) { return (s + GU_AW - 1 < 32 ? 1 : 0) + (s >= 24 ? 1 : 0) + gu_dma(s); }
__host__ __device__ constexpr int gu_younger(int k) {
    int c = 0, s = k - GU_AW + 1;
    if (s < 0) { c = GU_AW - 2 - k; s = 0; } else { c = gu_ops(s) - 1; ++s; }
    for (; s < k; ++s) c += gu_ops(s);
    return c;
}

struct GU { u32x16 r0, r1, st; u32x8 w0, w1, w2; f32x16 a0, a1, a2; unsigned wa[3], sa, s0, sv, ss, l16, budget, dm, ds, dv, dm2, ds2, dm3, ds3; rsrc_t rs, rd; };
template <int i> __device__ __forceinline__ void gu_steps(GU& g) {
    constexpr int AW = GU_AW, j = i + AW - 1, n = i + 1;
    unsigned tq;
#define GU_RD(R, B) "ds_read_b64_tr_b16 v[" R ":" R "+1], %[" B "] offset:%c[no]\n\tds_read_b64_tr_b16 v[" R "+2:" R "+3], %[" B "] offset:%c[no]+768\n\t"
    if constexpr (i == 0)
        asm volatile(".set tagj, 0\n\t.rept 7\n\tbuffer_load_dwordx4 v[140+4*tagj:143+4*tagj], %[l16], %[rs], %[sa] offen offset:(tagj&3)*1024 sc1\n\t"
                     ".if (tagj&3) == 3\n\ts_add_u32 %[sa], %[sa], 0x1000\n\t.endif\n\t.set tagj, tagj+1\n\t.endr\n\t" GU_RD("112", "b0") GU_RD("116", "b1") GU_RD("124", "b2")
                     "s_waitcnt vmcnt(6) lgkmcnt(0)\n\t" TAG_T1 TAG_T2 TAG_T3(140, 8)
                     : "={v[140:155]}"(g.r0), "={v[156:171]}"(g.r1), "={v[172:187]}"(g.st), "={v[112:119]}"(g.w0), "={v[124:131]}"(g.w1), "={v[132:139]}"(g.w2), [sa] "+s"(g.sa),
                       "+{v[64:79]}"(g.a0), "+{v[80:95]}"(g.a1), "+{v[96:111]}"(g.a2), [bud] "+s"(g.budget), [t] "=&s"(tq)
                     : [l16] "v"(g.l16), [rs] "s"(g.rs), [s0] "s"(g.s0), [b0] "v"(g.wa[0]), [b1] "v"(g.wa[1]), [b2] "v"(g.wa[2]), [no] "n"(0), [cr] "n"(140), [ci] "n"(0), [cnt] "n"(AW - 1)
                     : "vcc", "scc", "v120", "memory");
    asm volatile(".if %c[i] == 15 || %c[i] == 23\n\ts_barrier\n\t.endif\n\t"
                 "v_mfma_f32_32x32x16_bf16 v[64:79], v[%c[c0]:%c[c0]+3], v[%c[ar]:%c[ar]+3], v[64:79]\n\t"
                 ".if %c[rd]\n\t" GU_RD("%c[n0]", "b0") GU_RD("%c[n1]", "b1") ".endif\n\t"
                 "v_mfma_f32_32x32x16_bf16 v[80:95], v[%c[c1]:%c[c1]+3], v[%c[ar]:%c[ar]+3], v[80:95]\n\t"
                 ".if %c[rd]\n\t" GU_RD("%c[n2]", "b2") ".endif\n\t"
                 ".if %c[ld]\n\t" TAG_LOAD ".endif\n\t"
                 ".if %c[sk] >= 0\n\tbuffer_load_dwordx2 v[172+2*%c[sk]:173+2*%c[sk]], %[sv], %[rs], %[ss] offen offset:%c[sk]*256 sc1\n\t.endif\n\t"
                 ".if %c[i] == 26\n\ts_mov_b32 %[dm], %[dm2]\n\ts_mov_b32 %[ds], %[ds2]\n\t.endif\n\t.if %c[i] == 29\n\ts_mov_b32 %[dm], %[dm3]\n\ts_mov_b32 %[ds], %[ds3]\n\t.endif\n\t"
                 ".if %c[dma]\n\ts_mov_b32 m0, %[dm]\n\ts_add_u32 %[dm], %[dm], 0x400\n\tbuffer_load_dwordx4 %[dv], %[rwd], %[ds] offen lds\n\ts_add_u32 %[ds], %[ds], 0x2000\n\t.endif\n\t"
                 "v_mfma_f32_32x32x16_bf16 v[96:111], v[%c[c2]:%c[c2]+3], v[%c[ar]:%c[ar]+3], v[96:111]\n\t"
                 ".if %c[cnt]\n\ts_waitcnt vmcnt(%c[vn])\n\t" TAG_T1 TAG_T2 "s_waitcnt lgkmcnt(0)\n\t" TAG_T3(140, 8) ".else\n\ts_waitcnt vmcnt(0)\n\ts_nop 15\n\t.endif"
                 : "+{v[64:79]}"(g.a0), "+{v[80:95]}"(g.a1), "+{v[96:111]}"(g.a2), "+{v[140:155]}"(g.r0), "+{v[156:171]}"(g.r1), "+{v[172:187]}"(g.st),
                   "+{v[112:119]}"(g.w0), "+{v[124:131]}"(g.w1), "+{v[132:139]}"(g.w2), [sa] "+s"(g.sa), [bud] "+s"(g.budget), [t] "=&s"(tq), [dm] "+s"(g.dm), [ds] "+s"(g.ds)
                 : [l16] "v"(g.l16), [rs] "s"(g.rs), [s0] "s"(g.s0), [dv] "v"(g.dv), [rwd] "s"(g.rd), [dm2] "s"(g.dm2), [ds2] "s"(g.ds2), [dm3] "s"(g.dm3), [ds3] "s"(g.ds3),
                   [dma] "n"(gu_dma(i)), [sv] "v"(g.sv), [ss] "s"(g.ss), [b0] "v"(g.wa[0]), [b1] "v"(g.wa[1]), [b2] "v"(g.wa[2]),
                   [ld] "n"(j < 32 ? 1 : 0), [lr] "n"(140 + 4 * (j % AW)), [lo] "n"((j & 3) * 1024), [sk] "n"(i >= 24 ? i - 24 : -1), [rd] "n"(n < 32 ? 1 : 0), [no] "n"(n * 1536),
                   [n0] "n"(n & 1 ? 128 : 112), [n1] "n"(n & 1 ? 132 : 116), [n2] "n"(n & 1 ? 136 : 124), [c0] "n"(i & 1 ? 128 : 112), [c1] "n"(i & 1 ? 132 : 116), [c2] "n"(i & 1 ? 136 : 124),
                   [vn] "n"(n < 32 ? gu_younger(n) : 0), [ar] "n"(140 + 4 * (i % AW)), [i] "n"(i), [cr] "n"(140 + 4 * (n % AW)), [ci] "n"(n), [cnt] "n"(j < 32 ? AW - 1 : 31 - i)
                 : "vcc", "scc", "m0", "v120", "memory");
    if constexpr (i + 1 < 32) gu_steps<i + 1>(g);
}

__global__ __launch_bounds__(512, 2) void k_block(const u16* __restrict__ x, const u16* __restrict__ wqkv, const u16* __restrict__ wo, const u16* __restrict__ wg,
                                                  const u16* __restrict__ wu, const u16* __restrict__ wd, u16* __restrict__ out, char* __restrict__ ws) {
    extern __shared__ __attribute__((aligned(16))) char smem[];
    const unsigned lds0 = (unsigned)(uintptr_t)(LDSAS char*)smem;
    const int tid = threadIdx.x, lane = tid & 63;
    const int w = __builtin_amdgcn_readfirstlane(tid >> 6);
    const int b = blockIdx.x, xcd = b & 7, bi = b >> 3;
    const int r32 = lane & 31, h = lane >> 5;
    const rsrc_t rs = mk_rsrc(ws, WS_END);
    const int wt = w & 1, kh = (w >> 1) & 1;
    STAMP_DECL;
    unsigned ep;
    {
    const int rt = (xcd >> 1) * 8 + (bi & 7), cb = (xcd & 1) * 4 + (bi >> 3), rot = bi & 3;
    const int wc = (w >> 1) & 1, hw = w - 4;
    STAMP(0, 0);
    const rsrc_t rw = mk_rsrc(wqkv, D * 3 * D * 2);
    const unsigned wv = (lane >> 3) * 3072 + (((lane & 7) ^ (((lane >> 4) & 1) << 2)) << 4);
    const unsigned fa = lds0 + K1_FLG;
    if (tid < 12) *(LDSAS unsigned*)(fa + tid * 4) = 0u;
    if (w >= 4) {
        BAR_L();
        const unsigned pa = fa + hw * 4;
        auto issue = [&](int s) {
            const unsigned so = RFL(((((s & 3) + rot) & 3) * 128 + hw * 32) * 3072 + (s >> 2) * 1024 + cb * 128);
            asm volatile("s_nop 4" :: "s"(so));
#pragma unroll
            for (int i = 0; i < 4; ++i)
                asm volatile("s_mov_b32 m0, %0\n\ts_nop 0\n\tbuffer_load_dwordx4 %1, %2, %3 offen lds"
                             :: "s"(lds0 + k1_slot(s) + (4 * hw + i) * 1024), "v"(wv), "s"(rw), "s"(so + i * 8 * 3072) : "m0", "memory");
        };
#define K1_LAND(N, s) asm volatile("s_waitcnt vmcnt(" #N ")\n\tds_write_b32 %0, %1" :: "v"(pa), "v"((s) + 1u) : "memory")
        issue(0); issue(1);
        issue(2); K1_LAND(8, 0);
        issue(3); K1_LAND(8, 1);
        issue(4); K1_LAND(8, 2); K1_LAND(4, 3); K1_LAND(0, 4);
        lds_spin_ge(fa + 28, 4);
        issue(5);
        lds_spin_ge(fa + 20, 4);
        issue(6); issue(7); K1_LAND(8, 5);
        issue(8); K1_LAND(8, 6);
        issue(9); K1_LAND(8, 7);
        lds_spin_ge(fa + 24, 4);
        issue(10); K1_LAND(8, 8);
        issue(11); K1_LAND(8, 9);
        K1_LAND(4, 10); K1_LAND(0, 11);
        if (w == 4) { unsigned e; asm volatile("s_load_dword %0, %1, 0\n\ts_waitcnt lgkmcnt(0)" : "=s"(e) : "s"(ws) : "memory"); *(LDSAS unsigned*)(fa + 48) = e + 1u; }
        else {
            unsigned long long pc; unsigned len, o = bi * 128 + (w - 5) * 24576, t, d0, d1, d2, d3;
#define KCP(D) "s_min_u32 %[t], %[o], %[len]\n\ts_load_dword " D ", %[pc], %[t]\n\ts_add_u32 %[o], %[o], 0x1000\n\t"
            asm volatile("s_getpc_b64 %[pc]\n.Lqkv_pc:\n\ts_mov_b32 %[len], .Lmain_end-.Lqkv_pc-4\n\t" KCP("%[d0]") KCP("%[d1]") KCP("%[d2]") KCP("%[d3]") KCP("%[d0]") KCP("%[d1]") "s_waitcnt lgkmcnt(0)"
                         : [pc] "=&s"(pc), [len] "=&s"(len), [o] "+s"(o), [t] "=&s"(t), [d0] "=&s"(d0), [d1] "=&s"(d1), [d2] "=&s"(d2), [d3] "=&s"(d3) :: "scc", "memory");
        }
        BAR_L();
        ep = RFL(*(const LDSAS unsigned*)(fa + 48));
        if (w == 4) { __builtin_amdgcn_s_sleep(POLL_DELAY_Q); wait_flags(rs, WS_FQ + (xcd * 2 + (bi >> 4)) * 8192, 64, ep, lane); }
    } else {
    {
        const rsrc_t rx = mk_rsrc(x, S * D * 2);
        u32x4 r[2][8];
#pragma unroll
        for (int p = 0; p < 2; ++p)
#pragma unroll
            for (int g = 0; g < 8; ++g)
                r[p][g] = __builtin_amdgcn_raw_buffer_load_b128(rx, (lane >> 3) * 1024 + (((lane & 7) ^ ((4 * p + (lane >> 4)) & 7)) << 4) + g * 128, (rt * 64 + w * 16 + p * 8) * 1024, K1_X_AUX);
        SBAR(); BAR_L(); SBAR();
#pragma unroll
        for (int p = 0; p < 2; ++p) {
            float s0 = 0.f, s1 = 0.f, q0 = 0.f, q1 = 0.f;
#pragma unroll
            for (int g = 0; g < 8; ++g) { s0 = dot2(r[p][g][0], ONES2, s0); q0 = dot2(r[p][g][0], r[p][g][0], q0); s1 = dot2(r[p][g][1], ONES2, s1); q1 = dot2(r[p][g][1], r[p][g][1], q1);
                                          s0 = dot2(r[p][g][2], ONES2, s0); q0 = dot2(r[p][g][2], r[p][g][2], q0); s1 = dot2(r[p][g][3], ONES2, s1); q1 = dot2(r[p][g][3], r[p][g][3], q1); }
            if (p == 0) STAMP(0, 1);
            float sum = s0 + s1, sq = q0 + q1;
            sum = dpp_add<0xB1>(sum); sq = dpp_add<0xB1>(sq); sum = dpp_add<0x4E>(sum); sq = dpp_add<0x4E>(sq); sum = dpp_add<0x141>(sum); sq = dpp_add<0x141>(sq);
            const float mean = sum * (1.f / D);
            const float rstd = __builtin_amdgcn_rsqf(__builtin_fmaxf(sq * (1.f / D) - mean * mean, 0.f) + LN_EPS), nb = -mean * rstd;
#pragma unroll
            for (int g = 0; g < 8; g += 2) {
                f32x2 t[8];
#pragma unroll
                for (int i = 0; i < 8; ++i) { const unsigned v = r[p][g + (i >> 2)][i & 3]; t[i] = f32x2{bflo(v), bfhi(v)}; }
                asm volatile("" : "+v"(t[0]), "+v"(t[1]), "+v"(t[2]), "+v"(t[3]), "+v"(t[4]), "+v"(t[5]), "+v"(t[6]), "+v"(t[7]));
#pragma unroll
                for (int i = 0; i < 8; ++i) t[i] = t[i] * rstd + nb;
                asm volatile("" : "+v"(t[0]), "+v"(t[1]), "+v"(t[2]), "+v"(t[3]), "+v"(t[4]), "+v"(t[5]), "+v"(t[6]), "+v"(t[7]));
                u32x4 y0, y1;
#pragma unroll
                for (int i = 0; i < 4; ++i) { y0[i] = pk2(t[i][0], t[i][1]); y1[i] = pk2(t[4 + i][0], t[4 + i][1]); }
                *(LDSAS u32x4*)(lds0 + (2 * w + p) * 1024 + lane * 16 + g * 8192) = y0;
                *(LDSAS u32x4*)(lds0 + (2 * w + p) * 1024 + lane * 16 + g * 8192 + 8192) = y1;
            }
            SBAR();
        }
    }
    lds_inc(fa + 16, lane);
    STAMP(0, 3);
    u32x16 xa0, xa1, xa2, xa3, xa4, xa5, xa6, xa7;
    u32x4 w0, w1, w2, w3, w4, fl;
    f32x16 acA, acB;
    unsigned xb[4], tq;
#pragma unroll
    for (int m = 0; m < 4; ++m) xb[m] = lds0 + (32 * wt + r32) * 128 + (((2 * m + h) ^ ((r32 >> 1) & 7)) << 4) + rot * 16384;
    const int q4 = (lane & 15) >> 2, p4 = lane & 3, blk = (lane >> 4) & 1;
    const unsigned tA = lds0 + (8 * h + q4) * 128 + (((4 * wc + 2 * blk + (p4 >> 1)) ^ (((q4 >> 1) & 1) << 2)) << 4) + 8 * (p4 & 1);
    const unsigned tB = tA + 65536, tC = tA + 131072;
    const int tt = rt * 2 + wt, hd = cb * 4 + wc * 2;
#define KXR(R, m, O) "ds_read_b128 v[" #R ":" #R "+3], %[x" #m "] offset:" #O "\n\t"
#define KXN(m) "v_add_u32 %[x" #m "], 0x4000, %[x" #m "]\n\tv_and_b32 %[x" #m "], 0xffff, %[x" #m "]\n\t"
#define KCHK(ST) "v_min3_u32 v116, v116, v117, v118\n\tv_min_u32 v116, v116, v119\n\ts_nop 0\n\tv_readfirstlane_b32 %[t], v116\n\ts_nop 0\n\ts_cmp_gt_u32 %[t], " ST "\n\t"
#define KSPIN(ST) "1:\n\tds_read_b128 v[116:119], %[fa]\n\ts_waitcnt lgkmcnt(0)\n\t" KCHK(ST) "s_cbranch_scc0 1b\n\t"
#define KWR(R, O) "ds_read_b64_tr_b16 v[" #R ":" #R "+1], %[tb] offset:" #O "\n\tds_read_b64_tr_b16 v[" #R "+2:" #R "+3], %[tb] offset:" #O "+512\n\t"
    asm volatile("3:\n\tds_read_b32 v116, %[fa] offset:16\n\ts_waitcnt lgkmcnt(0)\n\tv_readfirstlane_b32 %[t], v116\n\ts_nop 0\n\ts_cmp_gt_u32 %[t], 3\n\ts_cbranch_scc0 3b\n\t"
                 KXR(128, 0, 0) KXR(132, 1, 0) KXR(136, 2, 0) KXR(140, 3, 0) KXR(144, 0, 8192) KXR(148, 1, 8192) KXR(152, 2, 8192) KXR(156, 3, 8192) KXN(0) KXN(1) KXN(2) KXN(3)
                 KSPIN("0") KWR(96, 0) KWR(100, 2048) KWR(104, 4096) KWR(108, 6144)
                 : "={v[116:119]}"(fl), [t] "=&s"(tq), "={v[96:99]}"(w0), "={v[100:103]}"(w1), "={v[104:107]}"(w2), "={v[108:111]}"(w3), "={v[112:115]}"(w4), "={v[64:79]}"(acA), "={v[80:95]}"(acB),
                   "={v[128:143]}"(xa0), "={v[144:159]}"(xa1), "={v[160:175]}"(xa2), "={v[176:191]}"(xa3), "={v[192:207]}"(xa4), "={v[208:223]}"(xa5), "={v[224:239]}"(xa6), "={v[240:255]}"(xa7),
                   [x0] "+v"(xb[0]), [x1] "+v"(xb[1]), [x2] "+v"(xb[2]), [x3] "+v"(xb[3])
                 : [fa] "v"(fa), [tb] "v"(tB) : "scc", "memory");
    STAMP(0, 4);
    auto epi = [&](int m, const f32x16& acc, int hh) {
        const float sc = 0.25f * LOG2E;
        u32x4 o;
#pragma unroll
        for (int i = 0; i < 4; ++i) o[i] = m == 0 ? pk2(acc[8 * hh + 2 * i] * sc, acc[8 * hh + 2 * i + 1] * sc) : pk2(acc[8 * hh + 2 * i], acc[8 * hh + 2 * i + 1]);
        if (m == 0) __builtin_amdgcn_raw_buffer_store_b128(o, rs, lane * 16, WS_Q + ((hd + hh) * 64 + tt) * 1024, K1_ST_AUX);
        else if (m == 1) __builtin_amdgcn_raw_buffer_store_b128(o, rs, lane * 16, WS_KV + (hd + hh) * KV_HEAD + (tt >> 3) * KV_CHUNK + (tt & 7) * 1024, K1_ST_AUX);
        else __builtin_amdgcn_raw_buffer_store_b128(o, rs, (r32 >> 4) * KV_HEAD + (16 * h + (lane & 15)) * 16 + hh * 512,
                                                    WS_KV + hd * KV_HEAD + (tt >> 3) * KV_CHUNK + 8192 + (tt & 7) * 1024, K1_ST_AUX);
    };
    sfor<0, 96>([&](auto G) {
        constexpr int g = G.value, n = g + 4, m = g >> 5;
        constexpr unsigned o = k1_slot(n >> 3) + (n & 7) * 2048, oi = o & 65535;
        constexpr int aR = m == 1 ? 80 : 64, wR = 96 + 4 * (g % 5), nR = 96 + 4 * (n % 5), xR = 128 + 4 * (g & 31);
        constexpr int nx = (g - 4 >= 0 && g - 4 < 12) + (g - 3 >= 0 && g - 3 < 12) + (g - 2 >= 0 && g - 2 < 12) + (g - 1 >= 0 && g - 1 < 12), wc_ = n >= 96 ? 0 : (8 + 2 * nx > 14 ? 14 : 8 + 2 * nx);
#define KRD(B) "ds_read_b64_tr_b16 v[%c[nr]:%c[nr]+1], %[" B "] offset:%c[oi]\n\tds_read_b64_tr_b16 v[%c[nr]+2:%c[nr]+3], %[" B "] offset:%c[oi]+512\n\t"
        asm volatile(".if %c[bar] == 1\n\t" KCHK("%c[st]") "s_cbranch_scc0 1f\n2:\n\t.subsection 1\n" KSPIN("%c[st]") "s_branch 2b\n\t.subsection 0\n\t.endif\n\t"
                     ".if %c[bar] == 2\n\tds_read_b128 v[116:119], %[fa]\n\t.endif\n\t"
                     ".if %c[rd] == 1\n\t" KRD("tb") ".endif\n\t.if %c[rd] == 2\n\t" KRD("tc") ".endif\n\t.if %c[rd] == 3\n\t" KRD("ta") ".endif\n\t"
                     "s_waitcnt lgkmcnt(%c[wn])\n\t"
                     ".if %c[md] == 0\n\tv_mfma_f32_32x32x16_bf16 v[%c[ar]:%c[ar]+15], v[%c[wr]:%c[wr]+3], v[%c[xr]:%c[xr]+3], v[%c[ar]:%c[ar]+15]\n\t.endif\n\t"
                     ".if %c[md] == 1\n\tv_mfma_f32_32x32x16_bf16 v[%c[ar]:%c[ar]+15], v[%c[xr]:%c[xr]+3], v[%c[wr]:%c[wr]+3], v[%c[ar]:%c[ar]+15]\n\t.endif\n\t"
                     ".if %c[md] == 2\n\tv_mfma_f32_32x32x16_bf16 v[%c[ar]:%c[ar]+15], v[%c[xr]:%c[xr]+3], v[%c[wr]:%c[wr]+3], 0\n\t.endif\n\t"
                     ".if %c[md] == 3\n\tv_mfma_f32_32x32x16_bf16 v[%c[ar]:%c[ar]+15], v[%c[wr]:%c[wr]+3], v[%c[xr]:%c[xr]+3], 0\n\t.endif\n\t"
                     ".if %c[xm] == 1\n\tds_read_b128 v[%c[xd]:%c[xd]+3], %[x0] offset:%c[xo]\n\tds_read_b128 v[%c[xd]+4:%c[xd]+7], %[x1] offset:%c[xo]\n\t.endif\n\t"
                     ".if %c[xm] == 2\n\tds_read_b128 v[%c[xd]:%c[xd]+3], %[x2] offset:%c[xo]\n\tds_read_b128 v[%c[xd]+4:%c[xd]+7], %[x3] offset:%c[xo]\n\t.endif\n\t"
                     ".if %c[xn]\n\t" KXN(0) KXN(1) KXN(2) KXN(3) ".endif"
                     : "+{v[64:79]}"(acA), "+{v[80:95]}"(acB), "+{v[96:99]}"(w0), "+{v[100:103]}"(w1), "+{v[104:107]}"(w2), "+{v[108:111]}"(w3), "+{v[112:115]}"(w4),
                       "+{v[128:143]}"(xa0), "+{v[144:159]}"(xa1), "+{v[160:175]}"(xa2), "+{v[176:191]}"(xa3), "+{v[192:207]}"(xa4), "+{v[208:223]}"(xa5), "+{v[224:239]}"(xa6), "+{v[240:255]}"(xa7),
                       "+{v[116:119]}"(fl), [t] "=&s"(tq), [x0] "+v"(xb[0]), [x1] "+v"(xb[1]), [x2] "+v"(xb[2]), [x3] "+v"(xb[3])
                     : [ta] "v"(tA), [tb] "v"(tB), [tc] "v"(tC), [fa] "v"(fa), [st] "n"(n >> 3), [wn] "n"(wc_),
                       [bar] "n"(K1_ABL ? 0 : n < 96 && (n & 7) == 0 ? 1 : n < 92 && (n & 7) == 4 ? 2 : 0), [rd] "n"(n >= 96 ? 0 : o < 65536 ? 3 : o < 131072 ? 1 : 2), [oi] "n"(oi), [nr] "n"(nR),
                       [md] "n"(m < 2 ? ((g & 31) == 0 ? 3 : 0) : g == 64 ? 2 : 1), [ar] "n"(aR), [wr] "n"(wR), [xr] "n"(xR),
                       [xm] "n"(g < 12 ? 1 + (g & 1) : 0), [xd] "n"(160 + 8 * g), [xo] "n"(((g >> 1) & 1) * 8192), [xn] "n"(g < 11 && (g & 3) == 3 ? 1 : 0) : "scc", "memory");
        if (g == 7 && lane == 0) asm volatile("ds_add_u32 %0, %1" :: "v"(fa + 28), "v"(1u) : "memory");
        if (g == 16 && lane == 0) asm volatile("ds_add_u32 %0, %1" :: "v"(fa + 20), "v"(1u) : "memory");
        if (g == 23 && lane == 0) asm volatile("ds_add_u32 %0, %1" :: "v"(fa + 24), "v"(1u) : "memory");
        if (g == 36 || g == 38) epi(0, acA, (g - 36) >> 1);
        if (g == 68 || g == 70) epi(1, acB, (g - 68) >> 1);
    });
    STAMP(0, 2);
    asm volatile("s_nop 15" : "+{v[64:79]}"(acA));
    epi(2, acA, 0); epi(2, acA, 1);
    SBAR(); BAR_L();
    ep = RFL(*(const LDSAS unsigned*)(fa + 48));
    WAITV(0);
    if (lane == 0) __builtin_amdgcn_raw_buffer_store_b32(ep, rs, 0, WS_FQ + ((cb * 2 + wc) * 64 + tt) * 128, AUX_SC1);
    STAMP(0, 5);
    FLUSH(0);
    }
    }

    const int rt = xcd * 4 + (bi >> 3), ct = bi & 7;
    u32x4 hreg[2];
    u32x8 xres;

    auto issue_wo = [&](int first, int n) {
        const rsrc_t rwo = mk_rsrc(wo, D * D * 2);
        const unsigned v = w64_voff(lane);
        for (int p = first; p < first + n; ++p) dma16(rwo, v, p * 8192 + ct * 128, lds0 + L_WO + p * 1024);
    };
    auto issue_dn = [&](int i0, int n, unsigned dst, int hw) {
        const rsrc_t rwd = mk_rsrc(wd, F * D * 2);
        const unsigned v = w64_voff(lane);
        const unsigned so = ((43 * (hw >> 1) + i0) * 16 + (hw & 1) * 8) * 1024 + ct * 128;
        for (int i = 0; i < n; ++i) dma16(rwd, v, so + i * 16384, lds0 + dst + (hw & 1) * 1024 + i * 2048);
    };
    auto gu_wa = [&](int n) -> unsigned { const unsigned blk = (lane >> 4) & 1;
        return lds0 + L_GU + blk * 49152 + (((4 * h + ((lane & 15) >> 2)) * 96 + n * 32 + (lane & 3) * 8) ^ (blk * 128)); };
    float cs[3] = {0.f, 0.f, 0.f};
    auto gu_colsum = [&]() {
        u32x4 f[3][4];
#pragma unroll
        for (int n = 0; n < 3; ++n) {
            const unsigned a0 = gu_wa(n) + w * 4 * 1536;
#pragma unroll
            for (int i = 0; i < 4; ++i) f[n][i] = __builtin_bit_cast(u32x4, lds_tr2(a0 + i * 1536, a0 + i * 1536 + 768));
        }
        SBAR();
#pragma unroll
        for (int i = 0; i < 4; ++i)
#pragma unroll
            for (int e = 0; e < 4; ++e)
#pragma unroll
                for (int n = 0; n < 3; ++n) cs[n] = dot2(f[n][i][e], ONES2, cs[n]);
#pragma unroll
        for (int n = 0; n < 3; ++n) cs[n] = swap_add(cs[n]);
    };

    unsigned budget = 1u << 16;
    const unsigned l16 = lane * 16;

    {
        const int head = xcd * 4 + (bi >> 3), qb = bi & 7, wq = w & 3, g = w >> 2, qt = qb * 8 + wq * 2;
        const unsigned kvs = WS_KV + head * KV_HEAD;
        unsigned ks = kvs + g * 4 * KV_CHUNK + wq * 4096, km = lds0 + g * 32768 + wq * 4096;
        const rsrc_t rg = mk_rsrc(g ? wu : wg, (bi < 29 && ABL_GU != 1) ? D * F * 2 : 0), rwd = mk_rsrc(wd, F * D * 2), rwo = mk_rsrc(wo, D * D * 2), rx = mk_rsrc(x, S * D * 2);
        unsigned gv0, gv1, gv2;
        { unsigned c = lane ^ (g * 8); gv0 = (c / 6) * (F * 2) + (c % 6) * 16; c = (64 + lane) ^ (g * 8); gv1 = (c / 6) * (F * 2) + (c % 6) * 16;
          c = (128 + lane) ^ (g * 8); gv2 = (c / 6) * (F * 2) + (c % 6) * 16; }
        if (ABL_GU == 2) { gv0 = l16; gv1 = l16 + 1024; gv2 = l16 + 2048; }
        unsigned gs = ABL_GU == 2 ? wq * 4 * 32 * (F * 2) + bi * 3072 : wq * 4 * 32 * (F * 2) + bi * 96, gm = lds0 + L_GU + g * 49152 + wq * 12288;
        const unsigned pvd = lane < 43 ? (bi * 344 + w * 43 + lane) * 128 : 0x80000000u, pvo = lane < 16 ? (bi * 128 + w * 16 + lane) * 128 : 0x80000000u;
        const unsigned wv = w64_voff(lane), xv = (rt * 64 + wt * 32 + r32) * 1024 + (ct * 64 + kh * 32 + 4 * h) * 2;
        const unsigned kb = lds0 + g * 32768 + l16, vb = lds0 + g * 32768 + 8192 + (16 * h + (r32 & 15)) * 16;
        const unsigned cst = (r32 & 27) == 16 ? ONES2 : 0u;
        const unsigned long long msk = 0x0000ffff0000ffffull;
        const unsigned fo = WS_O + ((qt + g) * 32 + head) * 1024, fh = WS_H + ((rt * 2 + wt) * 32 + ct * 4 + (w >> 1)) * 1024, fg = WS_HH + ((xcd * 8 + w) * NU + bi * 3) * 1024;
        const unsigned fsv = w == 0 ? WS_ST + ((rt * 2 + h) * 16 + ct * 2) * 256 + r32 * 16 : 0x80000000u, fg2 = bi < 28 ? l16 : 0x80000000u, fg0 = bi < 29 ? l16 : 0x80000000u;
        const unsigned fpv = tid == 0 ? WS_FP + b * 4 : 0x80000000u;
        u32x4 ones = {~0u, ~0u, ~0u, ~0u}, fpl = {0u, 0u, 0u, 0u};
        unsigned pfd = 0, epv = ep;
        xres = u32x8{};
        s16x8 q0, q1;
        STAMP(1, 0);
        SBAR();
#define XKV "s_mov_b32 m0, %[km]\n\ts_add_u32 %[km], %[km], 0x400\n\tbuffer_load_dwordx4 %[l16], %[rs], %[ks] offen sc1 lds\n\ts_add_u32 %[ks], %[ks], 0x400\n\t"
        asm volatile("s_barrier\n\tbuffer_load_dwordx4 v[128:131], %[l16], %[rs], %[qs] offen sc1\n\tbuffer_load_dwordx4 v[156:159], %[l16], %[rs], %[qs] offen offset:1024 sc1\n\t"
                     XKV XKV XKV XKV "s_add_u32 %[km], %[km], 0x3000\n\ts_add_u32 %[ks], %[ks], 0x3000\n\ts_waitcnt vmcnt(0)\n\ts_barrier\n\t" XKV XKV XKV XKV
                     : "={v[128:131]}"(q0), "={v[156:159]}"(q1), [km] "+s"(km), [ks] "+s"(ks)
                     : [l16] "v"(l16), [rs] "s"(rs), [qs] "s"(WS_Q + (head * 64 + qt) * 1024) : "m0", "scc", "memory");
        km -= 16384 + 4096; ks += KV_CHUNK - 4096;
        STAMP(1, 1);
        f32x16 n0, n1, sA, sB = {}, o0 = {}, o1 = {};
        s16x8 kf = lds_b128(kb), v0, v1, v1b;
        {
            const f32x16 z = {};
            const s16x8 kz = lds_b128(lds0 + l16);
            const u32x4 vv = __builtin_bit_cast(u32x4, lds_b128(vb)), cc = {cst, cst, cst, cst};
            const f32x16 s0 = MFMA(kz, q0, z), s1 = MFMA(kz, q1, z);
            float m0 = __builtin_fmaxf(s0[0], s0[1]), m1 = __builtin_fmaxf(s1[0], s1[1]);
#pragma unroll
            for (int i = 2; i < 16; i += 2) { m0 = __builtin_fmaxf(__builtin_fmaxf(m0, s0[i]), s0[i + 1]); m1 = __builtin_fmaxf(__builtin_fmaxf(m1, s1[i]), s1[i + 1]); }
            m0 = swap_max(m0); m1 = swap_max(m1);
#pragma unroll
            for (int i = 0; i < 16; ++i) { n0[i] = -m0; n1[i] = -m1; }
            sA = MFMA(kf, q0, n0);
            v0 = __builtin_bit_cast(s16x8, r32 < 16 ? vv : cc); v1 = v1b = __builtin_bit_cast(s16x8, cc);
        }
        u32x4 pA = {0u, 0u, 0u, 0u}, pB = {0u, 0u, 0u, 0u};
#define XE(C, i) "v_exp_f32 v[" #C "+" #i "], v[" #C "+" #i "]\n\t"
#define XC(P, C, i) "v_cvt_pk_bf16_f32 v" #P ", v[" #C "+" #i "], v[" #C "+" #i "+1]\n\t"
#define XG1(C) XE(C, 0) XE(C, 1) XE(C, 2) XE(C, 3) XC(148, C, 0) XE(C, 4) XE(C, 5) XC(149, C, 2)
#define XG2(C) XE(C, 6) XE(C, 7) XC(150, C, 4) XE(C, 8) XC(151, C, 6) XE(C, 9) XE(C, 10)
#define XG3(C) XE(C, 11) XC(152, C, 8) XE(C, 12) XE(C, 13) XC(153, C, 10) XE(C, 14) XE(C, 15) XC(154, C, 12) XC(155, C, 14)
#define XM(D, A, B, C) "v_mfma_f32_32x32x16_bf16 v[" #D ":" #D "+15], v[" #A ":" #A "+3], v[" #B ":" #B "+3], v[" #C ":" #C "+15]\n\t"
#define XRK(BASE, O) "ds_read_b128 v[132:135], " BASE " offset:" #O "\n\t"
#define XRV(R, BASE, O) "s_mov_b64 exec, %[msk]\n\tds_read_b128 v[" #R ":" #R "+3], " BASE " offset:" #O "\n\ts_mov_b64 exec, -1\n\t"
#define XW(n) "s_waitcnt lgkmcnt(" #n ")\n\t"
#define XTILE(VA, RK, RV1, WA, X1, VB, X2, RV0) \
        XM(80, 132, 156, 176) RK XG1(64) XM(160, VA, 152, 160) RV1 XG2(64) XW(WA) XM(96, 140, 148, 96) XG3(64) \
        XW(0) XM(64, 132, 128, 112) X1 XG1(80) XM(96, VB, 152, 96) X2 XG2(80) XM(160, 140, 148, 160) RV0 XG3(80)
#define XGU(j) "s_mov_b32 m0, %[gm]\n\ts_add_u32 %[gm], %[gm], 0x400\n\tbuffer_load_dwordx4 %[gv" #j "], %[rg], %[gs] offen lds\n\t"
#define XGU2 XGU(2) "s_add_u32 %[gs], %[gs], 88064\n\t"
#define XPF "buffer_load_dword v200, %[pvd], %[rwd], 0 offen\n\tbuffer_load_dword v200, %[pvo], %[rwo], 0 offen\n\t"
#define XWO "s_mov_b32 m0, %[km]\n\ts_add_u32 %[km], %[km], 0x400\n\tbuffer_load_dwordx4 %[wv], %[rwo], %[ks] offen lds\n\ts_add_u32 %[ks], %[ks], 0x2000\n\t"
#define XST(V, SO, O) "buffer_store_dwordx4 v[204:207], " V ", %[rs], " SO " offen offset:" #O " sc1\n\t"
#define XFP "buffer_store_dword v201, %[fpv], %[rs], 0 offen sc1\n\t"
#define XFL "buffer_load_dwordx4 v[208:211], %[l16], %[rs], %[fp] offen sc1\n\t"
#define XXR(R, O) "buffer_load_dwordx2 v[" #R ":" #R "+1], %[xv], %[rx], 0 offen offset:" #O "\n\t"
#define AOPS : "+{v[64:79]}"(sA), "+{v[80:95]}"(sB), "+{v[96:111]}"(o0), "+{v[160:175]}"(o1), "+{v[112:127]}"(n0), "+{v[176:191]}"(n1), "+{v[128:131]}"(q0), "+{v[156:159]}"(q1), \
               "+{v[132:135]}"(kf), "+{v[140:143]}"(v0), "+{v[144:147]}"(v1), "+{v[136:139]}"(v1b), "+{v[148:151]}"(pA), "+{v[152:155]}"(pB), "+{v[192:199]}"(xres), "+{v200}"(pfd), \
               "+{v201}"(epv), "+{v[204:207]}"(ones), "+{v[208:211]}"(fpl), \
               [km] "+s"(km), [ks] "+s"(ks), [gm] "+s"(gm), [gs] "+s"(gs) \
             : [ka] "v"(ka), [va] "v"(va), [kn] "v"(kn), [vn] "v"(vn), [msk] "s"(msk), [l16] "v"(l16), [rs] "s"(rs), [rg] "s"(rg), [gv0] "v"(gv0), [gv1] "v"(gv1), [gv2] "v"(gv2), \
               [pvd] "v"(pvd), [pvo] "v"(pvo), [wv] "v"(wv), [xv] "v"(xv), [rwd] "s"(rwd), [rwo] "s"(rwo), [rx] "s"(rx), \
               [fo] "s"(fo), [fh] "s"(fh), [fg] "s"(fg), [fsv] "v"(fsv), [fg0] "v"(fg0), [fg2] "v"(fg2), [fpv] "v"(fpv), [fp] "s"(WS_FP) : "m0", "scc", "memory"
#define XBAR(n) asm volatile("s_waitcnt vmcnt(" #n ") lgkmcnt(0)\n\ts_barrier" AOPS)
#define XROUND(A1, A2, B1, B2, C1, C2, D1, D2, E1, E2, F1, F2) asm volatile( \
            XTILE(136, XRK("%[ka]", 1024), XRV(144, "%[va]", 512), 2, A1, 144, A2, XRV(140, "%[va]", 1024)) \
            XTILE(144, XRK("%[ka]", 2048), XRV(144, "%[va]", 1536), 2, B1, 144, B2, XRV(140, "%[va]", 2048)) \
            XTILE(144, XRK("%[ka]", 3072), XRV(144, "%[va]", 2560), 2, C1, 144, C2, XRV(140, "%[va]", 3072)) \
            XTILE(144, XRK("%[ka]", 4096), XRV(144, "%[va]", 3584), 2, D1, 144, D2, XRV(140, "%[va]", 4096)) \
            XTILE(144, XRK("%[ka]", 5120), XRV(144, "%[va]", 4608), 2, E1, 144, E2, XRV(140, "%[va]", 5120)) \
            XTILE(144, XRK("%[ka]", 6144), XRV(144, "%[va]", 5632), 2, F1, 144, F2, XRV(140, "%[va]", 6144)) \
            XTILE(144, XRK("%[ka]", 7168), XRV(144, "%[va]", 6656), 2, XRV(136, "%[va]", 7680), 144, "", XRV(140, "%[va]", 7168)) AOPS)
#define XLAST(X1, X2) asm volatile(XTILE(144, XRK("%[kn]", 0), "", 1, X1, 136, X2, XRV(140, "%[vn]", 0)) AOPS)
        unsigned ka = kb, va = vb, kn = kb + 16384, vn = vb + 16384;
        asm volatile("s_nop 15" AOPS);
        STAMP(1, 7);
        XROUND(XGU(0), "", "", XST("%[l16]", "%[fo]", 0), XGU(1), XST("%[fsv]", "0", 0), "", XST("%[l16]", "%[fh]", 0), XGU2, "", XGU(0), "");
        STAMP(1, 8);
        XBAR(7);
        STAMP(1, 9);
        XLAST(XKV, XKV);
        ka = kb + 16384; va = vb + 16384; kn = kb; vn = vb;
        XROUND(XKV, XKV, XGU(1), XST("%[fg0]", "%[fg]", 0), XGU2, "", XST("%[fg0]", "%[fg]", 1024), XGU(0), "", XGU(1), XST("%[fg2]", "%[fg]", 2048), "");
        STAMP(1, 10);
        XBAR(7);
        STAMP(1, 11);
        km += 16384 - 4096; ks += KV_CHUNK - 4096;
        XLAST(XKV, XKV);
        ka = kb; va = vb; kn = kb + 16384; vn = vb + 16384;
        XROUND(XKV, XKV, XGU2, "", XGU(0), "", XGU(1), "", XGU2, "", XPF, "");
        STAMP(1, 12);
        XBAR(6);
        STAMP(1, 13);
        km = lds0 + g * 32768 + wq * 4096; ks = (32 * g + 4 * wq) * 8192 + ct * 128;
        XLAST(XFP, XWO);
        ka = kb + 16384; va = vb + 16384; kn = kb; vn = vb;
        XROUND(XWO, XWO, XWO, "", XXR(192, 0), "", XXR(194, 16), "", XXR(196, 32), "", XXR(198, 48), "");
        XLAST(XFL, "");
        STAMP(1, 14);
        asm volatile(XM(160, 136, 152, 160) "s_waitcnt vmcnt(0) lgkmcnt(0)\n\ts_barrier\n\ts_nop 15" AOPS);
        STAMP(1, 4);
        f32x16 o;
        {
            const f32x16& snd = g ? o0 : o1;
            const unsigned xa = lds0 + g * 32768 + 16384 + wq * 3072 + l16, xb = lds0 + (g ^ 1) * 32768 + 16384 + wq * 3072 + l16;
            *(LDSAS f32x4*)(xa) = f32x4{snd[0], snd[1], snd[2], snd[3]};
            *(LDSAS f32x4*)(xa + 1024) = f32x4{snd[4], snd[5], snd[6], snd[7]};
            *(LDSAS float*)(xa + 2048) = snd[8];
            if (tid == 0) *(LDSAS unsigned*)(lds0 + L_FLAG) = 0u;
            BAR_L();
            const f32x4 p0 = lds_f4(xb), p1 = lds_f4(xb + 1024); const float pl = *(const LDSAS float*)(xb + 2048);
            o = g ? o1 : o0;
#pragma unroll
            for (int i = 0; i < 4; ++i) { o[i] += p0[i]; o[4 + i] += p1[i]; }
            o[8] += pl;
        }
        bool bad = !(o[8] < 1.8446744e19f);
#pragma unroll
        for (int i = 0; i < 8; ++i) bad |= !(__builtin_fabsf(o[i]) < 3.0e38f);
        if (__any(bad) && lane == 0) *(LDSAS unsigned*)(lds0 + L_FLAG) = 1u;
        auto store_o = [&]() {
            const float inv = __builtin_amdgcn_rcpf(o[8]);
            u32x4 ov;
#pragma unroll
            for (int i = 0; i < 4; ++i) ov[i] = pk2(o[2 * i] * inv, o[2 * i + 1] * inv);
            __builtin_amdgcn_raw_buffer_store_b128(ov, rs, l16, WS_O + ((qt + g) * 32 + head) * 1024, AUX_SC1);
        };
        BAR_L();
        if (__builtin_amdgcn_readfirstlane(*(const LDSAS unsigned*)(lds0 + L_FLAG))) {
            BAR_L();
            const s16x8 qf = g ? q1 : q0;
            const u32x4 cc = {cst, cst, cst, cst};
            const unsigned vf = lds0 + 16384 + 8192 + (16 * h + (r32 & 15)) * 16;
            float m = -__builtin_inff();
            o = f32x16{};
            const f32x16 z = {};
#pragma unroll 1
            for (int c = 0; c < 8; ++c) {
                dma16<true>(rs, l16, kvs + c * KV_CHUNK + w * 2048, lds0 + 16384 + w * 2048);
                dma16<true>(rs, l16, kvs + c * KV_CHUNK + w * 2048 + 1024, lds0 + 16384 + w * 2048 + 1024);
                BAR_VL(0);
#pragma unroll 1
                for (int t = 0; t < 8; ++t) {
                    f32x16 s = MFMA(lds_b128(lds0 + 16384 + l16 + t * 1024), qf, z);
                    float mx = s[0];
#pragma unroll
                    for (int i = 1; i < 16; ++i) mx = __builtin_fmaxf(mx, s[i]);
                    mx = __builtin_fmaxf(swap_max(mx), m);
                    const float al = __builtin_amdgcn_exp2f(m - mx);
                    m = mx;
#pragma unroll
                    for (int i = 0; i < 16; ++i) { o[i] *= al; s[i] = __builtin_amdgcn_exp2f(s[i] - m); }
#pragma unroll
                    for (int i = 0; i < 4; ++i) { pA[i] = pk2(s[2 * i], s[2 * i + 1]); pB[i] = pk2(s[8 + 2 * i], s[9 + 2 * i]); }
                    const u32x4 a = __builtin_bit_cast(u32x4, lds_b128(vf + t * 1024)), c2 = __builtin_bit_cast(u32x4, lds_b128(vf + t * 1024 + 512));
                    o = MFMA(__builtin_bit_cast(s16x8, r32 < 16 ? a : cc), __builtin_bit_cast(s16x8, pA), o);
                    o = MFMA(__builtin_bit_cast(s16x8, r32 < 16 ? c2 : cc), __builtin_bit_cast(s16x8, pB), o);
                }
                BAR_L();
            }
        }
        store_o();
        STAMP(1, 5);
        BAR_L();
        if (w >= 4) issue_wo(16 + ((w - 4) >> 1) * 32 + (w & 1) * 8, 8);
        gu_colsum();
#pragma unroll 1
        for (unsigned spins = 0; spins < SPIN_CAP && !__all(fpl[0] == ep && fpl[1] == ep && fpl[2] == ep && fpl[3] == ep); ++spins) {
            __builtin_amdgcn_s_sleep(2);
            fpl = __builtin_amdgcn_raw_buffer_load_b128(rs, l16, WS_FP, AUX_SC1);
        }
        STAMP(1, 6);
        FLUSH(1);
    }

    {
        STAMP(2, 0);
        f32x16 a0, a1;
        if (w < 4) {
            const unsigned t0 = lds0 + L_WO + kh * 32768 + w64_tr(lane, kh), t1 = lds0 + L_WO + kh * 32768 + w64_tr(lane, kh ^ 1);
            G2 g2;
            g2.tb[0] = g2.tb[2] = g2.tb[4] = t0; g2.tb[1] = g2.tb[3] = g2.tb[5] = t1;
            g2.sa = g2.s0 = RFL(WS_O + ((rt * 2 + wt) * 32 + kh * 16) * 1024); g2.l16 = l16; g2.budget = budget; g2.rs = rs; g2.a1 = f32x16{};
#pragma unroll
            for (int i = 0; i < 8; ++i) { g2.a0[2 * i] = bflo(xres[i]); g2.a0[2 * i + 1] = bfhi(xres[i]); }
            __builtin_amdgcn_s_sleep(DELAY_O);
            STAMP(2, 1);
            g2_steps<0, 16, 0, 6, -1>(g2);
            a0 = g2.a0; a1 = g2.a1; STAMPV(15, budget - g2.budget); budget = g2.budget;
        } else BAR_VL(0);
        STAMP(2, 2);
        BAR_L();
        if (w < 4) {
#pragma unroll
            for (int g = 0; g < 4; ++g) *(LDSAS f32x4*)(lds0 + w * 4096 + g * 1024 + lane * 16) = f32x4{a1[4 * g], a1[4 * g + 1], a1[4 * g + 2], a1[4 * g + 3]};
        }
#pragma unroll
        for (int n = 0; n < 3; ++n) *(LDSAS float*)(lds0 + L_TAB + (w * 96 + n * 32 + r32) * 4) = cs[n];
        BAR_L();
        if (w >= 4) {
            if (tid < 256 + 96) {
                float t[8];
#pragma unroll
                for (int i = 0; i < 8; ++i) t[i] = *(const LDSAS float*)(lds0 + L_TAB + (i * 96 + tid - 256) * 4);
                *(LDSAS float*)(lds0 + L_TAB + 3072 + (tid - 256) * 4) = ((t[0] + t[1]) + (t[2] + t[3])) + ((t[4] + t[5]) + (t[6] + t[7]));
            }
        } else {
            f32x4 p[4];
#pragma unroll
            for (int g = 0; g < 4; ++g) p[g] = lds_f4(lds0 + (w ^ 2) * 4096 + g * 1024 + lane * 16);
            SBAR();
#pragma unroll
            for (int g = 0; g < 4; ++g) {
                hreg[g >> 1][(g & 1) * 2] = pk2(a0[4 * g] + p[g][0], a0[4 * g + 1] + p[g][1]);
                hreg[g >> 1][(g & 1) * 2 + 1] = pk2(a0[4 * g + 2] + p[g][2], a0[4 * g + 3] + p[g][3]);
            }
            const unsigned so = WS_H + ((rt * 2 + wt) * 32 + (ct * 2 + kh) * 2) * 1024;
            __builtin_amdgcn_raw_buffer_store_b128(hreg[0], rs, lane * 16, so, AUX_SC1);
            __builtin_amdgcn_raw_buffer_store_b128(hreg[1], rs, lane * 16 + 1024, so, AUX_SC1);
            SBAR();
            float sm = 0.f, sq = 0.f;
#pragma unroll
            for (int i = 0; i < 8; ++i) { sm = dot2(hreg[i >> 2][i & 3], ONES2, sm); sq = dot2(hreg[i >> 2][i & 3], hreg[i >> 2][i & 3], sq); }
            sm = swap_add(sm); sq = swap_add(sq);
            if (h == 0) __builtin_amdgcn_raw_buffer_store_b64(u32x2{__float_as_uint(sm), __float_as_uint(sq)}, rs, r32 * 8,
                                                              WS_ST + ((rt * 2 + wt) * 16 + ct * 2 + kh) * 256, AUX_SC1);
        }
        STAMP(2, 3);
        BAR_L();
        if (w >= 4) { issue_dn(0, 15, ((w - 4) >> 1) * 15 * 2048, w - 4); if (bi >= 29) issue_dn(15, 24, L_GU + ((w - 4) >> 1) * 49152, w - 4); }
        STAMP(2, 5);
        FLUSH(2);
    }

    {
        STAMP(3, 0);
        if (bi < 29) {
            const int tt = xcd * 8 + w;
            GU gu;
            gu.a0 = gu.a1 = gu.a2 = f32x16{};
            gu.sa = gu.s0 = RFL(WS_H + tt * 32768); gu.sv = 8 * h * 256 + r32 * 8; gu.ss = RFL(WS_ST + tt * 4096); gu.l16 = l16; gu.budget = budget; gu.rs = rs;
            gu.dm = RFL(lds0 + L_GU + (w >> 2) * 49152 + (w & 3) * 6144); gu.ds = RFL(((43 * (w >> 2) + 15) * 16 + (w & 3) * 48) * 1024 + ct * 128);
            gu.dm2 = RFL(lds0 + L_GU + (w >> 2) * 49152 + 24576 + (w & 3) * 3072); gu.ds2 = RFL(((43 * (w >> 2) + 27) * 16 + (w & 3) * 24) * 1024 + ct * 128);
            gu.dm3 = RFL(lds0 + L_TAB + (w >> 2) * 2048 + (w & 1) * 1024); gu.ds3 = RFL(((43 * (w >> 2) + 39) * 16 + (w & 1) * 8) * 1024 + ct * 128);
            gu.dv = w64_voff(lane); gu.rd = mk_rsrc(wd, F * D * 2);
            gu.wa[0] = gu_wa(0); gu.wa[1] = gu_wa(1); gu.wa[2] = gu_wa(2);
            f32x4 ctab[3][4];
#pragma unroll
            for (int n = 0; n < 3; ++n)
#pragma unroll
                for (int g = 0; g < 4; ++g) ctab[n][g] = lds_f4(lds0 + L_TAB + 3072 + (n * 32 + 8 * g + 4 * h) * 4);
            if (w < 4) __builtin_amdgcn_s_sleep(DELAY_H);
            STAMP(3, 2);
            gu_steps<0>(gu);
            STAMPV(15, budget - gu.budget); budget = gu.budget;
            const f32x16 acc[3] = {gu.a0, gu.a1, gu.a2};
            u32x16 st = gu.st;
            const unsigned sv = gu.sv, ss = gu.ss;
            STAMP(3, 3);
#pragma unroll 1
            for (;;) {
                unsigned mx = st[0];
#pragma unroll
                for (int i = 1; i < 16; ++i) mx = st[i] > mx ? st[i] : mx;
                if (!__any(mx == ~0u) || budget == 0) break;
                --budget;
#pragma unroll
                for (int i = 0; i < 8; ++i) { const u32x2 v = __builtin_amdgcn_raw_buffer_load_b64(rs, sv + i * 256, ss, AUX_SC1); st[2 * i] = v[0]; st[2 * i + 1] = v[1]; }
            }
            float sm = 0.f, sq = 0.f;
#pragma unroll
            for (int i = 0; i < 8; ++i) { sm += __uint_as_float(st[2 * i]); sq += __uint_as_float(st[2 * i + 1]); }
            sm = swap_add(sm); sq = swap_add(sq);
            const float mean = sm * (1.f / D);
            const float var = __builtin_fmaxf(sq * (1.f / D) - mean * mean, 0.f) + LN_EPS;
            const float rstd = __builtin_amdgcn_rsqf(var), sd = var * rstd, ga = -LOG2E * rstd;
            auto epi = [&](int n) {
                u32x4 ov; float y[8];
#pragma unroll
                for (int g = 0; g < 2; ++g) {
                    const f32x4 cg = ctab[n][g], cu = ctab[n][2 + g];
#pragma unroll
                    for (int i = 0; i < 4; ++i) {
                        const float e = __builtin_amdgcn_exp2f(__builtin_fmaf(-mean, cg[i], acc[n][4 * g + i]) * ga);
                        y[4 * g + i] = __builtin_fmaf(-mean, cu[i], acc[n][8 + 4 * g + i]) * __builtin_amdgcn_rcpf(__builtin_fmaf(e, sd, sd));
                    }
                }
#pragma unroll
                for (int i = 0; i < 4; ++i) ov[i] = pk2(y[2 * i], y[2 * i + 1]);
                const int u = bi * 3 + n;
                if (u < NU) __builtin_amdgcn_raw_buffer_store_b128(ov, rs, lane * 16, WS_HH + (tt * NU + u) * 1024, AUX_SC1);
            };
            epi(0); epi(1);
            epi(2);
        }
        STAMP(3, 4);
        if (bi >= 29) WAITV(0);
        BAR_L();
        STAMP(3, 5);
        if (w >= 4) {
            if (bi < 29) issue_dn(33, 6, L_GU + ((w - 4) >> 1) * 49152 + 36864, w - 4); else issue_dn(39, 1, L_TAB + ((w - 4) >> 1) * 2048, w - 4);
            STAMP(3, 6);
        }
        FLUSH(3);
    }

    {
        STAMP(4, 0);
        if (b == 0 && tid == 0) __builtin_amdgcn_raw_buffer_store_b32(ep, rs, 0, WS_EP, AUX_SC1);
        f32x16 a0, a1;
        if (w < 4) {
            const unsigned t0 = lds0 + w64_tr(lane, kh), t1 = lds0 + w64_tr(lane, kh ^ 1);
            G2 g2;
            g2.tb[0] = t0 + kh * 15 * 2048; g2.tb[1] = t1 + kh * 15 * 2048; g2.tb[2] = t0 + L_GU + kh * 24 * 2048; g2.tb[3] = t1 + L_GU + kh * 24 * 2048;
            g2.tb[4] = t0 + L_TAB + kh * 2048; g2.tb[5] = t1 + L_TAB + kh * 2048;
            g2.sa = g2.s0 = RFL(WS_HH + ((rt * 2 + wt) * NU + kh * 43) * 1024); g2.l16 = l16; g2.budget = budget; g2.rs = rs; g2.a1 = f32x16{};
#pragma unroll
            for (int i = 0; i < 8; ++i) { g2.a0[2 * i] = bflo(hreg[i >> 2][i & 3]); g2.a0[2 * i + 1] = bfhi(hreg[i >> 2][i & 3]); }
            __builtin_amdgcn_s_sleep(DELAY_G);
            STAMP(4, 1);
            g2_steps<0, 43, 1, DN_B0, 38>(g2);
            a0 = g2.a0; a1 = g2.a1; STAMPV(15, budget - g2.budget);
        } else {
            BAR_VL(0);
            issue_dn(40, 3, ((w - 4) >> 1) * 15 * 2048, w - 4);
            BAR_VL(0);
        }
        STAMP(4, 2);
        if (w < 4) {
#pragma unroll
            for (int g = 0; g < 4; ++g) *(LDSAS f32x4*)(lds0 + 8192 + w * 4096 + g * 1024 + lane * 16) = f32x4{a1[4 * g], a1[4 * g + 1], a1[4 * g + 2], a1[4 * g + 3]};
        }
        BAR_L();
        if (w < 4) {
            f32x4 p[4];
#pragma unroll
            for (int g = 0; g < 4; ++g) p[g] = lds_f4(lds0 + 8192 + (w ^ 2) * 4096 + g * 1024 + lane * 16);
            SBAR();
            unsigned y[8];
#pragma unroll
            for (int g = 0; g < 4; ++g) {
                y[2 * g] = pk2(a0[4 * g] + p[g][0], a0[4 * g + 1] + p[g][1]);
                y[2 * g + 1] = pk2(a0[4 * g + 2] + p[g][2], a0[4 * g + 3] + p[g][3]);
            }
            const rsrc_t ro = mk_rsrc(out, S * D * 2);
            const unsigned vo = (rt * 64 + wt * 32 + r32) * 1024 + (ct * 64 + kh * 32 + 8 * h) * 2;
#pragma unroll
            for (int g = 0; g < 2; ++g) {
                auto e0 = __builtin_amdgcn_permlane32_swap(y[4 * g], y[4 * g + 2], false, false);
                auto e1 = __builtin_amdgcn_permlane32_swap(y[4 * g + 1], y[4 * g + 3], false, false);
                __builtin_amdgcn_raw_buffer_store_b128(u32x4{e0[0], e1[0], e0[1], e1[1]}, ro, vo + g * 32, 0, OUT_AUX);
            }
        }
        STAMP(4, 3);
        STAMP(4, 4);
        FLUSH(4);
        asm volatile(".subsection 2\n.Lmain_end:\n\t.fill 64, 4, 0xBF800000\n\t.subsection 0");
    }
}

extern "C" void kernel_launch(void* const* d_in, const int* in_sizes, int n_in,
                              void* d_out, int out_size, void* d_ws, size_t ws_size,
                              hipStream_t stream) {
    const u16* x = (const u16*)d_in[0];
    const u16* wqkv = (const u16*)d_in[1];
    const u16* wo = (const u16*)d_in[2];
    const u16* wg = (const u16*)d_in[4];
    const u16* wu = (const u16*)d_in[5];
    const u16* wd = (const u16*)d_in[6];
    u16* out = (u16*)d_out;
    char* ws = (char*)d_ws;
    (void)hipFuncSetAttribute((const void*)k_block, hipFuncAttributeMaxDynamicSharedMemorySize, K2_LDS);
    k_block<<<256, 512, K2_LDS, stream>>>(x, wqkv, wo, wg, wu, wd, out, ws);
}
```

```cpp
#include <hip/hip_runtime.h>
#include <hip/hip_bf16.h>
#include <stdint.h>
#pragma clang diagnostic ignored "-Winline-asm"

typedef short s16x8 __attribute__((ext_vector_type(8)));
typedef short s16x4 __attribute__((ext_vector_type(4)));
typedef float f32x16 __attribute__((ext_vector_type(16)));
typedef float f32x4 __attribute__((ext_vector_type(4)));
typedef float f32x2 __attribute__((ext_vector_type(2)));
typedef unsigned u32x4 __attribute__((ext_vector_type(4)));
typedef unsigned u32x2 __attribute__((ext_vector_type(2)));
typedef unsigned u32x8 __attribute__((ext_vector_type(8)));
typedef __bf16 bf16x2 __attribute__((ext_vector_type(2)));
typedef unsigned short u16;
#define LDSAS __attribute__((address_space(3)))
#define GAS __attribute__((address_space(1)))
typedef __amdgpu_buffer_rsrc_t rsrc_t;

constexpr int S = 2048, D = 512, NH = 32, F = 1376, NU = F / 16;
constexpr float LN_EPS = 1e-5f;
constexpr float LOG2E = 1.4426950408889634f;

constexpr unsigned KV_CHUNK = 16384;
constexpr unsigned KV_HEAD = 8 * KV_CHUNK;
constexpr unsigned WS_EP = 0;
constexpr unsigned WS_FQ = 4096;
constexpr unsigned WS_FP = WS_FQ + 16 * 64 * 128;
constexpr unsigned WS_Q = WS_FP + 4096;
constexpr unsigned WS_KV = WS_Q + NH * 64 * 1024;
constexpr unsigned WS_O = WS_KV + NH * KV_HEAD;
constexpr unsigned WS_H = WS_O + 64 * 32 * 1024;
constexpr unsigned WS_ST = WS_H + 64 * 32 * 1024;
constexpr unsigned WS_HH = WS_ST + 64 * 16 * 256;
constexpr unsigned WS_END = WS_HH + 64 * NU * 1024;
constexpr int AUX_SC1 = 16;
#define STAMP_DECL
#define FLUSH(k) do {} while (0)
#define STAMP(k, i) do {} while (0)

#define STAMPV(i, v) do {} while (0)
#define SBAR() __builtin_amdgcn_sched_barrier(0)
#define RFL(x) ((unsigned)__builtin_amdgcn_readfirstlane(x))
#define PIN(x) asm volatile("" : "+v"(x))
#define MFMA(a, b, c) __builtin_amdgcn_mfma_f32_32x32x16_bf16((a), (b), (c), 0, 0, 0)
#define WAITV(n) asm volatile("s_waitcnt vmcnt(" #n ")" ::: "memory")
#define BAR_L() asm volatile("s_waitcnt lgkmcnt(0)\n\ts_barrier" ::: "memory")
#define BAR_0() asm volatile("s_barrier" ::: "memory")
#define BAR_VL(n) asm volatile("s_waitcnt vmcnt(" #n ") lgkmcnt(0)\n\ts_barrier" ::: "memory")

__device__ __forceinline__ rsrc_t mk_rsrc(const void* p, unsigned bytes) {
    return __builtin_amdgcn_make_buffer_rsrc((void*)p, (short)0, (int)bytes, 0x00020000);
}
template <bool SC1 = false> __device__ __forceinline__ void dma16(rsrc_t r, unsigned voff, unsigned soff, unsigned lds) {
    if constexpr (SC1) asm volatile("s_nop 4\n\ts_mov_b32 m0, %0\n\ts_nop 0\n\tbuffer_load_dwordx4 %1, %2, %3 offen sc1 lds"
                 :: "s"(__builtin_amdgcn_readfirstlane(lds)), "v"(voff), "s"(r), "s"(__builtin_amdgcn_readfirstlane(soff)) : "m0", "memory");
    else asm volatile("s_nop 4\n\ts_mov_b32 m0, %0\n\ts_nop 0\n\tbuffer_load_dwordx4 %1, %2, %3 offen lds"
                 :: "s"(__builtin_amdgcn_readfirstlane(lds)), "v"(voff), "s"(r), "s"(__builtin_amdgcn_readfirstlane(soff)) : "m0", "memory");
}
#define ST128(v, r, voff, soff, aux) do { const u32x4 d_ = (v); __builtin_amdgcn_raw_buffer_store_b128(d_, r, voff, soff, aux); asm volatile("s_nop 1" :: "v"(d_)); } while (0)
__device__ __forceinline__ unsigned pk2(float lo, float hi) {
    f32x2 v = {lo, hi};
    return __builtin_bit_cast(unsigned, __builtin_convertvector(v, bf16x2));
}
__device__ __forceinline__ float dot2(unsigned a, unsigned b, float c) {
    return __builtin_amdgcn_fdot2_f32_bf16(__builtin_bit_cast(bf16x2, a), __builtin_bit_cast(bf16x2, b), c, false);
}
__device__ __forceinline__ float swap_add(float v) {
    auto r = __builtin_amdgcn_permlane32_swap(__float_as_uint(v), __float_as_uint(v), false, false);
    return __uint_as_float(r[0]) + __uint_as_float(r[1]);
}
__device__ __forceinline__ float swap_max(float v) {
    auto r = __builtin_amdgcn_permlane32_swap(__float_as_uint(v), __float_as_uint(v), false, false);
    return __builtin_fmaxf(__uint_as_float(r[0]), __uint_as_float(r[1]));
}
__device__ __forceinline__ s16x8 lds_b128(unsigned a) { return *(const LDSAS s16x8*)(a); }
__device__ __forceinline__ f32x4 lds_f4(unsigned a) { return *(const LDSAS f32x4*)(a); }
__device__ __forceinline__ s16x8 lds_tr2(unsigned a0, unsigned a1) {
    s16x4 lo = __builtin_amdgcn_ds_read_tr16_b64_v4i16((LDSAS s16x4*)(a0));
    s16x4 hi = __builtin_amdgcn_ds_read_tr16_b64_v4i16((LDSAS s16x4*)(a1));
    return __builtin_shufflevector(lo, hi, 0, 1, 2, 3, 4, 5, 6, 7);
}
__device__ __forceinline__ float bflo(unsigned v) { return __uint_as_float(v << 16); }
__device__ __forceinline__ float bfhi(unsigned v) { return __uint_as_float(v & 0xffff0000u); }
constexpr unsigned ONES2 = 0x3f803f80u;
template <int I> struct ic { static constexpr int value = I; constexpr operator int() const { return I; } };
template <int B, int E, class Fn> __device__ __forceinline__ void sfor(Fn&& fn) { if constexpr (B < E) { fn(ic<B>{}); sfor<B + 1, E>(fn); } }

#define K1_X_AUX 2
#define K1_ABL 0
#define K1_ST_AUX 16
constexpr unsigned K1_W = 65536, K1_FLG = K1_W + 5 * 16384, K1_ST = K1_FLG + 512, K1_CS = K1_ST + 512;
__host__ __device__ constexpr unsigned k1_slot(int s) { return s < 5 ? K1_W + s * 16384 : s == 5 ? K1_W : s < 10 ? (s - 6) * 16384 : K1_W + (s - 9) * 16384; }
__device__ __forceinline__ void lds_spin_ge(unsigned addr, unsigned want) {
#pragma unroll 1
    for (unsigned n = 0; n < (1u << 22); ++n) {
        unsigned v; asm volatile("ds_read_b32 %0, %1\n\ts_waitcnt lgkmcnt(0)" : "=v"(v) : "v"(addr) : "memory");
        if ((unsigned)__builtin_amdgcn_readfirstlane(v) >= want) break;
        __builtin_amdgcn_s_sleep(1);
    }
}
__device__ __forceinline__ void lds_post(unsigned addr, unsigned v) {
    asm volatile("s_waitcnt lgkmcnt(0)\n\tds_write_b32 %0, %1" :: "v"(addr), "v"(v) : "memory");
}
__device__ __forceinline__ void lds_inc(unsigned addr, int lane) {
    asm volatile("s_waitcnt lgkmcnt(0)" ::: "memory");
    if (lane == 0) asm volatile("ds_add_u32 %0, %1" :: "v"(addr), "v"(1u) : "memory");
}
typedef unsigned u32x16 __attribute__((ext_vector_type(16)));
template <int CTRL> __device__ __forceinline__ float dpp_add(float v) {
    return v + __builtin_bit_cast(float, __builtin_amdgcn_update_dpp(0, __builtin_bit_cast(int, v), CTRL, 0xf, 0xf, true));
}

constexpr unsigned K2_LDS = 163840;
constexpr unsigned L_WO = 0, L_GU = 65536;
constexpr unsigned L_TAB = 61440;
constexpr unsigned L_FLAG = 16384 + 12288;
constexpr unsigned SPIN_CAP = 1u << 20;
#define POLL_DELAY_Q 24
#define OUT_AUX 2
#define DN_B0 20
#define ABL_GU 0
#define DELAY_O 0
#define DELAY_H 8
#define DELAY_G 8
constexpr int GU_AW = 8, G2_AW = 12;
#define TAG_T1 "v_max3_u32 v120, v[%c[cr]], v[%c[cr]+1], v[%c[cr]+2]\n\t"
#define TAG_T2 "v_max_u32 v120, v120, v[%c[cr]+3]\n\tv_cmp_eq_u32 vcc, -1, v120\n\t"
#define TAG_RELOAD(RB, AW) "s_add_u32 %[t], %[s0], (tagj>>2)*4096\n\tbuffer_load_dwordx4 v[" #RB "+4*(tagj%%" #AW "):" #RB "+3+4*(tagj%%" #AW ")], %[l16], %[rs], %[t] offen offset:(tagj&3)*1024 sc1\n\t"
#define TAG_T3(RB, AW) \
    "s_cbranch_vccnz 5f\n6:\n\t" \
    ".subsection 1\n5:\n\ts_cmp_eq_u32 %[bud], 0\n\ts_cbranch_scc1 6b\n\ts_sub_u32 %[bud], %[bud], 1\n\t.set tagj, %c[ci]\n\t" TAG_RELOAD(RB, AW) \
    "s_waitcnt vmcnt(0)\n\t" TAG_T1 TAG_T2 "s_cbranch_vccnz 5b\n\t.rept %c[cnt]-1\n\t.set tagj, tagj+1\n\t" TAG_RELOAD(RB, AW) ".endr\n\ts_branch 6b\n\t.subsection 0\n\t"
#define TAG_LOAD "buffer_load_dwordx4 v[%c[lr]:%c[lr]+3], %[l16], %[rs], %[sa] offen offset:%c[lo] sc1\n\t.if %c[lo] == 3072\n\ts_add_u32 %[sa], %[sa], 0x1000\n\t.endif\n\t"

__device__ __forceinline__ void wait_flags(rsrc_t rs, unsigned base, int n, unsigned ep, int lane) {
    const unsigned v = (lane < n ? lane : n - 1) * 128;
#pragma unroll 1
    for (unsigned spins = 0; spins < SPIN_CAP; ++spins) {
        const unsigned f = __builtin_amdgcn_raw_buffer_load_b32(rs, v, base, AUX_SC1);
        if (__all(f == ep)) break;
        __builtin_amdgcn_s_sleep(1);
    }
    asm volatile("" ::: "memory");
}

__device__ __forceinline__ unsigned w64_voff(int lane) { return (lane >> 3) * 1024 + (((lane & 7) ^ (((lane >> 4) & 1) << 2)) << 4); }
__device__ __forceinline__ unsigned w64_tr(int lane, int n) {
    const int q = (lane & 15) >> 2, p = lane & 3, blk = (lane >> 4) & 1, h = lane >> 5;
    return (4 * h + q) * 128 + (((4 * n + 2 * blk + (p >> 1)) ^ (((q >> 1) & 1) << 2)) << 4) + 8 * (p & 1);
}

struct G2 { u32x16 r0, r1, r2; u32x8 w0, w1, w2; f32x16 a0, a1; unsigned tb[6], sa, s0, l16, budget; rsrc_t rs;
};
__host__ __device__ constexpr int g2_w(int DN, int n) {
    return !DN ? n * 2048 : n < 15 ? n * 2048 : n < 39 ? 65536 + (n - 15) * 2048 : n < 40 ? 131072 : (n - 40) * 2048;
}
template <int i, int N, int DN, int B0, int B1> __device__ __forceinline__ void g2_steps(G2& g) {
    constexpr int AW = G2_AW, j = i + AW - 1, n = i + 2, sel = n < N ? g2_w(DN, n) >> 16 : 3;
    unsigned tq;
#define G2_RD(P, R) "ds_read_b64_tr_b16 v[%c[nr]+" #R ":%c[nr]+" #R "+1], %[" P "] offset:%c[no]\n\tds_read_b64_tr_b16 v[%c[nr]+" #R "+2:%c[nr]+" #R "+3], %[" P "] offset:%c[no]+1024\n\t"
#define G2_RDS(P0, P1, P2, R) ".if %c[sel] == 0\n\t" G2_RD(P0, R) ".endif\n\t.if %c[sel] == 1\n\t" G2_RD(P1, R) ".endif\n\t.if %c[sel] == 2\n\t" G2_RD(P2, R) ".endif\n\t"
    if constexpr (i == 0)
        asm volatile(".set tagj, 0\n\t.rept 11\n\tbuffer_load_dwordx4 v[128+4*tagj:131+4*tagj], %[l16], %[rs], %[sa] offen offset:(tagj&3)*1024 sc1\n\t"
                     ".if (tagj&3) == 3\n\ts_add_u32 %[sa], %[sa], 0x1000\n\t.endif\n\t.set tagj, tagj+1\n\t.endr\n\t"
                     ".set tagj, 0\n\t.rept 2\n\tds_read_b64_tr_b16 v[96+8*tagj:97+8*tagj], %[b0] offset:tagj*2048\n\tds_read_b64_tr_b16 v[98+8*tagj:99+8*tagj], %[b0] offset:tagj*2048+1024\n\t"
                     "ds_read_b64_tr_b16 v[100+8*tagj:101+8*tagj], %[b1] offset:tagj*2048\n\tds_read_b64_tr_b16 v[102+8*tagj:103+8*tagj], %[b1] offset:tagj*2048+1024\n\t.set tagj, tagj+1\n\t.endr\n\t"
                     "s_waitcnt vmcnt(10) lgkmcnt(4)\n\t" TAG_T1 TAG_T2 TAG_T3(128, 12)
                     : "={v[128:143]}"(g.r0), "={v[144:159]}"(g.r1), "={v[160:175]}"(g.r2), "={v[96:103]}"(g.w0), "={v[104:111]}"(g.w1), "={v[112:119]}"(g.w2), [sa] "+s"(g.sa),
                       "+{v[64:79]}"(g.a0), "+{v[80:95]}"(g.a1), [bud] "+s"(g.budget), [t] "=&s"(tq)
                     : [l16] "v"(g.l16), [rs] "s"(g.rs), [s0] "s"(g.s0), [b0] "v"(g.tb[0]), [b1] "v"(g.tb[1]), [cr] "n"(128), [ci] "n"(0), [cnt] "n"(AW - 1) : "vcc", "scc", "v120", "memory");
    asm volatile("v_mfma_f32_32x32x16_bf16 v[64:79], v[%c[wr]:%c[wr]+3], v[%c[ar]:%c[ar]+3], v[64:79]\n\t"
                 ".if %c[ld]\n\t" TAG_LOAD ".endif\n\t"
                 ".if %c[bar]\n\ts_waitcnt lgkmcnt(0)\n\ts_barrier\n\t.endif\n\t"
                 G2_RDS("b0", "b2", "b4", 0)
                 ".if %c[cnt]\n\ts_waitcnt vmcnt(%c[vn])\n\t" TAG_T1 ".endif\n\t"
                 "v_mfma_f32_32x32x16_bf16 v[80:95], v[%c[wr]+4:%c[wr]+7], v[%c[ar]:%c[ar]+3], v[80:95]\n\t"
                 G2_RDS("b1", "b3", "b5", 4)
                 ".if %c[cnt]\n\t" TAG_T2 "s_waitcnt lgkmcnt(%c[ln])\n\t" TAG_T3(128, 12) ".else\n\ts_nop 15\n\t.endif"
                 : "+{v[64:79]}"(g.a0), "+{v[80:95]}"(g.a1), "+{v[128:143]}"(g.r0), "+{v[144:159]}"(g.r1), "+{v[160:175]}"(g.r2), "+{v[96:103]}"(g.w0), "+{v[104:111]}"(g.w1), "+{v[112:119]}"(g.w2),
                   [sa] "+s"(g.sa), [bud] "+s"(g.budget), [t] "=&s"(tq)
                 : [l16] "v"(g.l16), [rs] "s"(g.rs), [s0] "s"(g.s0), [b0] "v"(g.tb[0]), [b1] "v"(g.tb[1]), [b2] "v"(g.tb[2]), [b3] "v"(g.tb[3]), [b4] "v"(g.tb[4]), [b5] "v"(g.tb[5]),
                   [ld] "n"(j < N ? 1 : 0), [lr] "n"(128 + 4 * (j % AW)), [lo] "n"((j & 3) * 1024), [bar] "n"(i == B0 || i == B1 ? 1 : 0), [sel] "n"(sel),
                   [nr] "n"(96 + 8 * (n % 3)), [no] "n"(n < N ? g2_w(DN, n) & 65535 : 0), [vn] "n"(j < N ? AW - 2 : i + 1 < N ? N - 2 - i : 0), [ln] "n"(n < N ? 4 : 0),
                   [ar] "n"(128 + 4 * (i % AW)), [wr] "n"(96 + 8 * (i % 3)), [cr] "n"(128 + 4 * ((i + 1) % AW)), [ci] "n"(i + 1), [cnt] "n"(j < N ? AW - 1 : N - 1 - i) : "vcc", "scc", "v120", "memory");
    if constexpr (i + 1 < N) g2_steps<i + 1, N, DN, B0, B1>(g);
}
__host__ __device__ constexpr int gu_dma(int s) { return (s >= 16 && s < 24 && !(s & 1)) || (s >= 24 && s < 30); }
__host__ __device__ constexpr int gu_ops(int s) { return (s + GU_AW - 1 < 32 ? 1 : 0) + (s >= 2 && s < 10 ? 1 : 0) + gu_dma(s); }
__host__ __device__ constexpr int gu_younger(int k) {
    int c = 0, s = k - GU_AW + 1;
    if (s < 0) { c = GU_AW - 2 - k; s = 0; } else { c = gu_ops(s) - 1; ++s; }
    for (; s < k; ++s) c += gu_ops(s);
    return c;
}

struct GU { u32x16 r0, r1, st; u32x8 w0, w1, w2; f32x16 a0, a1, a2; unsigned wa[3], sa, s0, sv, ss, l16, budget, dm, ds, dv, dm2, ds2, dm3, ds3; rsrc_t rs, rd; };
template <int i, int E> __device__ __forceinline__ void gu_steps(GU& g) {
    constexpr int AW = GU_AW, j = i + AW - 1, n = i + 1;
    unsigned tq;
#define GU_RD(R, B) "ds_read_b64_tr_b16 v[" R ":" R "+1], %[" B "] offset:%c[no]\n\tds_read_b64_tr_b16 v[" R "+2:" R "+3], %[" B "] offset:%c[no]+768\n\t"
    if constexpr (i == 0)
        asm volatile(".set tagj, 0\n\t.rept 7\n\tbuffer_load_dwordx4 v[140+4*tagj:143+4*tagj], %[l16], %[rs], %[sa] offen offset:(tagj&3)*1024 sc1\n\t"
                     ".if (tagj&3) == 3\n\ts_add_u32 %[sa], %[sa], 0x1000\n\t.endif\n\t.set tagj, tagj+1\n\t.endr\n\t" GU_RD("112", "b0") GU_RD("116", "b1") GU_RD("124", "b2")
                     "s_waitcnt vmcnt(6) lgkmcnt(0)\n\t" TAG_T1 TAG_T2 TAG_T3(140, 8)
                     : "={v[140:155]}"(g.r0), "={v[156:171]}"(g.r1), "={v[172:187]}"(g.st), "={v[112:119]}"(g.w0), "={v[124:131]}"(g.w1), "={v[132:139]}"(g.w2), [sa] "+s"(g.sa),
                       "+{v[64:79]}"(g.a0), "+{v[80:95]}"(g.a1), "+{v[96:111]}"(g.a2), [bud] "+s"(g.budget), [t] "=&s"(tq)
                     : [l16] "v"(g.l16), [rs] "s"(g.rs), [s0] "s"(g.s0), [b0] "v"(g.wa[0]), [b1] "v"(g.wa[1]), [b2] "v"(g.wa[2]), [no] "n"(0), [cr] "n"(140), [ci] "n"(0), [cnt] "n"(AW - 1)
                     : "vcc", "scc", "v120", "memory");
    asm volatile(".if %c[i] == 15 || %c[i] == 23\n\ts_barrier\n\t.endif\n\t"
                 "v_mfma_f32_32x32x16_bf16 v[64:79], v[%c[c0]:%c[c0]+3], v[%c[ar]:%c[ar]+3], v[64:79]\n\t"
                 ".if %c[rd]\n\t" GU_RD("%c[n0]", "b0") GU_RD("%c[n1]", "b1") ".endif\n\t"
                 "v_mfma_f32_32x32x16_bf16 v[80:95], v[%c[c1]:%c[c1]+3], v[%c[ar]:%c[ar]+3], v[80:95]\n\t"
                 ".if %c[rd]\n\t" GU_RD("%c[n2]", "b2") ".endif\n\t"
                 ".if %c[ld]\n\t" TAG_LOAD ".endif\n\t"
                 ".if %c[sk] >= 0\n\tbuffer_load_dwordx2 v[172+2*%c[sk]:173+2*%c[sk]], %[sv], %[rs], %[ss] offen offset:%c[sk]*256 sc1\n\t.endif\n\t"
                 ".if %c[i] == 26\n\ts_mov_b32 %[dm], %[dm2]\n\ts_mov_b32 %[ds], %[ds2]\n\t.endif\n\t.if %c[i] == 29\n\ts_mov_b32 %[dm], %[dm3]\n\ts_mov_b32 %[ds], %[ds3]\n\t.endif\n\t"
                 ".if %c[dma]\n\ts_mov_b32 m0, %[dm]\n\ts_add_u32 %[dm], %[dm], 0x400\n\tbuffer_load_dwordx4 %[dv], %[rwd], %[ds] offen lds\n\ts_add_u32 %[ds], %[ds], 0x2000\n\t.endif\n\t"
                 "v_mfma_f32_32x32x16_bf16 v[96:111], v[%c[c2]:%c[c2]+3], v[%c[ar]:%c[ar]+3], v[96:111]\n\t"
                 ".if %c[cnt]\n\ts_waitcnt vmcnt(%c[vn])\n\t" TAG_T1 TAG_T2 "s_waitcnt lgkmcnt(0)\n\t" TAG_T3(140, 8) ".endif"
                 : "+{v[64:79]}"(g.a0), "+{v[80:95]}"(g.a1), "+{v[96:111]}"(g.a2), "+{v[140:155]}"(g.r0), "+{v[156:171]}"(g.r1), "+{v[172:187]}"(g.st),
                   "+{v[112:119]}"(g.w0), "+{v[124:131]}"(g.w1), "+{v[132:139]}"(g.w2), [sa] "+s"(g.sa), [bud] "+s"(g.budget), [t] "=&s"(tq), [dm] "+s"(g.dm), [ds] "+s"(g.ds)
                 : [l16] "v"(g.l16), [rs] "s"(g.rs), [s0] "s"(g.s0), [dv] "v"(g.dv), [rwd] "s"(g.rd), [dm2] "s"(g.dm2), [ds2] "s"(g.ds2), [dm3] "s"(g.dm3), [ds3] "s"(g.ds3),
                   [dma] "n"(gu_dma(i)), [sv] "v"(g.sv), [ss] "s"(g.ss), [b0] "v"(g.wa[0]), [b1] "v"(g.wa[1]), [b2] "v"(g.wa[2]),
                   [ld] "n"(j < 32 ? 1 : 0), [lr] "n"(140 + 4 * (j % AW)), [lo] "n"((j & 3) * 1024), [sk] "n"(i >= 2 && i < 10 ? i - 2 : -1), [rd] "n"(n < 32 ? 1 : 0), [no] "n"(n * 1536),
                   [n0] "n"(n & 1 ? 128 : 112), [n1] "n"(n & 1 ? 132 : 116), [n2] "n"(n & 1 ? 136 : 124), [c0] "n"(i & 1 ? 128 : 112), [c1] "n"(i & 1 ? 132 : 116), [c2] "n"(i & 1 ? 136 : 124),
                   [vn] "n"(n < 32 ? gu_younger(n) : 0), [ar] "n"(140 + 4 * (i % AW)), [i] "n"(i), [cr] "n"(140 + 4 * (n % AW)), [ci] "n"(n), [cnt] "n"(j < 32 ? AW - 1 : 31 - i)
                 : "vcc", "scc", "m0", "v120", "memory");
    if constexpr (i + 1 < E) gu_steps<i + 1, E>(g);
}

__global__ __launch_bounds__(512, 2) void k_block(const u16* __restrict__ x, const u16* __restrict__ wqkv, const u16* __restrict__ wo, const u16* __restrict__ wg,
                                                  const u16* __restrict__ wu, const u16* __restrict__ wd, u16* __restrict__ out, char* __restrict__ ws) {
    extern __shared__ __attribute__((aligned(16))) char smem[];
    const unsigned lds0 = (unsigned)(uintptr_t)(LDSAS char*)smem;
    const int tid = threadIdx.x, lane = tid & 63;
    const int w = __builtin_amdgcn_readfirstlane(tid >> 6);
    const int b = blockIdx.x, xcd = b & 7, bi = b >> 3;
    const int r32 = lane & 31, h = lane >> 5;
    const rsrc_t rs = mk_rsrc(ws, WS_END);
    const int wt = w & 1, kh = (w >> 1) & 1;
    STAMP_DECL;
    unsigned ep;
    {
    const int rt = (xcd >> 1) * 8 + (bi & 7), cb = (xcd & 1) * 4 + (bi >> 3), rot = bi & 3;
    const int wc = (w >> 1) & 1, hw = w - 4;
    STAMP(0, 0);
    const rsrc_t rw = mk_rsrc(wqkv, D * 3 * D * 2);
    const unsigned wv = (lane >> 3) * 3072 + (((lane & 7) ^ (((lane >> 4) & 1) << 2)) << 4);
    const unsigned fa = lds0 + K1_FLG;
    if (tid < 12) *(LDSAS unsigned*)(fa + tid * 4) = 0u;
    if (w >= 4) {
        BAR_L();
        const unsigned pa = fa + hw * 4;
        auto issue = [&](int s) {
            const unsigned so = RFL(((((s & 3) + rot) & 3) * 128 + hw * 32) * 3072 + (s >> 2) * 1024 + cb * 128);
            asm volatile("s_nop 4" :: "s"(so));
#pragma unroll
            for (int i = 0; i < 4; ++i)
                asm volatile("s_mov_b32 m0, %0\n\ts_nop 0\n\tbuffer_load_dwordx4 %1, %2, %3 offen lds"
                             :: "s"(lds0 + k1_slot(s) + (4 * hw + i) * 1024), "v"(wv), "s"(rw), "s"(so + i * 8 * 3072) : "m0", "memory");
        };
#define K1_LAND(N, s) asm volatile("s_waitcnt vmcnt(" #N ")\n\tds_write_b32 %0, %1" :: "v"(pa), "v"((s) + 1u) : "memory")
        issue(0); issue(1);
        issue(2); K1_LAND(8, 0);
        issue(3); K1_LAND(8, 1);
        issue(4); K1_LAND(8, 2); K1_LAND(4, 3); K1_LAND(0, 4);
        lds_spin_ge(fa + 28, 4);
        issue(5);
        lds_spin_ge(fa + 20, 4);
        issue(6); issue(7); K1_LAND(8, 5);
        issue(8); K1_LAND(8, 6);
        issue(9); K1_LAND(8, 7);
        lds_spin_ge(fa + 24, 4);
        issue(10); K1_LAND(8, 8);
        issue(11); K1_LAND(8, 9);
        K1_LAND(4, 10); K1_LAND(0, 11);
        if (w == 4) { unsigned e; asm volatile("s_load_dword %0, %1, 0\n\ts_waitcnt lgkmcnt(0)" : "=s"(e) : "s"(ws) : "memory"); *(LDSAS unsigned*)(fa + 48) = e + 1u; }
        else {
            unsigned long long pc; unsigned len, o = bi * 128 + (w - 5) * 24576, t, d0, d1, d2, d3;
#define KCP(D) "s_min_u32 %[t], %[o], %[len]\n\ts_load_dword " D ", %[pc], %[t]\n\ts_add_u32 %[o], %[o], 0x1000\n\t"
            asm volatile("s_getpc_b64 %[pc]\n.Lqkv_pc:\n\ts_mov_b32 %[len], .Lmain_end-.Lqkv_pc-4\n\t" KCP("%[d0]") KCP("%[d1]") KCP("%[d2]") KCP("%[d3]") KCP("%[d0]") KCP("%[d1]") "s_waitcnt lgkmcnt(0)"
                         : [pc] "=&s"(pc), [len] "=&s"(len), [o] "+s"(o), [t] "=&s"(t), [d0] "=&s"(d0), [d1] "=&s"(d1), [d2] "=&s"(d2), [d3] "=&s"(d3) :: "scc", "memory");
        }
        BAR_L();
        ep = RFL(*(const LDSAS unsigned*)(fa + 48));
        if (w == 4) { __builtin_amdgcn_s_sleep(POLL_DELAY_Q); wait_flags(rs, WS_FQ + (xcd * 2 + (bi >> 4)) * 8192, 64, ep, lane); }
    } else {
    {
        const rsrc_t rx = mk_rsrc(x, S * D * 2);
        u32x4 r[2][8];
#pragma unroll
        for (int p = 0; p < 2; ++p)
#pragma unroll
            for (int g = 0; g < 8; ++g)
                r[p][g] = __builtin_amdgcn_raw_buffer_load_b128(rx, (lane >> 3) * 1024 + (((lane & 7) ^ ((4 * p + (lane >> 4)) & 7)) << 4) + g * 128, (rt * 64 + w * 16 + p * 8) * 1024, K1_X_AUX);
        SBAR(); BAR_L(); SBAR();
#pragma unroll
        for (int p = 0; p < 2; ++p) {
            float s0 = 0.f, s1 = 0.f, q0 = 0.f, q1 = 0.f;
#pragma unroll
            for (int g = 0; g < 8; ++g) { s0 = dot2(r[p][g][0], ONES2, s0); q0 = dot2(r[p][g][0], r[p][g][0], q0); s1 = dot2(r[p][g][1], ONES2, s1); q1 = dot2(r[p][g][1], r[p][g][1], q1);
                                          s0 = dot2(r[p][g][2], ONES2, s0); q0 = dot2(r[p][g][2], r[p][g][2], q0); s1 = dot2(r[p][g][3], ONES2, s1); q1 = dot2(r[p][g][3], r[p][g][3], q1); }
            if (p == 0) STAMP(0, 1);
            float sum = s0 + s1, sq = q0 + q1;
            sum = dpp_add<0xB1>(sum); sq = dpp_add<0xB1>(sq); sum = dpp_add<0x4E>(sum); sq = dpp_add<0x4E>(sq); sum = dpp_add<0x141>(sum); sq = dpp_add<0x141>(sq);
            const float mean = sum * (1.f / D);
            const float rstd = __builtin_amdgcn_rsqf(__builtin_fmaxf(sq * (1.f / D) - mean * mean, 0.f) + LN_EPS), nb = -mean * rstd;
#pragma unroll
            for (int g = 0; g < 8; g += 2) {
                f32x2 t[8];
#pragma unroll
                for (int i = 0; i < 8; ++i) { const unsigned v = r[p][g + (i >> 2)][i & 3]; t[i] = f32x2{bflo(v), bfhi(v)}; }
                asm volatile("" : "+v"(t[0]), "+v"(t[1]), "+v"(t[2]), "+v"(t[3]), "+v"(t[4]), "+v"(t[5]), "+v"(t[6]), "+v"(t[7]));
#pragma unroll
                for (int i = 0; i < 8; ++i) t[i] = t[i] * rstd + nb;
                asm volatile("" : "+v"(t[0]), "+v"(t[1]), "+v"(t[2]), "+v"(t[3]), "+v"(t[4]), "+v"(t[5]), "+v"(t[6]), "+v"(t[7]));
                u32x4 y0, y1;
#pragma unroll
                for (int i = 0; i < 4; ++i) { y0[i] = pk2(t[i][0], t[i][1]); y1[i] = pk2(t[4 + i][0], t[4 + i][1]); }
                *(LDSAS u32x4*)(lds0 + (2 * w + p) * 1024 + lane * 16 + g * 8192) = y0;
                *(LDSAS u32x4*)(lds0 + (2 * w + p) * 1024 + lane * 16 + g * 8192 + 8192) = y1;
            }
            SBAR();
        }
    }
    lds_inc(fa + 16, lane);
    STAMP(0, 3);
    u32x16 xa0, xa1, xa2, xa3, xa4, xa5, xa6, xa7;
    u32x4 w0, w1, w2, w3, w4, fl;
    f32x16 acA, acB;
    unsigned xb[4], tq;
#pragma unroll
    for (int m = 0; m < 4; ++m) xb[m] = lds0 + (32 * wt + r32) * 128 + (((2 * m + h) ^ ((r32 >> 1) & 7)) << 4) + rot * 16384;
    const int q4 = (lane & 15) >> 2, p4 = lane & 3, blk = (lane >> 4) & 1;
    const unsigned tA = lds0 + (8 * h + q4) * 128 + (((4 * wc + 2 * blk + (p4 >> 1)) ^ (((q4 >> 1) & 1) << 2)) << 4) + 8 * (p4 & 1);
    const unsigned tB = tA + 65536, tC = tA + 131072;
    const int tt = rt * 2 + wt, hd = cb * 4 + wc * 2;
#define KXR(R, m, O) "ds_read_b128 v[" #R ":" #R "+3], %[x" #m "] offset:" #O "\n\t"
#define KXN(m) "v_add_u32 %[x" #m "], 0x4000, %[x" #m "]\n\tv_and_b32 %[x" #m "], 0xffff, %[x" #m "]\n\t"
#define KCHK(ST) "v_min3_u32 v116, v116, v117, v118\n\tv_min_u32 v116, v116, v119\n\ts_nop 0\n\tv_readfirstlane_b32 %[t], v116\n\ts_nop 0\n\ts_cmp_gt_u32 %[t], " ST "\n\t"
#define KSPIN(ST) "1:\n\tds_read_b128 v[116:119], %[fa]\n\ts_waitcnt lgkmcnt(0)\n\t" KCHK(ST) "s_cbranch_scc0 1b\n\t"
#define KWR(R, O) "ds_read_b64_tr_b16 v[" #R ":" #R "+1], %[tb] offset:" #O "\n\tds_read_b64_tr_b16 v[" #R "+2:" #R "+3], %[tb] offset:" #O "+512\n\t"
    asm volatile("3:\n\tds_read_b32 v116, %[fa] offset:16\n\ts_waitcnt lgkmcnt(0)\n\tv_readfirstlane_b32 %[t], v116\n\ts_nop 0\n\ts_cmp_gt_u32 %[t], 3\n\ts_cbranch_scc0 3b\n\t"
                 KXR(128, 0, 0) KXR(132, 1, 0) KXR(136, 2, 0) KXR(140, 3, 0) KXR(144, 0, 8192) KXR(148, 1, 8192) KXR(152, 2, 8192) KXR(156, 3, 8192) KXN(0) KXN(1) KXN(2) KXN(3)
                 KSPIN("0") KWR(96, 0) KWR(100, 2048) KWR(104, 4096) KWR(108, 6144)
                 : "={v[116:119]}"(fl), [t] "=&s"(tq), "={v[96:99]}"(w0), "={v[100:103]}"(w1), "={v[104:107]}"(w2), "={v[108:111]}"(w3), "={v[112:115]}"(w4), "={v[64:79]}"(acA), "={v[80:95]}"(acB),
                   "={v[128:143]}"(xa0), "={v[144:159]}"(xa1), "={v[160:175]}"(xa2), "={v[176:191]}"(xa3), "={v[192:207]}"(xa4), "={v[208:223]}"(xa5), "={v[224:239]}"(xa6), "={v[240:255]}"(xa7),
                   [x0] "+v"(xb[0]), [x1] "+v"(xb[1]), [x2] "+v"(xb[2]), [x3] "+v"(xb[3])
                 : [fa] "v"(fa), [tb] "v"(tB) : "scc", "memory");
    STAMP(0, 4);
    auto epi = [&](int m, const f32x16& acc, int hh) {
        const float sc = 0.25f * LOG2E;
        u32x4 o;
#pragma unroll
        for (int i = 0; i < 4; ++i) o[i] = m == 0 ? pk2(acc[8 * hh + 2 * i] * sc, acc[8 * hh + 2 * i + 1] * sc) : pk2(acc[8 * hh + 2 * i], acc[8 * hh + 2 * i + 1]);
        if (m == 0) ST128(o, rs, lane * 16, WS_Q + ((hd + hh) * 64 + tt) * 1024, K1_ST_AUX);
        else if (m == 1) ST128(o, rs, lane * 16, WS_KV + (hd + hh) * KV_HEAD + (tt >> 3) * KV_CHUNK + (tt & 7) * 1024, K1_ST_AUX);
        else ST128(o, rs, (r32 >> 4) * KV_HEAD + (16 * h + (lane & 15)) * 16 + hh * 512,
                                                    WS_KV + hd * KV_HEAD + (tt >> 3) * KV_CHUNK + 8192 + (tt & 7) * 1024, K1_ST_AUX);
    };
    sfor<0, 96>([&](auto G) {
        constexpr int g = G.value, n = g + 4, m = g >> 5;
        constexpr unsigned o = k1_slot(n >> 3) + (n & 7) * 2048, oi = o & 65535;
        constexpr int aR = m == 1 ? 80 : 64, wR = 96 + 4 * (g % 5), nR = 96 + 4 * (n % 5), xR = 128 + 4 * (g & 31);
        constexpr int nx = (g - 4 >= 0 && g - 4 < 12) + (g - 3 >= 0 && g - 3 < 12) + (g - 2 >= 0 && g - 2 < 12) + (g - 1 >= 0 && g - 1 < 12), wc_ = n >= 96 ? 0 : (8 + 2 * nx > 14 ? 14 : 8 + 2 * nx);
#define KRD(B) "ds_read_b64_tr_b16 v[%c[nr]:%c[nr]+1], %[" B "] offset:%c[oi]\n\tds_read_b64_tr_b16 v[%c[nr]+2:%c[nr]+3], %[" B "] offset:%c[oi]+512\n\t"
        asm volatile(".if %c[bar] == 1\n\t" KCHK("%c[st]") "s_cbranch_scc0 1f\n2:\n\t.subsection 1\n" KSPIN("%c[st]") "s_branch 2b\n\t.subsection 0\n\t.endif\n\t"
                     ".if %c[bar] == 2\n\tds_read_b128 v[116:119], %[fa]\n\t.endif\n\t"
                     ".if %c[rd] == 1\n\t" KRD("tb") ".endif\n\t.if %c[rd] == 2\n\t" KRD("tc") ".endif\n\t.if %c[rd] == 3\n\t" KRD("ta") ".endif\n\t"
                     "s_waitcnt lgkmcnt(%c[wn])\n\t"
                     ".if %c[md] == 0\n\tv_mfma_f32_32x32x16_bf16 v[%c[ar]:%c[ar]+15], v[%c[wr]:%c[wr]+3], v[%c[xr]:%c[xr]+3], v[%c[ar]:%c[ar]+15]\n\t.endif\n\t"
                     ".if %c[md] == 1\n\tv_mfma_f32_32x32x16_bf16 v[%c[ar]:%c[ar]+15], v[%c[xr]:%c[xr]+3], v[%c[wr]:%c[wr]+3], v[%c[ar]:%c[ar]+15]\n\t.endif\n\t"
                     ".if %c[md] == 2\n\tv_mfma_f32_32x32x16_bf16 v[%c[ar]:%c[ar]+15], v[%c[xr]:%c[xr]+3], v[%c[wr]:%c[wr]+3], 0\n\t.endif\n\t"
                     ".if %c[md] == 3\n\tv_mfma_f32_32x32x16_bf16 v[%c[ar]:%c[ar]+15], v[%c[wr]:%c[wr]+3], v[%c[xr]:%c[xr]+3], 0\n\t.endif\n\t"
                     ".if %c[xm] == 1\n\tds_read_b128 v[%c[xd]:%c[xd]+3], %[x0] offset:%c[xo]\n\tds_read_b128 v[%c[xd]+4:%c[xd]+7], %[x1] offset:%c[xo]\n\t.endif\n\t"
                     ".if %c[xm] == 2\n\tds_read_b128 v[%c[xd]:%c[xd]+3], %[x2] offset:%c[xo]\n\tds_read_b128 v[%c[xd]+4:%c[xd]+7], %[x3] offset:%c[xo]\n\t.endif\n\t"
                     ".if %c[xn]\n\t" KXN(0) KXN(1) KXN(2) KXN(3) ".endif"
                     : "+{v[64:79]}"(acA), "+{v[80:95]}"(acB), "+{v[96:99]}"(w0), "+{v[100:103]}"(w1), "+{v[104:107]}"(w2), "+{v[108:111]}"(w3), "+{v[112:115]}"(w4),
                       "+{v[128:143]}"(xa0), "+{v[144:159]}"(xa1), "+{v[160:175]}"(xa2), "+{v[176:191]}"(xa3), "+{v[192:207]}"(xa4), "+{v[208:223]}"(xa5), "+{v[224:239]}"(xa6), "+{v[240:255]}"(xa7),
                       "+{v[116:119]}"(fl), [t] "=&s"(tq), [x0] "+v"(xb[0]), [x1] "+v"(xb[1]), [x2] "+v"(xb[2]), [x3] "+v"(xb[3])
                     : [ta] "v"(tA), [tb] "v"(tB), [tc] "v"(tC), [fa] "v"(fa), [st] "n"(n >> 3), [wn] "n"(wc_),
                       [bar] "n"(K1_ABL ? 0 : n < 96 && (n & 7) == 0 ? 1 : n < 92 && (n & 7) == 4 ? 2 : 0), [rd] "n"(n >= 96 ? 0 : o < 65536 ? 3 : o < 131072 ? 1 : 2), [oi] "n"(oi), [nr] "n"(nR),
                       [md] "n"(m < 2 ? ((g & 31) == 0 ? 3 : 0) : g == 64 ? 2 : 1), [ar] "n"(aR), [wr] "n"(wR), [xr] "n"(xR),
                       [xm] "n"(g < 12 ? 1 + (g & 1) : 0), [xd] "n"(160 + 8 * g), [xo] "n"(((g >> 1) & 1) * 8192), [xn] "n"(g < 11 && (g & 3) == 3 ? 1 : 0) : "scc", "memory");
        if (g == 7 && lane == 0) asm volatile("ds_add_u32 %0, %1" :: "v"(fa + 28), "v"(1u) : "memory");
        if (g == 16 && lane == 0) asm volatile("ds_add_u32 %0, %1" :: "v"(fa + 20), "v"(1u) : "memory");
        if (g == 23 && lane == 0) asm volatile("ds_add_u32 %0, %1" :: "v"(fa + 24), "v"(1u) : "memory");
        if (g == 36 || g == 38) epi(0, acA, (g - 36) >> 1);
        if (g == 68 || g == 70) epi(1, acB, (g - 68) >> 1);
    });
    STAMP(0, 2);
    asm volatile("s_nop 15" : "+{v[64:79]}"(acA));
    epi(2, acA, 0); epi(2, acA, 1);
    SBAR(); BAR_L();
    ep = RFL(*(const LDSAS unsigned*)(fa + 48));
    WAITV(0);
    if (lane == 0) __builtin_amdgcn_raw_buffer_store_b32(ep, rs, 0, WS_FQ + ((cb * 2 + wc) * 64 + tt) * 128, AUX_SC1);
    STAMP(0, 5);
    FLUSH(0);
    }
    }

    const int rt = xcd * 4 + (bi >> 3), ct = bi & 7;
    u32x4 hreg[2];
    u32x8 xres;

    auto issue_wo = [&](int first, int n) {
        const rsrc_t rwo = mk_rsrc(wo, D * D * 2);
        const unsigned v = w64_voff(lane);
        for (int p = first; p < first + n; ++p) dma16(rwo, v, p * 8192 + ct * 128, lds0 + L_WO + p * 1024);
    };
    auto issue_dn = [&](int i0, int n, unsigned dst, int hw) {
        const rsrc_t rwd = mk_rsrc(wd, F * D * 2);
        const unsigned v = w64_voff(lane);
        const unsigned so = ((43 * (hw >> 1) + i0) * 16 + (hw & 1) * 8) * 1024 + ct * 128;
        for (int i = 0; i < n; ++i) dma16(rwd, v, so + i * 16384, lds0 + dst + (hw & 1) * 1024 + i * 2048);
    };
    auto gu_wa = [&](int n) -> unsigned { const unsigned blk = (lane >> 4) & 1;
        return lds0 + L_GU + blk * 49152 + (((4 * h + ((lane & 15) >> 2)) * 96 + n * 32 + (lane & 3) * 8) ^ (blk * 128)); };
    float cs[3] = {0.f, 0.f, 0.f};
    auto gu_colsum = [&]() {
        u32x4 f[3][4];
#pragma unroll
        for (int n = 0; n < 3; ++n) {
            const unsigned a0 = gu_wa(n) + w * 4 * 1536;
#pragma unroll
            for (int i = 0; i < 4; ++i) f[n][i] = __builtin_bit_cast(u32x4, lds_tr2(a0 + i * 1536, a0 + i * 1536 + 768));
        }
        SBAR();
#pragma unroll
        for (int i = 0; i < 4; ++i)
#pragma unroll
            for (int e = 0; e < 4; ++e)
#pragma unroll
                for (int n = 0; n < 3; ++n) cs[n] = dot2(f[n][i][e], ONES2, cs[n]);
#pragma unroll
        for (int n = 0; n < 3; ++n) cs[n] = swap_add(cs[n]);
    };

    unsigned budget = 1u << 16;
    const unsigned l16 = lane * 16;

    {
        const int head = xcd * 4 + (bi >> 3), qb = bi & 7, wq = w & 3, g = w >> 2, qt = qb * 8 + wq * 2;
        const unsigned kvs = WS_KV + head * KV_HEAD;
        unsigned ks = kvs + g * 4 * KV_CHUNK + wq * 4096, km = lds0 + g * 32768 + wq * 4096;
        const rsrc_t rg = mk_rsrc(g ? wu : wg, (bi < 29 && ABL_GU != 1) ? D * F * 2 : 0), rwd = mk_rsrc(wd, F * D * 2), rwo = mk_rsrc(wo, D * D * 2), rx = mk_rsrc(x, S * D * 2);
        unsigned gv0, gv1, gv2;
        { unsigned c = lane ^ (g * 8); gv0 = (c / 6) * (F * 2) + (c % 6) * 16; c = (64 + lane) ^ (g * 8); gv1 = (c / 6) * (F * 2) + (c % 6) * 16;
          c = (128 + lane) ^ (g * 8); gv2 = (c / 6) * (F * 2) + (c % 6) * 16; }
        if (ABL_GU == 2) { gv0 = l16; gv1 = l16 + 1024; gv2 = l16 + 2048; }
        unsigned gs = ABL_GU == 2 ? wq * 4 * 32 * (F * 2) + bi * 3072 : wq * 4 * 32 * (F * 2) + bi * 96, gm = lds0 + L_GU + g * 49152 + wq * 12288;
        const unsigned pvd = lane < 43 ? (bi * 344 + w * 43 + lane) * 128 : 0x80000000u, pvo = lane < 16 ? (bi * 128 + w * 16 + lane) * 128 : 0x80000000u;
        const unsigned wv = w64_voff(lane), xv = (rt * 64 + wt * 32 + r32) * 1024 + (ct * 64 + kh * 32 + 4 * h) * 2;
        const unsigned kb = lds0 + g * 32768 + l16, vb = lds0 + g * 32768 + 8192 + (16 * h + (r32 & 15)) * 16;
        const unsigned cst = (r32 & 27) == 16 ? ONES2 : 0u;
        const unsigned long long msk = 0x0000ffff0000ffffull;
        const unsigned fo = WS_O + ((qt + g) * 32 + head) * 1024, fh = WS_H + ((rt * 2 + wt) * 32 + ct * 4 + (w >> 1)) * 1024, fg = WS_HH + ((xcd * 8 + w) * NU + bi * 3) * 1024;
        const unsigned fsv = w == 0 ? WS_ST + ((rt * 2 + h) * 16 + ct * 2) * 256 + r32 * 16 : 0x80000000u, fg2 = bi < 28 ? l16 : 0x80000000u, fg0 = bi < 29 ? l16 : 0x80000000u;
        const unsigned fpv = tid == 0 ? WS_FP + b * 4 : 0x80000000u;
        u32x4 ones = {~0u, ~0u, ~0u, ~0u}, fpl = {0u, 0u, 0u, 0u};
        unsigned pfd = 0, epv = ep;
        xres = u32x8{};
        s16x8 q0, q1;
        f32x16 n0, n1, sA, sB = {}, o0 = {}, o1 = {};
        u32x4 pA = {0u, 0u, 0u, 0u}, pB = {0u, 0u, 0u, 0u};
        s16x8 v1 = __builtin_bit_cast(s16x8, u32x4{cst, cst, cst, cst}), v1b = v1;
        STAMP(1, 0);
        SBAR();
#define XKV "s_mov_b32 m0, %[km]\n\ts_add_u32 %[km], %[km], 0x400\n\tbuffer_load_dwordx4 %[l16], %[rs], %[ks] offen sc1 lds\n\ts_add_u32 %[ks], %[ks], 0x400\n\t"
        asm volatile("s_barrier\n\tbuffer_load_dwordx4 v[128:131], %[l16], %[rs], %[qs] offen sc1\n\tbuffer_load_dwordx4 v[156:159], %[l16], %[rs], %[qs1] offen sc1\n\t"
                     XKV XKV XKV XKV "s_add_u32 %[km], %[km], 0x3000\n\ts_add_u32 %[ks], %[ks], 0x3000\n\ts_waitcnt vmcnt(0)\n\ts_barrier\n\t" XKV XKV XKV XKV
                     : "={v[128:131]}"(q0), "={v[156:159]}"(q1), [km] "+s"(km), [ks] "+s"(ks), "+{v[80:95]}"(sB), "+{v[96:111]}"(o0), "+{v[160:175]}"(o1),
                       "+{v[144:147]}"(v1), "+{v[136:139]}"(v1b), "+{v[148:151]}"(pA), "+{v[152:155]}"(pB), "+{v[192:199]}"(xres), "+{v200}"(pfd), "+{v201}"(epv), "+{v[204:207]}"(ones), "+{v[208:211]}"(fpl)
                     : [l16] "v"(l16), [rs] "s"(rs), [qs] "s"(RFL(WS_Q + (head * 64 + qt + g) * 1024)), [qs1] "s"(RFL(WS_Q + (head * 64 + qt + 1 - g) * 1024)) : "m0", "scc", "memory");
        km -= 16384 + 4096; ks += KV_CHUNK - 4096;
        STAMP(1, 1);
        s16x8 kf = lds_b128(kb), v0;
        {
            const f32x16 z = {};
            const s16x8 kz = lds_b128(lds0 + l16);
            const u32x4 vv = __builtin_bit_cast(u32x4, lds_b128(vb)), cc = {cst, cst, cst, cst};
            const f32x16 s0 = MFMA(kz, q0, z), s1 = MFMA(kz, q1, z);
            float m0 = __builtin_fmaxf(s0[0], s0[1]), m1 = __builtin_fmaxf(s1[0], s1[1]);
#pragma unroll
            for (int i = 2; i < 16; i += 2) { m0 = __builtin_fmaxf(__builtin_fmaxf(m0, s0[i]), s0[i + 1]); m1 = __builtin_fmaxf(__builtin_fmaxf(m1, s1[i]), s1[i + 1]); }
            m0 = swap_max(m0); m1 = swap_max(m1);
#pragma unroll
            for (int i = 0; i < 16; ++i) { n0[i] = -m0; n1[i] = -m1; }
            sA = MFMA(kf, q0, n0);
            v0 = __builtin_bit_cast(s16x8, r32 < 16 ? vv : cc);
        }
#define XE(C, i) "v_exp_f32 v[" #C "+" #i "], v[" #C "+" #i "]\n\t"
#define XC(P, C, i) "v_cvt_pk_bf16_f32 v" #P ", v[" #C "+" #i "], v[" #C "+" #i "+1]\n\t"
#define XG1(C) XE(C, 0) XE(C, 1) XE(C, 2) XE(C, 3) XC(148, C, 0) XE(C, 4) XE(C, 5) XC(149, C, 2)
#define XG2(C) XE(C, 6) XE(C, 7) XC(150, C, 4) XE(C, 8) XC(151, C, 6) XE(C, 9) XE(C, 10)
#define XG3(C) XE(C, 11) XC(152, C, 8) XE(C, 12) XE(C, 13) XC(153, C, 10) XE(C, 14) XE(C, 15) XC(154, C, 12) XC(155, C, 14)
#define XM(D, A, B, C) "v_mfma_f32_32x32x16_bf16 v[" #D ":" #D "+15], v[" #A ":" #A "+3], v[" #B ":" #B "+3], v[" #C ":" #C "+15]\n\t"
#define XRK(BASE, O) "ds_read_b128 v[132:135], " BASE " offset:" #O "\n\t"
#define XRV(R, BASE, O) "s_mov_b64 exec, %[msk]\n\tds_read_b128 v[" #R ":" #R "+3], " BASE " offset:" #O "\n\ts_mov_b64 exec, -1\n\t"
#define XW(n) "s_waitcnt lgkmcnt(" #n ")\n\t"
#define XTILE(VA, RK, RV1, WA, X1, VB, X2, RV0) \
        XM(80, 132, 156, 176) RK XG1(64) XM(160, VA, 152, 160) RV1 XG2(64) XW(WA) XM(96, 140, 148, 96) XG3(64) \
        XW(0) XM(64, 132, 128, 112) X1 XG1(80) XM(96, VB, 152, 96) X2 XG2(80) XM(160, 140, 148, 160) RV0 XG3(80)
#define XGU(j) "s_mov_b32 m0, %[gm]\n\ts_add_u32 %[gm], %[gm], 0x400\n\tbuffer_load_dwordx4 %[gv" #j "], %[rg], %[gs] offen lds\n\t"
#define XGU2 XGU(2) "s_add_u32 %[gs], %[gs], 88064\n\t"
#define XPF "buffer_load_dword v200, %[pvd], %[rwd], 0 offen\n\tbuffer_load_dword v200, %[pvo], %[rwo], 0 offen\n\t"
#define XWO "s_mov_b32 m0, %[km]\n\ts_add_u32 %[km], %[km], 0x400\n\tbuffer_load_dwordx4 %[wv], %[rwo], %[ks] offen lds\n\ts_add_u32 %[ks], %[ks], 0x2000\n\t"
#define XST(V, SO, O) "buffer_store_dwordx4 v[204:207], " V ", %[rs], " SO " offen offset:" #O " sc1\n\t"
#define XFP "buffer_store_dword v201, %[fpv], %[rs], 0 offen sc1\n\t"
#define XFL "buffer_load_dwordx4 v[208:211], %[l16], %[rs], %[fp] offen sc1\n\t"
#define XXR(R, O) "buffer_load_dwordx2 v[" #R ":" #R "+1], %[xv], %[rx], 0 offen offset:" #O "\n\t"
#define AOPS : "+{v[64:79]}"(sA), "+{v[80:95]}"(sB), "+{v[96:111]}"(o0), "+{v[160:175]}"(o1), "+{v[112:127]}"(n0), "+{v[176:191]}"(n1), "+{v[128:131]}"(q0), "+{v[156:159]}"(q1), \
               "+{v[132:135]}"(kf), "+{v[140:143]}"(v0), "+{v[144:147]}"(v1), "+{v[136:139]}"(v1b), "+{v[148:151]}"(pA), "+{v[152:155]}"(pB), "+{v[192:199]}"(xres), "+{v200}"(pfd), \
               "+{v201}"(epv), "+{v[204:207]}"(ones), "+{v[208:211]}"(fpl), \
               [km] "+s"(km), [ks] "+s"(ks), [gm] "+s"(gm), [gs] "+s"(gs) \
             : [ka] "v"(ka), [va] "v"(va), [kn] "v"(kn), [vn] "v"(vn), [msk] "s"(msk), [l16] "v"(l16), [rs] "s"(rs), [rg] "s"(rg), [gv0] "v"(gv0), [gv1] "v"(gv1), [gv2] "v"(gv2), \
               [pvd] "v"(pvd), [pvo] "v"(pvo), [wv] "v"(wv), [xv] "v"(xv), [rwd] "s"(rwd), [rwo] "s"(rwo), [rx] "s"(rx), \
               [fo] "s"(fo), [fh] "s"(fh), [fg] "s"(fg), [fsv] "v"(fsv), [fg0] "v"(fg0), [fg2] "v"(fg2), [fpv] "v"(fpv), [fp] "s"(WS_FP) : "m0", "scc", "memory"
#define XBAR(n) asm volatile("s_waitcnt vmcnt(" #n ") lgkmcnt(0)\n\ts_barrier" AOPS)
#define XROUND(A1, A2, B1, B2, C1, C2, D1, D2, E1, E2, F1, F2) asm volatile( \
            XTILE(136, XRK("%[ka]", 1024), XRV(144, "%[va]", 512), 2, A1, 144, A2, XRV(140, "%[va]", 1024)) \
            XTILE(144, XRK("%[ka]", 2048), XRV(144, "%[va]", 1536), 2, B1, 144, B2, XRV(140, "%[va]", 2048)) \
            XTILE(144, XRK("%[ka]", 3072), XRV(144, "%[va]", 2560), 2, C1, 144, C2, XRV(140, "%[va]", 3072)) \
            XTILE(144, XRK("%[ka]", 4096), XRV(144, "%[va]", 3584), 2, D1, 144, D2, XRV(140, "%[va]", 4096)) \
            XTILE(144, XRK("%[ka]", 5120), XRV(144, "%[va]", 4608), 2, E1, 144, E2, XRV(140, "%[va]", 5120)) \
            XTILE(144, XRK("%[ka]", 6144), XRV(144, "%[va]", 5632), 2, F1, 144, F2, XRV(140, "%[va]", 6144)) \
            XTILE(144, XRK("%[ka]", 7168), XRV(144, "%[va]", 6656), 2, XRV(136, "%[va]", 7680), 144, "", XRV(140, "%[va]", 7168)) AOPS)
#define XLAST(X1, X2) asm volatile(XTILE(144, XRK("%[kn]", 0), "", 1, X1, 136, X2, XRV(140, "%[vn]", 0)) AOPS)
        unsigned ka = kb, va = vb, kn = kb + 16384, vn = vb + 16384;
        asm volatile("s_nop 15" AOPS);
        STAMP(1, 7);
        XROUND(XGU(0), "", "", XST("%[l16]", "%[fo]", 0), XGU(1), XST("%[fsv]", "0", 0), "", XST("%[l16]", "%[fh]", 0), XGU2, "", XGU(0), "");
        STAMP(1, 8);
        XBAR(7);
        STAMP(1, 9);
        XLAST(XKV, XKV);
        ka = kb + 16384; va = vb + 16384; kn = kb; vn = vb;
        XROUND(XKV, XKV, XGU(1), XST("%[fg0]", "%[fg]", 0), XGU2, "", XST("%[fg0]", "%[fg]", 1024), XGU(0), "", XGU(1), XST("%[fg2]", "%[fg]", 2048), "");
        STAMP(1, 10);
        XBAR(7);
        STAMP(1, 11);
        km += 16384 - 4096; ks += KV_CHUNK - 4096;
        XLAST(XKV, XKV);
        ka = kb; va = vb; kn = kb + 16384; vn = vb + 16384;
        XROUND(XKV, XKV, XGU2, "", XGU(0), "", XGU(1), "", XGU2, "", XPF, "");
        STAMP(1, 12);
        XBAR(6);
        STAMP(1, 13);
        km = lds0 + g * 32768 + wq * 4096; ks = (32 * g + 4 * wq) * 8192 + ct * 128;
        XLAST(XFP, XWO);
        ka = kb + 16384; va = vb + 16384; kn = kb; vn = vb;
        XROUND(XWO, XWO, XWO, "", XXR(192, 0), "", XXR(194, 16), "", XXR(196, 32), "", XXR(198, 48), "");
        XLAST(XFL, "");
        STAMP(1, 14);
        asm volatile(XM(160, 136, 152, 160) "s_waitcnt vmcnt(0) lgkmcnt(0)\n\ts_barrier\n\ts_nop 15" AOPS);
        STAMP(1, 4);
        f32x16 o = o0;
        const unsigned xa = lds0 + g * 32768 + 16384 + wq * 3072 + l16, xb = lds0 + (g ^ 1) * 32768 + 16384 + wq * 3072 + l16;
        *(LDSAS f32x4*)(xa) = f32x4{o1[0], o1[1], o1[2], o1[3]};
        *(LDSAS f32x4*)(xa + 1024) = f32x4{o1[4], o1[5], o1[6], o1[7]};
        *(LDSAS float*)(xa + 2048) = o1[8];
        {
            float big = __builtin_fmaxf(o0[8], o1[8]) * 1e19f;
            float chk = o0[8] + o1[8];
#pragma unroll
            for (int i = 0; i < 8; ++i) { big = __builtin_fmaxf(__builtin_fmaxf(big, __builtin_fabsf(o0[i])), __builtin_fabsf(o1[i])); chk += o0[i] + o1[i]; }
            *(LDSAS unsigned*)(lds0 + L_FLAG + w * 4) = __any(!(big < 1e38f) || chk != chk);
        }
        BAR_L();
        const f32x4 p0 = lds_f4(xb), p1 = lds_f4(xb + 1024); const float pl = *(const LDSAS float*)(xb + 2048);
        const u32x4 f0 = *(const LDSAS u32x4*)(lds0 + L_FLAG), f1 = *(const LDSAS u32x4*)(lds0 + L_FLAG + 16);
#pragma unroll
        for (int i = 0; i < 4; ++i) { o[i] += p0[i]; o[4 + i] += p1[i]; }
        o[8] += pl;
        if (__builtin_expect(RFL(f0[0] | f0[1] | f0[2] | f0[3] | f1[0] | f1[1] | f1[2] | f1[3]), 0)) {
            BAR_L();
            const s16x8 qf = q0;
            const u32x4 cc = {cst, cst, cst, cst};
            const unsigned vf = lds0 + 16384 + 8192 + (16 * h + (r32 & 15)) * 16;
            float m = -__builtin_inff();
            o = f32x16{};
            const f32x16 z = {};
#pragma unroll 1
            for (int c = 0; c < 8; ++c) {
                dma16<true>(rs, l16, kvs + c * KV_CHUNK + w * 2048, lds0 + 16384 + w * 2048);
                dma16<true>(rs, l16, kvs + c * KV_CHUNK + w * 2048 + 1024, lds0 + 16384 + w * 2048 + 1024);
                BAR_VL(0);
#pragma unroll 1
                for (int t = 0; t < 8; ++t) {
                    f32x16 s = MFMA(lds_b128(lds0 + 16384 + l16 + t * 1024), qf, z);
                    float mx = s[0];
#pragma unroll
                    for (int i = 1; i < 16; ++i) mx = __builtin_fmaxf(mx, s[i]);
                    mx = __builtin_fmaxf(swap_max(mx), m);
                    const float al = __builtin_amdgcn_exp2f(m - mx);
                    m = mx;
#pragma unroll
                    for (int i = 0; i < 16; ++i) { o[i] *= al; s[i] = __builtin_amdgcn_exp2f(s[i] - m); }
#pragma unroll
                    for (int i = 0; i < 4; ++i) { pA[i] = pk2(s[2 * i], s[2 * i + 1]); pB[i] = pk2(s[8 + 2 * i], s[9 + 2 * i]); }
                    const u32x4 a = __builtin_bit_cast(u32x4, lds_b128(vf + t * 1024)), c2 = __builtin_bit_cast(u32x4, lds_b128(vf + t * 1024 + 512));
                    o = MFMA(__builtin_bit_cast(s16x8, r32 < 16 ? a : cc), __builtin_bit_cast(s16x8, pA), o);
                    o = MFMA(__builtin_bit_cast(s16x8, r32 < 16 ? c2 : cc), __builtin_bit_cast(s16x8, pB), o);
                }
                BAR_L();
            }
        }
        {
            const float inv = __builtin_amdgcn_rcpf(o[8]);
            u32x4 ov;
#pragma unroll
            for (int i = 0; i < 4; ++i) ov[i] = pk2(o[2 * i] * inv, o[2 * i + 1] * inv);
            ST128(ov, rs, l16, fo, AUX_SC1);
        }
        STAMP(1, 5);
        BAR_L();
        if (w >= 4) issue_wo(16 + ((w - 4) >> 1) * 32 + (w & 1) * 8, 8);
        gu_colsum();
#pragma unroll 1
        for (unsigned spins = 0; spins < SPIN_CAP && !__all(fpl[0] == ep && fpl[1] == ep && fpl[2] == ep && fpl[3] == ep); ++spins) {
            __builtin_amdgcn_s_sleep(2);
            fpl = __builtin_amdgcn_raw_buffer_load_b128(rs, l16, WS_FP, AUX_SC1);
        }
        STAMP(1, 6);
        FLUSH(1);
    }

    {
        STAMP(2, 0);
        f32x16 a0, a1;
        if (w < 4) {
            const unsigned t0 = lds0 + L_WO + kh * 32768 + w64_tr(lane, kh), t1 = lds0 + L_WO + kh * 32768 + w64_tr(lane, kh ^ 1);
            G2 g2;
            g2.tb[0] = g2.tb[2] = g2.tb[4] = t0; g2.tb[1] = g2.tb[3] = g2.tb[5] = t1;
            g2.sa = g2.s0 = RFL(WS_O + ((rt * 2 + wt) * 32 + kh * 16) * 1024); g2.l16 = l16; g2.budget = budget; g2.rs = rs; g2.a1 = f32x16{};
#pragma unroll
            for (int i = 0; i < 8; ++i) { g2.a0[2 * i] = bflo(xres[i]); g2.a0[2 * i + 1] = bfhi(xres[i]); }
            __builtin_amdgcn_s_sleep(DELAY_O);
            STAMP(2, 1);
            g2_steps<0, 16, 0, 6, -1>(g2);
            a0 = g2.a0; a1 = g2.a1; STAMPV(15, budget - g2.budget); budget = g2.budget;
        } else BAR_VL(0);
        STAMP(2, 2);
        BAR_L();
        if (w < 4) {
#pragma unroll
            for (int g = 0; g < 4; ++g) *(LDSAS f32x4*)(lds0 + w * 4096 + g * 1024 + lane * 16) = f32x4{a1[4 * g], a1[4 * g + 1], a1[4 * g + 2], a1[4 * g + 3]};
        }
#pragma unroll
        for (int n = 0; n < 3; ++n) *(LDSAS float*)(lds0 + L_TAB + (w * 96 + n * 32 + r32) * 4) = cs[n];
        BAR_L();
        if (w >= 4) {
            if (tid < 256 + 96) {
                float t[8];
#pragma unroll
                for (int i = 0; i < 8; ++i) t[i] = *(const LDSAS float*)(lds0 + L_TAB + (i * 96 + tid - 256) * 4);
                *(LDSAS float*)(lds0 + L_TAB + 3072 + (tid - 256) * 4) = ((t[0] + t[1]) + (t[2] + t[3])) + ((t[4] + t[5]) + (t[6] + t[7]));
            }
        } else {
            f32x4 p[4];
#pragma unroll
            for (int g = 0; g < 4; ++g) p[g] = lds_f4(lds0 + (w ^ 2) * 4096 + g * 1024 + lane * 16);
            SBAR();
#pragma unroll
            for (int g = 0; g < 4; ++g) {
                hreg[g >> 1][(g & 1) * 2] = pk2(a0[4 * g] + p[g][0], a0[4 * g + 1] + p[g][1]);
                hreg[g >> 1][(g & 1) * 2 + 1] = pk2(a0[4 * g + 2] + p[g][2], a0[4 * g + 3] + p[g][3]);
            }
            const unsigned so = WS_H + ((rt * 2 + wt) * 32 + (ct * 2 + kh) * 2) * 1024;
            ST128(hreg[0], rs, lane * 16, so, AUX_SC1);
            ST128(hreg[1], rs, lane * 16 + 1024, so, AUX_SC1);
            SBAR();
            float sm = 0.f, sq = 0.f;
#pragma unroll
            for (int i = 0; i < 8; ++i) { sm = dot2(hreg[i >> 2][i & 3], ONES2, sm); sq = dot2(hreg[i >> 2][i & 3], hreg[i >> 2][i & 3], sq); }
            sm = swap_add(sm); sq = swap_add(sq);
            if (h == 0) __builtin_amdgcn_raw_buffer_store_b64(u32x2{__float_as_uint(sm), __float_as_uint(sq)}, rs, r32 * 8,
                                                              WS_ST + ((rt * 2 + wt) * 16 + ct * 2 + kh) * 256, AUX_SC1);
        }
        STAMP(2, 3);
        BAR_L();
        if (w >= 4) { issue_dn(0, 15, ((w - 4) >> 1) * 15 * 2048, w - 4); if (bi >= 29) issue_dn(15, 24, L_GU + ((w - 4) >> 1) * 49152, w - 4); }
        STAMP(2, 5);
        FLUSH(2);
    }

    {
        STAMP(3, 0);
        if (bi < 29) {
            const int tt = xcd * 8 + w;
            GU gu;
            gu.a0 = gu.a1 = gu.a2 = f32x16{};
            gu.sa = gu.s0 = RFL(WS_H + tt * 32768); gu.sv = 8 * h * 256 + r32 * 8; gu.ss = RFL(WS_ST + tt * 4096); gu.l16 = l16; gu.budget = budget; gu.rs = rs;
            gu.dm = RFL(lds0 + L_GU + (w >> 2) * 49152 + (w & 3) * 6144); gu.ds = RFL(((43 * (w >> 2) + 15) * 16 + (w & 3) * 48) * 1024 + ct * 128);
            gu.dm2 = RFL(lds0 + L_GU + (w >> 2) * 49152 + 24576 + (w & 3) * 3072); gu.ds2 = RFL(((43 * (w >> 2) + 27) * 16 + (w & 3) * 24) * 1024 + ct * 128);
            gu.dm3 = RFL(lds0 + L_TAB + (w >> 2) * 2048 + (w & 1) * 1024); gu.ds3 = RFL(((43 * (w >> 2) + 39) * 16 + (w & 1) * 8) * 1024 + ct * 128);
            gu.dv = w64_voff(lane); gu.rd = mk_rsrc(wd, F * D * 2);
            gu.wa[0] = gu_wa(0); gu.wa[1] = gu_wa(1); gu.wa[2] = gu_wa(2);
            float csl[3];
#pragma unroll
            for (int n = 0; n < 3; ++n) csl[n] = *(const LDSAS float*)(lds0 + L_TAB + 3072 + (n * 32 + r32) * 4);
            if (w < 4) __builtin_amdgcn_s_sleep(DELAY_H);
            STAMP(3, 2);
            gu_steps<0, 18>(gu);
#pragma unroll 1
            for (;;) {
                unsigned mx = gu.st[0];
#pragma unroll
                for (int i = 1; i < 16; ++i) mx = gu.st[i] > mx ? gu.st[i] : mx;
                if (__builtin_expect(!__any(mx == ~0u) || gu.budget == 0, 1)) break;
                --gu.budget;
                asm volatile(".set tagj, 0\n\t.rept 8\n\tbuffer_load_dwordx2 v[172+2*tagj:173+2*tagj], %[sv], %[rs], %[ss] offen offset:tagj*256 sc1\n\t.set tagj, tagj+1\n\t.endr\n\ts_waitcnt vmcnt(0)"
                             : "+{v[172:187]}"(gu.st), "+{v[140:155]}"(gu.r0), "+{v[156:171]}"(gu.r1), "+{v[112:119]}"(gu.w0), "+{v[124:131]}"(gu.w1), "+{v[132:139]}"(gu.w2),
                               "+{v[64:79]}"(gu.a0), "+{v[80:95]}"(gu.a1), "+{v[96:111]}"(gu.a2) : [sv] "v"(gu.sv), [rs] "s"(rs), [ss] "s"(gu.ss) : "memory");
            }
            float sm = 0.f, sq = 0.f;
#pragma unroll
            for (int i = 0; i < 8; ++i) { sm += __uint_as_float(gu.st[2 * i]); sq += __uint_as_float(gu.st[2 * i + 1]); }
            sm = swap_add(sm); sq = swap_add(sq);
            const float mean = sm * (1.f / D);
            const float var = __builtin_fmaxf(sq * (1.f / D) - mean * mean, 0.f) + LN_EPS;
            float rstd = __builtin_amdgcn_rsqf(var), sd = var * rstd, ga = -LOG2E * rstd;
            u32x4 mf, cf[3];
            {
                const unsigned mh = pk2(-mean, -mean);
                const unsigned ml = pk2(-mean - bflo(mh), 0.f);
                mf = h ? u32x4{0u, 0u, 0u, 0u} : u32x4{(mh & 0xffffu) | (ml << 16), mh & 0xffffu, 0u, 0u};
#pragma unroll
                for (int n = 0; n < 3; ++n) { const unsigned ch = pk2(csl[n], csl[n]); cf[n] = u32x4{ch, pk2(csl[n] - bflo(ch), 0.f), 0u, 0u}; PIN(cf[n]); }
                PIN(mf); PIN(sd); PIN(ga);
            }
            gu_steps<18, 32>(gu);
            asm volatile("v_mfma_f32_32x32x16_bf16 v[64:79], %[c0], %[m], v[64:79]\n\tv_mfma_f32_32x32x16_bf16 v[80:95], %[c1], %[m], v[80:95]\n\t"
                         "v_mfma_f32_32x32x16_bf16 v[96:111], %[c2], %[m], v[96:111]\n\ts_nop 10"
                         : "+{v[64:79]}"(gu.a0), "+{v[80:95]}"(gu.a1), "+{v[96:111]}"(gu.a2) : [c0] "v"(cf[0]), [c1] "v"(cf[1]), [c2] "v"(cf[2]), [m] "v"(mf));
            STAMP(3, 3);
            STAMPV(15, budget - gu.budget); budget = gu.budget;
            auto epi = [&](int n, const f32x16& acc) {
                f32x2 e[4], y[4];
#pragma unroll
                for (int i = 0; i < 4; ++i) e[i] = f32x2{acc[2 * i], acc[2 * i + 1]} * ga;
#pragma unroll
                for (int i = 0; i < 4; ++i) e[i] = f32x2{__builtin_amdgcn_exp2f(e[i][0]), __builtin_amdgcn_exp2f(e[i][1])} * sd + sd;
#pragma unroll
                for (int i = 0; i < 4; ++i) y[i] = f32x2{acc[8 + 2 * i], acc[9 + 2 * i]} * f32x2{__builtin_amdgcn_rcpf(e[i][0]), __builtin_amdgcn_rcpf(e[i][1])};
                const u32x4 ov = {pk2(y[0][0], y[0][1]), pk2(y[1][0], y[1][1]), pk2(y[2][0], y[2][1]), pk2(y[3][0], y[3][1])};
                ST128(ov, rs, (n < 2 || bi < 28) ? l16 : 0x80000000u, WS_HH + (tt * NU + bi * 3 + n) * 1024, AUX_SC1);
            };
            epi(0, gu.a0);
            asm volatile("" : "+{v[80:95]}"(gu.a1));
            epi(1, gu.a1);
            asm volatile("" : "+{v[96:111]}"(gu.a2));
            epi(2, gu.a2);
        }
        STAMP(3, 4);
        if (bi >= 29) WAITV(0);
        BAR_VL(3);
        STAMP(3, 5);
        if (w >= 4) {
            if (bi < 29) issue_dn(33, 6, L_GU + ((w - 4) >> 1) * 49152 + 36864, w - 4); else issue_dn(39, 1, L_TAB + ((w - 4) >> 1) * 2048, w - 4);
            STAMP(3, 6);
        }
        FLUSH(3);
    }

    {
        STAMP(4, 0);
        if (b == 0 && tid == 0) __builtin_amdgcn_raw_buffer_store_b32(ep, rs, 0, WS_EP, AUX_SC1);
        f32x16 a0, a1;
        if (w < 4) {
            const unsigned t0 = lds0 + w64_tr(lane, kh), t1 = lds0 + w64_tr(lane, kh ^ 1);
            G2 g2;
            g2.tb[0] = t0 + kh * 15 * 2048; g2.tb[1] = t1 + kh * 15 * 2048; g2.tb[2] = t0 + L_GU + kh * 24 * 2048; g2.tb[3] = t1 + L_GU + kh * 24 * 2048;
            g2.tb[4] = t0 + L_TAB + kh * 2048; g2.tb[5] = t1 + L_TAB + kh * 2048;
            g2.sa = g2.s0 = RFL(WS_HH + ((rt * 2 + wt) * NU + kh * 43) * 1024); g2.l16 = l16; g2.budget = budget; g2.rs = rs; g2.a1 = f32x16{};
#pragma unroll
            for (int i = 0; i < 8; ++i) { g2.a0[2 * i] = bflo(hreg[i >> 2][i & 3]); g2.a0[2 * i + 1] = bfhi(hreg[i >> 2][i & 3]); }
            __builtin_amdgcn_s_sleep(DELAY_G);
            STAMP(4, 1);
            g2_steps<0, 43, 1, DN_B0, 38>(g2);
            a0 = g2.a0; a1 = g2.a1; STAMPV(15, budget - g2.budget);
        } else {
            BAR_VL(0);
            issue_dn(40, 3, ((w - 4) >> 1) * 15 * 2048, w - 4);
            BAR_VL(0);
        }
        STAMP(4, 2);
        if (w < 4) {
#pragma unroll
            for (int g = 0; g < 4; ++g) *(LDSAS f32x4*)(lds0 + 8192 + w * 4096 + g * 1024 + lane * 16) = f32x4{a1[4 * g], a1[4 * g + 1], a1[4 * g + 2], a1[4 * g + 3]};
        }
        BAR_L();
        if (w < 4) {
            f32x4 p[4];
#pragma unroll
            for (int g = 0; g < 4; ++g) p[g] = lds_f4(lds0 + 8192 + (w ^ 2) * 4096 + g * 1024 + lane * 16);
            SBAR();
            unsigned y[8];
#pragma unroll
            for (int g = 0; g < 4; ++g) {
                y[2 * g] = pk2(a0[4 * g] + p[g][0], a0[4 * g + 1] + p[g][1]);
                y[2 * g + 1] = pk2(a0[4 * g + 2] + p[g][2], a0[4 * g + 3] + p[g][3]);
            }
            const rsrc_t ro = mk_rsrc(out, S * D * 2);
            const unsigned vo = (rt * 64 + wt * 32 + r32) * 1024 + (ct * 64 + kh * 32 + 8 * h) * 2;
#pragma unroll
            for (int g = 0; g < 2; ++g) {
                auto e0 = __builtin_amdgcn_permlane32_swap(y[4 * g], y[4 * g + 2], false, false);
                auto e1 = __builtin_amdgcn_permlane32_swap(y[4 * g + 1], y[4 * g + 3], false, false);
                ST128((u32x4{e0[0], e1[0], e0[1], e1[1]}), ro, vo + g * 32, 0, OUT_AUX);
            }
        }
        STAMP(4, 3);
        STAMP(4, 4);
        FLUSH(4);
        asm volatile(".subsection 2\n.Lmain_end:\n\t.fill 64, 4, 0xBF800000\n\t.subsection 0");
    }
}

extern "C" void kernel_launch(void* const* d_in, const int* in_sizes, int n_in,
                              void* d_out, int out_size, void* d_ws, size_t ws_size,
                              hipStream_t stream) {
    const u16* x = (const u16*)d_in[0];
    const u16* wqkv = (const u16*)d_in[1];
    const u16* wo = (const u16*)d_in[2];
    const u16* wg = (const u16*)d_in[4];
    const u16* wu = (const u16*)d_in[5];
    const u16* wd = (const u16*)d_in[6];
    u16* out = (u16*)d_out;
    char* ws = (char*)d_ws;
    (void)hipFuncSetAttribute((const void*)k_block, hipFuncAttributeMaxDynamicSharedMemorySize, K2_LDS);
    k_block<<<256, 512, K2_LDS, stream>>>(x, wqkv, wo, wg, wu, wd, out, ws);
}
```

```cpp
#define PROBE 0
#include <hip/hip_runtime.h>
#include <hip/hip_bf16.h>
#include <stdint.h>
#pragma clang diagnostic ignored "-Winline-asm"

typedef short s16x8 __attribute__((ext_vector_type(8)));
typedef short s16x4 __attribute__((ext_vector_type(4)));
typedef float f32x16 __attribute__((ext_vector_type(16)));
typedef float f32x4 __attribute__((ext_vector_type(4)));
typedef float f32x2 __attribute__((ext_vector_type(2)));
typedef unsigned u32x4 __attribute__((ext_vector_type(4)));
typedef unsigned u32x2 __attribute__((ext_vector_type(2)));
typedef unsigned u32x8 __attribute__((ext_vector_type(8)));
typedef __bf16 bf16x2 __attribute__((ext_vector_type(2)));
typedef unsigned short u16;
#define LDSAS __attribute__((address_space(3)))
#define GAS __attribute__((address_space(1)))
typedef __amdgpu_buffer_rsrc_t rsrc_t;

constexpr int S = 2048, D = 512, NH = 32, F = 1376, NU = F / 16;
constexpr float LN_EPS = 1e-5f;
constexpr float LOG2E = 1.4426950408889634f;

constexpr unsigned KV_CHUNK = 16384;
constexpr unsigned KV_HEAD = 8 * KV_CHUNK;
constexpr unsigned WS_EP = 0;
constexpr unsigned WS_FQ = 4096;
constexpr unsigned WS_FP = WS_FQ + 16 * 64 * 128;
constexpr unsigned WS_Q = WS_FP + 4096;
constexpr unsigned WS_KV = WS_Q + NH * 64 * 1024;
constexpr unsigned WS_O = WS_KV + NH * KV_HEAD;
constexpr unsigned WS_H = WS_O + 64 * 32 * 1024;
constexpr unsigned WS_ST = WS_H + 64 * 32 * 1024;
constexpr unsigned WS_HH = WS_ST + 64 * 16 * 256;
constexpr unsigned WS_END = WS_HH + 64 * NU * 1024;
constexpr int AUX_SC1 = 16;
#define PROBE_BLOCK 0
#define STAMP_DECL unsigned tp0_ = 0, tp_ = 0
#define STAMP(k, i) do { if ((k) * 16 + (i) == 0) tp0_ = (unsigned)__builtin_amdgcn_s_memrealtime(); if ((k) * 16 + (i) == PROBE) { __builtin_amdgcn_sched_barrier(0); \
    tp_ = (unsigned)__builtin_amdgcn_s_memrealtime(); asm volatile("" : "+s"(tp_)); __builtin_amdgcn_sched_barrier(0); } } while (0)
#define FLUSH(k) do { if ((k) == 4 && blockIdx.x == PROBE_BLOCK && w == 0) { unsigned lim_ = tp_ - tp0_; if (lim_ > 20000) lim_ = 20000; \
    while ((unsigned)__builtin_amdgcn_s_memrealtime() - tp0_ < 6000 + lim_) __builtin_amdgcn_s_sleep(2); } } while (0)

#define STAMPV(i, v) do {} while (0)
#define SBAR() __builtin_amdgcn_sched_barrier(0)
#define RFL(x) ((unsigned)__builtin_amdgcn_readfirstlane(x))
#define PIN(x) asm volatile("" : "+v"(x))
#define MFMA(a, b, c) __builtin_amdgcn_mfma_f32_32x32x16_bf16((a), (b), (c), 0, 0, 0)
#define WAITV(n) asm volatile("s_waitcnt vmcnt(" #n ")" ::: "memory")
#define BAR_L() asm volatile("s_waitcnt lgkmcnt(0)\n\ts_barrier" ::: "memory")
#define BAR_0() asm volatile("s_barrier" ::: "memory")
#define BAR_VL(n) asm volatile("s_waitcnt vmcnt(" #n ") lgkmcnt(0)\n\ts_barrier" ::: "memory")

__device__ __forceinline__ rsrc_t mk_rsrc(const void* p, unsigned bytes) {
    return __builtin_amdgcn_make_buffer_rsrc((void*)p, (short)0, (int)bytes, 0x00020000);
}
template <bool SC1 = false> __device__ __forceinline__ void dma16(rsrc_t r, unsigned voff, unsigned soff, unsigned lds) {
    if constexpr (SC1) asm volatile("s_nop 4\n\ts_mov_b32 m0, %0\n\ts_nop 0\n\tbuffer_load_dwordx4 %1, %2, %3 offen sc1 lds"
                 :: "s"(__builtin_amdgcn_readfirstlane(lds)), "v"(voff), "s"(r), "s"(__builtin_amdgcn_readfirstlane(soff)) : "m0", "memory");
    else asm volatile("s_nop 4\n\ts_mov_b32 m0, %0\n\ts_nop 0\n\tbuffer_load_dwordx4 %1, %2, %3 offen lds"
                 :: "s"(__builtin_amdgcn_readfirstlane(lds)), "v"(voff), "s"(r), "s"(__builtin_amdgcn_readfirstlane(soff)) : "m0", "memory");
}
#define ST128(v, r, voff, soff, aux) do { const u32x4 d_ = (v); __builtin_amdgcn_raw_buffer_store_b128(d_, r, voff, soff, aux); asm volatile("s_nop 1" :: "v"(d_)); } while (0)
__device__ __forceinline__ unsigned pk2(float lo, float hi) {
    f32x2 v = {lo, hi};
    return __builtin_bit_cast(unsigned, __builtin_convertvector(v, bf16x2));
}
__device__ __forceinline__ float dot2(unsigned a, unsigned b, float c) {
    return __builtin_amdgcn_fdot2_f32_bf16(__builtin_bit_cast(bf16x2, a), __builtin_bit_cast(bf16x2, b), c, false);
}
__device__ __forceinline__ float swap_add(float v) {
    auto r = __builtin_amdgcn_permlane32_swap(__float_as_uint(v), __float_as_uint(v), false, false);
    return __uint_as_float(r[0]) + __uint_as_float(r[1]);
}
__device__ __forceinline__ float swap_max(float v) {
    auto r = __builtin_amdgcn_permlane32_swap(__float_as_uint(v), __float_as_uint(v), false, false);
    return __builtin_fmaxf(__uint_as_float(r[0]), __uint_as_float(r[1]));
}
__device__ __forceinline__ s16x8 lds_b128(unsigned a) { return *(const LDSAS s16x8*)(a); }
__device__ __forceinline__ f32x4 lds_f4(unsigned a) { return *(const LDSAS f32x4*)(a); }
__device__ __forceinline__ s16x8 lds_tr2(unsigned a0, unsigned a1) {
    s16x4 lo = __builtin_amdgcn_ds_read_tr16_b64_v4i16((LDSAS s16x4*)(a0));
    s16x4 hi = __builtin_amdgcn_ds_read_tr16_b64_v4i16((LDSAS s16x4*)(a1));
    return __builtin_shufflevector(lo, hi, 0, 1, 2, 3, 4, 5, 6, 7);
}
__device__ __forceinline__ float bflo(unsigned v) { return __uint_as_float(v << 16); }
__device__ __forceinline__ float bfhi(unsigned v) { return __uint_as_float(v & 0xffff0000u); }
constexpr unsigned ONES2 = 0x3f803f80u;
template <int I> struct ic { static constexpr int value = I; constexpr operator int() const { return I; } };
template <int B, int E, class Fn> __device__ __forceinline__ void sfor(Fn&& fn) { if constexpr (B < E) { fn(ic<B>{}); sfor<B + 1, E>(fn); } }

#define K1_X_AUX 2
#define K1_ABL 0
#define K1_ST_AUX 16
constexpr unsigned K1_W = 65536, K1_FLG = K1_W + 5 * 16384, K1_ST = K1_FLG + 512, K1_CS = K1_ST + 512;
__host__ __device__ constexpr unsigned k1_slot(int s) { return s < 5 ? K1_W + s * 16384 : s == 5 ? K1_W : s < 10 ? (s - 6) * 16384 : K1_W + (s - 9) * 16384; }
__device__ __forceinline__ void lds_spin_ge(unsigned addr, unsigned want) {
#pragma unroll 1
    for (unsigned n = 0; n < (1u << 22); ++n) {
        unsigned v; asm volatile("ds_read_b32 %0, %1\n\ts_waitcnt lgkmcnt(0)" : "=v"(v) : "v"(addr) : "memory");
        if ((unsigned)__builtin_amdgcn_readfirstlane(v) >= want) break;
        __builtin_amdgcn_s_sleep(1);
    }
}
__device__ __forceinline__ void lds_post(unsigned addr, unsigned v) {
    asm volatile("s_waitcnt lgkmcnt(0)\n\tds_write_b32 %0, %1" :: "v"(addr), "v"(v) : "memory");
}
__device__ __forceinline__ void lds_inc(unsigned addr, int lane) {
    asm volatile("s_waitcnt lgkmcnt(0)" ::: "memory");
    if (lane == 0) asm volatile("ds_add_u32 %0, %1" :: "v"(addr), "v"(1u) : "memory");
}
typedef unsigned u32x16 __attribute__((ext_vector_type(16)));
template <int CTRL> __device__ __forceinline__ float dpp_add(float v) {
    return v + __builtin_bit_cast(float, __builtin_amdgcn_update_dpp(0, __builtin_bit_cast(int, v), CTRL, 0xf, 0xf, true));
}

constexpr unsigned K2_LDS = 163840;
constexpr unsigned L_WO = 0, L_GU = 65536;
constexpr unsigned L_TAB = 61440;
constexpr unsigned L_FLAG = 16384 + 12288;
constexpr unsigned SPIN_CAP = 1u << 20;
#define POLL_DELAY_Q 24
#define OUT_AUX 2
#define DN_B0 20
#define ABL_GU 0
#define DELAY_O 0
#define DELAY_H 8
#define DELAY_G 8
constexpr int GU_AW = 8, G2_AW = 12;
#define TAG_T1 "v_max3_u32 v120, v[%c[cr]], v[%c[cr]+1], v[%c[cr]+2]\n\t"
#define TAG_T2 "v_max_u32 v120, v120, v[%c[cr]+3]\n\tv_cmp_eq_u32 vcc, -1, v120\n\t"
#define TAG_RELOAD(RB, AW) "s_add_u32 %[t], %[s0], (tagj>>2)*4096\n\tbuffer_load_dwordx4 v[" #RB "+4*(tagj%%" #AW "):" #RB "+3+4*(tagj%%" #AW ")], %[l16], %[rs], %[t] offen offset:(tagj&3)*1024 sc1\n\t"
#define TAG_T3(RB, AW) \
    "s_cbranch_vccnz 5f\n6:\n\t" \
    ".subsection 1\n5:\n\ts_cmp_eq_u32 %[bud], 0\n\ts_cbranch_scc1 6b\n\ts_sub_u32 %[bud], %[bud], 1\n\t.set tagj, %c[ci]\n\t" TAG_RELOAD(RB, AW) \
    "s_waitcnt vmcnt(0)\n\t" TAG_T1 TAG_T2 "s_cbranch_vccnz 5b\n\t.rept %c[cnt]-1\n\t.set tagj, tagj+1\n\t" TAG_RELOAD(RB, AW) ".endr\n\ts_branch 6b\n\t.subsection 0\n\t"
#define TAG_LOAD "buffer_load_dwordx4 v[%c[lr]:%c[lr]+3], %[l16], %[rs], %[sa] offen offset:%c[lo] sc1\n\t.if %c[lo] == 3072\n\ts_add_u32 %[sa], %[sa], 0x1000\n\t.endif\n\t"

__device__ __forceinline__ void wait_flags(rsrc_t rs, unsigned base, int n, unsigned ep, int lane) {
    const unsigned v = (lane < n ? lane : n - 1) * 128;
#pragma unroll 1
    for (unsigned spins = 0; spins < SPIN_CAP; ++spins) {
        const unsigned f = __builtin_amdgcn_raw_buffer_load_b32(rs, v, base, AUX_SC1);
        if (__all(f == ep)) break;
        __builtin_amdgcn_s_sleep(1);
    }
    asm volatile("" ::: "memory");
}

__device__ __forceinline__ unsigned w64_voff(int lane) { return (lane >> 3) * 1024 + (((lane & 7) ^ (((lane >> 4) & 1) << 2)) << 4); }
__device__ __forceinline__ unsigned w64_tr(int lane, int n) {
    const int q = (lane & 15) >> 2, p = lane & 3, blk = (lane >> 4) & 1, h = lane >> 5;
    return (4 * h + q) * 128 + (((4 * n + 2 * blk + (p >> 1)) ^ (((q >> 1) & 1) << 2)) << 4) + 8 * (p & 1);
}

struct G2 { u32x16 r0, r1, r2; u32x8 w0, w1, w2; f32x16 a0, a1; unsigned tb[6], sa, s0, l16, budget; rsrc_t rs;
};
__host__ __device__ constexpr int g2_w(int DN, int n) {
    return !DN ? n * 2048 : n < 15 ? n * 2048 : n < 39 ? 65536 + (n - 15) * 2048 : n < 40 ? 131072 : (n - 40) * 2048;
}
template <int i, int N, int DN, int B0, int B1> __device__ __forceinline__ void g2_steps(G2& g) {
    constexpr int AW = G2_AW, j = i + AW - 1, n = i + 2, sel = n < N ? g2_w(DN, n) >> 16 : 3;
    unsigned tq;
#define G2_RD(P, R) "ds_read_b64_tr_b16 v[%c[nr]+" #R ":%c[nr]+" #R "+1], %[" P "] offset:%c[no]\n\tds_read_b64_tr_b16 v[%c[nr]+" #R "+2:%c[nr]+" #R "+3], %[" P "] offset:%c[no]+1024\n\t"
#define G2_RDS(P0, P1, P2, R) ".if %c[sel] == 0\n\t" G2_RD(P0, R) ".endif\n\t.if %c[sel] == 1\n\t" G2_RD(P1, R) ".endif\n\t.if %c[sel] == 2\n\t" G2_RD(P2, R) ".endif\n\t"
    if constexpr (i == 0)
        asm volatile(".set tagj, 0\n\t.rept 11\n\tbuffer_load_dwordx4 v[128+4*tagj:131+4*tagj], %[l16], %[rs], %[sa] offen offset:(tagj&3)*1024 sc1\n\t"
                     ".if (tagj&3) == 3\n\ts_add_u32 %[sa], %[sa], 0x1000\n\t.endif\n\t.set tagj, tagj+1\n\t.endr\n\t"
                     ".set tagj, 0\n\t.rept 2\n\tds_read_b64_tr_b16 v[96+8*tagj:97+8*tagj], %[b0] offset:tagj*2048\n\tds_read_b64_tr_b16 v[98+8*tagj:99+8*tagj], %[b0] offset:tagj*2048+1024\n\t"
                     "ds_read_b64_tr_b16 v[100+8*tagj:101+8*tagj], %[b1] offset:tagj*2048\n\tds_read_b64_tr_b16 v[102+8*tagj:103+8*tagj], %[b1] offset:tagj*2048+1024\n\t.set tagj, tagj+1\n\t.endr\n\t"
                     "s_waitcnt vmcnt(10) lgkmcnt(4)\n\t" TAG_T1 TAG_T2 TAG_T3(128, 12)
                     : "={v[128:143]}"(g.r0), "={v[144:159]}"(g.r1), "={v[160:175]}"(g.r2), "={v[96:103]}"(g.w0), "={v[104:111]}"(g.w1), "={v[112:119]}"(g.w2), [sa] "+s"(g.sa),
                       "+{v[64:79]}"(g.a0), "+{v[80:95]}"(g.a1), [bud] "+s"(g.budget), [t] "=&s"(tq)
                     : [l16] "v"(g.l16), [rs] "s"(g.rs), [s0] "s"(g.s0), [b0] "v"(g.tb[0]), [b1] "v"(g.tb[1]), [cr] "n"(128), [ci] "n"(0), [cnt] "n"(AW - 1) : "vcc", "scc", "v120", "memory");
    asm volatile("v_mfma_f32_32x32x16_bf16 v[64:79], v[%c[wr]:%c[wr]+3], v[%c[ar]:%c[ar]+3], v[64:79]\n\t"
                 ".if %c[ld]\n\t" TAG_LOAD ".endif\n\t"
                 ".if %c[bar]\n\ts_waitcnt lgkmcnt(0)\n\ts_barrier\n\t.endif\n\t"
                 G2_RDS("b0", "b2", "b4", 0)
                 ".if %c[cnt]\n\ts_waitcnt vmcnt(%c[vn])\n\t" TAG_T1 ".endif\n\t"
                 "v_mfma_f32_32x32x16_bf16 v[80:95], v[%c[wr]+4:%c[wr]+7], v[%c[ar]:%c[ar]+3], v[80:95]\n\t"
                 G2_RDS("b1", "b3", "b5", 4)
                 ".if %c[cnt]\n\t" TAG_T2 "s_waitcnt lgkmcnt(%c[ln])\n\t" TAG_T3(128, 12) ".else\n\ts_nop 15\n\t.endif"
                 : "+{v[64:79]}"(g.a0), "+{v[80:95]}"(g.a1), "+{v[128:143]}"(g.r0), "+{v[144:159]}"(g.r1), "+{v[160:175]}"(g.r2), "+{v[96:103]}"(g.w0), "+{v[104:111]}"(g.w1), "+{v[112:119]}"(g.w2),
                   [sa] "+s"(g.sa), [bud] "+s"(g.budget), [t] "=&s"(tq)
                 : [l16] "v"(g.l16), [rs] "s"(g.rs), [s0] "s"(g.s0), [b0] "v"(g.tb[0]), [b1] "v"(g.tb[1]), [b2] "v"(g.tb[2]), [b3] "v"(g.tb[3]), [b4] "v"(g.tb[4]), [b5] "v"(g.tb[5]),
                   [ld] "n"(j < N ? 1 : 0), [lr] "n"(128 + 4 * (j % AW)), [lo] "n"((j & 3) * 1024), [bar] "n"(i == B0 || i == B1 ? 1 : 0), [sel] "n"(sel),
                   [nr] "n"(96 + 8 * (n % 3)), [no] "n"(n < N ? g2_w(DN, n) & 65535 : 0), [vn] "n"(j < N ? AW - 2 : i + 1 < N ? N - 2 - i : 0), [ln] "n"(n < N ? 4 : 0),
                   [ar] "n"(128 + 4 * (i % AW)), [wr] "n"(96 + 8 * (i % 3)), [cr] "n"(128 + 4 * ((i + 1) % AW)), [ci] "n"(i + 1), [cnt] "n"(j < N ? AW - 1 : N - 1 - i) : "vcc", "scc", "v120", "memory");
    if constexpr (i + 1 < N) g2_steps<i + 1, N, DN, B0, B1>(g);
}
__host__ __device__ constexpr int gu_dma(int s) { return (s >= 16 && s < 24 && !(s & 1)) || (s >= 24 && s < 30); }
__host__ __device__ constexpr int gu_ops(int s) { return (s + GU_AW - 1 < 32 ? 1 : 0) + (s >= 2 && s < 10 ? 1 : 0) + gu_dma(s); }
__host__ __device__ constexpr int gu_younger(int k) {
    int c = 0, s = k - GU_AW + 1;
    if (s < 0) { c = GU_AW - 2 - k; s = 0; } else { c = gu_ops(s) - 1; ++s; }
    for (; s < k; ++s) c += gu_ops(s);
    return c;
}

struct GU { u32x16 r0, r1, st; u32x8 w0, w1, w2; f32x16 a0, a1, a2; unsigned wa[3], sa, s0, sv, ss, l16, budget, dm, ds, dv, dm2, ds2, dm3, ds3; rsrc_t rs, rd; };
template <int i, int E> __device__ __forceinline__ void gu_steps(GU& g) {
    constexpr int AW = GU_AW, j = i + AW - 1, n = i + 1;
    unsigned tq;
#define GU_RD(R, B) "ds_read_b64_tr_b16 v[" R ":" R "+1], %[" B "] offset:%c[no]\n\tds_read_b64_tr_b16 v[" R "+2:" R "+3], %[" B "] offset:%c[no]+768\n\t"
    if constexpr (i == 0)
        asm volatile(".set tagj, 0\n\t.rept 7\n\tbuffer_load_dwordx4 v[140+4*tagj:143+4*tagj], %[l16], %[rs], %[sa] offen offset:(tagj&3)*1024 sc1\n\t"
                     ".if (tagj&3) == 3\n\ts_add_u32 %[sa], %[sa], 0x1000\n\t.endif\n\t.set tagj, tagj+1\n\t.endr\n\t" GU_RD("112", "b0") GU_RD("116", "b1") GU_RD("124", "b2")
                     "s_waitcnt vmcnt(6) lgkmcnt(0)\n\t" TAG_T1 TAG_T2 TAG_T3(140, 8)
                     : "={v[140:155]}"(g.r0), "={v[156:171]}"(g.r1), "={v[172:187]}"(g.st), "={v[112:119]}"(g.w0), "={v[124:131]}"(g.w1), "={v[132:139]}"(g.w2), [sa] "+s"(g.sa),
                       "+{v[64:79]}"(g.a0), "+{v[80:95]}"(g.a1), "+{v[96:111]}"(g.a2), [bud] "+s"(g.budget), [t] "=&s"(tq)
                     : [l16] "v"(g.l16), [rs] "s"(g.rs), [s0] "s"(g.s0), [b0] "v"(g.wa[0]), [b1] "v"(g.wa[1]), [b2] "v"(g.wa[2]), [no] "n"(0), [cr] "n"(140), [ci] "n"(0), [cnt] "n"(AW - 1)
                     : "vcc", "scc", "v120", "memory");
    asm volatile(".if %c[i] == 15 || %c[i] == 23\n\ts_barrier\n\t.endif\n\t"
                 "v_mfma_f32_32x32x16_bf16 v[64:79], v[%c[c0]:%c[c0]+3], v[%c[ar]:%c[ar]+3], v[64:79]\n\t"
                 ".if %c[rd]\n\t" GU_RD("%c[n0]", "b0") GU_RD("%c[n1]", "b1") ".endif\n\t"
                 "v_mfma_f32_32x32x16_bf16 v[80:95], v[%c[c1]:%c[c1]+3], v[%c[ar]:%c[ar]+3], v[80:95]\n\t"
                 ".if %c[rd]\n\t" GU_RD("%c[n2]", "b2") ".endif\n\t"
                 ".if %c[ld]\n\t" TAG_LOAD ".endif\n\t"
                 ".if %c[sk] >= 0\n\tbuffer_load_dwordx2 v[172+2*%c[sk]:173+2*%c[sk]], %[sv], %[rs], %[ss] offen offset:%c[sk]*256 sc1\n\t.endif\n\t"
                 ".if %c[i] == 26\n\ts_mov_b32 %[dm], %[dm2]\n\ts_mov_b32 %[ds], %[ds2]\n\t.endif\n\t.if %c[i] == 29\n\ts_mov_b32 %[dm], %[dm3]\n\ts_mov_b32 %[ds], %[ds3]\n\t.endif\n\t"
                 ".if %c[dma]\n\ts_mov_b32 m0, %[dm]\n\ts_add_u32 %[dm], %[dm], 0x400\n\tbuffer_load_dwordx4 %[dv], %[rwd], %[ds] offen lds\n\ts_add_u32 %[ds], %[ds], 0x2000\n\t.endif\n\t"
                 "v_mfma_f32_32x32x16_bf16 v[96:111], v[%c[c2]:%c[c2]+3], v[%c[ar]:%c[ar]+3], v[96:111]\n\t"
                 ".if %c[cnt]\n\ts_waitcnt vmcnt(%c[vn])\n\t" TAG_T1 TAG_T2 "s_waitcnt lgkmcnt(0)\n\t" TAG_T3(140, 8) ".endif"
                 : "+{v[64:79]}"(g.a0), "+{v[80:95]}"(g.a1), "+{v[96:111]}"(g.a2), "+{v[140:155]}"(g.r0), "+{v[156:171]}"(g.r1), "+{v[172:187]}"(g.st),
                   "+{v[112:119]}"(g.w0), "+{v[124:131]}"(g.w1), "+{v[132:139]}"(g.w2), [sa] "+s"(g.sa), [bud] "+s"(g.budget), [t] "=&s"(tq), [dm] "+s"(g.dm), [ds] "+s"(g.ds)
                 : [l16] "v"(g.l16), [rs] "s"(g.rs), [s0] "s"(g.s0), [dv] "v"(g.dv), [rwd] "s"(g.rd), [dm2] "s"(g.dm2), [ds2] "s"(g.ds2), [dm3] "s"(g.dm3), [ds3] "s"(g.ds3),
                   [dma] "n"(gu_dma(i)), [sv] "v"(g.sv), [ss] "s"(g.ss), [b0] "v"(g.wa[0]), [b1] "v"(g.wa[1]), [b2] "v"(g.wa[2]),
                   [ld] "n"(j < 32 ? 1 : 0), [lr] "n"(140 + 4 * (j % AW)), [lo] "n"((j & 3) * 1024), [sk] "n"(i >= 2 && i < 10 ? i - 2 : -1), [rd] "n"(n < 32 ? 1 : 0), [no] "n"(n * 1536),
                   [n0] "n"(n & 1 ? 128 : 112), [n1] "n"(n & 1 ? 132 : 116), [n2] "n"(n & 1 ? 136 : 124), [c0] "n"(i & 1 ? 128 : 112), [c1] "n"(i & 1 ? 132 : 116), [c2] "n"(i & 1 ? 136 : 124),
                   [vn] "n"(n < 32 ? gu_younger(n) : 0), [ar] "n"(140 + 4 * (i % AW)), [i] "n"(i), [cr] "n"(140 + 4 * (n % AW)), [ci] "n"(n), [cnt] "n"(j < 32 ? AW - 1 : 31 - i)
                 : "vcc", "scc", "m0", "v120", "memory");
    if constexpr (i + 1 < E) gu_steps<i + 1, E>(g);
}

__global__ __launch_bounds__(512, 2) void k_block(const u16* __restrict__ x, const u16* __restrict__ wqkv, const u16* __restrict__ wo, const u16* __restrict__ wg,
                                                  const u16* __restrict__ wu, const u16* __restrict__ wd, u16* __restrict__ out, char* __restrict__ ws) {
    extern __shared__ __attribute__((aligned(16))) char smem[];
    const unsigned lds0 = (unsigned)(uintptr_t)(LDSAS char*)smem;
    const int tid = threadIdx.x, lane = tid & 63;
    const int w = __builtin_amdgcn_readfirstlane(tid >> 6);
    const int b = blockIdx.x, xcd = b & 7, bi = b >> 3;
    const int r32 = lane & 31, h = lane >> 5;
    const rsrc_t rs = mk_rsrc(ws, WS_END);
    const int wt = w & 1, kh = (w >> 1) & 1;
    STAMP_DECL;
    unsigned ep;
    {
    const int rt = (xcd >> 1) * 8 + (bi & 7), cb = (xcd & 1) * 4 + (bi >> 3), rot = bi & 3;
    const int wc = (w >> 1) & 1, hw = w - 4;
    STAMP(0, 0);
    const rsrc_t rw = mk_rsrc(wqkv, D * 3 * D * 2);
    const unsigned wv = (lane >> 3) * 3072 + (((lane & 7) ^ (((lane >> 4) & 1) << 2)) << 4);
    const unsigned fa = lds0 + K1_FLG;
    if (tid < 12) *(LDSAS unsigned*)(fa + tid * 4) = 0u;
    if (w >= 4) {
        BAR_L();
        const unsigned pa = fa + hw * 4;
        auto issue = [&](int s) {
            const unsigned so = RFL(((((s & 3) + rot) & 3) * 128 + hw * 32) * 3072 + (s >> 2) * 1024 + cb * 128);
            asm volatile("s_nop 4" :: "s"(so));
#pragma unroll
            for (int i = 0; i < 4; ++i)
                asm volatile("s_mov_b32 m0, %0\n\ts_nop 0\n\tbuffer_load_dwordx4 %1, %2, %3 offen lds"
                             :: "s"(lds0 + k1_slot(s) + (4 * hw + i) * 1024), "v"(wv), "s"(rw), "s"(so + i * 8 * 3072) : "m0", "memory");
        };
#define K1_LAND(N, s) asm volatile("s_waitcnt vmcnt(" #N ")\n\tds_write_b32 %0, %1" :: "v"(pa), "v"((s) + 1u) : "memory")
        issue(0); issue(1);
        issue(2); K1_LAND(8, 0);
        issue(3); K1_LAND(8, 1);
        issue(4); K1_LAND(8, 2); K1_LAND(4, 3); K1_LAND(0, 4);
        lds_spin_ge(fa + 28, 4);
        issue(5);
        lds_spin_ge(fa + 20, 4);
        issue(6); issue(7); K1_LAND(8, 5);
        issue(8); K1_LAND(8, 6);
        issue(9); K1_LAND(8, 7);
        lds_spin_ge(fa + 24, 4);
        issue(10); K1_LAND(8, 8);
        issue(11); K1_LAND(8, 9);
        K1_LAND(4, 10); K1_LAND(0, 11);
        if (w == 4) { unsigned e; asm volatile("s_load_dword %0, %1, 0\n\ts_waitcnt lgkmcnt(0)" : "=s"(e) : "s"(ws) : "memory"); *(LDSAS unsigned*)(fa + 48) = e + 1u; }
        else {
            unsigned long long pc; unsigned len, o = bi * 128 + (w - 5) * 24576, t, d0, d1, d2, d3;
#define KCP(D) "s_min_u32 %[t], %[o], %[len]\n\ts_load_dword " D ", %[pc], %[t]\n\ts_add_u32 %[o], %[o], 0x1000\n\t"
            asm volatile("s_getpc_b64 %[pc]\n.Lqkv_pc:\n\ts_mov_b32 %[len], .Lmain_end-.Lqkv_pc-4\n\t" KCP("%[d0]") KCP("%[d1]") KCP("%[d2]") KCP("%[d3]") KCP("%[d0]") KCP("%[d1]") "s_waitcnt lgkmcnt(0)"
                         : [pc] "=&s"(pc), [len] "=&s"(len), [o] "+s"(o), [t] "=&s"(t), [d0] "=&s"(d0), [d1] "=&s"(d1), [d2] "=&s"(d2), [d3] "=&s"(d3) :: "scc", "memory");
        }
        BAR_L();
        ep = RFL(*(const LDSAS unsigned*)(fa + 48));
        if (w == 4) { __builtin_amdgcn_s_sleep(POLL_DELAY_Q); wait_flags(rs, WS_FQ + (xcd * 2 + (bi >> 4)) * 8192, 64, ep, lane); }
    } else {
    {
        const rsrc_t rx = mk_rsrc(x, S * D * 2);
        u32x4 r[2][8];
#pragma unroll
        for (int p = 0; p < 2; ++p)
#pragma unroll
            for (int g = 0; g < 8; ++g)
                r[p][g] = __builtin_amdgcn_raw_buffer_load_b128(rx, (lane >> 3) * 1024 + (((lane & 7) ^ ((4 * p + (lane >> 4)) & 7)) << 4) + g * 128, (rt * 64 + w * 16 + p * 8) * 1024, K1_X_AUX);
        SBAR(); BAR_L(); SBAR();
#pragma unroll
        for (int p = 0; p < 2; ++p) {
            float s0 = 0.f, s1 = 0.f, q0 = 0.f, q1 = 0.f;
#pragma unroll
            for (int g = 0; g < 8; ++g) { s0 = dot2(r[p][g][0], ONES2, s0); q0 = dot2(r[p][g][0], r[p][g][0], q0); s1 = dot2(r[p][g][1], ONES2, s1); q1 = dot2(r[p][g][1], r[p][g][1], q1);
                                          s0 = dot2(r[p][g][2], ONES2, s0); q0 = dot2(r[p][g][2], r[p][g][2], q0); s1 = dot2(r[p][g][3], ONES2, s1); q1 = dot2(r[p][g][3], r[p][g][3], q1); }
            if (p == 0) STAMP(0, 1);
            float sum = s0 + s1, sq = q0 + q1;
            sum = dpp_add<0xB1>(sum); sq = dpp_add<0xB1>(sq); sum = dpp_add<0x4E>(sum); sq = dpp_add<0x4E>(sq); sum = dpp_add<0x141>(sum); sq = dpp_add<0x141>(sq);
            const float mean = sum * (1.f / D);
            const float rstd = __builtin_amdgcn_rsqf(__builtin_fmaxf(sq * (1.f / D) - mean * mean, 0.f) + LN_EPS), nb = -mean * rstd;
#pragma unroll
            for (int g = 0; g < 8; g += 2) {
                f32x2 t[8];
#pragma unroll
                for (int i = 0; i < 8; ++i) { const unsigned v = r[p][g + (i >> 2)][i & 3]; t[i] = f32x2{bflo(v), bfhi(v)}; }
                asm volatile("" : "+v"(t[0]), "+v"(t[1]), "+v"(t[2]), "+v"(t[3]), "+v"(t[4]), "+v"(t[5]), "+v"(t[6]), "+v"(t[7]));
#pragma unroll
                for (int i = 0; i < 8; ++i) t[i] = t[i] * rstd + nb;
                asm volatile("" : "+v"(t[0]), "+v"(t[1]), "+v"(t[2]), "+v"(t[3]), "+v"(t[4]), "+v"(t[5]), "+v"(t[6]), "+v"(t[7]));
                u32x4 y0, y1;
#pragma unroll
                for (int i = 0; i < 4; ++i) { y0[i] = pk2(t[i][0], t[i][1]); y1[i] = pk2(t[4 + i][0], t[4 + i][1]); }
                *(LDSAS u32x4*)(lds0 + (2 * w + p) * 1024 + lane * 16 + g * 8192) = y0;
                *(LDSAS u32x4*)(lds0 + (2 * w + p) * 1024 + lane * 16 + g * 8192 + 8192) = y1;
            }
            SBAR();
        }
    }
    lds_inc(fa + 16, lane);
    STAMP(0, 3);
    u32x16 xa0, xa1, xa2, xa3, xa4, xa5, xa6, xa7;
    u32x4 w0, w1, w2, w3, w4, fl;
    f32x16 acA, acB;
    unsigned xb[4], tq;
#pragma unroll
    for (int m = 0; m < 4; ++m) xb[m] = lds0 + (32 * wt + r32) * 128 + (((2 * m + h) ^ ((r32 >> 1) & 7)) << 4) + rot * 16384;
    const int q4 = (lane & 15) >> 2, p4 = lane & 3, blk = (lane >> 4) & 1;
    const unsigned tA = lds0 + (8 * h + q4) * 128 + (((4 * wc + 2 * blk + (p4 >> 1)) ^ (((q4 >> 1) & 1) << 2)) << 4) + 8 * (p4 & 1);
    const unsigned tB = tA + 65536, tC = tA + 131072;
    const int tt = rt * 2 + wt, hd = cb * 4 + wc * 2;
#define KXR(R, m, O) "ds_read_b128 v[" #R ":" #R "+3], %[x" #m "] offset:" #O "\n\t"
#define KXN(m) "v_add_u32 %[x" #m "], 0x4000, %[x" #m "]\n\tv_and_b32 %[x" #m "], 0xffff, %[x" #m "]\n\t"
#define KCHK(ST) "v_min3_u32 v116, v116, v117, v118\n\tv_min_u32 v116, v116, v119\n\ts_nop 0\n\tv_readfirstlane_b32 %[t], v116\n\ts_nop 0\n\ts_cmp_gt_u32 %[t], " ST "\n\t"
#define KSPIN(ST) "1:\n\tds_read_b128 v[116:119], %[fa]\n\ts_waitcnt lgkmcnt(0)\n\t" KCHK(ST) "s_cbranch_scc0 1b\n\t"
#define KWR(R, O) "ds_read_b64_tr_b16 v[" #R ":" #R "+1], %[tb] offset:" #O "\n\tds_read_b64_tr_b16 v[" #R "+2:" #R "+3], %[tb] offset:" #O "+512\n\t"
    asm volatile("3:\n\tds_read_b32 v116, %[fa] offset:16\n\ts_waitcnt lgkmcnt(0)\n\tv_readfirstlane_b32 %[t], v116\n\ts_nop 0\n\ts_cmp_gt_u32 %[t], 3\n\ts_cbranch_scc0 3b\n\t"
                 KXR(128, 0, 0) KXR(132, 1, 0) KXR(136, 2, 0) KXR(140, 3, 0) KXR(144, 0, 8192) KXR(148, 1, 8192) KXR(152, 2, 8192) KXR(156, 3, 8192) KXN(0) KXN(1) KXN(2) KXN(3)
                 KSPIN("0") KWR(96, 0) KWR(100, 2048) KWR(104, 4096) KWR(108, 6144)
                 : "={v[116:119]}"(fl), [t] "=&s"(tq), "={v[96:99]}"(w0), "={v[100:103]}"(w1), "={v[104:107]}"(w2), "={v[108:111]}"(w3), "={v[112:115]}"(w4), "={v[64:79]}"(acA), "={v[80:95]}"(acB),
                   "={v[128:143]}"(xa0), "={v[144:159]}"(xa1), "={v[160:175]}"(xa2), "={v[176:191]}"(xa3), "={v[192:207]}"(xa4), "={v[208:223]}"(xa5), "={v[224:239]}"(xa6), "={v[240:255]}"(xa7),
                   [x0] "+v"(xb[0]), [x1] "+v"(xb[1]), [x2] "+v"(xb[2]), [x3] "+v"(xb[3])
                 : [fa] "v"(fa), [tb] "v"(tB) : "scc", "memory");
    STAMP(0, 4);
    auto epi = [&](int m, const f32x16& acc, int hh) {
        const float sc = 0.25f * LOG2E;
        u32x4 o;
#pragma unroll
        for (int i = 0; i < 4; ++i) o[i] = m == 0 ? pk2(acc[8 * hh + 2 * i] * sc, acc[8 * hh + 2 * i + 1] * sc) : pk2(acc[8 * hh + 2 * i], acc[8 * hh + 2 * i + 1]);
        if (m == 0) ST128(o, rs, lane * 16, WS_Q + ((hd + hh) * 64 + tt) * 1024, K1_ST_AUX);
        else if (m == 1) ST128(o, rs, lane * 16, WS_KV + (hd + hh) * KV_HEAD + (tt >> 3) * KV_CHUNK + (tt & 7) * 1024, K1_ST_AUX);
        else ST128(o, rs, (r32 >> 4) * KV_HEAD + (16 * h + (lane & 15)) * 16 + hh * 512,
                                                    WS_KV + hd * KV_HEAD + (tt >> 3) * KV_CHUNK + 8192 + (tt & 7) * 1024, K1_ST_AUX);
    };
    sfor<0, 96>([&](auto G) {
        constexpr int g = G.value, n = g + 4, m = g >> 5;
        constexpr unsigned o = k1_slot(n >> 3) + (n & 7) * 2048, oi = o & 65535;
        constexpr int aR = m == 1 ? 80 : 64, wR = 96 + 4 * (g % 5), nR = 96 + 4 * (n % 5), xR = 128 + 4 * (g & 31);
        constexpr int nx = (g - 4 >= 0 && g - 4 < 12) + (g - 3 >= 0 && g - 3 < 12) + (g - 2 >= 0 && g - 2 < 12) + (g - 1 >= 0 && g - 1 < 12), wc_ = n >= 96 ? 0 : (8 + 2 * nx > 14 ? 14 : 8 + 2 * nx);
#define KRD(B) "ds_read_b64_tr_b16 v[%c[nr]:%c[nr]+1], %[" B "] offset:%c[oi]\n\tds_read_b64_tr_b16 v[%c[nr]+2:%c[nr]+3], %[" B "] offset:%c[oi]+512\n\t"
        asm volatile(".if %c[bar] == 1\n\t" KCHK("%c[st]") "s_cbranch_scc0 1f\n2:\n\t.subsection 1\n" KSPIN("%c[st]") "s_branch 2b\n\t.subsection 0\n\t.endif\n\t"
                     ".if %c[bar] == 2\n\tds_read_b128 v[116:119], %[fa]\n\t.endif\n\t"
                     ".if %c[rd] == 1\n\t" KRD("tb") ".endif\n\t.if %c[rd] == 2\n\t" KRD("tc") ".endif\n\t.if %c[rd] == 3\n\t" KRD("ta") ".endif\n\t"
                     "s_waitcnt lgkmcnt(%c[wn])\n\t"
                     ".if %c[md] == 0\n\tv_mfma_f32_32x32x16_bf16 v[%c[ar]:%c[ar]+15], v[%c[wr]:%c[wr]+3], v[%c[xr]:%c[xr]+3], v[%c[ar]:%c[ar]+15]\n\t.endif\n\t"
                     ".if %c[md] == 1\n\tv_mfma_f32_32x32x16_bf16 v[%c[ar]:%c[ar]+15], v[%c[xr]:%c[xr]+3], v[%c[wr]:%c[wr]+3], v[%c[ar]:%c[ar]+15]\n\t.endif\n\t"
                     ".if %c[md] == 2\n\tv_mfma_f32_32x32x16_bf16 v[%c[ar]:%c[ar]+15], v[%c[xr]:%c[xr]+3], v[%c[wr]:%c[wr]+3], 0\n\t.endif\n\t"
                     ".if %c[md] == 3\n\tv_mfma_f32_32x32x16_bf16 v[%c[ar]:%c[ar]+15], v[%c[wr]:%c[wr]+3], v[%c[xr]:%c[xr]+3], 0\n\t.endif\n\t"
                     ".if %c[xm] == 1\n\tds_read_b128 v[%c[xd]:%c[xd]+3], %[x0] offset:%c[xo]\n\tds_read_b128 v[%c[xd]+4:%c[xd]+7], %[x1] offset:%c[xo]\n\t.endif\n\t"
                     ".if %c[xm] == 2\n\tds_read_b128 v[%c[xd]:%c[xd]+3], %[x2] offset:%c[xo]\n\tds_read_b128 v[%c[xd]+4:%c[xd]+7], %[x3] offset:%c[xo]\n\t.endif\n\t"
                     ".if %c[xn]\n\t" KXN(0) KXN(1) KXN(2) KXN(3) ".endif"
                     : "+{v[64:79]}"(acA), "+{v[80:95]}"(acB), "+{v[96:99]}"(w0), "+{v[100:103]}"(w1), "+{v[104:107]}"(w2), "+{v[108:111]}"(w3), "+{v[112:115]}"(w4),
                       "+{v[128:143]}"(xa0), "+{v[144:159]}"(xa1), "+{v[160:175]}"(xa2), "+{v[176:191]}"(xa3), "+{v[192:207]}"(xa4), "+{v[208:223]}"(xa5), "+{v[224:239]}"(xa6), "+{v[240:255]}"(xa7),
                       "+{v[116:119]}"(fl), [t] "=&s"(tq), [x0] "+v"(xb[0]), [x1] "+v"(xb[1]), [x2] "+v"(xb[2]), [x3] "+v"(xb[3])
                     : [ta] "v"(tA), [tb] "v"(tB), [tc] "v"(tC), [fa] "v"(fa), [st] "n"(n >> 3), [wn] "n"(wc_),
                       [bar] "n"(K1_ABL ? 0 : n < 96 && (n & 7) == 0 ? 1 : n < 92 && (n & 7) == 4 ? 2 : 0), [rd] "n"(n >= 96 ? 0 : o < 65536 ? 3 : o < 131072 ? 1 : 2), [oi] "n"(oi), [nr] "n"(nR),
                       [md] "n"(m < 2 ? ((g & 31) == 0 ? 3 : 0) : g == 64 ? 2 : 1), [ar] "n"(aR), [wr] "n"(wR), [xr] "n"(xR),
                       [xm] "n"(g < 12 ? 1 + (g & 1) : 0), [xd] "n"(160 + 8 * g), [xo] "n"(((g >> 1) & 1) * 8192), [xn] "n"(g < 11 && (g & 3) == 3 ? 1 : 0) : "scc", "memory");
        if (g == 7 && lane == 0) asm volatile("ds_add_u32 %0, %1" :: "v"(fa + 28), "v"(1u) : "memory");
        if (g == 16 && lane == 0) asm volatile("ds_add_u32 %0, %1" :: "v"(fa + 20), "v"(1u) : "memory");
        if (g == 23 && lane == 0) asm volatile("ds_add_u32 %0, %1" :: "v"(fa + 24), "v"(1u) : "memory");
        if (g == 36 || g == 38) epi(0, acA, (g - 36) >> 1);
        if (g == 68 || g == 70) epi(1, acB, (g - 68) >> 1);
    });
    STAMP(0, 2);
    asm volatile("s_nop 15" : "+{v[64:79]}"(acA));
    epi(2, acA, 0); epi(2, acA, 1);
    SBAR(); BAR_L();
    ep = RFL(*(const LDSAS unsigned*)(fa + 48));
    WAITV(0);
    if (lane == 0) __builtin_amdgcn_raw_buffer_store_b32(ep, rs, 0, WS_FQ + ((cb * 2 + wc) * 64 + tt) * 128, AUX_SC1);
    STAMP(0, 5);
    FLUSH(0);
    }
    }

    const int rt = xcd * 4 + (bi >> 3), ct = bi & 7;
    u32x4 hreg[2];
    u32x8 xres;

    auto issue_wo = [&](int first, int n) {
        const rsrc_t rwo = mk_rsrc(wo, D * D * 2);
        const unsigned v = w64_voff(lane);
        for (int p = first; p < first + n; ++p) dma16(rwo, v, p * 8192 + ct * 128, lds0 + L_WO + p * 1024);
    };
    auto issue_dn = [&](int i0, int n, unsigned dst, int hw) {
        const rsrc_t rwd = mk_rsrc(wd, F * D * 2);
        const unsigned v = w64_voff(lane);
        const unsigned so = ((43 * (hw >> 1) + i0) * 16 + (hw & 1) * 8) * 1024 + ct * 128;
        for (int i = 0; i < n; ++i) dma16(rwd, v, so + i * 16384, lds0 + dst + (hw & 1) * 1024 + i * 2048);
    };
    auto gu_wa = [&](int n) -> unsigned { const unsigned blk = (lane >> 4) & 1;
        return lds0 + L_GU + blk * 49152 + (((4 * h + ((lane & 15) >> 2)) * 96 + n * 32 + (lane & 3) * 8) ^ (blk * 128)); };
    float cs[3] = {0.f, 0.f, 0.f};
    auto gu_colsum = [&]() {
        u32x4 f[3][4];
#pragma unroll
        for (int n = 0; n < 3; ++n) {
            const unsigned a0 = gu_wa(n) + w * 4 * 1536;
#pragma unroll
            for (int i = 0; i < 4; ++i) f[n][i] = __builtin_bit_cast(u32x4, lds_tr2(a0 + i * 1536, a0 + i * 1536 + 768));
        }
        SBAR();
#pragma unroll
        for (int i = 0; i < 4; ++i)
#pragma unroll
            for (int e = 0; e < 4; ++e)
#pragma unroll
                for (int n = 0; n < 3; ++n) cs[n] = dot2(f[n][i][e], ONES2, cs[n]);
#pragma unroll
        for (int n = 0; n < 3; ++n) cs[n] = swap_add(cs[n]);
    };

    unsigned budget = 1u << 16;
    const unsigned l16 = lane * 16;

    {
        const int head = xcd * 4 + (bi >> 3), qb = bi & 7, wq = w & 3, g = w >> 2, qt = qb * 8 + wq * 2;
        const unsigned kvs = WS_KV + head * KV_HEAD;
        unsigned ks = kvs + g * 4 * KV_CHUNK + wq * 4096, km = lds0 + g * 32768 + wq * 4096;
        const rsrc_t rg = mk_rsrc(g ? wu : wg, (bi < 29 && ABL_GU != 1) ? D * F * 2 : 0), rwd = mk_rsrc(wd, F * D * 2), rwo = mk_rsrc(wo, D * D * 2), rx = mk_rsrc(x, S * D * 2);
        unsigned gv0, gv1, gv2;
        { unsigned c = lane ^ (g * 8); gv0 = (c / 6) * (F * 2) + (c % 6) * 16; c = (64 + lane) ^ (g * 8); gv1 = (c / 6) * (F * 2) + (c % 6) * 16;
          c = (128 + lane) ^ (g * 8); gv2 = (c / 6) * (F * 2) + (c % 6) * 16; }
        if (ABL_GU == 2) { gv0 = l16; gv1 = l16 + 1024; gv2 = l16 + 2048; }
        unsigned gs = ABL_GU == 2 ? wq * 4 * 32 * (F * 2) + bi * 3072 : wq * 4 * 32 * (F * 2) + bi * 96, gm = lds0 + L_GU + g * 49152 + wq * 12288;
        const unsigned pvd = lane < 43 ? (bi * 344 + w * 43 + lane) * 128 : 0x80000000u, pvo = lane < 16 ? (bi * 128 + w * 16 + lane) * 128 : 0x80000000u;
        const unsigned wv = w64_voff(lane), xv = (rt * 64 + wt * 32 + r32) * 1024 + (ct * 64 + kh * 32 + 4 * h) * 2;
        const unsigned kb = lds0 + g * 32768 + l16, vb = lds0 + g * 32768 + 8192 + (16 * h + (r32 & 15)) * 16;
        const unsigned cst = (r32 & 27) == 16 ? ONES2 : 0u;
        const unsigned long long msk = 0x0000ffff0000ffffull;
        const unsigned fo = WS_O + ((qt + g) * 32 + head) * 1024, fh = WS_H + ((rt * 2 + wt) * 32 + ct * 4 + (w >> 1)) * 1024, fg = WS_HH + ((xcd * 8 + w) * NU + bi * 3) * 1024;
        const unsigned fsv = w == 0 ? WS_ST + ((rt * 2 + h) * 16 + ct * 2) * 256 + r32 * 16 : 0x80000000u, fg2 = bi < 28 ? l16 : 0x80000000u, fg0 = bi < 29 ? l16 : 0x80000000u;
        const unsigned fpv = tid == 0 ? WS_FP + b * 4 : 0x80000000u;
        u32x4 ones = {~0u, ~0u, ~0u, ~0u}, fpl = {0u, 0u, 0u, 0u};
        unsigned pfd = 0, epv = ep;
        xres = u32x8{};
        s16x8 q0, q1;
        f32x16 n0, n1, sA, sB = {}, o0 = {}, o1 = {};
        u32x4 pA = {0u, 0u, 0u, 0u}, pB = {0u, 0u, 0u, 0u};
        s16x8 v1 = __builtin_bit_cast(s16x8, u32x4{cst, cst, cst, cst}), v1b = v1;
        STAMP(1, 0);
        SBAR();
#define XKV "s_mov_b32 m0, %[km]\n\ts_add_u32 %[km], %[km], 0x400\n\tbuffer_load_dwordx4 %[l16], %[rs], %[ks] offen sc1 lds\n\ts_add_u32 %[ks], %[ks], 0x400\n\t"
        asm volatile("s_barrier\n\tbuffer_load_dwordx4 v[128:131], %[l16], %[rs], %[qs] offen sc1\n\tbuffer_load_dwordx4 v[156:159], %[l16], %[rs], %[qs1] offen sc1\n\t"
                     XKV XKV XKV XKV "s_add_u32 %[km], %[km], 0x3000\n\ts_add_u32 %[ks], %[ks], 0x3000\n\ts_waitcnt vmcnt(0)\n\ts_barrier\n\t" XKV XKV XKV XKV
                     : "={v[128:131]}"(q0), "={v[156:159]}"(q1), [km] "+s"(km), [ks] "+s"(ks), "+{v[80:95]}"(sB), "+{v[96:111]}"(o0), "+{v[160:175]}"(o1),
                       "+{v[144:147]}"(v1), "+{v[136:139]}"(v1b), "+{v[148:151]}"(pA), "+{v[152:155]}"(pB), "+{v[192:199]}"(xres), "+{v200}"(pfd), "+{v201}"(epv), "+{v[204:207]}"(ones), "+{v[208:211]}"(fpl)
                     : [l16] "v"(l16), [rs] "s"(rs), [qs] "s"(RFL(WS_Q + (head * 64 + qt + g) * 1024)), [qs1] "s"(RFL(WS_Q + (head * 64 + qt + 1 - g) * 1024)) : "m0", "scc", "memory");
        km -= 16384 + 4096; ks += KV_CHUNK - 4096;
        STAMP(1, 1);
        s16x8 kf = lds_b128(kb), v0;
        {
            const f32x16 z = {};
            const s16x8 kz = lds_b128(lds0 + l16);
            const u32x4 vv = __builtin_bit_cast(u32x4, lds_b128(vb)), cc = {cst, cst, cst, cst};
            const f32x16 s0 = MFMA(kz, q0, z), s1 = MFMA(kz, q1, z);
            float m0 = __builtin_fmaxf(s0[0], s0[1]), m1 = __builtin_fmaxf(s1[0], s1[1]);
#pragma unroll
            for (int i = 2; i < 16; i += 2) { m0 = __builtin_fmaxf(__builtin_fmaxf(m0, s0[i]), s0[i + 1]); m1 = __builtin_fmaxf(__builtin_fmaxf(m1, s1[i]), s1[i + 1]); }
            m0 = swap_max(m0); m1 = swap_max(m1);
#pragma unroll
            for (int i = 0; i < 16; ++i) { n0[i] = -m0; n1[i] = -m1; }
            sA = MFMA(kf, q0, n0);
            v0 = __builtin_bit_cast(s16x8, r32 < 16 ? vv : cc);
        }
#define XE(C, i) "v_exp_f32 v[" #C "+" #i "], v[" #C "+" #i "]\n\t"
#define XC(P, C, i) "v_cvt_pk_bf16_f32 v" #P ", v[" #C "+" #i "], v[" #C "+" #i "+1]\n\t"
#define XG1(C) XE(C, 0) XE(C, 1) XE(C, 2) XE(C, 3) XC(148, C, 0) XE(C, 4) XE(C, 5) XC(149, C, 2)
#define XG2(C) XE(C, 6) XE(C, 7) XC(150, C, 4) XE(C, 8) XC(151, C, 6) XE(C, 9) XE(C, 10)
#define XG3(C) XE(C, 11) XC(152, C, 8) XE(C, 12) XE(C, 13) XC(153, C, 10) XE(C, 14) XE(C, 15) XC(154, C, 12) XC(155, C, 14)
#define XM(D, A, B, C) "v_mfma_f32_32x32x16_bf16 v[" #D ":" #D "+15], v[" #A ":" #A "+3], v[" #B ":" #B "+3], v[" #C ":" #C "+15]\n\t"
#define XRK(BASE, O) "ds_read_b128 v[132:135], " BASE " offset:" #O "\n\t"
#define XRV(R, BASE, O) "s_mov_b64 exec, %[msk]\n\tds_read_b128 v[" #R ":" #R "+3], " BASE " offset:" #O "\n\ts_mov_b64 exec, -1\n\t"
#define XW(n) "s_waitcnt lgkmcnt(" #n ")\n\t"
#define XTILE(VA, RK, RV1, WA, X1, VB, X2, RV0) \
        XM(80, 132, 156, 176) RK XG1(64) XM(160, VA, 152, 160) RV1 XG2(64) XW(WA) XM(96, 140, 148, 96) XG3(64) \
        XW(0) XM(64, 132, 128, 112) X1 XG1(80) XM(96, VB, 152, 96) X2 XG2(80) XM(160, 140, 148, 160) RV0 XG3(80)
#define XGU(j) "s_mov_b32 m0, %[gm]\n\ts_add_u32 %[gm], %[gm], 0x400\n\tbuffer_load_dwordx4 %[gv" #j "], %[rg], %[gs] offen lds\n\t"
#define XGU2 XGU(2) "s_add_u32 %[gs], %[gs], 88064\n\t"
#define XPF "buffer_load_dword v200, %[pvd], %[rwd], 0 offen\n\tbuffer_load_dword v200, %[pvo], %[rwo], 0 offen\n\t"
#define XWO "s_mov_b32 m0, %[km]\n\ts_add_u32 %[km], %[km], 0x400\n\tbuffer_load_dwordx4 %[wv], %[rwo], %[ks] offen lds\n\ts_add_u32 %[ks], %[ks], 0x2000\n\t"
#define XST(V, SO, O) "buffer_store_dwordx4 v[204:207], " V ", %[rs], " SO " offen offset:" #O " sc1\n\t"
#define XFP "buffer_store_dword v201, %[fpv], %[rs], 0 offen sc1\n\t"
#define XFL "buffer_load_dwordx4 v[208:211], %[l16], %[rs], %[fp] offen sc1\n\t"
#define XXR(R, O) "buffer_load_dwordx2 v[" #R ":" #R "+1], %[xv], %[rx], 0 offen offset:" #O "\n\t"
#define AOPS : "+{v[64:79]}"(sA), "+{v[80:95]}"(sB), "+{v[96:111]}"(o0), "+{v[160:175]}"(o1), "+{v[112:127]}"(n0), "+{v[176:191]}"(n1), "+{v[128:131]}"(q0), "+{v[156:159]}"(q1), \
               "+{v[132:135]}"(kf), "+{v[140:143]}"(v0), "+{v[144:147]}"(v1), "+{v[136:139]}"(v1b), "+{v[148:151]}"(pA), "+{v[152:155]}"(pB), "+{v[192:199]}"(xres), "+{v200}"(pfd), \
               "+{v201}"(epv), "+{v[204:207]}"(ones), "+{v[208:211]}"(fpl), \
               [km] "+s"(km), [ks] "+s"(ks), [gm] "+s"(gm), [gs] "+s"(gs) \
             : [ka] "v"(ka), [va] "v"(va), [kn] "v"(kn), [vn] "v"(vn), [msk] "s"(msk), [l16] "v"(l16), [rs] "s"(rs), [rg] "s"(rg), [gv0] "v"(gv0), [gv1] "v"(gv1), [gv2] "v"(gv2), \
               [pvd] "v"(pvd), [pvo] "v"(pvo), [wv] "v"(wv), [xv] "v"(xv), [rwd] "s"(rwd), [rwo] "s"(rwo), [rx] "s"(rx), \
               [fo] "s"(fo), [fh] "s"(fh), [fg] "s"(fg), [fsv] "v"(fsv), [fg0] "v"(fg0), [fg2] "v"(fg2), [fpv] "v"(fpv), [fp] "s"(WS_FP) : "m0", "scc", "memory"
#define XBAR(n) asm volatile("s_waitcnt vmcnt(" #n ") lgkmcnt(0)\n\ts_barrier" AOPS)
#define XROUND(A1, A2, B1, B2, C1, C2, D1, D2, E1, E2, F1, F2) asm volatile( \
            XTILE(136, XRK("%[ka]", 1024), XRV(144, "%[va]", 512), 2, A1, 144, A2, XRV(140, "%[va]", 1024)) \
            XTILE(144, XRK("%[ka]", 2048), XRV(144, "%[va]", 1536), 2, B1, 144, B2, XRV(140, "%[va]", 2048)) \
            XTILE(144, XRK("%[ka]", 3072), XRV(144, "%[va]", 2560), 2, C1, 144, C2, XRV(140, "%[va]", 3072)) \
            XTILE(144, XRK("%[ka]", 4096), XRV(144, "%[va]", 3584), 2, D1, 144, D2, XRV(140, "%[va]", 4096)) \
            XTILE(144, XRK("%[ka]", 5120), XRV(144, "%[va]", 4608), 2, E1, 144, E2, XRV(140, "%[va]", 5120)) \
            XTILE(144, XRK("%[ka]", 6144), XRV(144, "%[va]", 5632), 2, F1, 144, F2, XRV(140, "%[va]", 6144)) \
            XTILE(144, XRK("%[ka]", 7168), XRV(144, "%[va]", 6656), 2, XRV(136, "%[va]", 7680), 144, "", XRV(140, "%[va]", 7168)) AOPS)
#define XLAST(X1, X2) asm volatile(XTILE(144, XRK("%[kn]", 0), "", 1, X1, 136, X2, XRV(140, "%[vn]", 0)) AOPS)
        unsigned ka = kb, va = vb, kn = kb + 16384, vn = vb + 16384;
        asm volatile("s_nop 15" AOPS);
        STAMP(1, 7);
        XROUND(XGU(0), "", "", XST("%[l16]", "%[fo]", 0), XGU(1), XST("%[fsv]", "0", 0), "", XST("%[l16]", "%[fh]", 0), XGU2, "", XGU(0), "");
        STAMP(1, 8);
        XBAR(7);
        STAMP(1, 9);
        XLAST(XKV, XKV);
        ka = kb + 16384; va = vb + 16384; kn = kb; vn = vb;
        XROUND(XKV, XKV, XGU(1), XST("%[fg0]", "%[fg]", 0), XGU2, "", XST("%[fg0]", "%[fg]", 1024), XGU(0), "", XGU(1), XST("%[fg2]", "%[fg]", 2048), "");
        STAMP(1, 10);
        XBAR(7);
        STAMP(1, 11);
        km += 16384 - 4096; ks += KV_CHUNK - 4096;
        XLAST(XKV, XKV);
        ka = kb; va = vb; kn = kb + 16384; vn = vb + 16384;
        XROUND(XKV, XKV, XGU2, "", XGU(0), "", XGU(1), "", XGU2, "", XPF, "");
        STAMP(1, 12);
        XBAR(6);
        STAMP(1, 13);
        km = lds0 + g * 32768 + wq * 4096; ks = (32 * g + 4 * wq) * 8192 + ct * 128;
        XLAST(XFP, XWO);
        ka = kb + 16384; va = vb + 16384; kn = kb; vn = vb;
        XROUND(XWO, XWO, XWO, "", XXR(192, 0), "", XXR(194, 16), "", XXR(196, 32), "", XXR(198, 48), "");
        XLAST(XFL, "");
        STAMP(1, 14);
        asm volatile(XM(160, 136, 152, 160) "s_waitcnt vmcnt(0) lgkmcnt(0)\n\ts_barrier\n\ts_nop 15" AOPS);
        STAMP(1, 4);
        f32x16 o = o0;
        const unsigned xa = lds0 + g * 32768 + 16384 + wq * 3072 + l16, xb = lds0 + (g ^ 1) * 32768 + 16384 + wq * 3072 + l16;
        *(LDSAS f32x4*)(xa) = f32x4{o1[0], o1[1], o1[2], o1[3]};
        *(LDSAS f32x4*)(xa + 1024) = f32x4{o1[4], o1[5], o1[6], o1[7]};
        *(LDSAS float*)(xa + 2048) = o1[8];
        {
            float big = __builtin_fmaxf(o0[8], o1[8]) * 1e19f;
            float chk = o0[8] + o1[8];
#pragma unroll
            for (int i = 0; i < 8; ++i) { big = __builtin_fmaxf(__builtin_fmaxf(big, __builtin_fabsf(o0[i])), __builtin_fabsf(o1[i])); chk += o0[i] + o1[i]; }
            *(LDSAS unsigned*)(lds0 + L_FLAG + w * 4) = __any(!(big < 1e38f) || chk != chk);
        }
        BAR_L();
        const f32x4 p0 = lds_f4(xb), p1 = lds_f4(xb + 1024); const float pl = *(const LDSAS float*)(xb + 2048);
        const u32x4 f0 = *(const LDSAS u32x4*)(lds0 + L_FLAG), f1 = *(const LDSAS u32x4*)(lds0 + L_FLAG + 16);
#pragma unroll
        for (int i = 0; i < 4; ++i) { o[i] += p0[i]; o[4 + i] += p1[i]; }
        o[8] += pl;
        if (__builtin_expect(RFL(f0[0] | f0[1] | f0[2] | f0[3] | f1[0] | f1[1] | f1[2] | f1[3]), 0)) {
            BAR_L();
            const s16x8 qf = q0;
            const u32x4 cc = {cst, cst, cst, cst};
            const unsigned vf = lds0 + 16384 + 8192 + (16 * h + (r32 & 15)) * 16;
            float m = -__builtin_inff();
            o = f32x16{};
            const f32x16 z = {};
#pragma unroll 1
            for (int c = 0; c < 8; ++c) {
                dma16<true>(rs, l16, kvs + c * KV_CHUNK + w * 2048, lds0 + 16384 + w * 2048);
                dma16<true>(rs, l16, kvs + c * KV_CHUNK + w * 2048 + 1024, lds0 + 16384 + w * 2048 + 1024);
                BAR_VL(0);
#pragma unroll 1
                for (int t = 0; t < 8; ++t) {
                    f32x16 s = MFMA(lds_b128(lds0 + 16384 + l16 + t * 1024), qf, z);
                    float mx = s[0];
#pragma unroll
                    for (int i = 1; i < 16; ++i) mx = __builtin_fmaxf(mx, s[i]);
                    mx = __builtin_fmaxf(swap_max(mx), m);
                    const float al = __builtin_amdgcn_exp2f(m - mx);
                    m = mx;
#pragma unroll
                    for (int i = 0; i < 16; ++i) { o[i] *= al; s[i] = __builtin_amdgcn_exp2f(s[i] - m); }
#pragma unroll
                    for (int i = 0; i < 4; ++i) { pA[i] = pk2(s[2 * i], s[2 * i + 1]); pB[i] = pk2(s[8 + 2 * i], s[9 + 2 * i]); }
                    const u32x4 a = __builtin_bit_cast(u32x4, lds_b128(vf + t * 1024)), c2 = __builtin_bit_cast(u32x4, lds_b128(vf + t * 1024 + 512));
                    o = MFMA(__builtin_bit_cast(s16x8, r32 < 16 ? a : cc), __builtin_bit_cast(s16x8, pA), o);
                    o = MFMA(__builtin_bit_cast(s16x8, r32 < 16 ? c2 : cc), __builtin_bit_cast(s16x8, pB), o);
                }
                BAR_L();
            }
        }
        {
            const float inv = __builtin_amdgcn_rcpf(o[8]);
            u32x4 ov;
#pragma unroll
            for (int i = 0; i < 4; ++i) ov[i] = pk2(o[2 * i] * inv, o[2 * i + 1] * inv);
            ST128(ov, rs, l16, fo, AUX_SC1);
        }
        STAMP(1, 5);
        BAR_L();
        if (w >= 4) issue_wo(16 + ((w - 4) >> 1) * 32 + (w & 1) * 8, 8);
        gu_colsum();
#pragma unroll 1
        for (unsigned spins = 0; spins < SPIN_CAP && !__all(fpl[0] == ep && fpl[1] == ep && fpl[2] == ep && fpl[3] == ep); ++spins) {
            __builtin_amdgcn_s_sleep(2);
            fpl = __builtin_amdgcn_raw_buffer_load_b128(rs, l16, WS_FP, AUX_SC1);
        }
        STAMP(1, 6);
        FLUSH(1);
    }

    {
        STAMP(2, 0);
        f32x16 a0, a1;
        if (w < 4) {
            const unsigned t0 = lds0 + L_WO + kh * 32768 + w64_tr(lane, kh), t1 = lds0 + L_WO + kh * 32768 + w64_tr(lane, kh ^ 1);
            G2 g2;
            g2.tb[0] = g2.tb[2] = g2.tb[4] = t0; g2.tb[1] = g2.tb[3] = g2.tb[5] = t1;
            g2.sa = g2.s0 = RFL(WS_O + ((rt * 2 + wt) * 32 + kh * 16) * 1024); g2.l16 = l16; g2.budget = budget; g2.rs = rs; g2.a1 = f32x16{};
#pragma unroll
            for (int i = 0; i < 8; ++i) { g2.a0[2 * i] = bflo(xres[i]); g2.a0[2 * i + 1] = bfhi(xres[i]); }
            __builtin_amdgcn_s_sleep(DELAY_O);
            STAMP(2, 1);
            g2_steps<0, 16, 0, 6, -1>(g2);
            a0 = g2.a0; a1 = g2.a1; STAMPV(15, budget - g2.budget); budget = g2.budget;
        } else BAR_VL(0);
        STAMP(2, 2);
        BAR_L();
        if (w < 4) {
#pragma unroll
            for (int g = 0; g < 4; ++g) *(LDSAS f32x4*)(lds0 + w * 4096 + g * 1024 + lane * 16) = f32x4{a1[4 * g], a1[4 * g + 1], a1[4 * g + 2], a1[4 * g + 3]};
        }
#pragma unroll
        for (int n = 0; n < 3; ++n) *(LDSAS float*)(lds0 + L_TAB + (w * 96 + n * 32 + r32) * 4) = cs[n];
        BAR_L();
        if (w >= 4) {
            if (tid < 256 + 96) {
                float t[8];
#pragma unroll
                for (int i = 0; i < 8; ++i) t[i] = *(const LDSAS float*)(lds0 + L_TAB + (i * 96 + tid - 256) * 4);
                *(LDSAS float*)(lds0 + L_TAB + 3072 + (tid - 256) * 4) = ((t[0] + t[1]) + (t[2] + t[3])) + ((t[4] + t[5]) + (t[6] + t[7]));
            }
        } else {
            f32x4 p[4];
#pragma unroll
            for (int g = 0; g < 4; ++g) p[g] = lds_f4(lds0 + (w ^ 2) * 4096 + g * 1024 + lane * 16);
            SBAR();
#pragma unroll
            for (int g = 0; g < 4; ++g) {
                hreg[g >> 1][(g & 1) * 2] = pk2(a0[4 * g] + p[g][0], a0[4 * g + 1] + p[g][1]);
                hreg[g >> 1][(g & 1) * 2 + 1] = pk2(a0[4 * g + 2] + p[g][2], a0[4 * g + 3] + p[g][3]);
            }
            const unsigned so = WS_H + ((rt * 2 + wt) * 32 + (ct * 2 + kh) * 2) * 1024;
            ST128(hreg[0], rs, lane * 16, so, AUX_SC1);
            ST128(hreg[1], rs, lane * 16 + 1024, so, AUX_SC1);
            SBAR();
            float sm = 0.f, sq = 0.f;
#pragma unroll
            for (int i = 0; i < 8; ++i) { sm = dot2(hreg[i >> 2][i & 3], ONES2, sm); sq = dot2(hreg[i >> 2][i & 3], hreg[i >> 2][i & 3], sq); }
            sm = swap_add(sm); sq = swap_add(sq);
            if (h == 0) __builtin_amdgcn_raw_buffer_store_b64(u32x2{__float_as_uint(sm), __float_as_uint(sq)}, rs, r32 * 8,
                                                              WS_ST + ((rt * 2 + wt) * 16 + ct * 2 + kh) * 256, AUX_SC1);
        }
        STAMP(2, 3);
        BAR_L();
        if (w >= 4) { issue_dn(0, 15, ((w - 4) >> 1) * 15 * 2048, w - 4); if (bi >= 29) issue_dn(15, 24, L_GU + ((w - 4) >> 1) * 49152, w - 4); }
        STAMP(2, 5);
        FLUSH(2);
    }

    {
        STAMP(3, 0);
        if (bi < 29) {
            const int tt = xcd * 8 + w;
            GU gu;
            gu.a0 = gu.a1 = gu.a2 = f32x16{};
            gu.sa = gu.s0 = RFL(WS_H + tt * 32768); gu.sv = 8 * h * 256 + r32 * 8; gu.ss = RFL(WS_ST + tt * 4096); gu.l16 = l16; gu.budget = budget; gu.rs = rs;
            gu.dm = RFL(lds0 + L_GU + (w >> 2) * 49152 + (w & 3) * 6144); gu.ds = RFL(((43 * (w >> 2) + 15) * 16 + (w & 3) * 48) * 1024 + ct * 128);
            gu.dm2 = RFL(lds0 + L_GU + (w >> 2) * 49152 + 24576 + (w & 3) * 3072); gu.ds2 = RFL(((43 * (w >> 2) + 27) * 16 + (w & 3) * 24) * 1024 + ct * 128);
            gu.dm3 = RFL(lds0 + L_TAB + (w >> 2) * 2048 + (w & 1) * 1024); gu.ds3 = RFL(((43 * (w >> 2) + 39) * 16 + (w & 1) * 8) * 1024 + ct * 128);
            gu.dv = w64_voff(lane); gu.rd = mk_rsrc(wd, F * D * 2);
            gu.wa[0] = gu_wa(0); gu.wa[1] = gu_wa(1); gu.wa[2] = gu_wa(2);
            float csl[3];
#pragma unroll
            for (int n = 0; n < 3; ++n) csl[n] = *(const LDSAS float*)(lds0 + L_TAB + 3072 + (n * 32 + r32) * 4);
            if (w < 4) __builtin_amdgcn_s_sleep(DELAY_H);
            STAMP(3, 2);
            gu_steps<0, 18>(gu);
#pragma unroll 1
            for (;;) {
                unsigned mx = gu.st[0];
#pragma unroll
                for (int i = 1; i < 16; ++i) mx = gu.st[i] > mx ? gu.st[i] : mx;
                if (__builtin_expect(!__any(mx == ~0u) || gu.budget == 0, 1)) break;
                --gu.budget;
                asm volatile(".set tagj, 0\n\t.rept 8\n\tbuffer_load_dwordx2 v[172+2*tagj:173+2*tagj], %[sv], %[rs], %[ss] offen offset:tagj*256 sc1\n\t.set tagj, tagj+1\n\t.endr\n\ts_waitcnt vmcnt(0)"
                             : "+{v[172:187]}"(gu.st), "+{v[140:155]}"(gu.r0), "+{v[156:171]}"(gu.r1), "+{v[112:119]}"(gu.w0), "+{v[124:131]}"(gu.w1), "+{v[132:139]}"(gu.w2),
                               "+{v[64:79]}"(gu.a0), "+{v[80:95]}"(gu.a1), "+{v[96:111]}"(gu.a2) : [sv] "v"(gu.sv), [rs] "s"(rs), [ss] "s"(gu.ss) : "memory");
            }
            float sm = 0.f, sq = 0.f;
#pragma unroll
            for (int i = 0; i < 8; ++i) { sm += __uint_as_float(gu.st[2 * i]); sq += __uint_as_float(gu.st[2 * i + 1]); }
            sm = swap_add(sm); sq = swap_add(sq);
            const float mean = sm * (1.f / D);
            const float var = __builtin_fmaxf(sq * (1.f / D) - mean * mean, 0.f) + LN_EPS;
            float rstd = __builtin_amdgcn_rsqf(var), sd = var * rstd, ga = -LOG2E * rstd;
            u32x4 mf, cf[3];
            {
                const unsigned mh = pk2(-mean, -mean);
                const unsigned ml = pk2(-mean - bflo(mh), 0.f);
                mf = h ? u32x4{0u, 0u, 0u, 0u} : u32x4{(mh & 0xffffu) | (ml << 16), mh & 0xffffu, 0u, 0u};
#pragma unroll
                for (int n = 0; n < 3; ++n) { const unsigned ch = pk2(csl[n], csl[n]); cf[n] = u32x4{ch, pk2(csl[n] - bflo(ch), 0.f), 0u, 0u}; PIN(cf[n]); }
                PIN(mf); PIN(sd); PIN(ga);
            }
            gu_steps<18, 32>(gu);
            asm volatile("v_mfma_f32_32x32x16_bf16 v[64:79], %[c0], %[m], v[64:79]\n\tv_mfma_f32_32x32x16_bf16 v[80:95], %[c1], %[m], v[80:95]\n\t"
                         "v_mfma_f32_32x32x16_bf16 v[96:111], %[c2], %[m], v[96:111]\n\ts_nop 10"
                         : "+{v[64:79]}"(gu.a0), "+{v[80:95]}"(gu.a1), "+{v[96:111]}"(gu.a2) : [c0] "v"(cf[0]), [c1] "v"(cf[1]), [c2] "v"(cf[2]), [m] "v"(mf));
            STAMP(3, 3);
            STAMPV(15, budget - gu.budget); budget = gu.budget;
            auto epi = [&](int n, const f32x16& acc) {
                f32x2 e[4], y[4];
#pragma unroll
                for (int i = 0; i < 4; ++i) e[i] = f32x2{acc[2 * i], acc[2 * i + 1]} * ga;
#pragma unroll
                for (int i = 0; i < 4; ++i) e[i] = f32x2{__builtin_amdgcn_exp2f(e[i][0]), __builtin_amdgcn_exp2f(e[i][1])} * sd + sd;
#pragma unroll
                for (int i = 0; i < 4; ++i) y[i] = f32x2{acc[8 + 2 * i], acc[9 + 2 * i]} * f32x2{__builtin_amdgcn_rcpf(e[i][0]), __builtin_amdgcn_rcpf(e[i][1])};
                const u32x4 ov = {pk2(y[0][0], y[0][1]), pk2(y[1][0], y[1][1]), pk2(y[2][0], y[2][1]), pk2(y[3][0], y[3][1])};
                ST128(ov, rs, (n < 2 || bi < 28) ? l16 : 0x80000000u, WS_HH + (tt * NU + bi * 3 + n) * 1024, AUX_SC1);
            };
            epi(0, gu.a0);
            asm volatile("" : "+{v[80:95]}"(gu.a1));
            epi(1, gu.a1);
            asm volatile("" : "+{v[96:111]}"(gu.a2));
            epi(2, gu.a2);
        }
        STAMP(3, 4);
        if (bi >= 29) WAITV(0);
        BAR_VL(3);
        STAMP(3, 5);
        if (w >= 4) {
            if (bi < 29) issue_dn(33, 6, L_GU + ((w - 4) >> 1) * 49152 + 36864, w - 4); else issue_dn(39, 1, L_TAB + ((w - 4) >> 1) * 2048, w - 4);
            STAMP(3, 6);
        }
        FLUSH(3);
    }

    {
        STAMP(4, 0);
        if (b == 0 && tid == 0) __builtin_amdgcn_raw_buffer_store_b32(ep, rs, 0, WS_EP, AUX_SC1);
        f32x16 a0, a1;
        if (w < 4) {
            const unsigned t0 = lds0 + w64_tr(lane, kh), t1 = lds0 + w64_tr(lane, kh ^ 1);
            G2 g2;
            g2.tb[0] = t0 + kh * 15 * 2048; g2.tb[1] = t1 + kh * 15 * 2048; g2.tb[2] = t0 + L_GU + kh * 24 * 2048; g2.tb[3] = t1 + L_GU + kh * 24 * 2048;
            g2.tb[4] = t0 + L_TAB + kh * 2048; g2.tb[5] = t1 + L_TAB + kh * 2048;
            g2.sa = g2.s0 = RFL(WS_HH + ((rt * 2 + wt) * NU + kh * 43) * 1024); g2.l16 = l16; g2.budget = budget; g2.rs = rs; g2.a1 = f32x16{};
#pragma unroll
            for (int i = 0; i < 8; ++i) { g2.a0[2 * i] = bflo(hreg[i >> 2][i & 3]); g2.a0[2 * i + 1] = bfhi(hreg[i >> 2][i & 3]); }
            __builtin_amdgcn_s_sleep(DELAY_G);
            STAMP(4, 1);
            g2_steps<0, 43, 1, DN_B0, 38>(g2);
            a0 = g2.a0; a1 = g2.a1; STAMPV(15, budget - g2.budget);
        } else {
            BAR_VL(0);
            issue_dn(40, 3, ((w - 4) >> 1) * 15 * 2048, w - 4);
            BAR_VL(0);
        }
        STAMP(4, 2);
        if (w < 4) {
#pragma unroll
            for (int g = 0; g < 4; ++g) *(LDSAS f32x4*)(lds0 + 8192 + w * 4096 + g * 1024 + lane * 16) = f32x4{a1[4 * g], a1[4 * g + 1], a1[4 * g + 2], a1[4 * g + 3]};
        }
        BAR_L();
        if (w < 4) {
            f32x4 p[4];
#pragma unroll
            for (int g = 0; g < 4; ++g) p[g] = lds_f4(lds0 + 8192 + (w ^ 2) * 4096 + g * 1024 + lane * 16);
            SBAR();
            unsigned y[8];
#pragma unroll
            for (int g = 0; g < 4; ++g) {
                y[2 * g] = pk2(a0[4 * g] + p[g][0], a0[4 * g + 1] + p[g][1]);
                y[2 * g + 1] = pk2(a0[4 * g + 2] + p[g][2], a0[4 * g + 3] + p[g][3]);
            }
            const rsrc_t ro = mk_rsrc(out, S * D * 2);
            const unsigned vo = (rt * 64 + wt * 32 + r32) * 1024 + (ct * 64 + kh * 32 + 8 * h) * 2;
#pragma unroll
            for (int g = 0; g < 2; ++g) {
                auto e0 = __builtin_amdgcn_permlane32_swap(y[4 * g], y[4 * g + 2], false, false);
                auto e1 = __builtin_amdgcn_permlane32_swap(y[4 * g + 1], y[4 * g + 3], false, false);
                ST128((u32x4{e0[0], e1[0], e0[1], e1[1]}), ro, vo + g * 32, 0, OUT_AUX);
            }
        }
        STAMP(4, 3);
        WAITV(0);
        STAMP(4, 4);
        FLUSH(4);
        asm volatile(".subsection 2\n.Lmain_end:\n\t.fill 64, 4, 0xBF800000\n\t.subsection 0");
    }
}

extern "C" void kernel_launch(void* const* d_in, const int* in_sizes, int n_in,
                              void* d_out, int out_size, void* d_ws, size_t ws_size,
                              hipStream_t stream) {
    const u16* x = (const u16*)d_in[0];
    const u16* wqkv = (const u16*)d_in[1];
    const u16* wo = (const u16*)d_in[2];
    const u16* wg = (const u16*)d_in[4];
    const u16* wu = (const u16*)d_in[5];
    const u16* wd = (const u16*)d_in[6];
    u16* out = (u16*)d_out;
    char* ws = (char*)d_ws;
    (void)hipFuncSetAttribute((const void*)k_block, hipFuncAttributeMaxDynamicSharedMemorySize, K2_LDS);
    k_block<<<256, 512, K2_LDS, stream>>>(x, wqkv, wo, wg, wu, wd, out, ws);
}
```

```cpp
#define PROBE 2
#include <hip/hip_runtime.h>
#include <hip/hip_bf16.h>
#include <stdint.h>
#pragma clang diagnostic ignored "-Winline-asm"

typedef short s16x8 __attribute__((ext_vector_type(8)));
typedef short s16x4 __attribute__((ext_vector_type(4)));
typedef float f32x16 __attribute__((ext_vector_type(16)));
typedef float f32x4 __attribute__((ext_vector_type(4)));
typedef float f32x2 __attribute__((ext_vector_type(2)));
typedef unsigned u32x4 __attribute__((ext_vector_type(4)));
typedef unsigned u32x2 __attribute__((ext_vector_type(2)));
typedef unsigned u32x8 __attribute__((ext_vector_type(8)));
typedef __bf16 bf16x2 __attribute__((ext_vector_type(2)));
typedef unsigned short u16;
#define LDSAS __attribute__((address_space(3)))
#define GAS __attribute__((address_space(1)))
typedef __amdgpu_buffer_rsrc_t rsrc_t;

constexpr int S = 2048, D = 512, NH = 32, F = 1376, NU = F / 16;
constexpr float LN_EPS = 1e-5f;
constexpr float LOG2E = 1.4426950408889634f;

constexpr unsigned KV_CHUNK = 16384;
constexpr unsigned KV_HEAD = 8 * KV_CHUNK;
constexpr unsigned WS_EP = 0;
constexpr unsigned WS_FQ = 4096;
constexpr unsigned WS_FP = WS_FQ + 16 * 64 * 128;
constexpr unsigned WS_Q = WS_FP + 4096;
constexpr unsigned WS_KV = WS_Q + NH * 64 * 1024;
constexpr unsigned WS_O = WS_KV + NH * KV_HEAD;
constexpr unsigned WS_H = WS_O + 64 * 32 * 1024;
constexpr unsigned WS_ST = WS_H + 64 * 32 * 1024;
constexpr unsigned WS_HH = WS_ST + 64 * 16 * 256;
constexpr unsigned WS_END = WS_HH + 64 * NU * 1024;
constexpr int AUX_SC1 = 16;
#define PROBE_BLOCK 0
#define STAMP_DECL unsigned tp0_ = 0, tp_ = 0
#define STAMP(k, i) do { if ((k) * 16 + (i) == 0) tp0_ = (unsigned)__builtin_amdgcn_s_memrealtime(); if ((k) * 16 + (i) == PROBE) { __builtin_amdgcn_sched_barrier(0); \
    tp_ = (unsigned)__builtin_amdgcn_s_memrealtime(); asm volatile("" : "+s"(tp_)); __builtin_amdgcn_sched_barrier(0); } } while (0)
#define FLUSH(k) do { if ((k) == 4 && blockIdx.x == PROBE_BLOCK && w == 0) { unsigned lim_ = tp_ - tp0_; if (lim_ > 20000) lim_ = 20000; \
    while ((unsigned)__builtin_amdgcn_s_memrealtime() - tp0_ < 6000 + lim_) __builtin_amdgcn_s_sleep(2); } } while (0)

#define STAMPV(i, v) do {} while (0)
#define SBAR() __builtin_amdgcn_sched_barrier(0)
#define RFL(x) ((unsigned)__builtin_amdgcn_readfirstlane(x))
#define PIN(x) asm volatile("" : "+v"(x))
#define MFMA(a, b, c) __builtin_amdgcn_mfma_f32_32x32x16_bf16((a), (b), (c), 0, 0, 0)
#define WAITV(n) asm volatile("s_waitcnt vmcnt(" #n ")" ::: "memory")
#define BAR_L() asm volatile("s_waitcnt lgkmcnt(0)\n\ts_barrier" ::: "memory")
#define BAR_0() asm volatile("s_barrier" ::: "memory")
#define BAR_VL(n) asm volatile("s_waitcnt vmcnt(" #n ") lgkmcnt(0)\n\ts_barrier" ::: "memory")

__device__ __forceinline__ rsrc_t mk_rsrc(const void* p, unsigned bytes) {
    return __builtin_amdgcn_make_buffer_rsrc((void*)p, (short)0, (int)bytes, 0x00020000);
}
template <bool SC1 = false> __device__ __forceinline__ void dma16(rsrc_t r, unsigned voff, unsigned soff, unsigned lds) {
    if constexpr (SC1) asm volatile("s_nop 4\n\ts_mov_b32 m0, %0\n\ts_nop 0\n\tbuffer_load_dwordx4 %1, %2, %3 offen sc1 lds"
                 :: "s"(__builtin_amdgcn_readfirstlane(lds)), "v"(voff), "s"(r), "s"(__builtin_amdgcn_readfirstlane(soff)) : "m0", "memory");
    else asm volatile("s_nop 4\n\ts_mov_b32 m0, %0\n\ts_nop 0\n\tbuffer_load_dwordx4 %1, %2, %3 offen lds"
                 :: "s"(__builtin_amdgcn_readfirstlane(lds)), "v"(voff), "s"(r), "s"(__builtin_amdgcn_readfirstlane(soff)) : "m0", "memory");
}
#define ST128(v, r, voff, soff, aux) do { const u32x4 d_ = (v); __builtin_amdgcn_raw_buffer_store_b128(d_, r, voff, soff, aux); asm volatile("s_nop 1" :: "v"(d_)); } while (0)
__device__ __forceinline__ unsigned pk2(float lo, float hi) {
    f32x2 v = {lo, hi};
    return __builtin_bit_cast(unsigned, __builtin_convertvector(v, bf16x2));
}
__device__ __forceinline__ float dot2(unsigned a, unsigned b, float c) {
    return __builtin_amdgcn_fdot2_f32_bf16(__builtin_bit_cast(bf16x2, a), __builtin_bit_cast(bf16x2, b), c, false);
}
__device__ __forceinline__ float swap_add(float v) {
    auto r = __builtin_amdgcn_permlane32_swap(__float_as_uint(v), __float_as_uint(v), false, false);
    return __uint_as_float(r[0]) + __uint_as_float(r[1]);
}
__device__ __forceinline__ float swap_max(float v) {
    auto r = __builtin_amdgcn_permlane32_swap(__float_as_uint(v), __float_as_uint(v), false, false);
    return __builtin_fmaxf(__uint_as_float(r[0]), __uint_as_float(r[1]));
}
__device__ __forceinline__ s16x8 lds_b128(unsigned a) { return *(const LDSAS s16x8*)(a); }
__device__ __forceinline__ f32x4 lds_f4(unsigned a) { return *(const LDSAS f32x4*)(a); }
__device__ __forceinline__ s16x8 lds_tr2(unsigned a0, unsigned a1) {
    s16x4 lo = __builtin_amdgcn_ds_read_tr16_b64_v4i16((LDSAS s16x4*)(a0));
    s16x4 hi = __builtin_amdgcn_ds_read_tr16_b64_v4i16((LDSAS s16x4*)(a1));
    return __builtin_shufflevector(lo, hi, 0, 1, 2, 3, 4, 5, 6, 7);
}
__device__ __forceinline__ float bflo(unsigned v) { return __uint_as_float(v << 16); }
__device__ __forceinline__ float bfhi(unsigned v) { return __uint_as_float(v & 0xffff0000u); }
constexpr unsigned ONES2 = 0x3f803f80u;
template <int I> struct ic { static constexpr int value = I; constexpr operator int() const { return I; } };
template <int B, int E, class Fn> __device__ __forceinline__ void sfor(Fn&& fn) { if constexpr (B < E) { fn(ic<B>{}); sfor<B + 1, E>(fn); } }

#define K1_X_AUX 2
#define K1_ABL 0
#define CODE_PF 1
#define K1_POST_AT 83
#define DELAY_Q 0
constexpr unsigned K1_W = 65536, K1_FLG = K1_W + 5 * 16384, K1_ST = K1_FLG + 512, K1_CS = K1_ST + 512;
__host__ __device__ constexpr unsigned k1_slot(int s) { return s < 5 ? K1_W + s * 16384 : s == 5 ? K1_W : s < 10 ? (s - 6) * 16384 : K1_W + (s - 9) * 16384; }
__device__ __forceinline__ void lds_spin_ge(unsigned addr, unsigned want) {
#pragma unroll 1
    for (unsigned n = 0; n < (1u << 22); ++n) {
        unsigned v; asm volatile("ds_read_b32 %0, %1\n\ts_waitcnt lgkmcnt(0)" : "=v"(v) : "v"(addr) : "memory");
        if ((unsigned)__builtin_amdgcn_readfirstlane(v) >= want) break;
        __builtin_amdgcn_s_sleep(1);
    }
}
__device__ __forceinline__ void lds_post(unsigned addr, unsigned v) {
    asm volatile("s_waitcnt lgkmcnt(0)\n\tds_write_b32 %0, %1" :: "v"(addr), "v"(v) : "memory");
}
__device__ __forceinline__ void lds_inc(unsigned addr, int lane) {
    asm volatile("s_waitcnt lgkmcnt(0)" ::: "memory");
    if (lane == 0) asm volatile("ds_add_u32 %0, %1" :: "v"(addr), "v"(1u) : "memory");
}
typedef unsigned u32x16 __attribute__((ext_vector_type(16)));
template <int CTRL> __device__ __forceinline__ float dpp_add(float v) {
    return v + __builtin_bit_cast(float, __builtin_amdgcn_update_dpp(0, __builtin_bit_cast(int, v), CTRL, 0xf, 0xf, true));
}

constexpr unsigned K2_LDS = 163840;
constexpr unsigned L_WO = 0, L_GU = 65536;
constexpr unsigned L_TAB = 61440;
constexpr unsigned L_FLAG = 16384 + 12288;
constexpr unsigned SPIN_CAP = 1u << 20;
#define POLL_DELAY_Q 6
#define OUT_AUX 2
#define DN_B0 20
#define ABL_GU 0
#define DELAY_O 0
#define DELAY_H 8
#define DELAY_G 8
constexpr int GU_AW = 8, G2_AW = 12;
#define TAG_T1 "v_max3_u32 v120, v[%c[cr]], v[%c[cr]+1], v[%c[cr]+2]\n\t"
#define TAG_T2 "v_max_u32 v120, v120, v[%c[cr]+3]\n\tv_cmp_eq_u32 vcc, -1, v120\n\t"
#define TAG_RELOAD(RB, AW) "s_add_u32 %[t], %[s0], (tagj>>2)*4096\n\tbuffer_load_dwordx4 v[" #RB "+4*(tagj%%" #AW "):" #RB "+3+4*(tagj%%" #AW ")], %[l16], %[rs], %[t] offen offset:(tagj&3)*1024 sc1\n\t"
#define TAG_T3(RB, AW) \
    "s_cbranch_vccnz 5f\n6:\n\t" \
    ".subsection 1\n5:\n\ts_cmp_eq_u32 %[bud], 0\n\ts_cbranch_scc1 6b\n\ts_sub_u32 %[bud], %[bud], 1\n\t.set tagj, %c[ci]\n\t" TAG_RELOAD(RB, AW) \
    "s_waitcnt vmcnt(0)\n\t" TAG_T1 TAG_T2 "s_cbranch_vccnz 5b\n\t.rept %c[cnt]-1\n\t.set tagj, tagj+1\n\t" TAG_RELOAD(RB, AW) ".endr\n\ts_branch 6b\n\t.subsection 0\n\t"
#define TAG_LOAD "buffer_load_dwordx4 v[%c[lr]:%c[lr]+3], %[l16], %[rs], %[sa] offen offset:%c[lo] sc1\n\t.if %c[lo] == 3072\n\ts_add_u32 %[sa], %[sa], 0x1000\n\t.endif\n\t"

__device__ __forceinline__ unsigned w64_voff(int lane) { return (lane >> 3) * 1024 + (((lane & 7) ^ (((lane >> 4) & 1) << 2)) << 4); }
__device__ __forceinline__ unsigned w64_tr(int lane, int n) {
    const int q = (lane & 15) >> 2, p = lane & 3, blk = (lane >> 4) & 1, h = lane >> 5;
    return (4 * h + q) * 128 + (((4 * n + 2 * blk + (p >> 1)) ^ (((q >> 1) & 1) << 2)) << 4) + 8 * (p & 1);
}

struct G2 { u32x16 r0, r1, r2; u32x8 w0, w1, w2; f32x16 a0, a1; unsigned tb[6], sa, s0, l16, budget; rsrc_t rs;
};
__host__ __device__ constexpr int g2_w(int DN, int n) {
    return !DN ? n * 2048 : n < 15 ? n * 2048 : n < 39 ? 65536 + (n - 15) * 2048 : n < 40 ? 131072 : (n - 40) * 2048;
}
template <int i, int N, int DN, int B0, int B1> __device__ __forceinline__ void g2_steps(G2& g) {
    constexpr int AW = G2_AW, j = i + AW - 1, n = i + 2, sel = n < N ? g2_w(DN, n) >> 16 : 3;
    unsigned tq;
#define G2_RD(P, R) "ds_read_b64_tr_b16 v[%c[nr]+" #R ":%c[nr]+" #R "+1], %[" P "] offset:%c[no]\n\tds_read_b64_tr_b16 v[%c[nr]+" #R "+2:%c[nr]+" #R "+3], %[" P "] offset:%c[no]+1024\n\t"
#define G2_RDS(P0, P1, P2, R) ".if %c[sel] == 0\n\t" G2_RD(P0, R) ".endif\n\t.if %c[sel] == 1\n\t" G2_RD(P1, R) ".endif\n\t.if %c[sel] == 2\n\t" G2_RD(P2, R) ".endif\n\t"
    if constexpr (i == 0)
        asm volatile(".set tagj, 0\n\t.rept 11\n\tbuffer_load_dwordx4 v[128+4*tagj:131+4*tagj], %[l16], %[rs], %[sa] offen offset:(tagj&3)*1024 sc1\n\t"
                     ".if (tagj&3) == 3\n\ts_add_u32 %[sa], %[sa], 0x1000\n\t.endif\n\t.set tagj, tagj+1\n\t.endr\n\t"
                     ".set tagj, 0\n\t.rept 2\n\tds_read_b64_tr_b16 v[96+8*tagj:97+8*tagj], %[b0] offset:tagj*2048\n\tds_read_b64_tr_b16 v[98+8*tagj:99+8*tagj], %[b0] offset:tagj*2048+1024\n\t"
                     "ds_read_b64_tr_b16 v[100+8*tagj:101+8*tagj], %[b1] offset:tagj*2048\n\tds_read_b64_tr_b16 v[102+8*tagj:103+8*tagj], %[b1] offset:tagj*2048+1024\n\t.set tagj, tagj+1\n\t.endr\n\t"
                     "s_waitcnt vmcnt(10) lgkmcnt(4)\n\t" TAG_T1 TAG_T2 TAG_T3(128, 12)
                     : "={v[128:143]}"(g.r0), "={v[144:159]}"(g.r1), "={v[160:175]}"(g.r2), "={v[96:103]}"(g.w0), "={v[104:111]}"(g.w1), "={v[112:119]}"(g.w2), [sa] "+s"(g.sa),
                       "+{v[64:79]}"(g.a0), "+{v[80:95]}"(g.a1), [bud] "+s"(g.budget), [t] "=&s"(tq)
                     : [l16] "v"(g.l16), [rs] "s"(g.rs), [s0] "s"(g.s0), [b0] "v"(g.tb[0]), [b1] "v"(g.tb[1]), [cr] "n"(128), [ci] "n"(0), [cnt] "n"(AW - 1) : "vcc", "scc", "v120", "memory");
    asm volatile("v_mfma_f32_32x32x16_bf16 v[64:79], v[%c[wr]:%c[wr]+3], v[%c[ar]:%c[ar]+3], v[64:79]\n\t"
                 ".if %c[ld]\n\t" TAG_LOAD ".endif\n\t"
                 ".if %c[bar]\n\ts_waitcnt lgkmcnt(0)\n\ts_barrier\n\t.endif\n\t"
                 G2_RDS("b0", "b2", "b4", 0)
                 ".if %c[cnt]\n\ts_waitcnt vmcnt(%c[vn])\n\t" TAG_T1 ".endif\n\t"
                 "v_mfma_f32_32x32x16_bf16 v[80:95], v[%c[wr]+4:%c[wr]+7], v[%c[ar]:%c[ar]+3], v[80:95]\n\t"
                 G2_RDS("b1", "b3", "b5", 4)
                 ".if %c[cnt]\n\t" TAG_T2 "s_waitcnt lgkmcnt(%c[ln])\n\t" TAG_T3(128, 12) ".else\n\ts_nop 15\n\t.endif"
                 : "+{v[64:79]}"(g.a0), "+{v[80:95]}"(g.a1), "+{v[128:143]}"(g.r0), "+{v[144:159]}"(g.r1), "+{v[160:175]}"(g.r2), "+{v[96:103]}"(g.w0), "+{v[104:111]}"(g.w1), "+{v[112:119]}"(g.w2),
                   [sa] "+s"(g.sa), [bud] "+s"(g.budget), [t] "=&s"(tq)
                 : [l16] "v"(g.l16), [rs] "s"(g.rs), [s0] "s"(g.s0), [b0] "v"(g.tb[0]), [b1] "v"(g.tb[1]), [b2] "v"(g.tb[2]), [b3] "v"(g.tb[3]), [b4] "v"(g.tb[4]), [b5] "v"(g.tb[5]),
                   [ld] "n"(j < N ? 1 : 0), [lr] "n"(128 + 4 * (j % AW)), [lo] "n"((j & 3) * 1024), [bar] "n"(i == B0 || i == B1 ? 1 : 0), [sel] "n"(sel),
                   [nr] "n"(96 + 8 * (n % 3)), [no] "n"(n < N ? g2_w(DN, n) & 65535 : 0), [vn] "n"(j < N ? AW - 2 : i + 1 < N ? N - 2 - i : 0), [ln] "n"(n < N ? 4 : 0),
                   [ar] "n"(128 + 4 * (i % AW)), [wr] "n"(96 + 8 * (i % 3)), [cr] "n"(128 + 4 * ((i + 1) % AW)), [ci] "n"(i + 1), [cnt] "n"(j < N ? AW - 1 : N - 1 - i) : "vcc", "scc", "v120", "memory");
    if constexpr (i + 1 < N) g2_steps<i + 1, N, DN, B0, B1>(g);
}
__host__ __device__ constexpr int gu_dma(int s) { return (s >= 16 && s < 24 && !(s & 1)) || (s >= 24 && s < 30); }
__host__ __device__ constexpr int gu_ops(int s) { return (s + GU_AW - 1 < 32 ? 1 : 0) + (s >= 2 && s < 10 ? 1 : 0) + gu_dma(s); }
__host__ __device__ constexpr int gu_younger(int k) {
    int c = 0, s = k - GU_AW + 1;
    if (s < 0) { c = GU_AW - 2 - k; s = 0; } else { c = gu_ops(s) - 1; ++s; }
    for (; s < k; ++s) c += gu_ops(s);
    return c;
}

struct GU { u32x16 r0, r1, st; u32x8 w0, w1, w2; f32x16 a0, a1, a2; unsigned wa[3], sa, s0, sv, ss, l16, budget, dm, ds, dv, dm2, ds2, dm3, ds3; rsrc_t rs, rd; };
template <int i, int E> __device__ __forceinline__ void gu_steps(GU& g) {
    constexpr int AW = GU_AW, j = i + AW - 1, n = i + 1;
    unsigned tq;
#define GU_RD(R, B) "ds_read_b64_tr_b16 v[" R ":" R "+1], %[" B "] offset:%c[no]\n\tds_read_b64_tr_b16 v[" R "+2:" R "+3], %[" B "] offset:%c[no]+768\n\t"
    if constexpr (i == 0)
        asm volatile(".set tagj, 0\n\t.rept 7\n\tbuffer_load_dwordx4 v[140+4*tagj:143+4*tagj], %[l16], %[rs], %[sa] offen offset:(tagj&3)*1024 sc1\n\t"
                     ".if (tagj&3) == 3\n\ts_add_u32 %[sa], %[sa], 0x1000\n\t.endif\n\t.set tagj, tagj+1\n\t.endr\n\t" GU_RD("112", "b0") GU_RD("116", "b1") GU_RD("124", "b2")
                     "s_waitcnt vmcnt(6) lgkmcnt(0)\n\t" TAG_T1 TAG_T2 TAG_T3(140, 8)
                     : "={v[140:155]}"(g.r0), "={v[156:171]}"(g.r1), "={v[172:187]}"(g.st), "={v[112:119]}"(g.w0), "={v[124:131]}"(g.w1), "={v[132:139]}"(g.w2), [sa] "+s"(g.sa),
                       "+{v[64:79]}"(g.a0), "+{v[80:95]}"(g.a1), "+{v[96:111]}"(g.a2), [bud] "+s"(g.budget), [t] "=&s"(tq)
                     : [l16] "v"(g.l16), [rs] "s"(g.rs), [s0] "s"(g.s0), [b0] "v"(g.wa[0]), [b1] "v"(g.wa[1]), [b2] "v"(g.wa[2]), [no] "n"(0), [cr] "n"(140), [ci] "n"(0), [cnt] "n"(AW - 1)
                     : "vcc", "scc", "v120", "memory");
    asm volatile(".if %c[i] == 15 || %c[i] == 23\n\ts_barrier\n\t.endif\n\t"
                 "v_mfma_f32_32x32x16_bf16 v[64:79], v[%c[c0]:%c[c0]+3], v[%c[ar]:%c[ar]+3], v[64:79]\n\t"
                 ".if %c[rd]\n\t" GU_RD("%c[n0]", "b0") GU_RD("%c[n1]", "b1") ".endif\n\t"
                 "v_mfma_f32_32x32x16_bf16 v[80:95], v[%c[c1]:%c[c1]+3], v[%c[ar]:%c[ar]+3], v[80:95]\n\t"
                 ".if %c[rd]\n\t" GU_RD("%c[n2]", "b2") ".endif\n\t"
                 ".if %c[ld]\n\t" TAG_LOAD ".endif\n\t"
                 ".if %c[sk] >= 0\n\tbuffer_load_dwordx2 v[172+2*%c[sk]:173+2*%c[sk]], %[sv], %[rs], %[ss] offen offset:%c[sk]*256 sc1\n\t.endif\n\t"
                 ".if %c[i] == 26\n\ts_mov_b32 %[dm], %[dm2]\n\ts_mov_b32 %[ds], %[ds2]\n\t.endif\n\t.if %c[i] == 29\n\ts_mov_b32 %[dm], %[dm3]\n\ts_mov_b32 %[ds], %[ds3]\n\t.endif\n\t"
                 ".if %c[dma]\n\ts_mov_b32 m0, %[dm]\n\ts_add_u32 %[dm], %[dm], 0x400\n\tbuffer_load_dwordx4 %[dv], %[rwd], %[ds] offen lds\n\ts_add_u32 %[ds], %[ds], 0x2000\n\t.endif\n\t"
                 "v_mfma_f32_32x32x16_bf16 v[96:111], v[%c[c2]:%c[c2]+3], v[%c[ar]:%c[ar]+3], v[96:111]\n\t"
                 ".if %c[cnt]\n\ts_waitcnt vmcnt(%c[vn])\n\t" TAG_T1 TAG_T2 "s_waitcnt lgkmcnt(0)\n\t" TAG_T3(140, 8) ".endif"
                 : "+{v[64:79]}"(g.a0), "+{v[80:95]}"(g.a1), "+{v[96:111]}"(g.a2), "+{v[140:155]}"(g.r0), "+{v[156:171]}"(g.r1), "+{v[172:187]}"(g.st),
                   "+{v[112:119]}"(g.w0), "+{v[124:131]}"(g.w1), "+{v[132:139]}"(g.w2), [sa] "+s"(g.sa), [bud] "+s"(g.budget), [t] "=&s"(tq), [dm] "+s"(g.dm), [ds] "+s"(g.ds)
                 : [l16] "v"(g.l16), [rs] "s"(g.rs), [s0] "s"(g.s0), [dv] "v"(g.dv), [rwd] "s"(g.rd), [dm2] "s"(g.dm2), [ds2] "s"(g.ds2), [dm3] "s"(g.dm3), [ds3] "s"(g.ds3),
                   [dma] "n"(gu_dma(i)), [sv] "v"(g.sv), [ss] "s"(g.ss), [b0] "v"(g.wa[0]), [b1] "v"(g.wa[1]), [b2] "v"(g.wa[2]),
                   [ld] "n"(j < 32 ? 1 : 0), [lr] "n"(140 + 4 * (j % AW)), [lo] "n"((j & 3) * 1024), [sk] "n"(i >= 2 && i < 10 ? i - 2 : -1), [rd] "n"(n < 32 ? 1 : 0), [no] "n"(n * 1536),
                   [n0] "n"(n & 1 ? 128 : 112), [n1] "n"(n & 1 ? 132 : 116), [n2] "n"(n & 1 ? 136 : 124), [c0] "n"(i & 1 ? 128 : 112), [c1] "n"(i & 1 ? 132 : 116), [c2] "n"(i & 1 ? 136 : 124),
                   [vn] "n"(n < 32 ? gu_younger(n) : 0), [ar] "n"(140 + 4 * (i % AW)), [i] "n"(i), [cr] "n"(140 + 4 * (n % AW)), [ci] "n"(n), [cnt] "n"(j < 32 ? AW - 1 : 31 - i)
                 : "vcc", "scc", "m0", "v120", "memory");
    if constexpr (i + 1 < E) gu_steps<i + 1, E>(g);
}

__global__ __launch_bounds__(512, 2) void k_block(const u16* __restrict__ x, const u16* __restrict__ wqkv, const u16* wo, const u16* wg,
                                                  const u16* wu, const u16* wd, u16* out, char* __restrict__ ws) {
    extern __shared__ __attribute__((aligned(16))) char smem[];
    const unsigned lds0 = (unsigned)(uintptr_t)(LDSAS char*)smem;
    const int tid = threadIdx.x, lane = tid & 63;
    const int w = __builtin_amdgcn_readfirstlane(tid >> 6);
    const int b = blockIdx.x, xcd = b & 7, bi = b >> 3;
    const int r32 = lane & 31, h = lane >> 5;
    const rsrc_t rs = mk_rsrc(ws, WS_END);
    const int wt = w & 1, kh = (w >> 1) & 1;
    STAMP_DECL;
    unsigned ep;
#define EP_LOAD() do { asm volatile("s_load_dword %0, %1, 0\n\ts_waitcnt lgkmcnt(0)" : "=s"(ep) : "s"(ws) : "memory"); ep += 1u; } while (0)
    asm volatile("" : "+s"(wo), "+s"(wg), "+s"(wu), "+s"(wd), "+s"(out));
    const int head = xcd * 4 + (bi >> 3);
    const unsigned l16 = lane * 16;
    const int rt2 = xcd * 4 + (bi >> 3), ct = bi & 7;
    {
    const int rt = (xcd >> 1) * 8 + (bi & 7), cb = (xcd & 1) * 4 + (bi >> 3), rot = bi & 3;
    const int wc = (w >> 1) & 1, hw = w - 4;
    STAMP(0, 0);
    const rsrc_t rw = mk_rsrc(wqkv, D * 3 * D * 2);
    const unsigned wv = (lane >> 3) * 3072 + (((lane & 7) ^ (((lane >> 4) & 1) << 2)) << 4);
    const unsigned fa = lds0 + K1_FLG;
    if (tid < 12) *(LDSAS unsigned*)(fa + tid * 4) = 0u;
    if (w >= 4) {
        BAR_L();
        const unsigned pa = fa + hw * 4;
        auto issue = [&](int s) {
            const unsigned so = RFL(((((s & 3) + rot) & 3) * 128 + hw * 32) * 3072 + (((s >> 2) + 1) % 3) * 1024 + cb * 128);
            asm volatile("s_nop 4" :: "s"(so));
#pragma unroll
            for (int i = 0; i < 4; ++i)
                asm volatile("s_mov_b32 m0, %0\n\ts_nop 0\n\tbuffer_load_dwordx4 %1, %2, %3 offen lds"
                             :: "s"(lds0 + k1_slot(s) + (4 * hw + i) * 1024), "v"(wv), "s"(rw), "s"(so + i * 8 * 3072) : "m0", "memory");
        };
#define K1_LAND(N, s) asm volatile("s_waitcnt vmcnt(" #N ")\n\tds_write_b32 %0, %1" :: "v"(pa), "v"((s) + 1u) : "memory")
        issue(0); issue(1);
        issue(2); K1_LAND(8, 0);
        issue(3); K1_LAND(8, 1);
        issue(4); K1_LAND(8, 2); K1_LAND(4, 3); K1_LAND(0, 4);
        EP_LOAD();
        lds_spin_ge(fa + 28, 4);
        issue(5);
        lds_spin_ge(fa + 20, 4);
        issue(6); issue(7); K1_LAND(8, 5);
        issue(8); K1_LAND(8, 6);
        issue(9); K1_LAND(8, 7);
        lds_spin_ge(fa + 24, 4);
        issue(10); K1_LAND(8, 8);
        issue(11); K1_LAND(8, 9);
        K1_LAND(4, 10); K1_LAND(0, 11);
        if (w > 4) {
            unsigned long long pc; unsigned len, o = bi * 128 + (w - 5) * 24576, t, d0, d1, d2, d3;
#define KCP(D) "s_min_u32 %[t], %[o], %[len]\n\ts_load_dword " D ", %[pc], %[t]\n\ts_add_u32 %[o], %[o], 0x1000\n\t"
            asm volatile("s_getpc_b64 %[pc]\n.Lqkv_pc:\n\ts_mov_b32 %[len], .Lmain_end-.Lqkv_pc-4\n\t" KCP("%[d0]") KCP("%[d1]") KCP("%[d2]") KCP("%[d3]") KCP("%[d0]") KCP("%[d1]") "s_waitcnt lgkmcnt(0)"
                         : [pc] "=&s"(pc), [len] "=&s"(len), [o] "+s"(o), [t] "=&s"(t), [d0] "=&s"(d0), [d1] "=&s"(d1), [d2] "=&s"(d2), [d3] "=&s"(d3) :: "scc", "memory");
        }
        STAMP(0, 1);
    } else {
    {
        const rsrc_t rx = mk_rsrc(x, S * D * 2);
        u32x4 r[2][8];
#pragma unroll
        for (int p = 0; p < 2; ++p)
#pragma unroll
            for (int g = 0; g < 8; ++g)
                r[p][g] = __builtin_amdgcn_raw_buffer_load_b128(rx, (lane >> 3) * 1024 + (((lane & 7) ^ ((4 * p + (lane >> 4)) & 7)) << 4) + g * 128, (rt * 64 + w * 16 + p * 8) * 1024, K1_X_AUX);
        SBAR(); BAR_L();
        EP_LOAD();
        SBAR();
#pragma unroll
        for (int p = 0; p < 2; ++p) {
            float s0 = 0.f, s1 = 0.f, q0 = 0.f, q1 = 0.f;
#pragma unroll
            for (int g = 0; g < 8; ++g) { s0 = dot2(r[p][g][0], ONES2, s0); q0 = dot2(r[p][g][0], r[p][g][0], q0); s1 = dot2(r[p][g][1], ONES2, s1); q1 = dot2(r[p][g][1], r[p][g][1], q1);
                                          s0 = dot2(r[p][g][2], ONES2, s0); q0 = dot2(r[p][g][2], r[p][g][2], q0); s1 = dot2(r[p][g][3], ONES2, s1); q1 = dot2(r[p][g][3], r[p][g][3], q1); }
            if (p == 0) STAMP(0, 1);
            float sum = s0 + s1, sq = q0 + q1;
            sum = dpp_add<0xB1>(sum); sq = dpp_add<0xB1>(sq); sum = dpp_add<0x4E>(sum); sq = dpp_add<0x4E>(sq); sum = dpp_add<0x141>(sum); sq = dpp_add<0x141>(sq);
            const float mean = sum * (1.f / D);
            const float rstd = __builtin_amdgcn_rsqf(__builtin_fmaxf(sq * (1.f / D) - mean * mean, 0.f) + LN_EPS), nb = -mean * rstd;
#pragma unroll
            for (int g = 0; g < 8; g += 2) {
                f32x2 t[8];
#pragma unroll
                for (int i = 0; i < 8; ++i) { const unsigned v = r[p][g + (i >> 2)][i & 3]; t[i] = f32x2{bflo(v), bfhi(v)}; }
                asm volatile("" : "+v"(t[0]), "+v"(t[1]), "+v"(t[2]), "+v"(t[3]), "+v"(t[4]), "+v"(t[5]), "+v"(t[6]), "+v"(t[7]));
#pragma unroll
                for (int i = 0; i < 8; ++i) t[i] = t[i] * rstd + nb;
                asm volatile("" : "+v"(t[0]), "+v"(t[1]), "+v"(t[2]), "+v"(t[3]), "+v"(t[4]), "+v"(t[5]), "+v"(t[6]), "+v"(t[7]));
                u32x4 y0, y1;
#pragma unroll
                for (int i = 0; i < 4; ++i) { y0[i] = pk2(t[i][0], t[i][1]); y1[i] = pk2(t[4 + i][0], t[4 + i][1]); }
                *(LDSAS u32x4*)(lds0 + (2 * w + p) * 1024 + lane * 16 + g * 8192) = y0;
                *(LDSAS u32x4*)(lds0 + (2 * w + p) * 1024 + lane * 16 + g * 8192 + 8192) = y1;
            }
            SBAR();
        }
    }
    lds_inc(fa + 16, lane);
    STAMP(0, 3);
    u32x16 xa0, xa1, xa2, xa3, xa4, xa5, xa6, xa7;
    u32x4 w0, w1, w2, w3, w4, fl;
    f32x16 acA, acB;
    unsigned xb[4], tq;
#pragma unroll
    for (int m = 0; m < 4; ++m) xb[m] = lds0 + (32 * wt + r32) * 128 + (((2 * m + h) ^ ((r32 >> 1) & 7)) << 4) + rot * 16384;
    const int q4 = (lane & 15) >> 2, p4 = lane & 3, blk = (lane >> 4) & 1;
    const unsigned tA = lds0 + (8 * h + q4) * 128 + (((4 * wc + 2 * blk + (p4 >> 1)) ^ (((q4 >> 1) & 1) << 2)) << 4) + 8 * (p4 & 1);
    const unsigned tB = tA + 65536, tC = tA + 131072;
    const int tt = rt * 2 + wt, hd = cb * 4 + wc * 2;
#define KXR(R, m, O) "ds_read_b128 v[" #R ":" #R "+3], %[x" #m "] offset:" #O "\n\t"
#define KXN(m) "v_add_u32 %[x" #m "], 0x4000, %[x" #m "]\n\tv_and_b32 %[x" #m "], 0xffff, %[x" #m "]\n\t"
#define KCHK(ST) "v_min3_u32 v116, v116, v117, v118\n\tv_min_u32 v116, v116, v119\n\ts_nop 0\n\tv_readfirstlane_b32 %[t], v116\n\ts_nop 0\n\ts_cmp_gt_u32 %[t], " ST "\n\t"
#define KSPIN(ST) "1:\n\tds_read_b128 v[116:119], %[fa]\n\ts_waitcnt lgkmcnt(0)\n\t" KCHK(ST) "s_cbranch_scc0 1b\n\t"
#define KWR(R, O) "ds_read_b64_tr_b16 v[" #R ":" #R "+1], %[tb] offset:" #O "\n\tds_read_b64_tr_b16 v[" #R "+2:" #R "+3], %[tb] offset:" #O "+512\n\t"
    asm volatile("3:\n\tds_read_b32 v116, %[fa] offset:16\n\ts_waitcnt lgkmcnt(0)\n\tv_readfirstlane_b32 %[t], v116\n\ts_nop 0\n\ts_cmp_gt_u32 %[t], 3\n\ts_cbranch_scc0 3b\n\t"
                 KXR(128, 0, 0) KXR(132, 1, 0) KXR(136, 2, 0) KXR(140, 3, 0) KXR(144, 0, 8192) KXR(148, 1, 8192) KXR(152, 2, 8192) KXR(156, 3, 8192) KXN(0) KXN(1) KXN(2) KXN(3)
                 KSPIN("0") KWR(96, 0) KWR(100, 2048) KWR(104, 4096) KWR(108, 6144)
                 : "={v[116:119]}"(fl), [t] "=&s"(tq), "={v[96:99]}"(w0), "={v[100:103]}"(w1), "={v[104:107]}"(w2), "={v[108:111]}"(w3), "={v[112:115]}"(w4), "={v[64:79]}"(acA), "={v[80:95]}"(acB),
                   "={v[128:143]}"(xa0), "={v[144:159]}"(xa1), "={v[160:175]}"(xa2), "={v[176:191]}"(xa3), "={v[192:207]}"(xa4), "={v[208:223]}"(xa5), "={v[224:239]}"(xa6), "={v[240:255]}"(xa7),
                   [x0] "+v"(xb[0]), [x1] "+v"(xb[1]), [x2] "+v"(xb[2]), [x3] "+v"(xb[3])
                 : [fa] "v"(fa), [tb] "v"(tB) : "scc", "memory");
    STAMP(0, 4);
    auto epi = [&](int m, const f32x16& acc, int hh) {
        const float sc = 0.25f * LOG2E;
        u32x4 o;
#pragma unroll
        for (int i = 0; i < 4; ++i) o[i] = m == 2 ? pk2(acc[8 * hh + 2 * i] * sc, acc[8 * hh + 2 * i + 1] * sc) : pk2(acc[8 * hh + 2 * i], acc[8 * hh + 2 * i + 1]);
        if (m == 2) ST128(o, rs, l16, WS_Q + ((hd + hh) * 64 + tt) * 1024, AUX_SC1);
        else if (m == 0) ST128(o, rs, l16, WS_KV + (hd + hh) * KV_HEAD + (tt >> 3) * KV_CHUNK + (tt & 7) * 1024, AUX_SC1);
        else ST128(o, rs, (r32 >> 4) * KV_HEAD + (16 * h + (lane & 15)) * 16 + hh * 512,
                   WS_KV + hd * KV_HEAD + (tt >> 3) * KV_CHUNK + 8192 + (tt & 7) * 1024, AUX_SC1);
    };
    sfor<0, 96>([&](auto G) {
        constexpr int g = G.value, n = g + 4, m = g >> 5;
        constexpr unsigned o = k1_slot(n >> 3) + (n & 7) * 2048, oi = o & 65535;
        constexpr int aR = m == 1 ? 80 : 64, wR = 96 + 4 * (g % 5), nR = 96 + 4 * (n % 5), xR = 128 + 4 * (g & 31);
        constexpr int nx = (g - 4 >= 0 && g - 4 < 12) + (g - 3 >= 0 && g - 3 < 12) + (g - 2 >= 0 && g - 2 < 12) + (g - 1 >= 0 && g - 1 < 12), wc_ = n >= 96 ? 0 : (8 + 2 * nx > 14 ? 14 : 8 + 2 * nx);
#define KRD(B) "ds_read_b64_tr_b16 v[%c[nr]:%c[nr]+1], %[" B "] offset:%c[oi]\n\tds_read_b64_tr_b16 v[%c[nr]+2:%c[nr]+3], %[" B "] offset:%c[oi]+512\n\t"
        asm volatile(".if %c[bar] == 1\n\t" KCHK("%c[st]") "s_cbranch_scc0 1f\n2:\n\t.subsection 1\n" KSPIN("%c[st]") "s_branch 2b\n\t.subsection 0\n\t.endif\n\t"
                     ".if %c[bar] == 2\n\tds_read_b128 v[116:119], %[fa]\n\t.endif\n\t"
                     ".if %c[rd] == 1\n\t" KRD("tb") ".endif\n\t.if %c[rd] == 2\n\t" KRD("tc") ".endif\n\t.if %c[rd] == 3\n\t" KRD("ta") ".endif\n\t"
                     "s_waitcnt lgkmcnt(%c[wn])\n\t"
                     ".if %c[md] == 0\n\tv_mfma_f32_32x32x16_bf16 v[%c[ar]:%c[ar]+15], v[%c[wr]:%c[wr]+3], v[%c[xr]:%c[xr]+3], v[%c[ar]:%c[ar]+15]\n\t.endif\n\t"
                     ".if %c[md] == 1\n\tv_mfma_f32_32x32x16_bf16 v[%c[ar]:%c[ar]+15], v[%c[xr]:%c[xr]+3], v[%c[wr]:%c[wr]+3], v[%c[ar]:%c[ar]+15]\n\t.endif\n\t"
                     ".if %c[md] == 2\n\tv_mfma_f32_32x32x16_bf16 v[%c[ar]:%c[ar]+15], v[%c[xr]:%c[xr]+3], v[%c[wr]:%c[wr]+3], 0\n\t.endif\n\t"
                     ".if %c[md] == 3\n\tv_mfma_f32_32x32x16_bf16 v[%c[ar]:%c[ar]+15], v[%c[wr]:%c[wr]+3], v[%c[xr]:%c[xr]+3], 0\n\t.endif\n\t"
                     ".if %c[xm] == 1\n\tds_read_b128 v[%c[xd]:%c[xd]+3], %[x0] offset:%c[xo]\n\tds_read_b128 v[%c[xd]+4:%c[xd]+7], %[x1] offset:%c[xo]\n\t.endif\n\t"
                     ".if %c[xm] == 2\n\tds_read_b128 v[%c[xd]:%c[xd]+3], %[x2] offset:%c[xo]\n\tds_read_b128 v[%c[xd]+4:%c[xd]+7], %[x3] offset:%c[xo]\n\t.endif\n\t"
                     ".if %c[xn]\n\t" KXN(0) KXN(1) KXN(2) KXN(3) ".endif"
                     : "+{v[64:79]}"(acA), "+{v[80:95]}"(acB), "+{v[96:99]}"(w0), "+{v[100:103]}"(w1), "+{v[104:107]}"(w2), "+{v[108:111]}"(w3), "+{v[112:115]}"(w4),
                       "+{v[128:143]}"(xa0), "+{v[144:159]}"(xa1), "+{v[160:175]}"(xa2), "+{v[176:191]}"(xa3), "+{v[192:207]}"(xa4), "+{v[208:223]}"(xa5), "+{v[224:239]}"(xa6), "+{v[240:255]}"(xa7),
                       "+{v[116:119]}"(fl), [t] "=&s"(tq), [x0] "+v"(xb[0]), [x1] "+v"(xb[1]), [x2] "+v"(xb[2]), [x3] "+v"(xb[3])
                     : [ta] "v"(tA), [tb] "v"(tB), [tc] "v"(tC), [fa] "v"(fa), [st] "n"(n >> 3), [wn] "n"(wc_),
                       [bar] "n"(K1_ABL ? 0 : n < 96 && (n & 7) == 0 ? 1 : n < 92 && (n & 7) == 4 ? 2 : 0), [rd] "n"(n >= 96 ? 0 : o < 65536 ? 3 : o < 131072 ? 1 : 2), [oi] "n"(oi), [nr] "n"(nR),
                       [md] "n"(m != 1 ? ((g & 31) == 0 ? 3 : 0) : g == 32 ? 2 : 1), [ar] "n"(aR), [wr] "n"(wR), [xr] "n"(xR),
                       [xm] "n"(g < 12 ? 1 + (g & 1) : 0), [xd] "n"(160 + 8 * g), [xo] "n"(((g >> 1) & 1) * 8192), [xn] "n"(g < 11 && (g & 3) == 3 ? 1 : 0) : "scc", "memory");
        if (g == 7 && lane == 0) asm volatile("ds_add_u32 %0, %1" :: "v"(fa + 28), "v"(1u) : "memory");
        if (g == 16 && lane == 0) asm volatile("ds_add_u32 %0, %1" :: "v"(fa + 20), "v"(1u) : "memory");
        if (g == 23 && lane == 0) asm volatile("ds_add_u32 %0, %1" :: "v"(fa + 24), "v"(1u) : "memory");
        if (g == 2 || g == 10) ST128((u32x4{~0u, ~0u, ~0u, ~0u}), rs, l16, WS_Q + ((hd + (g >> 3)) * 64 + tt) * 1024, AUX_SC1);
        if (g == 36 || g == 38) epi(0, acA, (g - 36) >> 1);
        if (g == 67 || g == 68) epi(1, acB, g - 67);
        if (g == K1_POST_AT) {
            WAITV(0);
            __builtin_amdgcn_raw_buffer_store_b32(ep, rs, (lane * 4) | ((unsigned)(31 - lane) & 0x80000000u), WS_FQ + ((cb * 2 + wc) * 64 + tt) * 128, AUX_SC1);
            if (lane == 0) asm volatile("ds_add_u32 %0, %1" :: "v"(fa + 32), "v"(1u) : "memory");
        }
    });
    STAMP(0, 2);
    asm volatile("s_nop 15" : "+{v[64:79]}"(acA));
    epi(2, acA, 0); epi(2, acA, 1);
    if (lane == 0) asm volatile("ds_add_u32 %0, %1" :: "v"(fa + 40), "v"(1u) : "memory");
    STAMP(0, 5);
    }
    }
    SBAR();
    const int rt = rt2;
    u32x8 xres = {};
    u32x4 hreg[2];

    auto issue_wo = [&](int first, int n) {
        const rsrc_t rwo = mk_rsrc(wo, D * D * 2);
        const unsigned v = w64_voff(lane);
        for (int p = first; p < first + n; ++p) dma16(rwo, v, p * 8192 + ct * 128, lds0 + L_WO + p * 1024);
    };
    auto issue_dn = [&](int i0, int n, unsigned dst, int hw) {
        const rsrc_t rwd = mk_rsrc(wd, F * D * 2);
        const unsigned v = w64_voff(lane);
        const unsigned so = ((43 * (hw >> 1) + i0) * 16 + (hw & 1) * 8) * 1024 + ct * 128;
        for (int i = 0; i < n; ++i) dma16(rwd, v, so + i * 16384, lds0 + dst + (hw & 1) * 1024 + i * 2048);
    };
    auto gu_wa = [&](int n) -> unsigned { const unsigned blk = (lane >> 4) & 1;
        return lds0 + L_GU + blk * 49152 + (((4 * h + ((lane & 15) >> 2)) * 96 + n * 32 + (lane & 3) * 8) ^ (blk * 128)); };
    float cs[3] = {0.f, 0.f, 0.f};
    auto gu_colsum = [&]() {
        u32x4 f[3][4];
#pragma unroll
        for (int n = 0; n < 3; ++n) {
            const unsigned a0 = gu_wa(n) + w * 4 * 1536;
#pragma unroll
            for (int i = 0; i < 4; ++i) f[n][i] = __builtin_bit_cast(u32x4, lds_tr2(a0 + i * 1536, a0 + i * 1536 + 768));
        }
        SBAR();
#pragma unroll
        for (int i = 0; i < 4; ++i)
#pragma unroll
            for (int e = 0; e < 4; ++e)
#pragma unroll
                for (int n = 0; n < 3; ++n) cs[n] = dot2(f[n][i][e], ONES2, cs[n]);
#pragma unroll
        for (int n = 0; n < 3; ++n) cs[n] = swap_add(cs[n]);
    };

    unsigned budget = 1u << 16;

    {
        const int qb = bi & 7, wq = w & 3, g = w >> 2, qt = qb * 8 + wq * 2;
        const unsigned kvs = WS_KV + head * KV_HEAD;
        unsigned ks = kvs + g * 4 * KV_CHUNK + wq * 4096, km = lds0 + g * 32768 + wq * 4096;
        const rsrc_t rg = mk_rsrc(g ? wu : wg, (bi < 29 && ABL_GU != 1) ? D * F * 2 : 0), rwd = mk_rsrc(wd, F * D * 2), rwo = mk_rsrc(wo, D * D * 2), rx = mk_rsrc(x, S * D * 2);
        unsigned gs = wq * 4 * 32 * (F * 2) + bi * 96, gm = lds0 + L_GU + g * 49152 + wq * 12288;
        const unsigned long long msk = 0x0000ffff0000ffffull;
        constexpr unsigned OOB = 0x80000000u, SOOB = 0x40000000u;
        auto gu_voff = [&](unsigned c) { const unsigned r = __umul24(c, 171u) >> 10; return __umul24(r, F * 2 - 96u) + c * 16; };
        const unsigned gv0 = gu_voff(lane ^ (g * 8)), gv1 = gu_voff((64 + lane) ^ (g * 8)), gv2 = gu_voff((128 + lane) ^ (g * 8));
        const unsigned pvd = (lane * 128) | ((unsigned)(42 - lane) & OOB), pvo = (lane * 128) | ((unsigned)(15 - lane) & OOB), psd = RFL((bi * 344 + w * 43) * 128), pso = RFL((bi * 128 + w * 16) * 128);
        const unsigned wv = w64_voff(lane), xv = r32 * 1024 + 8 * h, xs = RFL((rt * 64 + wt * 32) * 1024 + (ct * 64 + kh * 32) * 2);
        const unsigned kb = lds0 + g * 32768 + l16, vb = lds0 + g * 32768 + 8192 + (16 * h + (r32 & 15)) * 16;
        const unsigned cst = (r32 & 27) == 16 ? ONES2 : 0u;
        const unsigned fsv = h * 4096 + r32 * 16, fs = RFL(w == 0 ? WS_ST + (rt * 32 + ct * 2) * 256 : SOOB), fpv = (unsigned)(0 - lane) & OOB, fps = RFL(w == 0 ? WS_FP + b * 4 : SOOB);
        u32x4 ones = {~0u, ~0u, ~0u, ~0u}, fpl = {0u, 0u, 0u, 0u}, pA = fpl, pB = fpl;
        unsigned pfd = 0, epv = ep;
        f32x16 sB = {}, o0 = {}, o1 = {};
        s16x8 v1 = __builtin_bit_cast(s16x8, u32x4{cst, cst, cst, cst}), v1b = v1;
        const unsigned fo = WS_O + ((qt + g) * 32 + head) * 1024, fh = WS_H + ((rt * 2 + wt) * 32 + ct * 4 + (w >> 1)) * 1024, fg = RFL(bi < 29 ? WS_HH + ((xcd * 8 + w) * NU + bi * 3) * 1024 : SOOB), fg2 = RFL(bi < 28 ? fg : SOOB);
        STAMP(0, 6);
        SBAR();
#define XKV "s_mov_b32 m0, %[km]\n\ts_add_u32 %[km], %[km], 0x400\n\tbuffer_load_dwordx4 %[l16], %[rs], %[ks] offen sc1 lds\n\ts_add_u32 %[ks], %[ks], 0x400\n\t"
        s16x8 q0, q1;
        unsigned lm = RFL(lds0 + ((w >> 1) & 1) * 32768 + (w & 1) * 8192), ls = RFL(kvs + ((w >> 1) & 1) * 4 * KV_CHUNK + (w & 1) * 8192), tq;
        km += 16384; ks += KV_CHUNK;
#define XSPIN(L, O, N) #L ":\n\ts_sleep 1\n\tds_read_b32 v132, %[fa] offset:" #O "\n\ts_sub_u32 %[cap], %[cap], 1\n\ts_cbranch_scc1 9f\n\ts_waitcnt lgkmcnt(0)\n\tv_readfirstlane_b32 %[t], v132\n\ts_nop 0\n\ts_cmp_lt_u32 %[t], " #N "\n\ts_cbranch_scc1 " #L "b\n\t"
#define XLD "s_mov_b32 m0, %[lm]\n\ts_add_u32 %[lm], %[lm], 0x400\n\tbuffer_load_dwordx4 %[l16], %[rs], %[ls] offen sc1 lds\n\ts_add_u32 %[ls], %[ls], 0x400\n\t"
        unsigned cap = 1u << 22;
        asm volatile("s_cmp_lt_u32 %[w], 4\n\ts_cbranch_scc1 8f\n\t" XSPIN(4, 32, 4)
                     "s_cmp_eq_u32 %[w], 4\n\ts_cbranch_scc0 6f\n\ts_sleep %c[pd]\n"
                     "5:\n\tbuffer_load_dword v132, %[pvf], %[rs], %[fq] offen sc1\n\ts_sub_u32 %[cap], %[cap], 1\n\ts_cbranch_scc1 9f\n\ts_waitcnt vmcnt(0)\n\tv_cmp_ne_u32 vcc, v201, v132\n\ts_cbranch_vccnz 5b\n\t"
                     "ds_add_u32 %[fa], v204 offset:36\n\ts_branch 7f\n" XSPIN(6, 36, 1)
                     "7:\n\t" XLD XLD XLD XLD XLD XLD XLD XLD XSPIN(4, 40, 4)
                     "9:\n8:\n\ts_sleep %c[dl]\n\tbuffer_load_dwordx4 v[128:131], %[l16], %[rs], %[qs] offen sc1\n\tbuffer_load_dwordx4 v[156:159], %[l16], %[rs], %[qs1] offen sc1\n"
                     "1:\n\ts_waitcnt vmcnt(0)\n\tv_max3_u32 v132, v128, v129, v130\n\tv_max3_u32 v132, v132, v131, v156\n\tv_max3_u32 v132, v132, v157, v158\n\tv_max_u32 v132, v132, v159\n\t"
                     "v_cmp_eq_u32 vcc, -1, v132\n\ts_cbranch_vccnz 3f\n2:\n\t.subsection 1\n3:\n\ts_cmp_eq_u32 %[bud], 0\n\ts_cbranch_scc1 2b\n\ts_sub_u32 %[bud], %[bud], 1\n\t"
                     "buffer_load_dwordx4 v[128:131], %[l16], %[rs], %[qs] offen sc1\n\tbuffer_load_dwordx4 v[156:159], %[l16], %[rs], %[qs1] offen sc1\n\ts_branch 1b\n\t.subsection 0\n\t"
                     "s_waitcnt lgkmcnt(0)\n\ts_barrier\n\t" XKV XKV XKV XKV
                     : "={v[128:131]}"(q0), "={v[156:159]}"(q1), [km] "+s"(km), [ks] "+s"(ks), [bud] "+s"(budget), [lm] "+s"(lm), [ls] "+s"(ls), [cap] "+s"(cap), [t] "=&s"(tq),
                       "+{v[80:95]}"(sB), "+{v[96:111]}"(o0), "+{v[160:175]}"(o1),
                       "+{v[144:147]}"(v1), "+{v[136:139]}"(v1b), "+{v[148:151]}"(pA), "+{v[152:155]}"(pB), "+{v[192:199]}"(xres), "+{v200}"(pfd), "+{v201}"(epv), "+{v[204:207]}"(ones), "+{v[208:211]}"(fpl)
                     : [l16] "v"(l16), [rs] "s"(rs), [qs] "s"(RFL(WS_Q + (head * 64 + qt + g) * 1024)), [qs1] "s"(RFL(WS_Q + (head * 64 + qt + 1 - g) * 1024)), [w] "s"(w),
                       [fa] "v"(lds0 + K1_FLG), [pvf] "v"(lane * 128), [fq] "s"(RFL(WS_FQ + (head >> 1) * 8192)), [pd] "n"(POLL_DELAY_Q), [dl] "n"(DELAY_Q),
                       "v"(gv0), "v"(gv1), "v"(gv2), "v"(pvd), "v"(pvo), "v"(wv), "v"(xv), "v"(kb), "v"(vb), "v"(fsv), "v"(fpv),
                       "s"(rg), "s"(rwd), "s"(rwo), "s"(rx), "s"(gs), "s"(gm), "s"(psd), "s"(pso), "s"(xs), "s"(fs), "s"(fps), "s"(fo), "s"(fh), "s"(fg), "s"(fg2)
                     : "m0", "scc", "vcc", "v132", "memory");
        STAMP(0, 7);
        FLUSH(0);
        km = RFL(km) - (16384 + 4096); ks = RFL(ks) + KV_CHUNK - 4096; budget = RFL(budget);
        STAMP(1, 0);
        STAMP(1, 1);
        f32x16 n0, n1, sA;
        s16x8 kf = lds_b128(kb), v0;
        {
            const f32x16 z = {};
            const s16x8 kz = lds_b128(lds0 + l16);
            const u32x4 vv = __builtin_bit_cast(u32x4, lds_b128(vb)), cc = {cst, cst, cst, cst};
            const f32x16 s0 = MFMA(kz, q0, z), s1 = MFMA(kz, q1, z);
            float m0 = __builtin_fmaxf(s0[0], s0[1]), m1 = __builtin_fmaxf(s1[0], s1[1]);
#pragma unroll
            for (int i = 2; i < 16; i += 2) { m0 = __builtin_fmaxf(__builtin_fmaxf(m0, s0[i]), s0[i + 1]); m1 = __builtin_fmaxf(__builtin_fmaxf(m1, s1[i]), s1[i + 1]); }
            m0 = swap_max(m0); m1 = swap_max(m1);
#pragma unroll
            for (int i = 0; i < 16; ++i) { n0[i] = -m0; n1[i] = -m1; }
            sA = MFMA(kf, q0, n0);
            v0 = __builtin_bit_cast(s16x8, r32 < 16 ? vv : cc);
        }
#define XE(C, i) "v_exp_f32 v[" #C "+" #i "], v[" #C "+" #i "]\n\t"
#define XC(P, C, i) "v_cvt_pk_bf16_f32 v" #P ", v[" #C "+" #i "], v[" #C "+" #i "+1]\n\t"
#define XG1(C) XE(C, 0) XE(C, 1) XE(C, 2) XE(C, 3) XC(148, C, 0) XE(C, 4) XE(C, 5) XC(149, C, 2)
#define XG2(C) XE(C, 6) XE(C, 7) XC(150, C, 4) XE(C, 8) XC(151, C, 6) XE(C, 9) XE(C, 10)
#define XG3(C) XE(C, 11) XC(152, C, 8) XE(C, 12) XE(C, 13) XC(153, C, 10) XE(C, 14) XE(C, 15) XC(154, C, 12) XC(155, C, 14)
#define XM(D, A, B, C) "v_mfma_f32_32x32x16_bf16 v[" #D ":" #D "+15], v[" #A ":" #A "+3], v[" #B ":" #B "+3], v[" #C ":" #C "+15]\n\t"
#define XRK(BASE, O) "ds_read_b128 v[132:135], " BASE " offset:" #O "\n\t"
#define XRV(R, BASE, O) "s_mov_b64 exec, %[msk]\n\tds_read_b128 v[" #R ":" #R "+3], " BASE " offset:" #O "\n\ts_mov_b64 exec, -1\n\t"
#define XW(n) "s_waitcnt lgkmcnt(" #n ")\n\t"
#define XTILE(VA, RK, RV1, WA, X1, VB, X2, RV0) \
        XM(80, 132, 156, 176) RK XG1(64) XM(160, VA, 152, 160) RV1 XG2(64) XW(WA) XM(96, 140, 148, 96) XG3(64) \
        XW(0) XM(64, 132, 128, 112) X1 XG1(80) XM(96, VB, 152, 96) X2 XG2(80) XM(160, 140, 148, 160) RV0 XG3(80)
#define XGU(j) "s_mov_b32 m0, %[gm]\n\ts_add_u32 %[gm], %[gm], 0x400\n\tbuffer_load_dwordx4 %[gv" #j "], %[rg], %[gs] offen lds\n\t"
#define XGU2 XGU(2) "s_add_u32 %[gs], %[gs], 88064\n\t"
#define XPF "buffer_load_dword v200, %[pvd], %[rwd], %[psd] offen\n\tbuffer_load_dword v200, %[pvo], %[rwo], %[pso] offen\n\t"
#define XWO "s_mov_b32 m0, %[km]\n\ts_add_u32 %[km], %[km], 0x400\n\tbuffer_load_dwordx4 %[wv], %[rwo], %[ks] offen lds\n\ts_add_u32 %[ks], %[ks], 0x2000\n\t"
#define XST(V, SO, O) "buffer_store_dwordx4 v[204:207], " V ", %[rs], " SO " offen offset:" #O " sc1\n\t"
#define XFP "buffer_store_dword v201, %[fpv], %[rs], %[fps] offen sc1\n\t"
#define XFL "buffer_load_dwordx4 v[208:211], %[l16], %[rs], %[fp] offen sc1\n\t"
#define XXR(R, O) "buffer_load_dwordx2 v[" #R ":" #R "+1], %[xv], %[rx], %[xs] offen offset:" #O "\n\t"
#define AOPS : "+{v[64:79]}"(sA), "+{v[80:95]}"(sB), "+{v[96:111]}"(o0), "+{v[160:175]}"(o1), "+{v[112:127]}"(n0), "+{v[176:191]}"(n1), "+{v[128:131]}"(q0), "+{v[156:159]}"(q1), \
               "+{v[132:135]}"(kf), "+{v[140:143]}"(v0), "+{v[144:147]}"(v1), "+{v[136:139]}"(v1b), "+{v[148:151]}"(pA), "+{v[152:155]}"(pB), "+{v[192:199]}"(xres), "+{v200}"(pfd), \
               "+{v201}"(epv), "+{v[204:207]}"(ones), "+{v[208:211]}"(fpl), \
               [km] "+s"(km), [ks] "+s"(ks), [gm] "+s"(gm), [gs] "+s"(gs) \
             : [ka] "v"(ka), [va] "v"(va), [kn] "v"(kn), [vn] "v"(vn), [msk] "s"(msk), [l16] "v"(l16), [rs] "s"(rs), [rg] "s"(rg), [gv0] "v"(gv0), [gv1] "v"(gv1), [gv2] "v"(gv2), \
               [pvd] "v"(pvd), [pvo] "v"(pvo), [wv] "v"(wv), [xv] "v"(xv), [xs] "s"(xs), [psd] "s"(psd), [pso] "s"(pso), [rwd] "s"(rwd), [rwo] "s"(rwo), [rx] "s"(rx), \
               [fo] "s"(fo), [fh] "s"(fh), [fg] "s"(fg), [fsv] "v"(fsv), [fs] "s"(fs), [fg2] "s"(fg2), [fpv] "v"(fpv), [fps] "s"(fps), [fp] "s"(WS_FP) : "m0", "scc", "memory"
#define XBAR(n) asm volatile("s_waitcnt vmcnt(" #n ") lgkmcnt(0)\n\ts_barrier" AOPS)
#define XROUND(A1, A2, B1, B2, C1, C2, D1, D2, E1, E2, F1, F2) asm volatile( \
            XTILE(136, XRK("%[ka]", 1024), XRV(144, "%[va]", 512), 2, A1, 144, A2, XRV(140, "%[va]", 1024)) \
            XTILE(144, XRK("%[ka]", 2048), XRV(144, "%[va]", 1536), 2, B1, 144, B2, XRV(140, "%[va]", 2048)) \
            XTILE(144, XRK("%[ka]", 3072), XRV(144, "%[va]", 2560), 2, C1, 144, C2, XRV(140, "%[va]", 3072)) \
            XTILE(144, XRK("%[ka]", 4096), XRV(144, "%[va]", 3584), 2, D1, 144, D2, XRV(140, "%[va]", 4096)) \
            XTILE(144, XRK("%[ka]", 5120), XRV(144, "%[va]", 4608), 2, E1, 144, E2, XRV(140, "%[va]", 5120)) \
            XTILE(144, XRK("%[ka]", 6144), XRV(144, "%[va]", 5632), 2, F1, 144, F2, XRV(140, "%[va]", 6144)) \
            XTILE(144, XRK("%[ka]", 7168), XRV(144, "%[va]", 6656), 2, XRV(136, "%[va]", 7680), 144, "", XRV(140, "%[va]", 7168)) AOPS)
#define XLAST(X1, X2) asm volatile(XTILE(144, XRK("%[kn]", 0), "", 1, X1, 136, X2, XRV(140, "%[vn]", 0)) AOPS)
        unsigned ka = kb, va = vb, kn = kb + 16384, vn = vb + 16384;
        asm volatile("s_nop 15" AOPS);
        STAMP(1, 7);
        XROUND(XGU(0), "", "", XST("%[l16]", "%[fo]", 0), XGU(1), XST("%[fsv]", "%[fs]", 0), "", XST("%[l16]", "%[fh]", 0), XGU2, "", XGU(0), "");
        STAMP(1, 8);
        XBAR(7);
        STAMP(1, 9);
        XLAST(XKV, XKV);
        ka = kb + 16384; va = vb + 16384; kn = kb; vn = vb;
        XROUND(XKV, XKV, XGU(1), XST("%[l16]", "%[fg]", 0), XGU2, "", XST("%[l16]", "%[fg]", 1024), XGU(0), "", XGU(1), XST("%[l16]", "%[fg2]", 2048), "");
        STAMP(1, 10);
        XBAR(7);
        STAMP(1, 11);
        km += 16384 - 4096; ks += KV_CHUNK - 4096;
        XLAST(XKV, XKV);
        ka = kb; va = vb; kn = kb + 16384; vn = vb + 16384;
        XROUND(XKV, XKV, XGU2, "", XGU(0), "", XGU(1), "", XGU2, "", XPF, "");
        STAMP(1, 12);
        XBAR(6);
        STAMP(1, 13);
        km = lds0 + g * 32768 + wq * 4096; ks = (32 * g + 4 * wq) * 8192 + ct * 128;
        XLAST(XFP, XWO);
        ka = kb + 16384; va = vb + 16384; kn = kb; vn = vb;
        XROUND(XWO, XWO, XWO, "", XXR(192, 0), "", XXR(194, 16), "", XXR(196, 32), "", XXR(198, 48), "");
        XLAST(XFL, "");
        STAMP(1, 14);
        asm volatile(XM(160, 136, 152, 160) "s_waitcnt vmcnt(0) lgkmcnt(0)\n\ts_barrier\n\ts_nop 15" AOPS);
        STAMP(1, 4);
        f32x16 o = o0;
        const unsigned xa = lds0 + g * 32768 + 16384 + wq * 3072 + l16, xb = lds0 + (g ^ 1) * 32768 + 16384 + wq * 3072 + l16;
        *(LDSAS f32x4*)(xa) = f32x4{o1[0], o1[1], o1[2], o1[3]};
        *(LDSAS f32x4*)(xa + 1024) = f32x4{o1[4], o1[5], o1[6], o1[7]};
        *(LDSAS float*)(xa + 2048) = o1[8];
        {
            float big = __builtin_fmaxf(o0[8], o1[8]) * 1e19f;
            float chk = o0[8] + o1[8];
#pragma unroll
            for (int i = 0; i < 8; ++i) { big = __builtin_fmaxf(__builtin_fmaxf(big, __builtin_fabsf(o0[i])), __builtin_fabsf(o1[i])); chk += o0[i] + o1[i]; }
            *(LDSAS unsigned*)(lds0 + L_FLAG + w * 4) = __any(!(big < 1e38f) || chk != chk);
        }
        BAR_L();
        const f32x4 p0 = lds_f4(xb), p1 = lds_f4(xb + 1024); const float pl = *(const LDSAS float*)(xb + 2048);
        const u32x4 f0 = *(const LDSAS u32x4*)(lds0 + L_FLAG), f1 = *(const LDSAS u32x4*)(lds0 + L_FLAG + 16);
#pragma unroll
        for (int i = 0; i < 4; ++i) { o[i] += p0[i]; o[4 + i] += p1[i]; }
        o[8] += pl;
        if (__builtin_expect(RFL(f0[0] | f0[1] | f0[2] | f0[3] | f1[0] | f1[1] | f1[2] | f1[3]), 0)) {
            BAR_L();
            const s16x8 qf = q0;
            const u32x4 cc = {cst, cst, cst, cst};
            const unsigned vf = lds0 + 16384 + 8192 + (16 * h + (r32 & 15)) * 16;
            float m = -__builtin_inff();
            o = f32x16{};
            const f32x16 z = {};
#pragma unroll 1
            for (int c = 0; c < 8; ++c) {
                dma16<true>(rs, l16, kvs + c * KV_CHUNK + w * 2048, lds0 + 16384 + w * 2048);
                dma16<true>(rs, l16, kvs + c * KV_CHUNK + w * 2048 + 1024, lds0 + 16384 + w * 2048 + 1024);
                BAR_VL(0);
#pragma unroll 1
                for (int t = 0; t < 8; ++t) {
                    f32x16 s = MFMA(lds_b128(lds0 + 16384 + l16 + t * 1024), qf, z);
                    float mx = s[0];
#pragma unroll
                    for (int i = 1; i < 16; ++i) mx = __builtin_fmaxf(mx, s[i]);
                    mx = __builtin_fmaxf(swap_max(mx), m);
                    const float al = __builtin_amdgcn_exp2f(m - mx);
                    m = mx;
#pragma unroll
                    for (int i = 0; i < 16; ++i) { o[i] *= al; s[i] = __builtin_amdgcn_exp2f(s[i] - m); }
#pragma unroll
                    for (int i = 0; i < 4; ++i) { pA[i] = pk2(s[2 * i], s[2 * i + 1]); pB[i] = pk2(s[8 + 2 * i], s[9 + 2 * i]); }
                    const u32x4 a = __builtin_bit_cast(u32x4, lds_b128(vf + t * 1024)), c2 = __builtin_bit_cast(u32x4, lds_b128(vf + t * 1024 + 512));
                    o = MFMA(__builtin_bit_cast(s16x8, r32 < 16 ? a : cc), __builtin_bit_cast(s16x8, pA), o);
                    o = MFMA(__builtin_bit_cast(s16x8, r32 < 16 ? c2 : cc), __builtin_bit_cast(s16x8, pB), o);
                }
                BAR_L();
            }
        }
        {
            const float inv = __builtin_amdgcn_rcpf(o[8]);
            u32x4 ov;
#pragma unroll
            for (int i = 0; i < 4; ++i) ov[i] = pk2(o[2 * i] * inv, o[2 * i + 1] * inv);
            ST128(ov, rs, l16, fo, AUX_SC1);
        }
        STAMP(1, 5);
        BAR_L();
        if (w >= 4) issue_wo(16 + ((w - 4) >> 1) * 32 + (w & 1) * 8, 8);
        gu_colsum();
#pragma unroll 1
        for (unsigned spins = 0; spins < SPIN_CAP && !__all(fpl[0] == ep && fpl[1] == ep && fpl[2] == ep && fpl[3] == ep); ++spins) {
            __builtin_amdgcn_s_sleep(2);
            fpl = __builtin_amdgcn_raw_buffer_load_b128(rs, l16, WS_FP, AUX_SC1);
        }
        STAMP(1, 6);
        FLUSH(1);
    }

    {
        STAMP(2, 0);
        f32x16 a0, a1;
        if (w < 4) {
            const unsigned t0 = lds0 + L_WO + kh * 32768 + w64_tr(lane, kh), t1 = lds0 + L_WO + kh * 32768 + w64_tr(lane, kh ^ 1);
            G2 g2;
            g2.tb[0] = g2.tb[2] = g2.tb[4] = t0; g2.tb[1] = g2.tb[3] = g2.tb[5] = t1;
            g2.sa = g2.s0 = RFL(WS_O + ((rt * 2 + wt) * 32 + kh * 16) * 1024); g2.l16 = l16; g2.budget = budget; g2.rs = rs; g2.a1 = f32x16{};
#pragma unroll
            for (int i = 0; i < 8; ++i) { g2.a0[2 * i] = bflo(xres[i]); g2.a0[2 * i + 1] = bfhi(xres[i]); }
            __builtin_amdgcn_s_sleep(DELAY_O);
            STAMP(2, 1);
            g2_steps<0, 16, 0, 6, -1>(g2);
            a0 = g2.a0; a1 = g2.a1; STAMPV(15, budget - g2.budget); budget = g2.budget;
        } else BAR_VL(0);
        STAMP(2, 2);
        BAR_L();
        if (w < 4) {
#pragma unroll
            for (int g = 0; g < 4; ++g) *(LDSAS f32x4*)(lds0 + w * 4096 + g * 1024 + lane * 16) = f32x4{a1[4 * g], a1[4 * g + 1], a1[4 * g + 2], a1[4 * g + 3]};
        }
#pragma unroll
        for (int n = 0; n < 3; ++n) *(LDSAS float*)(lds0 + L_TAB + (w * 96 + n * 32 + r32) * 4) = cs[n];
        BAR_L();
        if (w >= 4) {
            if (tid < 256 + 96) {
                float t[8];
#pragma unroll
                for (int i = 0; i < 8; ++i) t[i] = *(const LDSAS float*)(lds0 + L_TAB + (i * 96 + tid - 256) * 4);
                *(LDSAS float*)(lds0 + L_TAB + 3072 + (tid - 256) * 4) = ((t[0] + t[1]) + (t[2] + t[3])) + ((t[4] + t[5]) + (t[6] + t[7]));
            }
        } else {
            f32x4 p[4];
#pragma unroll
            for (int g = 0; g < 4; ++g) p[g] = lds_f4(lds0 + (w ^ 2) * 4096 + g * 1024 + lane * 16);
            SBAR();
#pragma unroll
            for (int g = 0; g < 4; ++g) {
                hreg[g >> 1][(g & 1) * 2] = pk2(a0[4 * g] + p[g][0], a0[4 * g + 1] + p[g][1]);
                hreg[g >> 1][(g & 1) * 2 + 1] = pk2(a0[4 * g + 2] + p[g][2], a0[4 * g + 3] + p[g][3]);
            }
            const unsigned so = WS_H + ((rt * 2 + wt) * 32 + (ct * 2 + kh) * 2) * 1024;
            ST128(hreg[0], rs, lane * 16, so, AUX_SC1);
            ST128(hreg[1], rs, lane * 16 + 1024, so, AUX_SC1);
            SBAR();
            float sm = 0.f, sq = 0.f;
#pragma unroll
            for (int i = 0; i < 8; ++i) { sm = dot2(hreg[i >> 2][i & 3], ONES2, sm); sq = dot2(hreg[i >> 2][i & 3], hreg[i >> 2][i & 3], sq); }
            sm = swap_add(sm); sq = swap_add(sq);
            if (h == 0) __builtin_amdgcn_raw_buffer_store_b64(u32x2{__float_as_uint(sm), __float_as_uint(sq)}, rs, r32 * 8,
                                                              WS_ST + ((rt * 2 + wt) * 16 + ct * 2 + kh) * 256, AUX_SC1);
        }
        STAMP(2, 3);
        BAR_L();
        if (w >= 4) { issue_dn(0, 15, ((w - 4) >> 1) * 15 * 2048, w - 4); if (bi >= 29) issue_dn(15, 24, L_GU + ((w - 4) >> 1) * 49152, w - 4); }
        STAMP(2, 5);
        FLUSH(2);
    }

    {
        STAMP(3, 0);
        if (bi < 29) {
            const int tt = xcd * 8 + w;
            GU gu;
            gu.a0 = gu.a1 = gu.a2 = f32x16{};
            gu.sa = gu.s0 = RFL(WS_H + tt * 32768); gu.sv = 8 * h * 256 + r32 * 8; gu.ss = RFL(WS_ST + tt * 4096); gu.l16 = l16; gu.budget = budget; gu.rs = rs;
            gu.dm = RFL(lds0 + L_GU + (w >> 2) * 49152 + (w & 3) * 6144); gu.ds = RFL(((43 * (w >> 2) + 15) * 16 + (w & 3) * 48) * 1024 + ct * 128);
            gu.dm2 = RFL(lds0 + L_GU + (w >> 2) * 49152 + 24576 + (w & 3) * 3072); gu.ds2 = RFL(((43 * (w >> 2) + 27) * 16 + (w & 3) * 24) * 1024 + ct * 128);
            gu.dm3 = RFL(lds0 + L_TAB + (w >> 2) * 2048 + (w & 1) * 1024); gu.ds3 = RFL(((43 * (w >> 2) + 39) * 16 + (w & 1) * 8) * 1024 + ct * 128);
            gu.dv = w64_voff(lane); gu.rd = mk_rsrc(wd, F * D * 2);
            gu.wa[0] = gu_wa(0); gu.wa[1] = gu_wa(1); gu.wa[2] = gu_wa(2);
            float csl[3];
#pragma unroll
            for (int n = 0; n < 3; ++n) csl[n] = *(const LDSAS float*)(lds0 + L_TAB + 3072 + (n * 32 + r32) * 4);
            if (w < 4) __builtin_amdgcn_s_sleep(DELAY_H);
            STAMP(3, 2);
            gu_steps<0, 18>(gu);
#pragma unroll 1
            for (;;) {
                unsigned mx = gu.st[0];
#pragma unroll
                for (int i = 1; i < 16; ++i) mx = gu.st[i] > mx ? gu.st[i] : mx;
                if (__builtin_expect(!__any(mx == ~0u) || gu.budget == 0, 1)) break;
                --gu.budget;
                asm volatile(".set tagj, 0\n\t.rept 8\n\tbuffer_load_dwordx2 v[172+2*tagj:173+2*tagj], %[sv], %[rs], %[ss] offen offset:tagj*256 sc1\n\t.set tagj, tagj+1\n\t.endr\n\ts_waitcnt vmcnt(0)"
                             : "+{v[172:187]}"(gu.st), "+{v[140:155]}"(gu.r0), "+{v[156:171]}"(gu.r1), "+{v[112:119]}"(gu.w0), "+{v[124:131]}"(gu.w1), "+{v[132:139]}"(gu.w2),
                               "+{v[64:79]}"(gu.a0), "+{v[80:95]}"(gu.a1), "+{v[96:111]}"(gu.a2) : [sv] "v"(gu.sv), [rs] "s"(rs), [ss] "s"(gu.ss) : "memory");
            }
            float sm = 0.f, sq = 0.f;
#pragma unroll
            for (int i = 0; i < 8; ++i) { sm += __uint_as_float(gu.st[2 * i]); sq += __uint_as_float(gu.st[2 * i + 1]); }
            sm = swap_add(sm); sq = swap_add(sq);
            const float mean = sm * (1.f / D);
            const float var = __builtin_fmaxf(sq * (1.f / D) - mean * mean, 0.f) + LN_EPS;
            float rstd = __builtin_amdgcn_rsqf(var), sd = var * rstd, ga = -LOG2E * rstd;
            u32x4 mf, cf[3];
            {
                const unsigned mh = pk2(-mean, -mean);
                const unsigned ml = pk2(-mean - bflo(mh), 0.f);
                mf = h ? u32x4{0u, 0u, 0u, 0u} : u32x4{(mh & 0xffffu) | (ml << 16), mh & 0xffffu, 0u, 0u};
#pragma unroll
                for (int n = 0; n < 3; ++n) { const unsigned ch = pk2(csl[n], csl[n]); cf[n] = u32x4{ch, pk2(csl[n] - bflo(ch), 0.f), 0u, 0u}; PIN(cf[n]); }
                PIN(mf); PIN(sd); PIN(ga);
            }
            gu_steps<18, 32>(gu);
            asm volatile("v_mfma_f32_32x32x16_bf16 v[64:79], %[c0], %[m], v[64:79]\n\tv_mfma_f32_32x32x16_bf16 v[80:95], %[c1], %[m], v[80:95]\n\t"
                         "v_mfma_f32_32x32x16_bf16 v[96:111], %[c2], %[m], v[96:111]\n\ts_nop 10"
                         : "+{v[64:79]}"(gu.a0), "+{v[80:95]}"(gu.a1), "+{v[96:111]}"(gu.a2) : [c0] "v"(cf[0]), [c1] "v"(cf[1]), [c2] "v"(cf[2]), [m] "v"(mf));
            STAMP(3, 3);
            STAMPV(15, budget - gu.budget); budget = gu.budget;
            auto epi = [&](int n, const f32x16& acc) {
                f32x2 e[4], y[4];
#pragma unroll
                for (int i = 0; i < 4; ++i) e[i] = f32x2{acc[2 * i], acc[2 * i + 1]} * ga;
#pragma unroll
                for (int i = 0; i < 4; ++i) e[i] = f32x2{__builtin_amdgcn_exp2f(e[i][0]), __builtin_amdgcn_exp2f(e[i][1])} * sd + sd;
#pragma unroll
                for (int i = 0; i < 4; ++i) y[i] = f32x2{acc[8 + 2 * i], acc[9 + 2 * i]} * f32x2{__builtin_amdgcn_rcpf(e[i][0]), __builtin_amdgcn_rcpf(e[i][1])};
                const u32x4 ov = {pk2(y[0][0], y[0][1]), pk2(y[1][0], y[1][1]), pk2(y[2][0], y[2][1]), pk2(y[3][0], y[3][1])};
                ST128(ov, rs, (n < 2 || bi < 28) ? l16 : 0x80000000u, WS_HH + (tt * NU + bi * 3 + n) * 1024, AUX_SC1);
            };
            epi(0, gu.a0);
            asm volatile("" : "+{v[80:95]}"(gu.a1));
            epi(1, gu.a1);
            asm volatile("" : "+{v[96:111]}"(gu.a2));
            epi(2, gu.a2);
        }
        STAMP(3, 4);
        if (bi >= 29) WAITV(0);
        BAR_VL(3);
        STAMP(3, 5);
        if (w >= 4) {
            if (bi < 29) issue_dn(33, 6, L_GU + ((w - 4) >> 1) * 49152 + 36864, w - 4); else issue_dn(39, 1, L_TAB + ((w - 4) >> 1) * 2048, w - 4);
            STAMP(3, 6);
        }
        FLUSH(3);
    }

    {
        STAMP(4, 0);
        if (b == 0 && tid == 0) __builtin_amdgcn_raw_buffer_store_b32(ep, rs, 0, WS_EP, AUX_SC1);
        f32x16 a0, a1;
        if (w < 4) {
            const unsigned t0 = lds0 + w64_tr(lane, kh), t1 = lds0 + w64_tr(lane, kh ^ 1);
            G2 g2;
            g2.tb[0] = t0 + kh * 15 * 2048; g2.tb[1] = t1 + kh * 15 * 2048; g2.tb[2] = t0 + L_GU + kh * 24 * 2048; g2.tb[3] = t1 + L_GU + kh * 24 * 2048;
            g2.tb[4] = t0 + L_TAB + kh * 2048; g2.tb[5] = t1 + L_TAB + kh * 2048;
            g2.sa = g2.s0 = RFL(WS_HH + ((rt * 2 + wt) * NU + kh * 43) * 1024); g2.l16 = l16; g2.budget = budget; g2.rs = rs; g2.a1 = f32x16{};
#pragma unroll
            for (int i = 0; i < 8; ++i) { g2.a0[2 * i] = bflo(hreg[i >> 2][i & 3]); g2.a0[2 * i + 1] = bfhi(hreg[i >> 2][i & 3]); }
            __builtin_amdgcn_s_sleep(DELAY_G);
            STAMP(4, 1);
            g2_steps<0, 43, 1, DN_B0, 38>(g2);
            a0 = g2.a0; a1 = g2.a1; STAMPV(15, budget - g2.budget);
        } else {
            BAR_VL(0);
            issue_dn(40, 3, ((w - 4) >> 1) * 15 * 2048, w - 4);
            BAR_VL(0);
        }
        STAMP(4, 2);
        if (w < 4) {
#pragma unroll
            for (int g = 0; g < 4; ++g) *(LDSAS f32x4*)(lds0 + 8192 + w * 4096 + g * 1024 + lane * 16) = f32x4{a1[4 * g], a1[4 * g + 1], a1[4 * g + 2], a1[4 * g + 3]};
        }
        BAR_L();
        if (w < 4) {
            f32x4 p[4];
#pragma unroll
            for (int g = 0; g < 4; ++g) p[g] = lds_f4(lds0 + 8192 + (w ^ 2) * 4096 + g * 1024 + lane * 16);
            SBAR();
            unsigned y[8];
#pragma unroll
            for (int g = 0; g < 4; ++g) {
                y[2 * g] = pk2(a0[4 * g] + p[g][0], a0[4 * g + 1] + p[g][1]);
                y[2 * g + 1] = pk2(a0[4 * g + 2] + p[g][2], a0[4 * g + 3] + p[g][3]);
            }
            const rsrc_t ro = mk_rsrc(out, S * D * 2);
            const unsigned vo = (rt * 64 + wt * 32 + r32) * 1024 + (ct * 64 + kh * 32 + 8 * h) * 2;
#pragma unroll
            for (int g = 0; g < 2; ++g) {
                auto e0 = __builtin_amdgcn_permlane32_swap(y[4 * g], y[4 * g + 2], false, false);
                auto e1 = __builtin_amdgcn_permlane32_swap(y[4 * g + 1], y[4 * g + 3], false, false);
                ST128((u32x4{e0[0], e1[0], e0[1], e1[1]}), ro, vo + g * 32, 0, OUT_AUX);
            }
        }
        STAMP(4, 3);
        WAITV(0);
        STAMP(4, 4);
        FLUSH(4);
        asm volatile(".subsection 2\n.Lmain_end:\n\t.fill 64, 4, 0xBF800000\n\t.subsection 0");
    }
}

extern "C" void kernel_launch(void* const* d_in, const int* in_sizes, int n_in,
                              void* d_out, int out_size, void* d_ws, size_t ws_size,
                              hipStream_t stream) {
    const u16* x = (const u16*)d_in[0];
    const u16* wqkv = (const u16*)d_in[1];
    const u16* wo = (const u16*)d_in[2];
    const u16* wg = (const u16*)d_in[4];
    const u16* wu = (const u16*)d_in[5];
    const u16* wd = (const u16*)d_in[6];
    u16* out = (u16*)d_out;
    char* ws = (char*)d_ws;
    (void)hipFuncSetAttribute((const void*)k_block, hipFuncAttributeMaxDynamicSharedMemorySize, K2_LDS);
    k_block<<<256, 512, K2_LDS, stream>>>(x, wqkv, wo, wg, wu, wd, out, ws);
}
```

```cpp
#include <hip/hip_runtime.h>
#include <hip/hip_bf16.h>
#include <stdint.h>
#pragma clang diagnostic ignored "-Winline-asm"

typedef short s16x8 __attribute__((ext_vector_type(8)));
typedef short s16x4 __attribute__((ext_vector_type(4)));
typedef float f32x16 __attribute__((ext_vector_type(16)));
typedef float f32x4 __attribute__((ext_vector_type(4)));
typedef float f32x2 __attribute__((ext_vector_type(2)));
typedef unsigned u32x4 __attribute__((ext_vector_type(4)));
typedef unsigned u32x2 __attribute__((ext_vector_type(2)));
typedef unsigned u32x8 __attribute__((ext_vector_type(8)));
typedef __bf16 bf16x2 __attribute__((ext_vector_type(2)));
typedef unsigned short u16;
#define LDSAS __attribute__((address_space(3)))
#define GAS __attribute__((address_space(1)))
typedef __amdgpu_buffer_rsrc_t rsrc_t;

constexpr int S = 2048, D = 512, NH = 32, F = 1376, NU = F / 16;
constexpr float LN_EPS = 1e-5f;
constexpr float LOG2E = 1.4426950408889634f;

constexpr unsigned KV_CHUNK = 16384;
constexpr unsigned KV_HEAD = 8 * KV_CHUNK;
constexpr unsigned WS_EP = 0;
constexpr unsigned WS_FQ = 4096;
constexpr unsigned WS_FP = WS_FQ + 16 * 64 * 128;
constexpr unsigned WS_Q = WS_FP + 4096;
constexpr unsigned WS_KV = WS_Q + NH * 64 * 1024;
constexpr unsigned WS_O = WS_KV + NH * KV_HEAD;
constexpr unsigned WS_H = WS_O + 64 * 32 * 1024;
constexpr unsigned WS_ST = WS_H + 64 * 32 * 1024;
constexpr unsigned WS_HH = WS_ST + 64 * 16 * 256;
constexpr unsigned WS_END = WS_HH + 64 * NU * 1024;
constexpr int AUX_SC1 = 16;
#define STAMP_DECL
#define FLUSH(k) do {} while (0)
#define STAMP(k, i) do {} while (0)

#define STAMPV(i, v) do {} while (0)
#define SBAR() __builtin_amdgcn_sched_barrier(0)
#define RFL(x) ((unsigned)__builtin_amdgcn_readfirstlane(x))
#define PIN(x) asm volatile("" : "+v"(x))
#define MFMA(a, b, c) __builtin_amdgcn_mfma_f32_32x32x16_bf16((a), (b), (c), 0, 0, 0)
#define WAITV(n) asm volatile("s_waitcnt vmcnt(" #n ")" ::: "memory")
#define BAR_L() asm volatile("s_waitcnt lgkmcnt(0)\n\ts_barrier" ::: "memory")
#define BAR_0() asm volatile("s_barrier" ::: "memory")
#define BAR_VL(n) asm volatile("s_waitcnt vmcnt(" #n ") lgkmcnt(0)\n\ts_barrier" ::: "memory")

__device__ __forceinline__ rsrc_t mk_rsrc(const void* p, unsigned bytes) {
    return __builtin_amdgcn_make_buffer_rsrc((void*)p, (short)0, (int)bytes, 0x00020000);
}
template <bool SC1 = false> __device__ __forceinline__ void dma16(rsrc_t r, unsigned voff, unsigned soff, unsigned lds) {
    if constexpr (SC1) asm volatile("s_nop 4\n\ts_mov_b32 m0, %0\n\ts_nop 0\n\tbuffer_load_dwordx4 %1, %2, %3 offen sc1 lds"
                 :: "s"(__builtin_amdgcn_readfirstlane(lds)), "v"(voff), "s"(r), "s"(__builtin_amdgcn_readfirstlane(soff)) : "m0", "memory");
    else asm volatile("s_nop 4\n\ts_mov_b32 m0, %0\n\ts_nop 0\n\tbuffer_load_dwordx4 %1, %2, %3 offen lds"
                 :: "s"(__builtin_amdgcn_readfirstlane(lds)), "v"(voff), "s"(r), "s"(__builtin_amdgcn_readfirstlane(soff)) : "m0", "memory");
}
#define ST128(v, r, voff, soff, aux) do { const u32x4 d_ = (v); __builtin_amdgcn_raw_buffer_store_b128(d_, r, voff, soff, aux); asm volatile("s_nop 1" :: "v"(d_)); } while (0)
__device__ __forceinline__ unsigned pk2(float lo, float hi) {
    f32x2 v = {lo, hi};
    return __builtin_bit_cast(unsigned, __builtin_convertvector(v, bf16x2));
}
__device__ __forceinline__ float dot2(unsigned a, unsigned b, float c) {
    return __builtin_amdgcn_fdot2_f32_bf16(__builtin_bit_cast(bf16x2, a), __builtin_bit_cast(bf16x2, b), c, false);
}
__device__ __forceinline__ float swap_add(float v) {
    auto r = __builtin_amdgcn_permlane32_swap(__float_as_uint(v), __float_as_uint(v), false, false);
    return __uint_as_float(r[0]) + __uint_as_float(r[1]);
}
__device__ __forceinline__ float swap_max(float v) {
    auto r = __builtin_amdgcn_permlane32_swap(__float_as_uint(v), __float_as_uint(v), false, false);
    return __builtin_fmaxf(__uint_as_float(r[0]), __uint_as_float(r[1]));
}
__device__ __forceinline__ s16x8 lds_b128(unsigned a) { return *(const LDSAS s16x8*)(a); }
__device__ __forceinline__ f32x4 lds_f4(unsigned a) { return *(const LDSAS f32x4*)(a); }
__device__ __forceinline__ s16x8 lds_tr2(unsigned a0, unsigned a1) {
    s16x4 lo = __builtin_amdgcn_ds_read_tr16_b64_v4i16((LDSAS s16x4*)(a0));
    s16x4 hi = __builtin_amdgcn_ds_read_tr16_b64_v4i16((LDSAS s16x4*)(a1));
    return __builtin_shufflevector(lo, hi, 0, 1, 2, 3, 4, 5, 6, 7);
}
__device__ __forceinline__ float bflo(unsigned v) { return __uint_as_float(v << 16); }
__device__ __forceinline__ float bfhi(unsigned v) { return __uint_as_float(v & 0xffff0000u); }
constexpr unsigned ONES2 = 0x3f803f80u;
template <int I> struct ic { static constexpr int value = I; constexpr operator int() const { return I; } };
template <int B, int E, class Fn> __device__ __forceinline__ void sfor(Fn&& fn) { if constexpr (B < E) { fn(ic<B>{}); sfor<B + 1, E>(fn); } }

#define K1_X_AUX 2
#define K1_ABL 0
#define CODE_PF 1
#define K1_POST_AT 83
#define DELAY_Q 0
constexpr unsigned K1_W = 65536, K1_FLG = K1_W + 5 * 16384, K1_ST = K1_FLG + 512, K1_CS = K1_ST + 512;
__host__ __device__ constexpr unsigned k1_slot(int s) { return s < 5 ? K1_W + s * 16384 : s == 5 ? K1_W : s < 10 ? (s - 6) * 16384 : K1_W + (s - 9) * 16384; }
__device__ __forceinline__ void lds_spin_ge(unsigned addr, unsigned want) {
#pragma unroll 1
    for (unsigned n = 0; n < (1u << 22); ++n) {
        unsigned v; asm volatile("ds_read_b32 %0, %1\n\ts_waitcnt lgkmcnt(0)" : "=v"(v) : "v"(addr) : "memory");
        if ((unsigned)__builtin_amdgcn_readfirstlane(v) >= want) break;
        __builtin_amdgcn_s_sleep(1);
    }
}
__device__ __forceinline__ void lds_post(unsigned addr, unsigned v) {
    asm volatile("s_waitcnt lgkmcnt(0)\n\tds_write_b32 %0, %1" :: "v"(addr), "v"(v) : "memory");
}
__device__ __forceinline__ void lds_inc(unsigned addr, int lane) {
    asm volatile("s_waitcnt lgkmcnt(0)" ::: "memory");
    if (lane == 0) asm volatile("ds_add_u32 %0, %1" :: "v"(addr), "v"(1u) : "memory");
}
typedef unsigned u32x16 __attribute__((ext_vector_type(16)));
template <int CTRL> __device__ __forceinline__ float dpp_add(float v) {
    return v + __builtin_bit_cast(float, __builtin_amdgcn_update_dpp(0, __builtin_bit_cast(int, v), CTRL, 0xf, 0xf, true));
}

constexpr unsigned K2_LDS = 163840;
constexpr unsigned L_WO = 0, L_GU = 65536;
constexpr unsigned L_TAB = 61440;
constexpr unsigned L_FLAG = 16384 + 12288;
constexpr unsigned SPIN_CAP = 1u << 20;
#define POLL_DELAY_Q 6
#define OUT_AUX 2
#define DN_B0 20
#define ABL_GU 0
#define DELAY_O 0
#define DELAY_H 8
#define DELAY_G 8
constexpr int GU_AW = 8, G2_AW = 12;
#define TAG_T1 "v_max3_u32 v120, v[%c[cr]], v[%c[cr]+1], v[%c[cr]+2]\n\t"
#define TAG_T2 "v_max_u32 v120, v120, v[%c[cr]+3]\n\tv_cmp_eq_u32 vcc, -1, v120\n\t"
#define TAG_RELOAD(RB, AW) "s_add_u32 %[t], %[s0], (tagj>>2)*4096\n\tbuffer_load_dwordx4 v[" #RB "+4*(tagj%%" #AW "):" #RB "+3+4*(tagj%%" #AW ")], %[l16], %[rs], %[t] offen offset:(tagj&3)*1024 sc1\n\t"
#define TAG_T3(RB, AW) \
    "s_cbranch_vccnz 5f\n6:\n\t" \
    ".subsection 1\n5:\n\ts_cmp_eq_u32 %[bud], 0\n\ts_cbranch_scc1 6b\n\ts_sub_u32 %[bud], %[bud], 1\n\t.set tagj, %c[ci]\n\t" TAG_RELOAD(RB, AW) \
    "s_waitcnt vmcnt(0)\n\t" TAG_T1 TAG_T2 "s_cbranch_vccnz 5b\n\t.rept %c[cnt]-1\n\t.set tagj, tagj+1\n\t" TAG_RELOAD(RB, AW) ".endr\n\ts_branch 6b\n\t.subsection 0\n\t"
#define TAG_LOAD "buffer_load_dwordx4 v[%c[lr]:%c[lr]+3], %[l16], %[rs], %[sa] offen offset:%c[lo] sc1\n\t.if %c[lo] == 3072\n\ts_add_u32 %[sa], %[sa], 0x1000\n\t.endif\n\t"

__device__ __forceinline__ unsigned w64_voff(int lane) { return (lane >> 3) * 1024 + (((lane & 7) ^ (((lane >> 4) & 1) << 2)) << 4); }
__device__ __forceinline__ unsigned w64_tr(int lane, int n) {
    const int q = (lane & 15) >> 2, p = lane & 3, blk = (lane >> 4) & 1, h = lane >> 5;
    return (4 * h + q) * 128 + (((4 * n + 2 * blk + (p >> 1)) ^ (((q >> 1) & 1) << 2)) << 4) + 8 * (p & 1);
}

struct G2 { u32x16 r0, r1, r2; u32x8 w0, w1, w2; f32x16 a0, a1; unsigned tb[6], sa, s0, l16, budget; rsrc_t rs;
};
__host__ __device__ constexpr int g2_w(int DN, int n) {
    return !DN ? n * 2048 : n < 15 ? n * 2048 : n < 39 ? 65536 + (n - 15) * 2048 : n < 40 ? 131072 : (n - 40) * 2048;
}
template <int i, int N, int DN, int B0, int B1> __device__ __forceinline__ void g2_steps(G2& g) {
    constexpr int AW = G2_AW, j = i + AW - 1, n = i + 2, sel = n < N ? g2_w(DN, n) >> 16 : 3;
    unsigned tq;
#define G2_RD(P, R) "ds_read_b64_tr_b16 v[%c[nr]+" #R ":%c[nr]+" #R "+1], %[" P "] offset:%c[no]\n\tds_read_b64_tr_b16 v[%c[nr]+" #R "+2:%c[nr]+" #R "+3], %[" P "] offset:%c[no]+1024\n\t"
#define G2_RDS(P0, P1, P2, R) ".if %c[sel] == 0\n\t" G2_RD(P0, R) ".endif\n\t.if %c[sel] == 1\n\t" G2_RD(P1, R) ".endif\n\t.if %c[sel] == 2\n\t" G2_RD(P2, R) ".endif\n\t"
    if constexpr (i == 0)
        asm volatile(".set tagj, 0\n\t.rept 11\n\tbuffer_load_dwordx4 v[128+4*tagj:131+4*tagj], %[l16], %[rs], %[sa] offen offset:(tagj&3)*1024 sc1\n\t"
                     ".if (tagj&3) == 3\n\ts_add_u32 %[sa], %[sa], 0x1000\n\t.endif\n\t.set tagj, tagj+1\n\t.endr\n\t"
                     ".set tagj, 0\n\t.rept 2\n\tds_read_b64_tr_b16 v[96+8*tagj:97+8*tagj], %[b0] offset:tagj*2048\n\tds_read_b64_tr_b16 v[98+8*tagj:99+8*tagj], %[b0] offset:tagj*2048+1024\n\t"
                     "ds_read_b64_tr_b16 v[100+8*tagj:101+8*tagj], %[b1] offset:tagj*2048\n\tds_read_b64_tr_b16 v[102+8*tagj:103+8*tagj], %[b1] offset:tagj*2048+1024\n\t.set tagj, tagj+1\n\t.endr\n\t"
                     "s_waitcnt vmcnt(10) lgkmcnt(4)\n\t" TAG_T1 TAG_T2 TAG_T3(128, 12)
                     : "={v[128:143]}"(g.r0), "={v[144:159]}"(g.r1), "={v[160:175]}"(g.r2), "={v[96:103]}"(g.w0), "={v[104:111]}"(g.w1), "={v[112:119]}"(g.w2), [sa] "+s"(g.sa),
                       "+{v[64:79]}"(g.a0), "+{v[80:95]}"(g.a1), [bud] "+s"(g.budget), [t] "=&s"(tq)
                     : [l16] "v"(g.l16), [rs] "s"(g.rs), [s0] "s"(g.s0), [b0] "v"(g.tb[0]), [b1] "v"(g.tb[1]), [cr] "n"(128), [ci] "n"(0), [cnt] "n"(AW - 1) : "vcc", "scc", "v120", "memory");
    asm volatile("v_mfma_f32_32x32x16_bf16 v[64:79], v[%c[wr]:%c[wr]+3], v[%c[ar]:%c[ar]+3], v[64:79]\n\t"
                 ".if %c[ld]\n\t" TAG_LOAD ".endif\n\t"
                 ".if %c[bar]\n\ts_waitcnt lgkmcnt(0)\n\ts_barrier\n\t.endif\n\t"
                 G2_RDS("b0", "b2", "b4", 0)
                 ".if %c[cnt]\n\ts_waitcnt vmcnt(%c[vn])\n\t" TAG_T1 ".endif\n\t"
                 "v_mfma_f32_32x32x16_bf16 v[80:95], v[%c[wr]+4:%c[wr]+7], v[%c[ar]:%c[ar]+3], v[80:95]\n\t"
                 G2_RDS("b1", "b3", "b5", 4)
                 ".if %c[cnt]\n\t" TAG_T2 "s_waitcnt lgkmcnt(%c[ln])\n\t" TAG_T3(128, 12) ".else\n\ts_nop 15\n\t.endif"
                 : "+{v[64:79]}"(g.a0), "+{v[80:95]}"(g.a1), "+{v[128:143]}"(g.r0), "+{v[144:159]}"(g.r1), "+{v[160:175]}"(g.r2), "+{v[96:103]}"(g.w0), "+{v[104:111]}"(g.w1), "+{v[112:119]}"(g.w2),
                   [sa] "+s"(g.sa), [bud] "+s"(g.budget), [t] "=&s"(tq)
                 : [l16] "v"(g.l16), [rs] "s"(g.rs), [s0] "s"(g.s0), [b0] "v"(g.tb[0]), [b1] "v"(g.tb[1]), [b2] "v"(g.tb[2]), [b3] "v"(g.tb[3]), [b4] "v"(g.tb[4]), [b5] "v"(g.tb[5]),
                   [ld] "n"(j < N ? 1 : 0), [lr] "n"(128 + 4 * (j % AW)), [lo] "n"((j & 3) * 1024), [bar] "n"(i == B0 || i == B1 ? 1 : 0), [sel] "n"(sel),
                   [nr] "n"(96 + 8 * (n % 3)), [no] "n"(n < N ? g2_w(DN, n) & 65535 : 0), [vn] "n"(j < N ? AW - 2 : i + 1 < N ? N - 2 - i : 0), [ln] "n"(n < N ? 4 : 0),
                   [ar] "n"(128 + 4 * (i % AW)), [wr] "n"(96 + 8 * (i % 3)), [cr] "n"(128 + 4 * ((i + 1) % AW)), [ci] "n"(i + 1), [cnt] "n"(j < N ? AW - 1 : N - 1 - i) : "vcc", "scc", "v120", "memory");
    if constexpr (i + 1 < N) g2_steps<i + 1, N, DN, B0, B1>(g);
}
__host__ __device__ constexpr int gu_dma(int s) { return (s >= 16 && s < 24 && !(s & 1)) || (s >= 24 && s < 30); }
__host__ __device__ constexpr int gu_ops(int s) { return (s + GU_AW - 1 < 32 ? 1 : 0) + (s >= 2 && s < 10 ? 1 : 0) + gu_dma(s); }
__host__ __device__ constexpr int gu_younger(int k) {
    int c = 0, s = k - GU_AW + 1;
    if (s < 0) { c = GU_AW - 2 - k; s = 0; } else { c = gu_ops(s) - 1; ++s; }
    for (; s < k; ++s) c += gu_ops(s);
    return c;
}

struct GU { u32x16 r0, r1, st; u32x8 w0, w1, w2; f32x16 a0, a1, a2; unsigned wa[3], sa, s0, sv, ss, l16, budget, dm, ds, dv, dm2, ds2, dm3, ds3; rsrc_t rs, rd; };
template <int i, int E> __device__ __forceinline__ void gu_steps(GU& g) {
    constexpr int AW = GU_AW, j = i + AW - 1, n = i + 1;
    unsigned tq;
#define GU_RD(R, B) "ds_read_b64_tr_b16 v[" R ":" R "+1], %[" B "] offset:%c[no]\n\tds_read_b64_tr_b16 v[" R "+2:" R "+3], %[" B "] offset:%c[no]+768\n\t"
    if constexpr (i == 0)
        asm volatile(".set tagj, 0\n\t.rept 7\n\tbuffer_load_dwordx4 v[140+4*tagj:143+4*tagj], %[l16], %[rs], %[sa] offen offset:(tagj&3)*1024 sc1\n\t"
                     ".if (tagj&3) == 3\n\ts_add_u32 %[sa], %[sa], 0x1000\n\t.endif\n\t.set tagj, tagj+1\n\t.endr\n\t" GU_RD("112", "b0") GU_RD("116", "b1") GU_RD("124", "b2")
                     "s_waitcnt vmcnt(6) lgkmcnt(0)\n\t" TAG_T1 TAG_T2 TAG_T3(140, 8)
                     : "={v[140:155]}"(g.r0), "={v[156:171]}"(g.r1), "={v[172:187]}"(g.st), "={v[112:119]}"(g.w0), "={v[124:131]}"(g.w1), "={v[132:139]}"(g.w2), [sa] "+s"(g.sa),
                       "+{v[64:79]}"(g.a0), "+{v[80:95]}"(g.a1), "+{v[96:111]}"(g.a2), [bud] "+s"(g.budget), [t] "=&s"(tq)
                     : [l16] "v"(g.l16), [rs] "s"(g.rs), [s0] "s"(g.s0), [b0] "v"(g.wa[0]), [b1] "v"(g.wa[1]), [b2] "v"(g.wa[2]), [no] "n"(0), [cr] "n"(140), [ci] "n"(0), [cnt] "n"(AW - 1)
                     : "vcc", "scc", "v120", "memory");
    asm volatile(".if %c[i] == 15 || %c[i] == 23\n\ts_barrier\n\t.endif\n\t"
                 "v_mfma_f32_32x32x16_bf16 v[64:79], v[%c[c0]:%c[c0]+3], v[%c[ar]:%c[ar]+3], v[64:79]\n\t"
                 ".if %c[rd]\n\t" GU_RD("%c[n0]", "b0") GU_RD("%c[n1]", "b1") ".endif\n\t"
                 "v_mfma_f32_32x32x16_bf16 v[80:95], v[%c[c1]:%c[c1]+3], v[%c[ar]:%c[ar]+3], v[80:95]\n\t"
                 ".if %c[rd]\n\t" GU_RD("%c[n2]", "b2") ".endif\n\t"
                 ".if %c[ld]\n\t" TAG_LOAD ".endif\n\t"
                 ".if %c[sk] >= 0\n\tbuffer_load_dwordx2 v[172+2*%c[sk]:173+2*%c[sk]], %[sv], %[rs], %[ss] offen offset:%c[sk]*256 sc1\n\t.endif\n\t"
                 ".if %c[i] == 26\n\ts_mov_b32 %[dm], %[dm2]\n\ts_mov_b32 %[ds], %[ds2]\n\t.endif\n\t.if %c[i] == 29\n\ts_mov_b32 %[dm], %[dm3]\n\ts_mov_b32 %[ds], %[ds3]\n\t.endif\n\t"
                 ".if %c[dma]\n\ts_mov_b32 m0, %[dm]\n\ts_add_u32 %[dm], %[dm], 0x400\n\tbuffer_load_dwordx4 %[dv], %[rwd], %[ds] offen lds\n\ts_add_u32 %[ds], %[ds], 0x2000\n\t.endif\n\t"
                 "v_mfma_f32_32x32x16_bf16 v[96:111], v[%c[c2]:%c[c2]+3], v[%c[ar]:%c[ar]+3], v[96:111]\n\t"
                 ".if %c[cnt]\n\ts_waitcnt vmcnt(%c[vn])\n\t" TAG_T1 TAG_T2 "s_waitcnt lgkmcnt(0)\n\t" TAG_T3(140, 8) ".endif"
                 : "+{v[64:79]}"(g.a0), "+{v[80:95]}"(g.a1), "+{v[96:111]}"(g.a2), "+{v[140:155]}"(g.r0), "+{v[156:171]}"(g.r1), "+{v[172:187]}"(g.st),
                   "+{v[112:119]}"(g.w0), "+{v[124:131]}"(g.w1), "+{v[132:139]}"(g.w2), [sa] "+s"(g.sa), [bud] "+s"(g.budget), [t] "=&s"(tq), [dm] "+s"(g.dm), [ds] "+s"(g.ds)
                 : [l16] "v"(g.l16), [rs] "s"(g.rs), [s0] "s"(g.s0), [dv] "v"(g.dv), [rwd] "s"(g.rd), [dm2] "s"(g.dm2), [ds2] "s"(g.ds2), [dm3] "s"(g.dm3), [ds3] "s"(g.ds3),
                   [dma] "n"(gu_dma(i)), [sv] "v"(g.sv), [ss] "s"(g.ss), [b0] "v"(g.wa[0]), [b1] "v"(g.wa[1]), [b2] "v"(g.wa[2]),
                   [ld] "n"(j < 32 ? 1 : 0), [lr] "n"(140 + 4 * (j % AW)), [lo] "n"((j & 3) * 1024), [sk] "n"(i >= 2 && i < 10 ? i - 2 : -1), [rd] "n"(n < 32 ? 1 : 0), [no] "n"(n * 1536),
                   [n0] "n"(n & 1 ? 128 : 112), [n1] "n"(n & 1 ? 132 : 116), [n2] "n"(n & 1 ? 136 : 124), [c0] "n"(i & 1 ? 128 : 112), [c1] "n"(i & 1 ? 132 : 116), [c2] "n"(i & 1 ? 136 : 124),
                   [vn] "n"(n < 32 ? gu_younger(n) : 0), [ar] "n"(140 + 4 * (i % AW)), [i] "n"(i), [cr] "n"(140 + 4 * (n % AW)), [ci] "n"(n), [cnt] "n"(j < 32 ? AW - 1 : 31 - i)
                 : "vcc", "scc", "m0", "v120", "memory");
    if constexpr (i + 1 < E) gu_steps<i + 1, E>(g);
}

__global__ __launch_bounds__(512, 2) void k_block(const u16* __restrict__ x, const u16* __restrict__ wqkv, const u16* wo, const u16* wg,
                                                  const u16* wu, const u16* wd, u16* out, char* __restrict__ ws) {
    extern __shared__ __attribute__((aligned(16))) char smem[];
    const unsigned lds0 = (unsigned)(uintptr_t)(LDSAS char*)smem;
    const int tid = threadIdx.x, lane = tid & 63;
    const int w = __builtin_amdgcn_readfirstlane(tid >> 6);
    const int b = blockIdx.x, xcd = b & 7, bi = b >> 3;
    const int r32 = lane & 31, h = lane >> 5;
    const rsrc_t rs = mk_rsrc(ws, WS_END);
    const int wt = w & 1, kh = (w >> 1) & 1;
    STAMP_DECL;
    unsigned ep;
#define EP_LOAD() do { asm volatile("s_load_dword %0, %1, 0\n\ts_waitcnt lgkmcnt(0)" : "=s"(ep) : "s"(ws) : "memory"); ep += 1u; } while (0)
    asm volatile("" : "+s"(wo), "+s"(wg), "+s"(wu), "+s"(wd), "+s"(out));
    const int head = xcd * 4 + (bi >> 3);
    const unsigned l16 = lane * 16;
    const int rt2 = xcd * 4 + (bi >> 3), ct = bi & 7;
    {
    const int rt = (xcd >> 1) * 8 + (bi & 7), cb = (xcd & 1) * 4 + (bi >> 3), rot = bi & 3;
    const int wc = (w >> 1) & 1, hw = w - 4;
    STAMP(0, 0);
    const rsrc_t rw = mk_rsrc(wqkv, D * 3 * D * 2);
    const unsigned wv = (lane >> 3) * 3072 + (((lane & 7) ^ (((lane >> 4) & 1) << 2)) << 4);
    const unsigned fa = lds0 + K1_FLG;
    if (tid < 12) *(LDSAS unsigned*)(fa + tid * 4) = 0u;
    if (w >= 4) {
        BAR_L();
        const unsigned pa = fa + hw * 4;
        auto issue = [&](int s) {
            const unsigned so = RFL(((((s & 3) + rot) & 3) * 128 + hw * 32) * 3072 + (((s >> 2) + 1) % 3) * 1024 + cb * 128);
            asm volatile("s_nop 4" :: "s"(so));
#pragma unroll
            for (int i = 0; i < 4; ++i)
                asm volatile("s_mov_b32 m0, %0\n\ts_nop 0\n\tbuffer_load_dwordx4 %1, %2, %3 offen lds"
                             :: "s"(lds0 + k1_slot(s) + (4 * hw + i) * 1024), "v"(wv), "s"(rw), "s"(so + i * 8 * 3072) : "m0", "memory");
        };
#define K1_LAND(N, s) asm volatile("s_waitcnt vmcnt(" #N ")\n\tds_write_b32 %0, %1" :: "v"(pa), "v"((s) + 1u) : "memory")
        issue(0); issue(1);
        issue(2); K1_LAND(8, 0);
        issue(3); K1_LAND(8, 1);
        issue(4); K1_LAND(8, 2); K1_LAND(4, 3); K1_LAND(0, 4);
        EP_LOAD();
        lds_spin_ge(fa + 28, 4);
        issue(5);
        lds_spin_ge(fa + 20, 4);
        issue(6); issue(7); K1_LAND(8, 5);
        issue(8); K1_LAND(8, 6);
        issue(9); K1_LAND(8, 7);
        lds_spin_ge(fa + 24, 4);
        issue(10); K1_LAND(8, 8);
        issue(11); K1_LAND(8, 9);
        K1_LAND(4, 10); K1_LAND(0, 11);
        if (w > 4) {
            unsigned long long pc; unsigned len, o = bi * 128 + (w - 5) * 24576, t, d0, d1, d2, d3;
#define KCP(D) "s_min_u32 %[t], %[o], %[len]\n\ts_load_dword " D ", %[pc], %[t]\n\ts_add_u32 %[o], %[o], 0x1000\n\t"
            asm volatile("s_getpc_b64 %[pc]\n.Lqkv_pc:\n\ts_mov_b32 %[len], .Lmain_end-.Lqkv_pc-4\n\t" KCP("%[d0]") KCP("%[d1]") KCP("%[d2]") KCP("%[d3]") KCP("%[d0]") KCP("%[d1]") "s_waitcnt lgkmcnt(0)"
                         : [pc] "=&s"(pc), [len] "=&s"(len), [o] "+s"(o), [t] "=&s"(t), [d0] "=&s"(d0), [d1] "=&s"(d1), [d2] "=&s"(d2), [d3] "=&s"(d3) :: "scc", "memory");
        }
        STAMP(0, 1);
    } else {
    {
        const rsrc_t rx = mk_rsrc(x, S * D * 2);
        u32x4 r[2][8];
#pragma unroll
        for (int p = 0; p < 2; ++p)
#pragma unroll
            for (int g = 0; g < 8; ++g)
                r[p][g] = __builtin_amdgcn_raw_buffer_load_b128(rx, (lane >> 3) * 1024 + (((lane & 7) ^ ((4 * p + (lane >> 4)) & 7)) << 4) + g * 128, (rt * 64 + w * 16 + p * 8) * 1024, K1_X_AUX);
        SBAR(); BAR_L();
        EP_LOAD();
        SBAR();
#pragma unroll
        for (int p = 0; p < 2; ++p) {
            float s0 = 0.f, s1 = 0.f, q0 = 0.f, q1 = 0.f;
#pragma unroll
            for (int g = 0; g < 8; ++g) { s0 = dot2(r[p][g][0], ONES2, s0); q0 = dot2(r[p][g][0], r[p][g][0], q0); s1 = dot2(r[p][g][1], ONES2, s1); q1 = dot2(r[p][g][1], r[p][g][1], q1);
                                          s0 = dot2(r[p][g][2], ONES2, s0); q0 = dot2(r[p][g][2], r[p][g][2], q0); s1 = dot2(r[p][g][3], ONES2, s1); q1 = dot2(r[p][g][3], r[p][g][3], q1); }
            if (p == 0) STAMP(0, 1);
            float sum = s0 + s1, sq = q0 + q1;
            sum = dpp_add<0xB1>(sum); sq = dpp_add<0xB1>(sq); sum = dpp_add<0x4E>(sum); sq = dpp_add<0x4E>(sq); sum = dpp_add<0x141>(sum); sq = dpp_add<0x141>(sq);
            const float mean = sum * (1.f / D);
            const float rstd = __builtin_amdgcn_rsqf(__builtin_fmaxf(sq * (1.f / D) - mean * mean, 0.f) + LN_EPS), nb = -mean * rstd;
#pragma unroll
            for (int g = 0; g < 8; g += 2) {
                f32x2 t[8];
#pragma unroll
                for (int i = 0; i < 8; ++i) { const unsigned v = r[p][g + (i >> 2)][i & 3]; t[i] = f32x2{bflo(v), bfhi(v)}; }
                asm volatile("" : "+v"(t[0]), "+v"(t[1]), "+v"(t[2]), "+v"(t[3]), "+v"(t[4]), "+v"(t[5]), "+v"(t[6]), "+v"(t[7]));
#pragma unroll
                for (int i = 0; i < 8; ++i) t[i] = t[i] * rstd + nb;
                asm volatile("" : "+v"(t[0]), "+v"(t[1]), "+v"(t[2]), "+v"(t[3]), "+v"(t[4]), "+v"(t[5]), "+v"(t[6]), "+v"(t[7]));
                u32x4 y0, y1;
#pragma unroll
                for (int i = 0; i < 4; ++i) { y0[i] = pk2(t[i][0], t[i][1]); y1[i] = pk2(t[4 + i][0], t[4 + i][1]); }
                *(LDSAS u32x4*)(lds0 + (2 * w + p) * 1024 + lane * 16 + g * 8192) = y0;
                *(LDSAS u32x4*)(lds0 + (2 * w + p) * 1024 + lane * 16 + g * 8192 + 8192) = y1;
            }
            SBAR();
        }
    }
    lds_inc(fa + 16, lane);
    STAMP(0, 3);
    u32x16 xa0, xa1, xa2, xa3, xa4, xa5, xa6, xa7;
    u32x4 w0, w1, w2, w3, w4, fl;
    f32x16 acA, acB;
    unsigned xb[4], tq;
#pragma unroll
    for (int m = 0; m < 4; ++m) xb[m] = lds0 + (32 * wt + r32) * 128 + (((2 * m + h) ^ ((r32 >> 1) & 7)) << 4) + rot * 16384;
    const int q4 = (lane & 15) >> 2, p4 = lane & 3, blk = (lane >> 4) & 1;
    const unsigned tA = lds0 + (8 * h + q4) * 128 + (((4 * wc + 2 * blk + (p4 >> 1)) ^ (((q4 >> 1) & 1) << 2)) << 4) + 8 * (p4 & 1);
    const unsigned tB = tA + 65536, tC = tA + 131072;
    const int tt = rt * 2 + wt, hd = cb * 4 + wc * 2;
#define KXR(R, m, O) "ds_read_b128 v[" #R ":" #R "+3], %[x" #m "] offset:" #O "\n\t"
#define KXN(m) "v_add_u32 %[x" #m "], 0x4000, %[x" #m "]\n\tv_and_b32 %[x" #m "], 0xffff, %[x" #m "]\n\t"
#define KCHK(ST) "v_min3_u32 v116, v116, v117, v118\n\tv_min_u32 v116, v116, v119\n\ts_nop 0\n\tv_readfirstlane_b32 %[t], v116\n\ts_nop 0\n\ts_cmp_gt_u32 %[t], " ST "\n\t"
#define KSPIN(ST) "1:\n\tds_read_b128 v[116:119], %[fa]\n\ts_waitcnt lgkmcnt(0)\n\t" KCHK(ST) "s_cbranch_scc0 1b\n\t"
#define KWR(R, O) "ds_read_b64_tr_b16 v[" #R ":" #R "+1], %[tb] offset:" #O "\n\tds_read_b64_tr_b16 v[" #R "+2:" #R "+3], %[tb] offset:" #O "+512\n\t"
    asm volatile("3:\n\tds_read_b32 v116, %[fa] offset:16\n\ts_waitcnt lgkmcnt(0)\n\tv_readfirstlane_b32 %[t], v116\n\ts_nop 0\n\ts_cmp_gt_u32 %[t], 3\n\ts_cbranch_scc0 3b\n\t"
                 KXR(128, 0, 0) KXR(132, 1, 0) KXR(136, 2, 0) KXR(140, 3, 0) KXR(144, 0, 8192) KXR(148, 1, 8192) KXR(152, 2, 8192) KXR(156, 3, 8192) KXN(0) KXN(1) KXN(2) KXN(3)
                 KSPIN("0") KWR(96, 0) KWR(100, 2048) KWR(104, 4096) KWR(108, 6144)
                 : "={v[116:119]}"(fl), [t] "=&s"(tq), "={v[96:99]}"(w0), "={v[100:103]}"(w1), "={v[104:107]}"(w2), "={v[108:111]}"(w3), "={v[112:115]}"(w4), "={v[64:79]}"(acA), "={v[80:95]}"(acB),
                   "={v[128:143]}"(xa0), "={v[144:159]}"(xa1), "={v[160:175]}"(xa2), "={v[176:191]}"(xa3), "={v[192:207]}"(xa4), "={v[208:223]}"(xa5), "={v[224:239]}"(xa6), "={v[240:255]}"(xa7),
                   [x0] "+v"(xb[0]), [x1] "+v"(xb[1]), [x2] "+v"(xb[2]), [x3] "+v"(xb[3])
                 : [fa] "v"(fa), [tb] "v"(tB) : "scc", "memory");
    STAMP(0, 4);
    auto epi = [&](int m, const f32x16& acc, int hh) {
        const float sc = 0.25f * LOG2E;
        u32x4 o;
#pragma unroll
        for (int i = 0; i < 4; ++i) o[i] = m == 2 ? pk2(acc[8 * hh + 2 * i] * sc, acc[8 * hh + 2 * i + 1] * sc) : pk2(acc[8 * hh + 2 * i], acc[8 * hh + 2 * i + 1]);
        if (m == 2) ST128(o, rs, l16, WS_Q + ((hd + hh) * 64 + tt) * 1024, AUX_SC1);
        else if (m == 0) ST128(o, rs, l16, WS_KV + (hd + hh) * KV_HEAD + (tt >> 3) * KV_CHUNK + (tt & 7) * 1024, AUX_SC1);
        else ST128(o, rs, (r32 >> 4) * KV_HEAD + (16 * h + (lane & 15)) * 16 + hh * 512,
                   WS_KV + hd * KV_HEAD + (tt >> 3) * KV_CHUNK + 8192 + (tt & 7) * 1024, AUX_SC1);
    };
    sfor<0, 96>([&](auto G) {
        constexpr int g = G.value, n = g + 4, m = g >> 5;
        constexpr unsigned o = k1_slot(n >> 3) + (n & 7) * 2048, oi = o & 65535;
        constexpr int aR = m == 1 ? 80 : 64, wR = 96 + 4 * (g % 5), nR = 96 + 4 * (n % 5), xR = 128 + 4 * (g & 31);
        constexpr int nx = (g - 4 >= 0 && g - 4 < 12) + (g - 3 >= 0 && g - 3 < 12) + (g - 2 >= 0 && g - 2 < 12) + (g - 1 >= 0 && g - 1 < 12), wc_ = n >= 96 ? 0 : (8 + 2 * nx > 14 ? 14 : 8 + 2 * nx);
#define KRD(B) "ds_read_b64_tr_b16 v[%c[nr]:%c[nr]+1], %[" B "] offset:%c[oi]\n\tds_read_b64_tr_b16 v[%c[nr]+2:%c[nr]+3], %[" B "] offset:%c[oi]+512\n\t"
        asm volatile(".if %c[bar] == 1\n\t" KCHK("%c[st]") "s_cbranch_scc0 1f\n2:\n\t.subsection 1\n" KSPIN("%c[st]") "s_branch 2b\n\t.subsection 0\n\t.endif\n\t"
                     ".if %c[bar] == 2\n\tds_read_b128 v[116:119], %[fa]\n\t.endif\n\t"
                     ".if %c[rd] == 1\n\t" KRD("tb") ".endif\n\t.if %c[rd] == 2\n\t" KRD("tc") ".endif\n\t.if %c[rd] == 3\n\t" KRD("ta") ".endif\n\t"
                     "s_waitcnt lgkmcnt(%c[wn])\n\t"
                     ".if %c[md] == 0\n\tv_mfma_f32_32x32x16_bf16 v[%c[ar]:%c[ar]+15], v[%c[wr]:%c[wr]+3], v[%c[xr]:%c[xr]+3], v[%c[ar]:%c[ar]+15]\n\t.endif\n\t"
                     ".if %c[md] == 1\n\tv_mfma_f32_32x32x16_bf16 v[%c[ar]:%c[ar]+15], v[%c[xr]:%c[xr]+3], v[%c[wr]:%c[wr]+3], v[%c[ar]:%c[ar]+15]\n\t.endif\n\t"
                     ".if %c[md] == 2\n\tv_mfma_f32_32x32x16_bf16 v[%c[ar]:%c[ar]+15], v[%c[xr]:%c[xr]+3], v[%c[wr]:%c[wr]+3], 0\n\t.endif\n\t"
                     ".if %c[md] == 3\n\tv_mfma_f32_32x32x16_bf16 v[%c[ar]:%c[ar]+15], v[%c[wr]:%c[wr]+3], v[%c[xr]:%c[xr]+3], 0\n\t.endif\n\t"
                     ".if %c[xm] == 1\n\tds_read_b128 v[%c[xd]:%c[xd]+3], %[x0] offset:%c[xo]\n\tds_read_b128 v[%c[xd]+4:%c[xd]+7], %[x1] offset:%c[xo]\n\t.endif\n\t"
                     ".if %c[xm] == 2\n\tds_read_b128 v[%c[xd]:%c[xd]+3], %[x2] offset:%c[xo]\n\tds_read_b128 v[%c[xd]+4:%c[xd]+7], %[x3] offset:%c[xo]\n\t.endif\n\t"
                     ".if %c[xn]\n\t" KXN(0) KXN(1) KXN(2) KXN(3) ".endif"
                     : "+{v[64:79]}"(acA), "+{v[80:95]}"(acB), "+{v[96:99]}"(w0), "+{v[100:103]}"(w1), "+{v[104:107]}"(w2), "+{v[108:111]}"(w3), "+{v[112:115]}"(w4),
                       "+{v[128:143]}"(xa0), "+{v[144:159]}"(xa1), "+{v[160:175]}"(xa2), "+{v[176:191]}"(xa3), "+{v[192:207]}"(xa4), "+{v[208:223]}"(xa5), "+{v[224:239]}"(xa6), "+{v[240:255]}"(xa7),
                       "+{v[116:119]}"(fl), [t] "=&s"(tq), [x0] "+v"(xb[0]), [x1] "+v"(xb[1]), [x2] "+v"(xb[2]), [x3] "+v"(xb[3])
                     : [ta] "v"(tA), [tb] "v"(tB), [tc] "v"(tC), [fa] "v"(fa), [st] "n"(n >> 3), [wn] "n"(wc_),
                       [bar] "n"(K1_ABL ? 0 : n < 96 && (n & 7) == 0 ? 1 : n < 92 && (n & 7) == 4 ? 2 : 0), [rd] "n"(n >= 96 ? 0 : o < 65536 ? 3 : o < 131072 ? 1 : 2), [oi] "n"(oi), [nr] "n"(nR),
                       [md] "n"(m != 1 ? ((g & 31) == 0 ? 3 : 0) : g == 32 ? 2 : 1), [ar] "n"(aR), [wr] "n"(wR), [xr] "n"(xR),
                       [xm] "n"(g < 12 ? 1 + (g & 1) : 0), [xd] "n"(160 + 8 * g), [xo] "n"(((g >> 1) & 1) * 8192), [xn] "n"(g < 11 && (g & 3) == 3 ? 1 : 0) : "scc", "memory");
        if (g == 7 && lane == 0) asm volatile("ds_add_u32 %0, %1" :: "v"(fa + 28), "v"(1u) : "memory");
        if (g == 16 && lane == 0) asm volatile("ds_add_u32 %0, %1" :: "v"(fa + 20), "v"(1u) : "memory");
        if (g == 23 && lane == 0) asm volatile("ds_add_u32 %0, %1" :: "v"(fa + 24), "v"(1u) : "memory");
        if (g == 2 || g == 10) ST128((u32x4{~0u, ~0u, ~0u, ~0u}), rs, l16, WS_Q + ((hd + (g >> 3)) * 64 + tt) * 1024, AUX_SC1);
        if (g == 36 || g == 38) epi(0, acA, (g - 36) >> 1);
        if (g == 67 || g == 68) epi(1, acB, g - 67);
        if (g == K1_POST_AT) {
            WAITV(0);
            __builtin_amdgcn_raw_buffer_store_b32(ep, rs, (lane * 4) | ((unsigned)(31 - lane) & 0x80000000u), WS_FQ + ((cb * 2 + wc) * 64 + tt) * 128, AUX_SC1);
            if (lane == 0) asm volatile("ds_add_u32 %0, %1" :: "v"(fa + 32), "v"(1u) : "memory");
        }
    });
    STAMP(0, 2);
    asm volatile("s_nop 15" : "+{v[64:79]}"(acA));
    epi(2, acA, 0); epi(2, acA, 1);
    if (lane == 0) asm volatile("ds_add_u32 %0, %1" :: "v"(fa + 40), "v"(1u) : "memory");
    STAMP(0, 5);
    }
    }
    SBAR();
    const int rt = rt2;
    u32x8 xres = {};
    u32x4 hreg[2];

    auto issue_wo = [&](int first, int n) {
        const rsrc_t rwo = mk_rsrc(wo, D * D * 2);
        const unsigned v = w64_voff(lane);
        for (int p = first; p < first + n; ++p) dma16(rwo, v, p * 8192 + ct * 128, lds0 + L_WO + p * 1024);
    };
    auto issue_dn = [&](int i0, int n, unsigned dst, int hw) {
        const rsrc_t rwd = mk_rsrc(wd, F * D * 2);
        const unsigned v = w64_voff(lane);
        const unsigned so = ((43 * (hw >> 1) + i0) * 16 + (hw & 1) * 8) * 1024 + ct * 128;
        for (int i = 0; i < n; ++i) dma16(rwd, v, so + i * 16384, lds0 + dst + (hw & 1) * 1024 + i * 2048);
    };
    auto gu_wa = [&](int n) -> unsigned { const unsigned blk = (lane >> 4) & 1;
        return lds0 + L_GU + blk * 49152 + (((4 * h + ((lane & 15) >> 2)) * 96 + n * 32 + (lane & 3) * 8) ^ (blk * 128)); };
    float cs[3] = {0.f, 0.f, 0.f};
    auto gu_colsum = [&]() {
        u32x4 f[3][4];
#pragma unroll
        for (int n = 0; n < 3; ++n) {
            const unsigned a0 = gu_wa(n) + w * 4 * 1536;
#pragma unroll
            for (int i = 0; i < 4; ++i) f[n][i] = __builtin_bit_cast(u32x4, lds_tr2(a0 + i * 1536, a0 + i * 1536 + 768));
        }
        SBAR();
#pragma unroll
        for (int i = 0; i < 4; ++i)
#pragma unroll
            for (int e = 0; e < 4; ++e)
#pragma unroll
                for (int n = 0; n < 3; ++n) cs[n] = dot2(f[n][i][e], ONES2, cs[n]);
#pragma unroll
        for (int n = 0; n < 3; ++n) cs[n] = swap_add(cs[n]);
    };

    unsigned budget = 1u << 16;

    {
        const int qb = bi & 7, wq = w & 3, g = w >> 2, qt = qb * 8 + wq * 2;
        const unsigned kvs = WS_KV + head * KV_HEAD;
        unsigned ks = kvs + g * 4 * KV_CHUNK + wq * 4096, km = lds0 + g * 32768 + wq * 4096;
        const rsrc_t rg = mk_rsrc(g ? wu : wg, (bi < 29 && ABL_GU != 1) ? D * F * 2 : 0), rwd = mk_rsrc(wd, F * D * 2), rwo = mk_rsrc(wo, D * D * 2), rx = mk_rsrc(x, S * D * 2);
        unsigned gs = wq * 4 * 32 * (F * 2) + bi * 96, gm = lds0 + L_GU + g * 49152 + wq * 12288;
        const unsigned long long msk = 0x0000ffff0000ffffull;
        constexpr unsigned OOB = 0x80000000u, SOOB = 0x40000000u;
        auto gu_voff = [&](unsigned c) { const unsigned r = __umul24(c, 171u) >> 10; return __umul24(r, F * 2 - 96u) + c * 16; };
        const unsigned gv0 = gu_voff(lane ^ (g * 8)), gv1 = gu_voff((64 + lane) ^ (g * 8)), gv2 = gu_voff((128 + lane) ^ (g * 8));
        const unsigned pvd = (lane * 128) | ((unsigned)(42 - lane) & OOB), pvo = (lane * 128) | ((unsigned)(15 - lane) & OOB), psd = RFL((bi * 344 + w * 43) * 128), pso = RFL((bi * 128 + w * 16) * 128);
        const unsigned wv = w64_voff(lane), xv = r32 * 1024 + 8 * h, xs = RFL((rt * 64 + wt * 32) * 1024 + (ct * 64 + kh * 32) * 2);
        const unsigned kb = lds0 + g * 32768 + l16, vb = lds0 + g * 32768 + 8192 + (16 * h + (r32 & 15)) * 16;
        const unsigned cst = (r32 & 27) == 16 ? ONES2 : 0u;
        const unsigned fsv = h * 4096 + r32 * 16, fs = RFL(w == 0 ? WS_ST + (rt * 32 + ct * 2) * 256 : SOOB), fpv = (unsigned)(0 - lane) & OOB, fps = RFL(w == 0 ? WS_FP + b * 4 : SOOB);
        u32x4 ones = {~0u, ~0u, ~0u, ~0u}, fpl = {0u, 0u, 0u, 0u}, pA = fpl, pB = fpl;
        unsigned pfd = 0, epv = ep;
        f32x16 sB = {}, o0 = {}, o1 = {};
        s16x8 v1 = __builtin_bit_cast(s16x8, u32x4{cst, cst, cst, cst}), v1b = v1;
        const unsigned fo = WS_O + ((qt + g) * 32 + head) * 1024, fh = WS_H + ((rt * 2 + wt) * 32 + ct * 4 + (w >> 1)) * 1024, fg = RFL(bi < 29 ? WS_HH + ((xcd * 8 + w) * NU + bi * 3) * 1024 : SOOB), fg2 = RFL(bi < 28 ? fg : SOOB);
        STAMP(0, 6);
        SBAR();
#define XKV "s_mov_b32 m0, %[km]\n\ts_add_u32 %[km], %[km], 0x400\n\tbuffer_load_dwordx4 %[l16], %[rs], %[ks] offen sc1 lds\n\ts_add_u32 %[ks], %[ks], 0x400\n\t"
        s16x8 q0, q1;
        unsigned lm = RFL(lds0 + ((w >> 1) & 1) * 32768 + (w & 1) * 8192), ls = RFL(kvs + ((w >> 1) & 1) * 4 * KV_CHUNK + (w & 1) * 8192), tq;
        km += 16384; ks += KV_CHUNK;
#define XSPIN(L, O, N) #L ":\n\ts_sleep 1\n\tds_read_b32 v132, %[fa] offset:" #O "\n\ts_sub_u32 %[cap], %[cap], 1\n\ts_cbranch_scc1 9f\n\ts_waitcnt lgkmcnt(0)\n\tv_readfirstlane_b32 %[t], v132\n\ts_nop 0\n\ts_cmp_lt_u32 %[t], " #N "\n\ts_cbranch_scc1 " #L "b\n\t"
#define XLD "s_mov_b32 m0, %[lm]\n\ts_add_u32 %[lm], %[lm], 0x400\n\tbuffer_load_dwordx4 %[l16], %[rs], %[ls] offen sc1 lds\n\ts_add_u32 %[ls], %[ls], 0x400\n\t"
        unsigned cap = 1u << 22;
        asm volatile("s_cmp_lt_u32 %[w], 4\n\ts_cbranch_scc1 8f\n\t" XSPIN(4, 32, 4)
                     "s_cmp_eq_u32 %[w], 4\n\ts_cbranch_scc0 6f\n\ts_sleep %c[pd]\n"
                     "5:\n\tbuffer_load_dword v132, %[pvf], %[rs], %[fq] offen sc1\n\ts_sub_u32 %[cap], %[cap], 1\n\ts_cbranch_scc1 9f\n\ts_waitcnt vmcnt(0)\n\tv_cmp_ne_u32 vcc, v201, v132\n\ts_cbranch_vccnz 5b\n\t"
                     "ds_add_u32 %[fa], v204 offset:36\n\ts_branch 7f\n" XSPIN(6, 36, 1)
                     "7:\n\t" XLD XLD XLD XLD XLD XLD XLD XLD XSPIN(4, 40, 4)
                     "9:\n8:\n\ts_sleep %c[dl]\n\tbuffer_load_dwordx4 v[128:131], %[l16], %[rs], %[qs] offen sc1\n\tbuffer_load_dwordx4 v[156:159], %[l16], %[rs], %[qs1] offen sc1\n"
                     "1:\n\ts_waitcnt vmcnt(0)\n\tv_max3_u32 v132, v128, v129, v130\n\tv_max3_u32 v132, v132, v131, v156\n\tv_max3_u32 v132, v132, v157, v158\n\tv_max_u32 v132, v132, v159\n\t"
                     "v_cmp_eq_u32 vcc, -1, v132\n\ts_cbranch_vccnz 3f\n2:\n\t.subsection 1\n3:\n\ts_cmp_eq_u32 %[bud], 0\n\ts_cbranch_scc1 2b\n\ts_sub_u32 %[bud], %[bud], 1\n\t"
                     "buffer_load_dwordx4 v[128:131], %[l16], %[rs], %[qs] offen sc1\n\tbuffer_load_dwordx4 v[156:159], %[l16], %[rs], %[qs1] offen sc1\n\ts_branch 1b\n\t.subsection 0\n\t"
                     "s_waitcnt lgkmcnt(0)\n\ts_barrier\n\t" XKV XKV XKV XKV
                     : "={v[128:131]}"(q0), "={v[156:159]}"(q1), [km] "+s"(km), [ks] "+s"(ks), [bud] "+s"(budget), [lm] "+s"(lm), [ls] "+s"(ls), [cap] "+s"(cap), [t] "=&s"(tq),
                       "+{v[80:95]}"(sB), "+{v[96:111]}"(o0), "+{v[160:175]}"(o1),
                       "+{v[144:147]}"(v1), "+{v[136:139]}"(v1b), "+{v[148:151]}"(pA), "+{v[152:155]}"(pB), "+{v[192:199]}"(xres), "+{v200}"(pfd), "+{v201}"(epv), "+{v[204:207]}"(ones), "+{v[208:211]}"(fpl)
                     : [l16] "v"(l16), [rs] "s"(rs), [qs] "s"(RFL(WS_Q + (head * 64 + qt + g) * 1024)), [qs1] "s"(RFL(WS_Q + (head * 64 + qt + 1 - g) * 1024)), [w] "s"(w),
                       [fa] "v"(lds0 + K1_FLG), [pvf] "v"(lane * 128), [fq] "s"(RFL(WS_FQ + (head >> 1) * 8192)), [pd] "n"(POLL_DELAY_Q), [dl] "n"(DELAY_Q),
                       "v"(gv0), "v"(gv1), "v"(gv2), "v"(pvd), "v"(pvo), "v"(wv), "v"(xv), "v"(kb), "v"(vb), "v"(fsv), "v"(fpv),
                       "s"(rg), "s"(rwd), "s"(rwo), "s"(rx), "s"(gs), "s"(gm), "s"(psd), "s"(pso), "s"(xs), "s"(fs), "s"(fps), "s"(fo), "s"(fh), "s"(fg), "s"(fg2)
                     : "m0", "scc", "vcc", "v132", "memory");
        STAMP(0, 7);
        FLUSH(0);
        km = RFL(km) - (16384 + 4096); ks = RFL(ks) + KV_CHUNK - 4096; budget = RFL(budget);
        STAMP(1, 0);
        STAMP(1, 1);
        f32x16 n0, n1, sA;
        s16x8 kf = lds_b128(kb), v0;
        {
            const f32x16 z = {};
            const s16x8 kz = lds_b128(lds0 + l16);
            const u32x4 vv = __builtin_bit_cast(u32x4, lds_b128(vb)), cc = {cst, cst, cst, cst};
            const f32x16 s0 = MFMA(kz, q0, z), s1 = MFMA(kz, q1, z);
            float m0 = __builtin_fmaxf(s0[0], s0[1]), m1 = __builtin_fmaxf(s1[0], s1[1]);
#pragma unroll
            for (int i = 2; i < 16; i += 2) { m0 = __builtin_fmaxf(__builtin_fmaxf(m0, s0[i]), s0[i + 1]); m1 = __builtin_fmaxf(__builtin_fmaxf(m1, s1[i]), s1[i + 1]); }
            m0 = swap_max(m0); m1 = swap_max(m1);
#pragma unroll
            for (int i = 0; i < 16; ++i) { n0[i] = -m0; n1[i] = -m1; }
            sA = MFMA(kf, q0, n0);
            v0 = __builtin_bit_cast(s16x8, r32 < 16 ? vv : cc);
        }
#define XE(C, i) "v_exp_f32 v[" #C "+" #i "], v[" #C "+" #i "]\n\t"
#define XC(P, C, i) "v_cvt_pk_bf16_f32 v" #P ", v[" #C "+" #i "], v[" #C "+" #i "+1]\n\t"
#define XG1(C) XE(C, 0) XE(C, 1) XE(C, 2) XE(C, 3) XC(148, C, 0) XE(C, 4) XE(C, 5) XC(149, C, 2)
#define XG2(C) XE(C, 6) XE(C, 7) XC(150, C, 4) XE(C, 8) XC(151, C, 6) XE(C, 9) XE(C, 10)
#define XG3(C) XE(C, 11) XC(152, C, 8) XE(C, 12) XE(C, 13) XC(153, C, 10) XE(C, 14) XE(C, 15) XC(154, C, 12) XC(155, C, 14)
#define XM(D, A, B, C) "v_mfma_f32_32x32x16_bf16 v[" #D ":" #D "+15], v[" #A ":" #A "+3], v[" #B ":" #B "+3], v[" #C ":" #C "+15]\n\t"
#define XRK(BASE, O) "ds_read_b128 v[132:135], " BASE " offset:" #O "\n\t"
#define XRV(R, BASE, O) "s_mov_b64 exec, %[msk]\n\tds_read_b128 v[" #R ":" #R "+3], " BASE " offset:" #O "\n\ts_mov_b64 exec, -1\n\t"
#define XW(n) "s_waitcnt lgkmcnt(" #n ")\n\t"
#define XTILE(VA, RK, RV1, WA, X1, VB, X2, RV0) \
        XM(80, 132, 156, 176) RK XG1(64) XM(160, VA, 152, 160) RV1 XG2(64) XW(WA) XM(96, 140, 148, 96) XG3(64) \
        XW(0) XM(64, 132, 128, 112) X1 XG1(80) XM(96, VB, 152, 96) X2 XG2(80) XM(160, 140, 148, 160) RV0 XG3(80)
#define XGU(j) "s_mov_b32 m0, %[gm]\n\ts_add_u32 %[gm], %[gm], 0x400\n\tbuffer_load_dwordx4 %[gv" #j "], %[rg], %[gs] offen lds\n\t"
#define XGU2 XGU(2) "s_add_u32 %[gs], %[gs], 88064\n\t"
#define XPF "buffer_load_dword v200, %[pvd], %[rwd], %[psd] offen\n\tbuffer_load_dword v200, %[pvo], %[rwo], %[pso] offen\n\t"
#define XWO "s_mov_b32 m0, %[km]\n\ts_add_u32 %[km], %[km], 0x400\n\tbuffer_load_dwordx4 %[wv], %[rwo], %[ks] offen lds\n\ts_add_u32 %[ks], %[ks], 0x2000\n\t"
#define XST(V, SO, O) "buffer_store_dwordx4 v[204:207], " V ", %[rs], " SO " offen offset:" #O " sc1\n\t"
#define XFP "buffer_store_dword v201, %[fpv], %[rs], %[fps] offen sc1\n\t"
#define XFL "buffer_load_dwordx4 v[208:211], %[l16], %[rs], %[fp] offen sc1\n\t"
#define XXR(R, O) "buffer_load_dwordx2 v[" #R ":" #R "+1], %[xv], %[rx], %[xs] offen offset:" #O "\n\t"
#define AOPS : "+{v[64:79]}"(sA), "+{v[80:95]}"(sB), "+{v[96:111]}"(o0), "+{v[160:175]}"(o1), "+{v[112:127]}"(n0), "+{v[176:191]}"(n1), "+{v[128:131]}"(q0), "+{v[156:159]}"(q1), \
               "+{v[132:135]}"(kf), "+{v[140:143]}"(v0), "+{v[144:147]}"(v1), "+{v[136:139]}"(v1b), "+{v[148:151]}"(pA), "+{v[152:155]}"(pB), "+{v[192:199]}"(xres), "+{v200}"(pfd), \
               "+{v201}"(epv), "+{v[204:207]}"(ones), "+{v[208:211]}"(fpl), \
               [km] "+s"(km), [ks] "+s"(ks), [gm] "+s"(gm), [gs] "+s"(gs) \
             : [ka] "v"(ka), [va] "v"(va), [kn] "v"(kn), [vn] "v"(vn), [msk] "s"(msk), [l16] "v"(l16), [rs] "s"(rs), [rg] "s"(rg), [gv0] "v"(gv0), [gv1] "v"(gv1), [gv2] "v"(gv2), \
               [pvd] "v"(pvd), [pvo] "v"(pvo), [wv] "v"(wv), [xv] "v"(xv), [xs] "s"(xs), [psd] "s"(psd), [pso] "s"(pso), [rwd] "s"(rwd), [rwo] "s"(rwo), [rx] "s"(rx), \
               [fo] "s"(fo), [fh] "s"(fh), [fg] "s"(fg), [fsv] "v"(fsv), [fs] "s"(fs), [fg2] "s"(fg2), [fpv] "v"(fpv), [fps] "s"(fps), [fp] "s"(WS_FP) : "m0", "scc", "memory"
#define XBAR(n) asm volatile("s_waitcnt vmcnt(" #n ") lgkmcnt(0)\n\ts_barrier" AOPS)
#define XROUND(A1, A2, B1, B2, C1, C2, D1, D2, E1, E2, F1, F2) asm volatile( \
            XTILE(136, XRK("%[ka]", 1024), XRV(144, "%[va]", 512), 2, A1, 144, A2, XRV(140, "%[va]", 1024)) \
            XTILE(144, XRK("%[ka]", 2048), XRV(144, "%[va]", 1536), 2, B1, 144, B2, XRV(140, "%[va]", 2048)) \
            XTILE(144, XRK("%[ka]", 3072), XRV(144, "%[va]", 2560), 2, C1, 144, C2, XRV(140, "%[va]", 3072)) \
            XTILE(144, XRK("%[ka]", 4096), XRV(144, "%[va]", 3584), 2, D1, 144, D2, XRV(140, "%[va]", 4096)) \
            XTILE(144, XRK("%[ka]", 5120), XRV(144, "%[va]", 4608), 2, E1, 144, E2, XRV(140, "%[va]", 5120)) \
            XTILE(144, XRK("%[ka]", 6144), XRV(144, "%[va]", 5632), 2, F1, 144, F2, XRV(140, "%[va]", 6144)) \
            XTILE(144, XRK("%[ka]", 7168), XRV(144, "%[va]", 6656), 2, XRV(136, "%[va]", 7680), 144, "", XRV(140, "%[va]", 7168)) AOPS)
#define XLAST(X1, X2) asm volatile(XTILE(144, XRK("%[kn]", 0), "", 1, X1, 136, X2, XRV(140, "%[vn]", 0)) AOPS)
        unsigned ka = kb, va = vb, kn = kb + 16384, vn = vb + 16384;
        asm volatile("s_nop 15" AOPS);
        STAMP(1, 7);
        XROUND(XGU(0), "", "", XST("%[l16]", "%[fo]", 0), XGU(1), XST("%[fsv]", "%[fs]", 0), "", XST("%[l16]", "%[fh]", 0), XGU2, "", XGU(0), "");
        STAMP(1, 8);
        XBAR(7);
        STAMP(1, 9);
        XLAST(XKV, XKV);
        ka = kb + 16384; va = vb + 16384; kn = kb; vn = vb;
        XROUND(XKV, XKV, XGU(1), XST("%[l16]", "%[fg]", 0), XGU2, "", XST("%[l16]", "%[fg]", 1024), XGU(0), "", XGU(1), XST("%[l16]", "%[fg2]", 2048), "");
        STAMP(1, 10);
        XBAR(7);
        STAMP(1, 11);
        km += 16384 - 4096; ks += KV_CHUNK - 4096;
        XLAST(XKV, XKV);
        ka = kb; va = vb; kn = kb + 16384; vn = vb + 16384;
        XROUND(XKV, XKV, XGU2, "", XGU(0), "", XGU(1), "", XGU2, "", XPF, "");
        STAMP(1, 12);
        XBAR(6);
        STAMP(1, 13);
        km = lds0 + g * 32768 + wq * 4096; ks = (32 * g + 4 * wq) * 8192 + ct * 128;
        XLAST(XFP, XWO);
        ka = kb + 16384; va = vb + 16384; kn = kb; vn = vb;
        XROUND(XWO, XWO, XWO, "", XXR(192, 0), "", XXR(194, 16), "", XXR(196, 32), "", XXR(198, 48), "");
        XLAST(XFL, "");
        STAMP(1, 14);
        asm volatile(XM(160, 136, 152, 160) "s_waitcnt vmcnt(0) lgkmcnt(0)\n\ts_barrier\n\ts_nop 15" AOPS);
        STAMP(1, 4);
        f32x16 o = o0;
        const unsigned xa = lds0 + g * 32768 + 16384 + wq * 3072 + l16, xb = lds0 + (g ^ 1) * 32768 + 16384 + wq * 3072 + l16;
        *(LDSAS f32x4*)(xa) = f32x4{o1[0], o1[1], o1[2], o1[3]};
        *(LDSAS f32x4*)(xa + 1024) = f32x4{o1[4], o1[5], o1[6], o1[7]};
        *(LDSAS float*)(xa + 2048) = o1[8];
        {
            float big = __builtin_fmaxf(o0[8], o1[8]) * 1e19f;
            float chk = o0[8] + o1[8];
#pragma unroll
            for (int i = 0; i < 8; ++i) { big = __builtin_fmaxf(__builtin_fmaxf(big, __builtin_fabsf(o0[i])), __builtin_fabsf(o1[i])); chk += o0[i] + o1[i]; }
            *(LDSAS unsigned*)(lds0 + L_FLAG + w * 4) = __any(!(big < 1e38f) || chk != chk);
        }
        BAR_L();
        const f32x4 p0 = lds_f4(xb), p1 = lds_f4(xb + 1024); const float pl = *(const LDSAS float*)(xb + 2048);
        const u32x4 f0 = *(const LDSAS u32x4*)(lds0 + L_FLAG), f1 = *(const LDSAS u32x4*)(lds0 + L_FLAG + 16);
#pragma unroll
        for (int i = 0; i < 4; ++i) { o[i] += p0[i]; o[4 + i] += p1[i]; }
        o[8] += pl;
        if (__builtin_expect(RFL(f0[0] | f0[1] | f0[2] | f0[3] | f1[0] | f1[1] | f1[2] | f1[3]), 0)) {
            BAR_L();
            const s16x8 qf = q0;
            const u32x4 cc = {cst, cst, cst, cst};
            const unsigned vf = lds0 + 16384 + 8192 + (16 * h + (r32 & 15)) * 16;
            float m = -__builtin_inff();
            o = f32x16{};
            const f32x16 z = {};
#pragma unroll 1
            for (int c = 0; c < 8; ++c) {
                dma16<true>(rs, l16, kvs + c * KV_CHUNK + w * 2048, lds0 + 16384 + w * 2048);
                dma16<true>(rs, l16, kvs + c * KV_CHUNK + w * 2048 + 1024, lds0 + 16384 + w * 2048 + 1024);
                BAR_VL(0);
#pragma unroll 1
                for (int t = 0; t < 8; ++t) {
                    f32x16 s = MFMA(lds_b128(lds0 + 16384 + l16 + t * 1024), qf, z);
                    float mx = s[0];
#pragma unroll
                    for (int i = 1; i < 16; ++i) mx = __builtin_fmaxf(mx, s[i]);
                    mx = __builtin_fmaxf(swap_max(mx), m);
                    const float al = __builtin_amdgcn_exp2f(m - mx);
                    m = mx;
#pragma unroll
                    for (int i = 0; i < 16; ++i) { o[i] *= al; s[i] = __builtin_amdgcn_exp2f(s[i] - m); }
#pragma unroll
                    for (int i = 0; i < 4; ++i) { pA[i] = pk2(s[2 * i], s[2 * i + 1]); pB[i] = pk2(s[8 + 2 * i], s[9 + 2 * i]); }
                    const u32x4 a = __builtin_bit_cast(u32x4, lds_b128(vf + t * 1024)), c2 = __builtin_bit_cast(u32x4, lds_b128(vf + t * 1024 + 512));
                    o = MFMA(__builtin_bit_cast(s16x8, r32 < 16 ? a : cc), __builtin_bit_cast(s16x8, pA), o);
                    o = MFMA(__builtin_bit_cast(s16x8, r32 < 16 ? c2 : cc), __builtin_bit_cast(s16x8, pB), o);
                }
                BAR_L();
            }
        }
        {
            const float inv = __builtin_amdgcn_rcpf(o[8]);
            u32x4 ov;
#pragma unroll
            for (int i = 0; i < 4; ++i) ov[i] = pk2(o[2 * i] * inv, o[2 * i + 1] * inv);
            ST128(ov, rs, l16, fo, AUX_SC1);
        }
        STAMP(1, 5);
        BAR_L();
        if (w >= 4) issue_wo(16 + ((w - 4) >> 1) * 32 + (w & 1) * 8, 8);
        gu_colsum();
#pragma unroll 1
        for (unsigned spins = 0; spins < SPIN_CAP && !__all(fpl[0] == ep && fpl[1] == ep && fpl[2] == ep && fpl[3] == ep); ++spins) {
            __builtin_amdgcn_s_sleep(2);
            fpl = __builtin_amdgcn_raw_buffer_load_b128(rs, l16, WS_FP, AUX_SC1);
        }
        STAMP(1, 6);
        FLUSH(1);
    }

    {
        STAMP(2, 0);
        f32x16 a0, a1;
        if (w < 4) {
            const unsigned t0 = lds0 + L_WO + kh * 32768 + w64_tr(lane, kh), t1 = lds0 + L_WO + kh * 32768 + w64_tr(lane, kh ^ 1);
            G2 g2;
            g2.tb[0] = g2.tb[2] = g2.tb[4] = t0; g2.tb[1] = g2.tb[3] = g2.tb[5] = t1;
            g2.sa = g2.s0 = RFL(WS_O + ((rt * 2 + wt) * 32 + kh * 16) * 1024); g2.l16 = l16; g2.budget = budget; g2.rs = rs; g2.a1 = f32x16{};
#pragma unroll
            for (int i = 0; i < 8; ++i) { g2.a0[2 * i] = bflo(xres[i]); g2.a0[2 * i + 1] = bfhi(xres[i]); }
            __builtin_amdgcn_s_sleep(DELAY_O);
            STAMP(2, 1);
            g2_steps<0, 16, 0, 6, -1>(g2);
            a0 = g2.a0; a1 = g2.a1; STAMPV(15, budget - g2.budget); budget = g2.budget;
        } else BAR_VL(0);
        STAMP(2, 2);
        BAR_L();
        if (w < 4) {
#pragma unroll
            for (int g = 0; g < 4; ++g) *(LDSAS f32x4*)(lds0 + w * 4096 + g * 1024 + lane * 16) = f32x4{a1[4 * g], a1[4 * g + 1], a1[4 * g + 2], a1[4 * g + 3]};
        }
#pragma unroll
        for (int n = 0; n < 3; ++n) *(LDSAS float*)(lds0 + L_TAB + (w * 96 + n * 32 + r32) * 4) = cs[n];
        BAR_L();
        if (w >= 4) {
            if (tid < 256 + 96) {
                float t[8];
#pragma unroll
                for (int i = 0; i < 8; ++i) t[i] = *(const LDSAS float*)(lds0 + L_TAB + (i * 96 + tid - 256) * 4);
                *(LDSAS float*)(lds0 + L_TAB + 3072 + (tid - 256) * 4) = ((t[0] + t[1]) + (t[2] + t[3])) + ((t[4] + t[5]) + (t[6] + t[7]));
            }
        } else {
            f32x4 p[4];
#pragma unroll
            for (int g = 0; g < 4; ++g) p[g] = lds_f4(lds0 + (w ^ 2) * 4096 + g * 1024 + lane * 16);
            SBAR();
#pragma unroll
            for (int g = 0; g < 4; ++g) {
                hreg[g >> 1][(g & 1) * 2] = pk2(a0[4 * g] + p[g][0], a0[4 * g + 1] + p[g][1]);
                hreg[g >> 1][(g & 1) * 2 + 1] = pk2(a0[4 * g + 2] + p[g][2], a0[4 * g + 3] + p[g][3]);
            }
            const unsigned so = WS_H + ((rt * 2 + wt) * 32 + (ct * 2 + kh) * 2) * 1024;
            ST128(hreg[0], rs, lane * 16, so, AUX_SC1);
            ST128(hreg[1], rs, lane * 16 + 1024, so, AUX_SC1);
            SBAR();
            float sm = 0.f, sq = 0.f;
#pragma unroll
            for (int i = 0; i < 8; ++i) { sm = dot2(hreg[i >> 2][i & 3], ONES2, sm); sq = dot2(hreg[i >> 2][i & 3], hreg[i >> 2][i & 3], sq); }
            sm = swap_add(sm); sq = swap_add(sq);
            if (h == 0) __builtin_amdgcn_raw_buffer_store_b64(u32x2{__float_as_uint(sm), __float_as_uint(sq)}, rs, r32 * 8,
                                                              WS_ST + ((rt * 2 + wt) * 16 + ct * 2 + kh) * 256, AUX_SC1);
        }
        STAMP(2, 3);
        BAR_L();
        if (w >= 4) { issue_dn(0, 15, ((w - 4) >> 1) * 15 * 2048, w - 4); if (bi >= 29) issue_dn(15, 24, L_GU + ((w - 4) >> 1) * 49152, w - 4); }
        STAMP(2, 5);
        FLUSH(2);
    }

    {
        STAMP(3, 0);
        if (bi < 29) {
            const int tt = xcd * 8 + w;
            GU gu;
            gu.a0 = gu.a1 = gu.a2 = f32x16{};
            gu.sa = gu.s0 = RFL(WS_H + tt * 32768); gu.sv = 8 * h * 256 + r32 * 8; gu.ss = RFL(WS_ST + tt * 4096); gu.l16 = l16; gu.budget = budget; gu.rs = rs;
            gu.dm = RFL(lds0 + L_GU + (w >> 2) * 49152 + (w & 3) * 6144); gu.ds = RFL(((43 * (w >> 2) + 15) * 16 + (w & 3) * 48) * 1024 + ct * 128);
            gu.dm2 = RFL(lds0 + L_GU + (w >> 2) * 49152 + 24576 + (w & 3) * 3072); gu.ds2 = RFL(((43 * (w >> 2) + 27) * 16 + (w & 3) * 24) * 1024 + ct * 128);
            gu.dm3 = RFL(lds0 + L_TAB + (w >> 2) * 2048 + (w & 1) * 1024); gu.ds3 = RFL(((43 * (w >> 2) + 39) * 16 + (w & 1) * 8) * 1024 + ct * 128);
            gu.dv = w64_voff(lane); gu.rd = mk_rsrc(wd, F * D * 2);
            gu.wa[0] = gu_wa(0); gu.wa[1] = gu_wa(1); gu.wa[2] = gu_wa(2);
            float csl[3];
#pragma unroll
            for (int n = 0; n < 3; ++n) csl[n] = *(const LDSAS float*)(lds0 + L_TAB + 3072 + (n * 32 + r32) * 4);
            if (w < 4) __builtin_amdgcn_s_sleep(DELAY_H);
            STAMP(3, 2);
            gu_steps<0, 18>(gu);
#pragma unroll 1
            for (;;) {
                unsigned mx = gu.st[0];
#pragma unroll
                for (int i = 1; i < 16; ++i) mx = gu.st[i] > mx ? gu.st[i] : mx;
                if (__builtin_expect(!__any(mx == ~0u) || gu.budget == 0, 1)) break;
                --gu.budget;
                asm volatile(".set tagj, 0\n\t.rept 8\n\tbuffer_load_dwordx2 v[172+2*tagj:173+2*tagj], %[sv], %[rs], %[ss] offen offset:tagj*256 sc1\n\t.set tagj, tagj+1\n\t.endr\n\ts_waitcnt vmcnt(0)"
                             : "+{v[172:187]}"(gu.st), "+{v[140:155]}"(gu.r0), "+{v[156:171]}"(gu.r1), "+{v[112:119]}"(gu.w0), "+{v[124:131]}"(gu.w1), "+{v[132:139]}"(gu.w2),
                               "+{v[64:79]}"(gu.a0), "+{v[80:95]}"(gu.a1), "+{v[96:111]}"(gu.a2) : [sv] "v"(gu.sv), [rs] "s"(rs), [ss] "s"(gu.ss) : "memory");
            }
            float sm = 0.f, sq = 0.f;
#pragma unroll
            for (int i = 0; i < 8; ++i) { sm += __uint_as_float(gu.st[2 * i]); sq += __uint_as_float(gu.st[2 * i + 1]); }
            sm = swap_add(sm); sq = swap_add(sq);
            const float mean = sm * (1.f / D);
            const float var = __builtin_fmaxf(sq * (1.f / D) - mean * mean, 0.f) + LN_EPS;
            float rstd = __builtin_amdgcn_rsqf(var), sd = var * rstd, ga = -LOG2E * rstd;
            u32x4 mf, cf[3];
            {
                const unsigned mh = pk2(-mean, -mean);
                const unsigned ml = pk2(-mean - bflo(mh), 0.f);
                mf = h ? u32x4{0u, 0u, 0u, 0u} : u32x4{(mh & 0xffffu) | (ml << 16), mh & 0xffffu, 0u, 0u};
#pragma unroll
                for (int n = 0; n < 3; ++n) { const unsigned ch = pk2(csl[n], csl[n]); cf[n] = u32x4{ch, pk2(csl[n] - bflo(ch), 0.f), 0u, 0u}; PIN(cf[n]); }
                PIN(mf); PIN(sd); PIN(ga);
            }
            gu_steps<18, 32>(gu);
            asm volatile("v_mfma_f32_32x32x16_bf16 v[64:79], %[c0], %[m], v[64:79]\n\tv_mfma_f32_32x32x16_bf16 v[80:95], %[c1], %[m], v[80:95]\n\t"
                         "v_mfma_f32_32x32x16_bf16 v[96:111], %[c2], %[m], v[96:111]\n\ts_nop 10"
                         : "+{v[64:79]}"(gu.a0), "+{v[80:95]}"(gu.a1), "+{v[96:111]}"(gu.a2) : [c0] "v"(cf[0]), [c1] "v"(cf[1]), [c2] "v"(cf[2]), [m] "v"(mf));
            STAMP(3, 3);
            STAMPV(15, budget - gu.budget); budget = gu.budget;
            auto epi = [&](int n, const f32x16& acc) {
                f32x2 e[4], y[4];
#pragma unroll
                for (int i = 0; i < 4; ++i) e[i] = f32x2{acc[2 * i], acc[2 * i + 1]} * ga;
#pragma unroll
                for (int i = 0; i < 4; ++i) e[i] = f32x2{__builtin_amdgcn_exp2f(e[i][0]), __builtin_amdgcn_exp2f(e[i][1])} * sd + sd;
#pragma unroll
                for (int i = 0; i < 4; ++i) y[i] = f32x2{acc[8 + 2 * i], acc[9 + 2 * i]} * f32x2{__builtin_amdgcn_rcpf(e[i][0]), __builtin_amdgcn_rcpf(e[i][1])};
                const u32x4 ov = {pk2(y[0][0], y[0][1]), pk2(y[1][0], y[1][1]), pk2(y[2][0], y[2][1]), pk2(y[3][0], y[3][1])};
                ST128(ov, rs, (n < 2 || bi < 28) ? l16 : 0x80000000u, WS_HH + (tt * NU + bi * 3 + n) * 1024, AUX_SC1);
            };
            epi(0, gu.a0);
            asm volatile("" : "+{v[80:95]}"(gu.a1));
            epi(1, gu.a1);
            asm volatile("" : "+{v[96:111]}"(gu.a2));
            epi(2, gu.a2);
        }
        STAMP(3, 4);
        if (bi >= 29) WAITV(0);
        BAR_VL(3);
        STAMP(3, 5);
        if (w >= 4) {
            if (bi < 29) issue_dn(33, 6, L_GU + ((w - 4) >> 1) * 49152 + 36864, w - 4); else issue_dn(39, 1, L_TAB + ((w - 4) >> 1) * 2048, w - 4);
            STAMP(3, 6);
        }
        FLUSH(3);
    }

    {
        STAMP(4, 0);
        if (b == 0 && tid == 0) __builtin_amdgcn_raw_buffer_store_b32(ep, rs, 0, WS_EP, AUX_SC1);
        f32x16 a0, a1;
        if (w < 4) {
            const unsigned t0 = lds0 + w64_tr(lane, kh), t1 = lds0 + w64_tr(lane, kh ^ 1);
            G2 g2;
            g2.tb[0] = t0 + kh * 15 * 2048; g2.tb[1] = t1 + kh * 15 * 2048; g2.tb[2] = t0 + L_GU + kh * 24 * 2048; g2.tb[3] = t1 + L_GU + kh * 24 * 2048;
            g2.tb[4] = t0 + L_TAB + kh * 2048; g2.tb[5] = t1 + L_TAB + kh * 2048;
            g2.sa = g2.s0 = RFL(WS_HH + ((rt * 2 + wt) * NU + kh * 43) * 1024); g2.l16 = l16; g2.budget = budget; g2.rs = rs; g2.a1 = f32x16{};
#pragma unroll
            for (int i = 0; i < 8; ++i) { g2.a0[2 * i] = bflo(hreg[i >> 2][i & 3]); g2.a0[2 * i + 1] = bfhi(hreg[i >> 2][i & 3]); }
            __builtin_amdgcn_s_sleep(DELAY_G);
            STAMP(4, 1);
            g2_steps<0, 43, 1, DN_B0, 38>(g2);
            a0 = g2.a0; a1 = g2.a1; STAMPV(15, budget - g2.budget);
        } else {
            BAR_VL(0);
            issue_dn(40, 3, ((w - 4) >> 1) * 15 * 2048, w - 4);
            BAR_VL(0);
        }
        STAMP(4, 2);
        if (w < 4) {
#pragma unroll
            for (int g = 0; g < 4; ++g) *(LDSAS f32x4*)(lds0 + 8192 + w * 4096 + g * 1024 + lane * 16) = f32x4{a1[4 * g], a1[4 * g + 1], a1[4 * g + 2], a1[4 * g + 3]};
        }
        BAR_L();
        if (w < 4) {
            f32x4 p[4];
#pragma unroll
            for (int g = 0; g < 4; ++g) p[g] = lds_f4(lds0 + 8192 + (w ^ 2) * 4096 + g * 1024 + lane * 16);
            SBAR();
            unsigned y[8];
#pragma unroll
            for (int g = 0; g < 4; ++g) {
                y[2 * g] = pk2(a0[4 * g] + p[g][0], a0[4 * g + 1] + p[g][1]);
                y[2 * g + 1] = pk2(a0[4 * g + 2] + p[g][2], a0[4 * g + 3] + p[g][3]);
            }
            const rsrc_t ro = mk_rsrc(out, S * D * 2);
            const unsigned vo = (rt * 64 + wt * 32 + r32) * 1024 + (ct * 64 + kh * 32 + 8 * h) * 2;
#pragma unroll
            for (int g = 0; g < 2; ++g) {
                auto e0 = __builtin_amdgcn_permlane32_swap(y[4 * g], y[4 * g + 2], false, false);
                auto e1 = __builtin_amdgcn_permlane32_swap(y[4 * g + 1], y[4 * g + 3], false, false);
                ST128((u32x4{e0[0], e1[0], e0[1], e1[1]}), ro, vo + g * 32, 0, OUT_AUX);
            }
        }
        STAMP(4, 3);
        STAMP(4, 4);
        FLUSH(4);
        asm volatile(".subsection 2\n.Lmain_end:\n\t.fill 64, 4, 0xBF800000\n\t.subsection 0");
    }
}

extern "C" void kernel_launch(void* const* d_in, const int* in_sizes, int n_in,
                              void* d_out, int out_size, void* d_ws, size_t ws_size,
                              hipStream_t stream) {
    const u16* x = (const u16*)d_in[0];
    const u16* wqkv = (const u16*)d_in[1];
    const u16* wo = (const u16*)d_in[2];
    const u16* wg = (const u16*)d_in[4];
    const u16* wu = (const u16*)d_in[5];
    const u16* wd = (const u16*)d_in[6];
    u16* out = (u16*)d_out;
    char* ws = (char*)d_ws;
    (void)hipFuncSetAttribute((const void*)k_block, hipFuncAttributeMaxDynamicSharedMemorySize, K2_LDS);
    k_block<<<256, 512, K2_LDS, stream>>>(x, wqkv, wo, wg, wu, wd, out, ws);
}
```

```cpp
#include <hip/hip_runtime.h>
#include <hip/hip_bf16.h>
#include <stdint.h>
#pragma clang diagnostic ignored "-Winline-asm"

typedef short s16x8 __attribute__((ext_vector_type(8)));
typedef short s16x4 __attribute__((ext_vector_type(4)));
typedef float f32x16 __attribute__((ext_vector_type(16)));
typedef float f32x4 __attribute__((ext_vector_type(4)));
typedef float f32x2 __attribute__((ext_vector_type(2)));
typedef unsigned u32x4 __attribute__((ext_vector_type(4)));
typedef unsigned u32x2 __attribute__((ext_vector_type(2)));
typedef unsigned u32x8 __attribute__((ext_vector_type(8)));
typedef __bf16 bf16x2 __attribute__((ext_vector_type(2)));
typedef unsigned short u16;
#define LDSAS __attribute__((address_space(3)))
#define GAS __attribute__((address_space(1)))
typedef __amdgpu_buffer_rsrc_t rsrc_t;

constexpr int S = 2048, D = 512, NH = 32, F = 1376, NU = F / 16;
constexpr float LN_EPS = 1e-5f;
constexpr float LOG2E = 1.4426950408889634f;

constexpr unsigned KV_CHUNK = 16384;
constexpr unsigned KV_HEAD = 8 * KV_CHUNK;
constexpr unsigned WS_EP = 0;
constexpr unsigned WS_FQ = 4096;
constexpr unsigned WS_FP = WS_FQ + 16 * 64 * 128;
constexpr unsigned WS_Q = WS_FP + 4096;
constexpr unsigned WS_KV = WS_Q + NH * 64 * 1024;
constexpr unsigned WS_O = WS_KV + NH * KV_HEAD;
constexpr unsigned WS_H = WS_O + 64 * 32 * 1024;
constexpr unsigned WS_ST = WS_H + 64 * 32 * 1024;
constexpr unsigned WS_HH = WS_ST + 64 * 16 * 256;
constexpr unsigned WS_END = WS_HH + 64 * NU * 1024;
constexpr int AUX_SC1 = 16;
#define STAMP_DECL
#define FLUSH(k) do {} while (0)
#define STAMP(k, i) do {} while (0)

#define STAMPV(i, v) do {} while (0)
#define SKEW(k) do {} while (0)
#define SBAR() __builtin_amdgcn_sched_barrier(0)
#define RFL(x) ((unsigned)__builtin_amdgcn_readfirstlane(x))
#define PIN(x) asm volatile("" : "+v"(x))
#define MFMA(a, b, c) __builtin_amdgcn_mfma_f32_32x32x16_bf16((a), (b), (c), 0, 0, 0)
#define WAITV(n) asm volatile("s_waitcnt vmcnt(" #n ")" ::: "memory")
#define BAR_L() asm volatile("s_waitcnt lgkmcnt(0)\n\ts_barrier" ::: "memory")
#define BAR_0() asm volatile("s_barrier" ::: "memory")
#define BAR_VL(n) asm volatile("s_waitcnt vmcnt(" #n ") lgkmcnt(0)\n\ts_barrier" ::: "memory")

__device__ __forceinline__ rsrc_t mk_rsrc(const void* p, unsigned bytes) {
    return __builtin_amdgcn_make_buffer_rsrc((void*)p, (short)0, (int)bytes, 0x00020000);
}
template <bool SC1 = false> __device__ __forceinline__ void dma16(rsrc_t r, unsigned voff, unsigned soff, unsigned lds) {
    if constexpr (SC1) asm volatile("s_nop 4\n\ts_mov_b32 m0, %0\n\ts_nop 0\n\tbuffer_load_dwordx4 %1, %2, %3 offen sc1 lds"
                 :: "s"(__builtin_amdgcn_readfirstlane(lds)), "v"(voff), "s"(r), "s"(__builtin_amdgcn_readfirstlane(soff)) : "m0", "memory");
    else asm volatile("s_nop 4\n\ts_mov_b32 m0, %0\n\ts_nop 0\n\tbuffer_load_dwordx4 %1, %2, %3 offen lds"
                 :: "s"(__builtin_amdgcn_readfirstlane(lds)), "v"(voff), "s"(r), "s"(__builtin_amdgcn_readfirstlane(soff)) : "m0", "memory");
}
#define ST128(v, r, voff, soff, aux) do { const u32x4 d_ = (v); __builtin_amdgcn_raw_buffer_store_b128(d_, r, voff, soff, aux); asm volatile("s_nop 1" :: "v"(d_)); } while (0)
__device__ __forceinline__ unsigned pk2(float lo, float hi) {
    f32x2 v = {lo, hi};
    return __builtin_bit_cast(unsigned, __builtin_convertvector(v, bf16x2));
}
__device__ __forceinline__ float dot2(unsigned a, unsigned b, float c) {
    return __builtin_amdgcn_fdot2_f32_bf16(__builtin_bit_cast(bf16x2, a), __builtin_bit_cast(bf16x2, b), c, false);
}
__device__ __forceinline__ float swap_add(float v) {
    auto r = __builtin_amdgcn_permlane32_swap(__float_as_uint(v), __float_as_uint(v), false, false);
    return __uint_as_float(r[0]) + __uint_as_float(r[1]);
}
__device__ __forceinline__ float swap_max(float v) {
    auto r = __builtin_amdgcn_permlane32_swap(__float_as_uint(v), __float_as_uint(v), false, false);
    return __builtin_fmaxf(__uint_as_float(r[0]), __uint_as_float(r[1]));
}
__device__ __forceinline__ s16x8 lds_b128(unsigned a) { return *(const LDSAS s16x8*)(a); }
__device__ __forceinline__ f32x4 lds_f4(unsigned a) { return *(const LDSAS f32x4*)(a); }
__device__ __forceinline__ s16x8 lds_tr2(unsigned a0, unsigned a1) {
    s16x4 lo = __builtin_amdgcn_ds_read_tr16_b64_v4i16((LDSAS s16x4*)(a0));
    s16x4 hi = __builtin_amdgcn_ds_read_tr16_b64_v4i16((LDSAS s16x4*)(a1));
    return __builtin_shufflevector(lo, hi, 0, 1, 2, 3, 4, 5, 6, 7);
}
__device__ __forceinline__ float bflo(unsigned v) { return __uint_as_float(v << 16); }
__device__ __forceinline__ float bfhi(unsigned v) { return __uint_as_float(v & 0xffff0000u); }
constexpr unsigned ONES2 = 0x3f803f80u;
template <int I> struct ic { static constexpr int value = I; constexpr operator int() const { return I; } };
template <int B, int E, class Fn> __device__ __forceinline__ void sfor(Fn&& fn) { if constexpr (B < E) { fn(ic<B>{}); sfor<B + 1, E>(fn); } }

#define K1_X_AUX 2
#define K1_ABL 0
#define CODE_PF 3
#define K1_POST_AT 92
#define K1_KV_AT 84
#define DELAY_Q 6
constexpr unsigned K1_W = 65536, K1_FLG = K1_W + 5 * 16384, K1_ST = K1_FLG + 512, K1_CS = K1_ST + 512;
__host__ __device__ constexpr unsigned k1_slot(int s) { return s < 5 ? K1_W + s * 16384 : s == 5 ? K1_W : s < 10 ? (s - 6) * 16384 : K1_W + (s - 9) * 16384; }
__device__ __forceinline__ void lds_spin_ge(unsigned addr, unsigned want) {
#pragma unroll 1
    for (unsigned n = 0; n < (1u << 22); ++n) {
        unsigned v; asm volatile("ds_read_b32 %0, %1\n\ts_waitcnt lgkmcnt(0)" : "=v"(v) : "v"(addr) : "memory");
        if ((unsigned)__builtin_amdgcn_readfirstlane(v) >= want) break;
        __builtin_amdgcn_s_sleep(1);
    }
}
__device__ __forceinline__ void lds_post(unsigned addr, unsigned v) {
    asm volatile("s_waitcnt lgkmcnt(0)\n\tds_write_b32 %0, %1" :: "v"(addr), "v"(v) : "memory");
}
__device__ __forceinline__ void lds_inc(unsigned addr, int lane) {
    asm volatile("s_waitcnt lgkmcnt(0)" ::: "memory");
    if (lane == 0) asm volatile("ds_add_u32 %0, %1" :: "v"(addr), "v"(1u) : "memory");
}
typedef unsigned u32x16 __attribute__((ext_vector_type(16)));
template <int CTRL> __device__ __forceinline__ float dpp_add(float v) {
    return v + __builtin_bit_cast(float, __builtin_amdgcn_update_dpp(0, __builtin_bit_cast(int, v), CTRL, 0xf, 0xf, true));
}

constexpr unsigned K2_LDS = 163840;
constexpr unsigned L_WO = 0, L_GU = 65536;
constexpr unsigned L_TAB = 61440;
constexpr unsigned L_FLAG = 16384 + 12288;
constexpr unsigned SPIN_CAP = 1u << 20;
#define POLL_DELAY_Q 2
#define OUT_AUX 2
#define DN_B0 20
#define ABL_GU 0
#define DELAY_O 0
#define DELAY_H 8
#define DELAY_G 8
constexpr int GU_AW = 8, G2_AW = 12;
#define TAG_T1 "v_max3_u32 v120, v[%c[cr]], v[%c[cr]+1], v[%c[cr]+2]\n\t"
#define TAG_T2 "v_max_u32 v120, v120, v[%c[cr]+3]\n\tv_cmp_eq_u32 vcc, -1, v120\n\t"
#define TAG_RELOAD(RB, AW) "s_add_u32 %[t], %[s0], (tagj>>2)*4096\n\tbuffer_load_dwordx4 v[" #RB "+4*(tagj%%" #AW "):" #RB "+3+4*(tagj%%" #AW ")], %[l16], %[rs], %[t] offen offset:(tagj&3)*1024 sc1\n\t"
#define TAG_T3(RB, AW) \
    "s_cbranch_vccnz 5f\n6:\n\t" \
    ".subsection 1\n5:\n\ts_cmp_eq_u32 %[bud], 0\n\ts_cbranch_scc1 6b\n\ts_sub_u32 %[bud], %[bud], 1\n\t.set tagj, %c[ci]\n\t" TAG_RELOAD(RB, AW) \
    "s_waitcnt vmcnt(0)\n\t" TAG_T1 TAG_T2 "s_cbranch_vccnz 5b\n\t.rept %c[cnt]-1\n\t.set tagj, tagj+1\n\t" TAG_RELOAD(RB, AW) ".endr\n\ts_branch 6b\n\t.subsection 0\n\t"
#define TAG_LOAD "buffer_load_dwordx4 v[%c[lr]:%c[lr]+3], %[l16], %[rs], %[sa] offen offset:%c[lo] sc1\n\t.if %c[lo] == 3072\n\ts_add_u32 %[sa], %[sa], 0x1000\n\t.endif\n\t"

__device__ __forceinline__ unsigned w64_voff(int lane) { return (lane >> 3) * 1024 + (((lane & 7) ^ (((lane >> 4) & 1) << 2)) << 4); }
__device__ __forceinline__ unsigned w64_tr(int lane, int n) {
    const int q = (lane & 15) >> 2, p = lane & 3, blk = (lane >> 4) & 1, h = lane >> 5;
    return (4 * h + q) * 128 + (((4 * n + 2 * blk + (p >> 1)) ^ (((q >> 1) & 1) << 2)) << 4) + 8 * (p & 1);
}

struct G2 { u32x16 r0, r1, r2; u32x8 w0, w1, w2; f32x16 a0, a1; unsigned tb[6], sa, s0, l16, budget; rsrc_t rs;
};
__host__ __device__ constexpr int g2_w(int DN, int n) {
    return !DN ? n * 2048 : n < 15 ? n * 2048 : n < 39 ? 65536 + (n - 15) * 2048 : n < 40 ? 131072 : (n - 40) * 2048;
}
template <int i, int N, int DN, int B0, int B1> __device__ __forceinline__ void g2_steps(G2& g) {
    constexpr int AW = G2_AW, j = i + AW - 1, n = i + 2, sel = n < N ? g2_w(DN, n) >> 16 : 3;
    unsigned tq;
#define G2_RD(P, R) "ds_read_b64_tr_b16 v[%c[nr]+" #R ":%c[nr]+" #R "+1], %[" P "] offset:%c[no]\n\tds_read_b64_tr_b16 v[%c[nr]+" #R "+2:%c[nr]+" #R "+3], %[" P "] offset:%c[no]+1024\n\t"
#define G2_RDS(P0, P1, P2, R) ".if %c[sel] == 0\n\t" G2_RD(P0, R) ".endif\n\t.if %c[sel] == 1\n\t" G2_RD(P1, R) ".endif\n\t.if %c[sel] == 2\n\t" G2_RD(P2, R) ".endif\n\t"
    if constexpr (i == 0)
        asm volatile(".set tagj, 0\n\t.rept 11\n\tbuffer_load_dwordx4 v[128+4*tagj:131+4*tagj], %[l16], %[rs], %[sa] offen offset:(tagj&3)*1024 sc1\n\t"
                     ".if (tagj&3) == 3\n\ts_add_u32 %[sa], %[sa], 0x1000\n\t.endif\n\t.set tagj, tagj+1\n\t.endr\n\t"
                     ".set tagj, 0\n\t.rept 2\n\tds_read_b64_tr_b16 v[96+8*tagj:97+8*tagj], %[b0] offset:tagj*2048\n\tds_read_b64_tr_b16 v[98+8*tagj:99+8*tagj], %[b0] offset:tagj*2048+1024\n\t"
                     "ds_read_b64_tr_b16 v[100+8*tagj:101+8*tagj], %[b1] offset:tagj*2048\n\tds_read_b64_tr_b16 v[102+8*tagj:103+8*tagj], %[b1] offset:tagj*2048+1024\n\t.set tagj, tagj+1\n\t.endr\n\t"
                     "s_waitcnt vmcnt(10) lgkmcnt(4)\n\t" TAG_T1 TAG_T2 TAG_T3(128, 12)
                     : "={v[128:143]}"(g.r0), "={v[144:159]}"(g.r1), "={v[160:175]}"(g.r2), "={v[96:103]}"(g.w0), "={v[104:111]}"(g.w1), "={v[112:119]}"(g.w2), [sa] "+s"(g.sa),
                       "+{v[64:79]}"(g.a0), "+{v[80:95]}"(g.a1), [bud] "+s"(g.budget), [t] "=&s"(tq)
                     : [l16] "v"(g.l16), [rs] "s"(g.rs), [s0] "s"(g.s0), [b0] "v"(g.tb[0]), [b1] "v"(g.tb[1]), [cr] "n"(128), [ci] "n"(0), [cnt] "n"(AW - 1) : "vcc", "scc", "v120", "memory");
    asm volatile("v_mfma_f32_32x32x16_bf16 v[64:79], v[%c[wr]:%c[wr]+3], v[%c[ar]:%c[ar]+3], v[64:79]\n\t"
                 ".if %c[ld]\n\t" TAG_LOAD ".endif\n\t"
                 ".if %c[bar]\n\ts_waitcnt lgkmcnt(0)\n\ts_barrier\n\t.endif\n\t"
                 G2_RDS("b0", "b2", "b4", 0)
                 ".if %c[cnt]\n\ts_waitcnt vmcnt(%c[vn])\n\t" TAG_T1 ".endif\n\t"
                 "v_mfma_f32_32x32x16_bf16 v[80:95], v[%c[wr]+4:%c[wr]+7], v[%c[ar]:%c[ar]+3], v[80:95]\n\t"
                 G2_RDS("b1", "b3", "b5", 4)
                 ".if %c[cnt]\n\t" TAG_T2 "s_waitcnt lgkmcnt(%c[ln])\n\t" TAG_T3(128, 12) ".else\n\ts_nop 15\n\t.endif"
                 : "+{v[64:79]}"(g.a0), "+{v[80:95]}"(g.a1), "+{v[128:143]}"(g.r0), "+{v[144:159]}"(g.r1), "+{v[160:175]}"(g.r2), "+{v[96:103]}"(g.w0), "+{v[104:111]}"(g.w1), "+{v[112:119]}"(g.w2),
                   [sa] "+s"(g.sa), [bud] "+s"(g.budget), [t] "=&s"(tq)
                 : [l16] "v"(g.l16), [rs] "s"(g.rs), [s0] "s"(g.s0), [b0] "v"(g.tb[0]), [b1] "v"(g.tb[1]), [b2] "v"(g.tb[2]), [b3] "v"(g.tb[3]), [b4] "v"(g.tb[4]), [b5] "v"(g.tb[5]),
                   [ld] "n"(j < N ? 1 : 0), [lr] "n"(128 + 4 * (j % AW)), [lo] "n"((j & 3) * 1024), [bar] "n"(i == B0 || i == B1 ? 1 : 0), [sel] "n"(sel),
                   [nr] "n"(96 + 8 * (n % 3)), [no] "n"(n < N ? g2_w(DN, n) & 65535 : 0), [vn] "n"(j < N ? AW - 2 : i + 1 < N ? N - 2 - i : 0), [ln] "n"(n < N ? 4 : 0),
                   [ar] "n"(128 + 4 * (i % AW)), [wr] "n"(96 + 8 * (i % 3)), [cr] "n"(128 + 4 * ((i + 1) % AW)), [ci] "n"(i + 1), [cnt] "n"(j < N ? AW - 1 : N - 1 - i) : "vcc", "scc", "v120", "memory");
    if constexpr (i + 1 < N) g2_steps<i + 1, N, DN, B0, B1>(g);
}
__host__ __device__ constexpr int gu_dma(int s) { return (s >= 16 && s < 24 && !(s & 1)) || (s >= 24 && s < 30); }
__host__ __device__ constexpr int gu_ops(int s) { return (s + GU_AW - 1 < 32 ? 1 : 0) + (s >= 2 && s < 10 ? 1 : 0) + gu_dma(s); }
__host__ __device__ constexpr int gu_younger(int k) {
    int c = 0, s = k - GU_AW + 1;
    if (s < 0) { c = GU_AW - 2 - k; s = 0; } else { c = gu_ops(s) - 1; ++s; }
    for (; s < k; ++s) c += gu_ops(s);
    return c;
}

struct GU { u32x16 r0, r1, st; u32x8 w0, w1, w2; f32x16 a0, a1, a2; unsigned wa[3], sa, s0, sv, ss, l16, budget, dm, ds, dv, dm2, ds2, dm3, ds3; rsrc_t rs, rd; };
template <int i, int E> __device__ __forceinline__ void gu_steps(GU& g) {
    constexpr int AW = GU_AW, j = i + AW - 1, n = i + 1;
    unsigned tq;
#define GU_RD(R, B) "ds_read_b64_tr_b16 v[" R ":" R "+1], %[" B "] offset:%c[no]\n\tds_read_b64_tr_b16 v[" R "+2:" R "+3], %[" B "] offset:%c[no]+768\n\t"
    if constexpr (i == 0)
        asm volatile(".set tagj, 0\n\t.rept 7\n\tbuffer_load_dwordx4 v[140+4*tagj:143+4*tagj], %[l16], %[rs], %[sa] offen offset:(tagj&3)*1024 sc1\n\t"
                     ".if (tagj&3) == 3\n\ts_add_u32 %[sa], %[sa], 0x1000\n\t.endif\n\t.set tagj, tagj+1\n\t.endr\n\t" GU_RD("112", "b0") GU_RD("116", "b1") GU_RD("124", "b2")
                     "s_waitcnt vmcnt(6) lgkmcnt(0)\n\t" TAG_T1 TAG_T2 TAG_T3(140, 8)
                     : "={v[140:155]}"(g.r0), "={v[156:171]}"(g.r1), "={v[172:187]}"(g.st), "={v[112:119]}"(g.w0), "={v[124:131]}"(g.w1), "={v[132:139]}"(g.w2), [sa] "+s"(g.sa),
                       "+{v[64:79]}"(g.a0), "+{v[80:95]}"(g.a1), "+{v[96:111]}"(g.a2), [bud] "+s"(g.budget), [t] "=&s"(tq)
                     : [l16] "v"(g.l16), [rs] "s"(g.rs), [s0] "s"(g.s0), [b0] "v"(g.wa[0]), [b1] "v"(g.wa[1]), [b2] "v"(g.wa[2]), [no] "n"(0), [cr] "n"(140), [ci] "n"(0), [cnt] "n"(AW - 1)
                     : "vcc", "scc", "v120", "memory");
    asm volatile(".if %c[i] == 15 || %c[i] == 23\n\ts_barrier\n\t.endif\n\t"
                 "v_mfma_f32_32x32x16_bf16 v[64:79], v[%c[c0]:%c[c0]+3], v[%c[ar]:%c[ar]+3], v[64:79]\n\t"
                 ".if %c[rd]\n\t" GU_RD("%c[n0]", "b0") GU_RD("%c[n1]", "b1") ".endif\n\t"
                 "v_mfma_f32_32x32x16_bf16 v[80:95], v[%c[c1]:%c[c1]+3], v[%c[ar]:%c[ar]+3], v[80:95]\n\t"
                 ".if %c[rd]\n\t" GU_RD("%c[n2]", "b2") ".endif\n\t"
                 ".if %c[ld]\n\t" TAG_LOAD ".endif\n\t"
                 ".if %c[sk] >= 0\n\tbuffer_load_dwordx2 v[172+2*%c[sk]:173+2*%c[sk]], %[sv], %[rs], %[ss] offen offset:%c[sk]*256 sc1\n\t.endif\n\t"
                 ".if %c[i] == 26\n\ts_mov_b32 %[dm], %[dm2]\n\ts_mov_b32 %[ds], %[ds2]\n\t.endif\n\t.if %c[i] == 29\n\ts_mov_b32 %[dm], %[dm3]\n\ts_mov_b32 %[ds], %[ds3]\n\t.endif\n\t"
                 ".if %c[dma]\n\ts_mov_b32 m0, %[dm]\n\ts_add_u32 %[dm], %[dm], 0x400\n\tbuffer_load_dwordx4 %[dv], %[rwd], %[ds] offen lds\n\ts_add_u32 %[ds], %[ds], 0x2000\n\t.endif\n\t"
                 "v_mfma_f32_32x32x16_bf16 v[96:111], v[%c[c2]:%c[c2]+3], v[%c[ar]:%c[ar]+3], v[96:111]\n\t"
                 ".if %c[cnt]\n\ts_waitcnt vmcnt(%c[vn])\n\t" TAG_T1 TAG_T2 "s_waitcnt lgkmcnt(0)\n\t" TAG_T3(140, 8) ".endif"
                 : "+{v[64:79]}"(g.a0), "+{v[80:95]}"(g.a1), "+{v[96:111]}"(g.a2), "+{v[140:155]}"(g.r0), "+{v[156:171]}"(g.r1), "+{v[172:187]}"(g.st),
                   "+{v[112:119]}"(g.w0), "+{v[124:131]}"(g.w1), "+{v[132:139]}"(g.w2), [sa] "+s"(g.sa), [bud] "+s"(g.budget), [t] "=&s"(tq), [dm] "+s"(g.dm), [ds] "+s"(g.ds)
                 : [l16] "v"(g.l16), [rs] "s"(g.rs), [s0] "s"(g.s0), [dv] "v"(g.dv), [rwd] "s"(g.rd), [dm2] "s"(g.dm2), [ds2] "s"(g.ds2), [dm3] "s"(g.dm3), [ds3] "s"(g.ds3),
                   [dma] "n"(gu_dma(i)), [sv] "v"(g.sv), [ss] "s"(g.ss), [b0] "v"(g.wa[0]), [b1] "v"(g.wa[1]), [b2] "v"(g.wa[2]),
                   [ld] "n"(j < 32 ? 1 : 0), [lr] "n"(140 + 4 * (j % AW)), [lo] "n"((j & 3) * 1024), [sk] "n"(i >= 2 && i < 10 ? i - 2 : -1), [rd] "n"(n < 32 ? 1 : 0), [no] "n"(n * 1536),
                   [n0] "n"(n & 1 ? 128 : 112), [n1] "n"(n & 1 ? 132 : 116), [n2] "n"(n & 1 ? 136 : 124), [c0] "n"(i & 1 ? 128 : 112), [c1] "n"(i & 1 ? 132 : 116), [c2] "n"(i & 1 ? 136 : 124),
                   [vn] "n"(n < 32 ? gu_younger(n) : 0), [ar] "n"(140 + 4 * (i % AW)), [i] "n"(i), [cr] "n"(140 + 4 * (n % AW)), [ci] "n"(n), [cnt] "n"(j < 32 ? AW - 1 : 31 - i)
                 : "vcc", "scc", "m0", "v120", "memory");
    if constexpr (i + 1 < E) gu_steps<i + 1, E>(g);
}

__global__ __launch_bounds__(512, 2) void k_block(const u16* __restrict__ x, const u16* __restrict__ wqkv, const u16* wo, const u16* wg,
                                                  const u16* wu, const u16* wd, u16* out, char* __restrict__ ws) {
    extern __shared__ __attribute__((aligned(16))) char smem[];
    const unsigned lds0 = (unsigned)(uintptr_t)(LDSAS char*)smem;
    const int tid = threadIdx.x, lane = tid & 63;
    const int w = __builtin_amdgcn_readfirstlane(tid >> 6);
    const int b = blockIdx.x, xcd = b & 7, bi = b >> 3;
    const int r32 = lane & 31, h = lane >> 5;
    const rsrc_t rs = mk_rsrc(ws, WS_END);
    const int wt = w & 1, kh = (w >> 1) & 1;
    STAMP_DECL;
    unsigned ep;
#define EP_LOAD() do { asm volatile("s_load_dword %0, %1, 0\n\ts_waitcnt lgkmcnt(0)" : "=s"(ep) : "s"(ws) : "memory"); ep += 1u; } while (0)
    asm volatile("" : "+s"(wo), "+s"(wg), "+s"(wu), "+s"(wd), "+s"(out));
    const int head = xcd * 4 + (bi >> 3);
    const unsigned l16 = lane * 16;
    const int rt2 = xcd * 4 + (bi >> 3), ct = bi & 7;
    {
    const int rt = (xcd >> 1) * 8 + (bi & 7), cb = (xcd & 1) * 4 + (bi >> 3), rot = bi & 3;
    const int wc = (w >> 1) & 1, hw = w - 4;
    STAMP(0, 0);
    SKEW(0);
    const rsrc_t rw = mk_rsrc(wqkv, D * 3 * D * 2);
    const unsigned wv = (lane >> 3) * 3072 + (((lane & 7) ^ (((lane >> 4) & 1) << 2)) << 4);
    const unsigned fa = lds0 + K1_FLG;
    if (tid < 12) *(LDSAS unsigned*)(fa + tid * 4) = 0u;
    if (w >= 4) {
        BAR_L();
        const unsigned pa = fa + hw * 4;
        auto issue = [&](int s) {
            const unsigned so = RFL(((((s & 3) + rot) & 3) * 128 + hw * 32) * 3072 + (((s >> 2) + 1) % 3) * 1024 + cb * 128);
            asm volatile("s_nop 4" :: "s"(so));
#pragma unroll
            for (int i = 0; i < 4; ++i)
                asm volatile("s_mov_b32 m0, %0\n\ts_nop 0\n\tbuffer_load_dwordx4 %1, %2, %3 offen lds"
                             :: "s"(lds0 + k1_slot(s) + (4 * hw + i) * 1024), "v"(wv), "s"(rw), "s"(so + i * 8 * 3072) : "m0", "memory");
        };
#define K1_LAND(N, s) asm volatile("s_waitcnt vmcnt(" #N ")\n\tds_write_b32 %0, %1" :: "v"(pa), "v"((s) + 1u) : "memory")
        issue(0); issue(1);
        issue(2); K1_LAND(8, 0);
        issue(3); K1_LAND(8, 1);
        issue(4); K1_LAND(8, 2); K1_LAND(4, 3); K1_LAND(0, 4);
        if (w > 4) {
            unsigned long long pc; unsigned len, o = bi * 128 + (w - 5) * 24576, t, d0, d1, d2, d3;
#define KCP(D) "s_min_u32 %[t], %[o], %[len]\n\ts_load_dword " D ", %[pc], %[t]\n\ts_add_u32 %[o], %[o], 0x1000\n\t"
            asm volatile("s_getpc_b64 %[pc]\n.Lqkv_pc:\n\ts_mov_b32 %[len], .Lmain_end-.Lqkv_pc-4\n\t" KCP("%[d0]") KCP("%[d1]") KCP("%[d2]") KCP("%[d3]") KCP("%[d0]") KCP("%[d1]") "s_waitcnt lgkmcnt(0)"
                         : [pc] "=&s"(pc), [len] "=&s"(len), [o] "+s"(o), [t] "=&s"(t), [d0] "=&s"(d0), [d1] "=&s"(d1), [d2] "=&s"(d2), [d3] "=&s"(d3) :: "scc", "memory");
        }
        EP_LOAD();
        SKEW(9);
        lds_spin_ge(fa + 28, 4);
        issue(5);
        lds_spin_ge(fa + 20, 4);
        issue(6); issue(7); K1_LAND(8, 5);
        issue(8); K1_LAND(8, 6);
        issue(9); K1_LAND(8, 7);
        lds_spin_ge(fa + 24, 4);
        issue(10); K1_LAND(8, 8);
        issue(11); K1_LAND(8, 9);
        {
            unsigned f, cap = 1u << 20;
            asm volatile("buffer_load_dword %[f], %[pv], %[rs], %[fq] offen sc1\n\ts_waitcnt vmcnt(5)\n\tds_write_b32 %[pa], %[c11]\n\ts_waitcnt vmcnt(1)\n\tds_write_b32 %[pa], %[c12]\n"
                         "1:\n\ts_waitcnt vmcnt(0)\n\tv_xor_b32 %[f], %[ep], %[f]\n\tv_lshlrev_b32 %[f], 1, %[f]\n\tv_cmp_ne_u32 vcc, 0, %[f]\n\ts_cbranch_vccz 2f\n\t"
                         "s_sub_u32 %[cap], %[cap], 1\n\ts_cbranch_scc1 2f\n\ts_sleep 2\n\tbuffer_load_dword %[f], %[pv], %[rs], %[fq] offen sc1\n\ts_branch 1b\n2:"
                         : [f] "=&v"(f), [cap] "+s"(cap) : [pv] "v"(lane * 128), [rs] "s"(rs), [fq] "s"(RFL(WS_FQ + (head >> 1) * 8192)), [pa] "v"(pa), [c11] "v"(11u), [c12] "v"(12u), [ep] "s"(ep)
                         : "vcc", "scc", "memory");
        }
        if (w == 4) lds_post(fa + 36, 1u);
        lds_spin_ge(fa + 32, 4);
        __builtin_amdgcn_s_sleep(POLL_DELAY_Q);
#pragma unroll
        for (int j = 0; j < 8; ++j)
            dma16<true>(rs, l16, WS_KV + head * KV_HEAD + (hw >> 1) * 4 * KV_CHUNK + ((hw & 1) * 8 + j) * 1024, lds0 + (hw >> 1) * 32768 + ((hw & 1) * 8 + j) * 1024);
        STAMP(0, 1);
    } else {
    {
        const rsrc_t rx = mk_rsrc(x, S * D * 2);
        u32x4 r[2][8];
#pragma unroll
        for (int p = 0; p < 2; ++p)
#pragma unroll
            for (int g = 0; g < 8; ++g)
                r[p][g] = __builtin_amdgcn_raw_buffer_load_b128(rx, (lane >> 3) * 1024 + (((lane & 7) ^ ((4 * p + (lane >> 4)) & 7)) << 4) + g * 128, (rt * 64 + w * 16 + p * 8) * 1024, K1_X_AUX);
        SBAR(); BAR_L();
        EP_LOAD();
        SBAR();
#pragma unroll
        for (int p = 0; p < 2; ++p) {
            float s0 = 0.f, s1 = 0.f, q0 = 0.f, q1 = 0.f;
#pragma unroll
            for (int g = 0; g < 8; ++g) { s0 = dot2(r[p][g][0], ONES2, s0); q0 = dot2(r[p][g][0], r[p][g][0], q0); s1 = dot2(r[p][g][1], ONES2, s1); q1 = dot2(r[p][g][1], r[p][g][1], q1);
                                          s0 = dot2(r[p][g][2], ONES2, s0); q0 = dot2(r[p][g][2], r[p][g][2], q0); s1 = dot2(r[p][g][3], ONES2, s1); q1 = dot2(r[p][g][3], r[p][g][3], q1); }
            if (p == 0) STAMP(0, 1);
            float sum = s0 + s1, sq = q0 + q1;
            sum = dpp_add<0xB1>(sum); sq = dpp_add<0xB1>(sq); sum = dpp_add<0x4E>(sum); sq = dpp_add<0x4E>(sq); sum = dpp_add<0x141>(sum); sq = dpp_add<0x141>(sq);
            const float mean = sum * (1.f / D);
            const float rstd = __builtin_amdgcn_rsqf(__builtin_fmaxf(sq * (1.f / D) - mean * mean, 0.f) + LN_EPS), nb = -mean * rstd;
#pragma unroll
            for (int g = 0; g < 8; g += 2) {
                f32x2 t[8];
#pragma unroll
                for (int i = 0; i < 8; ++i) { const unsigned v = r[p][g + (i >> 2)][i & 3]; t[i] = f32x2{bflo(v), bfhi(v)}; }
                asm volatile("" : "+v"(t[0]), "+v"(t[1]), "+v"(t[2]), "+v"(t[3]), "+v"(t[4]), "+v"(t[5]), "+v"(t[6]), "+v"(t[7]));
#pragma unroll
                for (int i = 0; i < 8; ++i) t[i] = t[i] * rstd + nb;
                asm volatile("" : "+v"(t[0]), "+v"(t[1]), "+v"(t[2]), "+v"(t[3]), "+v"(t[4]), "+v"(t[5]), "+v"(t[6]), "+v"(t[7]));
                u32x4 y0, y1;
#pragma unroll
                for (int i = 0; i < 4; ++i) { y0[i] = pk2(t[i][0], t[i][1]); y1[i] = pk2(t[4 + i][0], t[4 + i][1]); }
                *(LDSAS u32x4*)(lds0 + (2 * w + p) * 1024 + lane * 16 + g * 8192) = y0;
                *(LDSAS u32x4*)(lds0 + (2 * w + p) * 1024 + lane * 16 + g * 8192 + 8192) = y1;
            }
            SBAR();
        }
    }
    SKEW(8);
    lds_inc(fa + 16, lane);
    STAMP(0, 3);
    u32x16 xa0, xa1, xa2, xa3, xa4, xa5, xa6, xa7;
    u32x4 w0, w1, w2, w3, w4, fl;
    f32x16 acA, acB;
    unsigned xb[4], tq;
#pragma unroll
    for (int m = 0; m < 4; ++m) xb[m] = lds0 + (32 * wt + r32) * 128 + (((2 * m + h) ^ ((r32 >> 1) & 7)) << 4) + rot * 16384;
    const int q4 = (lane & 15) >> 2, p4 = lane & 3, blk = (lane >> 4) & 1;
    const unsigned tA = lds0 + (8 * h + q4) * 128 + (((4 * wc + 2 * blk + (p4 >> 1)) ^ (((q4 >> 1) & 1) << 2)) << 4) + 8 * (p4 & 1);
    const unsigned tB = tA + 65536, tC = tA + 131072;
    const int tt = rt * 2 + wt, hd = cb * 4 + wc * 2;
#define KXR(R, m, O) "ds_read_b128 v[" #R ":" #R "+3], %[x" #m "] offset:" #O "\n\t"
#define KXN(m) "v_add_u32 %[x" #m "], 0x4000, %[x" #m "]\n\tv_and_b32 %[x" #m "], 0xffff, %[x" #m "]\n\t"
#define KCHK(ST) "v_min3_u32 v116, v116, v117, v118\n\tv_min_u32 v116, v116, v119\n\ts_nop 0\n\tv_readfirstlane_b32 %[t], v116\n\ts_nop 0\n\ts_cmp_gt_u32 %[t], " ST "\n\t"
#define KSPIN(ST) "1:\n\tds_read_b128 v[116:119], %[fa]\n\ts_waitcnt lgkmcnt(0)\n\t" KCHK(ST) "s_cbranch_scc0 1b\n\t"
#define KWR(R, O) "ds_read_b64_tr_b16 v[" #R ":" #R "+1], %[tb] offset:" #O "\n\tds_read_b64_tr_b16 v[" #R "+2:" #R "+3], %[tb] offset:" #O "+512\n\t"
    asm volatile("3:\n\tds_read_b32 v116, %[fa] offset:16\n\ts_waitcnt lgkmcnt(0)\n\tv_readfirstlane_b32 %[t], v116\n\ts_nop 0\n\ts_cmp_gt_u32 %[t], 3\n\ts_cbranch_scc0 3b\n\t"
                 KXR(128, 0, 0) KXR(132, 1, 0) KXR(136, 2, 0) KXR(140, 3, 0) KXR(144, 0, 8192) KXR(148, 1, 8192) KXR(152, 2, 8192) KXR(156, 3, 8192) KXN(0) KXN(1) KXN(2) KXN(3)
                 KSPIN("0") KWR(96, 0) KWR(100, 2048) KWR(104, 4096) KWR(108, 6144)
                 : "={v[116:119]}"(fl), [t] "=&s"(tq), "={v[96:99]}"(w0), "={v[100:103]}"(w1), "={v[104:107]}"(w2), "={v[108:111]}"(w3), "={v[112:115]}"(w4), "={v[64:79]}"(acA), "={v[80:95]}"(acB),
                   "={v[128:143]}"(xa0), "={v[144:159]}"(xa1), "={v[160:175]}"(xa2), "={v[176:191]}"(xa3), "={v[192:207]}"(xa4), "={v[208:223]}"(xa5), "={v[224:239]}"(xa6), "={v[240:255]}"(xa7),
                   [x0] "+v"(xb[0]), [x1] "+v"(xb[1]), [x2] "+v"(xb[2]), [x3] "+v"(xb[3])
                 : [fa] "v"(fa), [tb] "v"(tB) : "scc", "memory");
    STAMP(0, 4);
    auto epi = [&](int m, const f32x16& acc, int hh) {
        const float sc = 0.25f * LOG2E;
        u32x4 o;
#pragma unroll
        for (int i = 0; i < 4; ++i) o[i] = m == 2 ? pk2(acc[8 * hh + 2 * i] * sc, acc[8 * hh + 2 * i + 1] * sc) : pk2(acc[8 * hh + 2 * i], acc[8 * hh + 2 * i + 1]);
        if (m == 2) ST128(o, rs, l16, WS_Q + ((hd + hh) * 64 + tt) * 1024, AUX_SC1);
        else if (m == 0) ST128(o, rs, l16, WS_KV + (hd + hh) * KV_HEAD + (tt >> 3) * KV_CHUNK + (tt & 7) * 1024, AUX_SC1);
        else ST128(o, rs, (r32 >> 4) * KV_HEAD + (16 * h + (lane & 15)) * 16 + hh * 512,
                   WS_KV + hd * KV_HEAD + (tt >> 3) * KV_CHUNK + 8192 + (tt & 7) * 1024, AUX_SC1);
    };
    const unsigned flv = (lane * 4) | ((unsigned)(31 - lane) & 0x80000000u), fls = RFL(WS_FQ + ((cb * 2 + wc) * 64 + tt) * 128);
    const unsigned kvf = RFL((tt & 31) < 8 ? WS_KV + hd * KV_HEAD + (tt >> 3) * KV_CHUNK + (tt & 7) * 1024 : 0x40000000u);
    sfor<0, 96>([&](auto G) {
        constexpr int g = G.value, n = g + 4, m = g >> 5;
        constexpr unsigned o = k1_slot(n >> 3) + (n & 7) * 2048, oi = o & 65535;
        constexpr int aR = m == 1 ? 80 : 64, wR = 96 + 4 * (g % 5), nR = 96 + 4 * (n % 5), xR = 128 + 4 * (g & 31);
        constexpr int nx = (g - 4 >= 0 && g - 4 < 12) + (g - 3 >= 0 && g - 3 < 12) + (g - 2 >= 0 && g - 2 < 12) + (g - 1 >= 0 && g - 1 < 12), wc_ = n >= 96 ? 0 : (8 + 2 * nx > 14 ? 14 : 8 + 2 * nx);
#define KRD(B) "ds_read_b64_tr_b16 v[%c[nr]:%c[nr]+1], %[" B "] offset:%c[oi]\n\tds_read_b64_tr_b16 v[%c[nr]+2:%c[nr]+3], %[" B "] offset:%c[oi]+512\n\t"
        asm volatile(".if %c[bar] == 1\n\t" KCHK("%c[st]") "s_cbranch_scc0 1f\n2:\n\t.subsection 1\n" KSPIN("%c[st]") "s_branch 2b\n\t.subsection 0\n\t.endif\n\t"
                     ".if %c[bar] == 2\n\tds_read_b128 v[116:119], %[fa]\n\t.endif\n\t"
                     ".if %c[rd] == 1\n\t" KRD("tb") ".endif\n\t.if %c[rd] == 2\n\t" KRD("tc") ".endif\n\t.if %c[rd] == 3\n\t" KRD("ta") ".endif\n\t"
                     "s_waitcnt lgkmcnt(%c[wn])\n\t"
                     ".if %c[md] == 0\n\tv_mfma_f32_32x32x16_bf16 v[%c[ar]:%c[ar]+15], v[%c[wr]:%c[wr]+3], v[%c[xr]:%c[xr]+3], v[%c[ar]:%c[ar]+15]\n\t.endif\n\t"
                     ".if %c[md] == 1\n\tv_mfma_f32_32x32x16_bf16 v[%c[ar]:%c[ar]+15], v[%c[xr]:%c[xr]+3], v[%c[wr]:%c[wr]+3], v[%c[ar]:%c[ar]+15]\n\t.endif\n\t"
                     ".if %c[md] == 2\n\tv_mfma_f32_32x32x16_bf16 v[%c[ar]:%c[ar]+15], v[%c[xr]:%c[xr]+3], v[%c[wr]:%c[wr]+3], 0\n\t.endif\n\t"
                     ".if %c[md] == 3\n\tv_mfma_f32_32x32x16_bf16 v[%c[ar]:%c[ar]+15], v[%c[wr]:%c[wr]+3], v[%c[xr]:%c[xr]+3], 0\n\t.endif\n\t"
                     ".if %c[xm] == 1\n\tds_read_b128 v[%c[xd]:%c[xd]+3], %[x0] offset:%c[xo]\n\tds_read_b128 v[%c[xd]+4:%c[xd]+7], %[x1] offset:%c[xo]\n\t.endif\n\t"
                     ".if %c[xm] == 2\n\tds_read_b128 v[%c[xd]:%c[xd]+3], %[x2] offset:%c[xo]\n\tds_read_b128 v[%c[xd]+4:%c[xd]+7], %[x3] offset:%c[xo]\n\t.endif\n\t"
                     ".if %c[xn]\n\t" KXN(0) KXN(1) KXN(2) KXN(3) ".endif"
                     : "+{v[64:79]}"(acA), "+{v[80:95]}"(acB), "+{v[96:99]}"(w0), "+{v[100:103]}"(w1), "+{v[104:107]}"(w2), "+{v[108:111]}"(w3), "+{v[112:115]}"(w4),
                       "+{v[128:143]}"(xa0), "+{v[144:159]}"(xa1), "+{v[160:175]}"(xa2), "+{v[176:191]}"(xa3), "+{v[192:207]}"(xa4), "+{v[208:223]}"(xa5), "+{v[224:239]}"(xa6), "+{v[240:255]}"(xa7),
                       "+{v[116:119]}"(fl), [t] "=&s"(tq), [x0] "+v"(xb[0]), [x1] "+v"(xb[1]), [x2] "+v"(xb[2]), [x3] "+v"(xb[3])
                     : [ta] "v"(tA), [tb] "v"(tB), [tc] "v"(tC), [fa] "v"(fa), [st] "n"(n >> 3), [wn] "n"(wc_),
                       [bar] "n"(K1_ABL ? 0 : n < 96 && (n & 7) == 0 ? 1 : n < 92 && (n & 7) == 4 ? 2 : 0), [rd] "n"(n >= 96 ? 0 : o < 65536 ? 3 : o < 131072 ? 1 : 2), [oi] "n"(oi), [nr] "n"(nR),
                       [md] "n"(m != 1 ? ((g & 31) == 0 ? 3 : 0) : g == 32 ? 2 : 1), [ar] "n"(aR), [wr] "n"(wR), [xr] "n"(xR),
                       [xm] "n"(g < 12 ? 1 + (g & 1) : 0), [xd] "n"(160 + 8 * g), [xo] "n"(((g >> 1) & 1) * 8192), [xn] "n"(g < 11 && (g & 3) == 3 ? 1 : 0) : "scc", "memory");
        if (g == 7 && lane == 0) asm volatile("ds_add_u32 %0, %1" :: "v"(fa + 28), "v"(1u) : "memory");
        if (g == 16 && lane == 0) asm volatile("ds_add_u32 %0, %1" :: "v"(fa + 20), "v"(1u) : "memory");
        if (g == 23 && lane == 0) asm volatile("ds_add_u32 %0, %1" :: "v"(fa + 24), "v"(1u) : "memory");
        if (g == 1 || g == 3) ST128((u32x4{~0u, ~0u, ~0u, ~0u}), rs, l16, WS_Q + ((hd + (g >> 1)) * 64 + tt) * 1024, AUX_SC1);
        if (g == 5 || g == 7 || g == 9 || g == 11) ST128((u32x4{~0u, ~0u, ~0u, ~0u}), rs, l16 + ((g - 5) >> 2) * KV_HEAD + (((g - 5) >> 1) & 1) * 8192, kvf, AUX_SC1);
        if (g == 35) { WAITV(0); __builtin_amdgcn_raw_buffer_store_b32(ep ^ 0x80000000u, rs, flv, fls, AUX_SC1); }
        if (g == 36 || g == 38) epi(0, acA, (g - 36) >> 1);
        if (g == 67 || g == 68) epi(1, acB, g - 67);
        if (g == K1_KV_AT && lane == 0) asm volatile("ds_add_u32 %0, %1" :: "v"(fa + 32), "v"(1u) : "memory");
        if (g == K1_POST_AT) { WAITV(0); __builtin_amdgcn_raw_buffer_store_b32(ep, rs, flv, fls, AUX_SC1); }
    });
    STAMP(0, 2);
    asm volatile("s_nop 15" : "+{v[64:79]}"(acA));
    epi(2, acA, 0); epi(2, acA, 1);
    if (lane == 0) asm volatile("ds_add_u32 %0, %1" :: "v"(fa + 40), "v"(1u) : "memory");
    STAMP(0, 5);
    }
    }
    SBAR();
    const int rt = rt2;
    u32x8 xres;
    u32x4 hreg[2];

    auto issue_wo = [&](int first, int n) {
        const rsrc_t rwo = mk_rsrc(wo, D * D * 2);
        const unsigned v = w64_voff(lane);
        for (int p = first; p < first + n; ++p) dma16(rwo, v, p * 8192 + ct * 128, lds0 + L_WO + p * 1024);
    };
    auto issue_dn = [&](int i0, int n, unsigned dst, int hw) {
        const rsrc_t rwd = mk_rsrc(wd, F * D * 2);
        const unsigned v = w64_voff(lane);
        const unsigned so = ((43 * (hw >> 1) + i0) * 16 + (hw & 1) * 8) * 1024 + ct * 128;
        for (int i = 0; i < n; ++i) dma16(rwd, v, so + i * 16384, lds0 + dst + (hw & 1) * 1024 + i * 2048);
    };
    auto gu_wa = [&](int n) -> unsigned { const unsigned blk = (lane >> 4) & 1;
        return lds0 + L_GU + blk * 49152 + (((4 * h + ((lane & 15) >> 2)) * 96 + n * 32 + (lane & 3) * 8) ^ (blk * 128)); };
    float cs[3] = {0.f, 0.f, 0.f};
    auto gu_colsum = [&]() {
        u32x4 f[3][4];
#pragma unroll
        for (int n = 0; n < 3; ++n) {
            const unsigned a0 = gu_wa(n) + w * 4 * 1536;
#pragma unroll
            for (int i = 0; i < 4; ++i) f[n][i] = __builtin_bit_cast(u32x4, lds_tr2(a0 + i * 1536, a0 + i * 1536 + 768));
        }
        SBAR();
#pragma unroll
        for (int i = 0; i < 4; ++i)
#pragma unroll
            for (int e = 0; e < 4; ++e)
#pragma unroll
                for (int n = 0; n < 3; ++n) cs[n] = dot2(f[n][i][e], ONES2, cs[n]);
#pragma unroll
        for (int n = 0; n < 3; ++n) cs[n] = swap_add(cs[n]);
    };

    unsigned budget = 1u << 16;

    {
        const int qb = bi & 7, wq = w & 3, g = w >> 2, qt = qb * 8 + wq * 2;
        s16x8 q0, q1;
        const unsigned qs0 = RFL(WS_Q + (head * 64 + qt + g) * 1024), qs1 = qs0 ^ 1024;
        unsigned tq, cap = 1u << 22;
#define XSPIN(L, O, N) #L ":\n\tds_read_b32 v132, %[fa] offset:" #O "\n\ts_sub_u32 %[cap], %[cap], 1\n\ts_cbranch_scc1 9f\n\ts_waitcnt lgkmcnt(0)\n\tv_readfirstlane_b32 %[t], v132\n\ts_sleep 1\n\ts_cmp_lt_u32 %[t], " #N "\n\ts_cbranch_scc1 " #L "b\n\t"
        SKEW(1);
        asm volatile(XSPIN(3, 40, 4) XSPIN(4, 36, 1) "s_sleep %c[dl]\n9:\n\tbuffer_load_dwordx4 v[128:131], %[l16], %[rs], %[qs] offen sc1\n\tbuffer_load_dwordx4 v[156:159], %[l16], %[rs], %[qs1] offen sc1"
                     : "={v[128:131]}"(q0), "={v[156:159]}"(q1), [cap] "+s"(cap), [t] "=&s"(tq)
                     : [l16] "v"(l16), [rs] "s"(rs), [qs] "s"(qs0), [qs1] "s"(qs1), [fa] "v"(lds0 + K1_FLG), [dl] "n"(DELAY_Q) : "scc", "v132", "memory");
        SBAR();
        const unsigned kvs = WS_KV + head * KV_HEAD;
        unsigned ks = kvs + (g * 4 + 1) * KV_CHUNK + wq * 4096, km = lds0 + g * 32768 + 16384 + wq * 4096;
        const rsrc_t rg = mk_rsrc(g ? wu : wg, (bi < 29 && ABL_GU != 1) ? D * F * 2 : 0), rwd = mk_rsrc(wd, F * D * 2), rwo = mk_rsrc(wo, D * D * 2), rx = mk_rsrc(x, S * D * 2);
        unsigned gs = wq * 4 * 32 * (F * 2) + bi * 96, gm = lds0 + L_GU + g * 49152 + wq * 12288;
        const unsigned long long msk = 0x0000ffff0000ffffull;
        constexpr unsigned OOB = 0x80000000u, SOOB = 0x40000000u;
        auto gu_voff = [&](unsigned c) { const unsigned r = __umul24(c, 171u) >> 10; return __umul24(r, F * 2 - 96u) + c * 16; };
        const unsigned gv0 = gu_voff(lane ^ (g * 8)), gv1 = gu_voff((64 + lane) ^ (g * 8)), gv2 = gu_voff((128 + lane) ^ (g * 8));
        const unsigned pvd = (lane * 128) | ((unsigned)(42 - lane) & OOB), pvo = (lane * 128) | ((unsigned)(15 - lane) & OOB), psd = RFL((bi * 344 + w * 43) * 128), pso = RFL((bi * 128 + w * 16) * 128);
        const unsigned wv = w64_voff(lane), xv = r32 * 1024 + 8 * h, xs = RFL((rt * 64 + wt * 32) * 1024 + (ct * 64 + kh * 32) * 2);
        const unsigned kb = lds0 + g * 32768 + l16, vb = lds0 + g * 32768 + 8192 + (16 * h + (r32 & 15)) * 16;
        const unsigned cst = (r32 & 27) == 16 ? ONES2 : 0u;
        const unsigned fsv = h * 4096 + r32 * 16, fs = RFL(w == 0 ? WS_ST + (rt * 32 + ct * 2) * 256 : SOOB), fpv = (unsigned)(0 - lane) & OOB, fps = RFL(w == 0 ? WS_FP + b * 4 : SOOB);
        u32x4 ones, fpl, pA, pB; unsigned pfd, epv, flq; f32x16 sB, o0, o1; s16x8 v1, v1b;
        const unsigned fo = WS_O + ((qt + g) * 32 + head) * 1024, fh = WS_H + ((rt * 2 + wt) * 32 + ct * 4 + (w >> 1)) * 1024, fg = RFL(bi < 29 ? WS_HH + ((xcd * 8 + w) * NU + bi * 3) * 1024 : SOOB), fg2 = RFL(bi < 28 ? fg : SOOB);
        STAMP(0, 6);
        SBAR();
#define XKV "s_mov_b32 m0, %[km]\n\ts_add_u32 %[km], %[km], 0x400\n\tbuffer_load_dwordx4 %[l16], %[rs], %[ks] offen sc1 lds\n\ts_add_u32 %[ks], %[ks], 0x400\n\t"
        const unsigned lm0 = RFL(lds0 + ((w >> 1) & 1) * 32768 + (w & 1) * 8192), ls0 = RFL(kvs + ((w >> 1) & 1) * 4 * KV_CHUNK + (w & 1) * 8192), fq = RFL(WS_FQ + (head >> 1) * 8192);
        const unsigned pvf = lane * 128;
        unsigned lm, ls;
#define XLD "s_mov_b32 m0, %[lm]\n\ts_add_u32 %[lm], %[lm], 0x400\n\tbuffer_load_dwordx4 %[l16], %[rs], %[ls] offen sc1 lds\n\ts_add_u32 %[ls], %[ls], 0x400\n\t"
#define XRB(R, O) "ds_read_b128 v[" #R ":" #R "+3], v133 offset:" #O "\n\t"
#define XMX(R) "v_max3_u32 v132, v132, v[" #R "], v[" #R "+1]\n\tv_max3_u32 v132, v132, v[" #R "+2], v[" #R "+3]\n\t"
        asm volatile(".set tagj, 0\n\t.rept 8\n\tv_mov_b64 v[96+tagj:97+tagj], 0\n\tv_mov_b64 v[160+tagj:161+tagj], 0\n\t.set tagj, tagj+2\n\t.endr\n\t"
                     "v_mov_b64 v[152:153], 0\n\tv_mov_b64 v[154:155], 0\n\tv_mov_b64 v[204:205], -1\n\tv_mov_b64 v[206:207], -1\n\tv_mov_b32 v201, %[ep]\n\t"
                     ".set tagj, 0\n\t.rept 4\n\tv_mov_b32 v[136+tagj], %[cst]\n\tv_mov_b32 v[144+tagj], %[cst]\n\t.set tagj, tagj+1\n\t.endr\n\t"
                     "s_cmp_lt_u32 %[w], 4\n\ts_cbranch_scc1 1f\n\tv_add_u32 v133, %[lm0], %[l16]\n\ts_waitcnt vmcnt(2)\n"
                     "5:\n\t" XRB(64, 0) XRB(68, 1024) XRB(72, 2048) XRB(76, 3072) XRB(112, 4096) XRB(116, 5120) XRB(120, 6144) XRB(124, 7168) "v_mov_b32 v132, 0\n\ts_waitcnt lgkmcnt(0)\n\t"
                     XMX(64) XMX(68) XMX(72) XMX(76) XMX(112) XMX(116) XMX(120) XMX(124) "v_cmp_eq_u32 vcc, -1, v132\n\ts_cbranch_vccnz 6f\n\t"
                     ".subsection 1\n6:\n\ts_cmp_eq_u32 %[bud], 0\n\ts_cbranch_scc1 1f\n\ts_sub_u32 %[bud], %[bud], 1\n\ts_mov_b32 %[lm], %[lm0]\n\ts_mov_b32 %[ls], %[ls0]\n\t"
                     XLD XLD XLD XLD XLD XLD XLD XLD "s_waitcnt vmcnt(0)\n\ts_branch 5b\n\t.subsection 0\n"
                     "1:\n\ts_waitcnt vmcnt(0)\n\tv_max3_u32 v132, v128, v129, v130\n\tv_max3_u32 v132, v132, v131, v156\n\tv_max3_u32 v132, v132, v157, v158\n\tv_max_u32 v132, v132, v159\n\t"
                     "v_cmp_eq_u32 vcc, -1, v132\n\ts_cbranch_vccnz 3f\n2:\n\t.subsection 1\n3:\n\ts_cmp_eq_u32 %[bud], 0\n\ts_cbranch_scc1 2b\n\ts_sub_u32 %[bud], %[bud], 1\n\t"
                     "buffer_load_dwordx4 v[128:131], %[l16], %[rs], %[qs] offen sc1\n\tbuffer_load_dwordx4 v[156:159], %[l16], %[rs], %[qs1] offen sc1\n\ts_branch 1b\n\t.subsection 0\n\t"
                     "s_barrier"
                     : "+{v[128:131]}"(q0), "+{v[156:159]}"(q1), [bud] "+s"(budget), [lm] "=&s"(lm), [ls] "=&s"(ls),
                       "={v[80:95]}"(sB), "={v[96:111]}"(o0), "={v[160:175]}"(o1), "={v[144:147]}"(v1), "={v[136:139]}"(v1b), "={v[148:151]}"(pA), "={v[152:155]}"(pB),
                       "={v[192:199]}"(xres), "={v200}"(pfd), "={v201}"(epv), "={v202}"(flq), "={v[204:207]}"(ones), "={v[208:211]}"(fpl)
                     : [l16] "v"(l16), [rs] "s"(rs), [qs] "s"(qs0), [qs1] "s"(qs1), [w] "s"(w), [ep] "s"(ep), [cst] "v"(cst), [lm0] "s"(lm0), [ls0] "s"(ls0),
                       "v"(gv0), "v"(gv1), "v"(gv2), "v"(pvd), "v"(pvo), "v"(wv), "v"(xv), "v"(kb), "v"(vb), "v"(fsv), "v"(fpv), "v"(pvf),
                       "s"(rg), "s"(rwd), "s"(rwo), "s"(rx), "s"(gs), "s"(gm), "s"(psd), "s"(pso), "s"(xs), "s"(fs), "s"(fps), "s"(fo), "s"(fh), "s"(fg), "s"(fg2), "s"(fq), "s"(km), "s"(ks)
                     : "m0", "scc", "vcc", "v132", "v133", "v64", "v65", "v66", "v67", "v68", "v69", "v70", "v71", "v72", "v73", "v74", "v75", "v76", "v77", "v78", "v79",
                       "v112", "v113", "v114", "v115", "v116", "v117", "v118", "v119", "v120", "v121", "v122", "v123", "v124", "v125", "v126", "v127", "memory");
        STAMP(0, 7);
        FLUSH(0);
        km = RFL(km); ks = RFL(ks); budget = RFL(budget);
        STAMP(1, 0);
        STAMP(1, 1);
        f32x16 n0, n1, sA;
        s16x8 kf = lds_b128(kb), v0;
        {
            const f32x16 z = {};
            const s16x8 kz = lds_b128(lds0 + l16);
            const u32x4 vv = __builtin_bit_cast(u32x4, lds_b128(vb)), cc = {cst, cst, cst, cst};
            const f32x16 s0 = MFMA(kz, q0, z), s1 = MFMA(kz, q1, z);
            float m0 = __builtin_fmaxf(s0[0], s0[1]), m1 = __builtin_fmaxf(s1[0], s1[1]);
#pragma unroll
            for (int i = 2; i < 16; i += 2) { m0 = __builtin_fmaxf(__builtin_fmaxf(m0, s0[i]), s0[i + 1]); m1 = __builtin_fmaxf(__builtin_fmaxf(m1, s1[i]), s1[i + 1]); }
            m0 = swap_max(m0); m1 = swap_max(m1);
#pragma unroll
            for (int i = 0; i < 16; ++i) { n0[i] = -m0; n1[i] = -m1; }
            sA = MFMA(kf, q0, n0);
            v0 = __builtin_bit_cast(s16x8, r32 < 16 ? vv : cc);
        }
#define XE(C, i) "v_exp_f32 v[" #C "+" #i "], v[" #C "+" #i "]\n\t"
#define XC(P, C, i) "v_cvt_pk_bf16_f32 v" #P ", v[" #C "+" #i "], v[" #C "+" #i "+1]\n\t"
#define XG1(C) XE(C, 0) XE(C, 1) XE(C, 2) XE(C, 3) XC(148, C, 0) XE(C, 4) XE(C, 5) XC(149, C, 2)
#define XG2(C) XE(C, 6) XE(C, 7) XC(150, C, 4) XE(C, 8) XC(151, C, 6) XE(C, 9) XE(C, 10)
#define XG3(C) XE(C, 11) XC(152, C, 8) XE(C, 12) XE(C, 13) XC(153, C, 10) XE(C, 14) XE(C, 15) XC(154, C, 12) XC(155, C, 14)
#define XM(D, A, B, C) "v_mfma_f32_32x32x16_bf16 v[" #D ":" #D "+15], v[" #A ":" #A "+3], v[" #B ":" #B "+3], v[" #C ":" #C "+15]\n\t"
#define XRK(BASE, O) "ds_read_b128 v[132:135], " BASE " offset:" #O "\n\t"
#define XRV(R, BASE, O) "s_mov_b64 exec, %[msk]\n\tds_read_b128 v[" #R ":" #R "+3], " BASE " offset:" #O "\n\ts_mov_b64 exec, -1\n\t"
#define XW(n) "s_waitcnt lgkmcnt(" #n ")\n\t"
#define XTILE(VA, RK, RV1, WA, X1, VB, X2, RV0) \
        XM(80, 132, 156, 176) RK XG1(64) XM(160, VA, 152, 160) RV1 XG2(64) XW(WA) XM(96, 140, 148, 96) XG3(64) \
        XW(0) XM(64, 132, 128, 112) X1 XG1(80) XM(96, VB, 152, 96) X2 XG2(80) XM(160, 140, 148, 160) RV0 XG3(80)
#define XGU(j) "s_mov_b32 m0, %[gm]\n\ts_add_u32 %[gm], %[gm], 0x400\n\tbuffer_load_dwordx4 %[gv" #j "], %[rg], %[gs] offen lds\n\t"
#define XGU2 XGU(2) "s_add_u32 %[gs], %[gs], 88064\n\t"
#define XPF "buffer_load_dword v200, %[pvd], %[rwd], %[psd] offen\n\tbuffer_load_dword v200, %[pvo], %[rwo], %[pso] offen\n\t"
#define XWO "s_mov_b32 m0, %[km]\n\ts_add_u32 %[km], %[km], 0x400\n\tbuffer_load_dwordx4 %[wv], %[rwo], %[ks] offen lds\n\ts_add_u32 %[ks], %[ks], 0x2000\n\t"
#define XST(V, SO, O) "buffer_store_dwordx4 v[204:207], " V ", %[rs], " SO " offen offset:" #O " sc1\n\t"
#define XFP "buffer_store_dword v201, %[fpv], %[rs], %[fps] offen sc1\n\t"
#define XFL "buffer_load_dwordx4 v[208:211], %[l16], %[rs], %[fp] offen sc1\n\t"
#define XQL "buffer_load_dword v202, %[pvf], %[rs], %[fq] offen sc1\n\t"
#define XQC(n) "s_waitcnt vmcnt(" #n ")\n\tv_cmp_ne_u32 vcc, v201, v202\n\ts_cbranch_vccnz 3f\n2:\n\t.subsection 1\n3:\n\ts_cmp_eq_u32 %[bud], 0\n\ts_cbranch_scc1 2b\n\ts_sub_u32 %[bud], %[bud], 1\n\t" \
               XQL "s_waitcnt vmcnt(0)\n\tv_cmp_ne_u32 vcc, v201, v202\n\ts_cbranch_vccnz 3b\n\ts_branch 2b\n\t.subsection 0\n\t"
#define XXR(R, O) "buffer_load_dwordx2 v[" #R ":" #R "+1], %[xv], %[rx], %[xs] offen offset:" #O "\n\t"
#define AOPS : "+{v[64:79]}"(sA), "+{v[80:95]}"(sB), "+{v[96:111]}"(o0), "+{v[160:175]}"(o1), "+{v[112:127]}"(n0), "+{v[176:191]}"(n1), "+{v[128:131]}"(q0), "+{v[156:159]}"(q1), \
               "+{v[132:135]}"(kf), "+{v[140:143]}"(v0), "+{v[144:147]}"(v1), "+{v[136:139]}"(v1b), "+{v[148:151]}"(pA), "+{v[152:155]}"(pB), "+{v[192:199]}"(xres), "+{v200}"(pfd), \
               "+{v201}"(epv), "+{v202}"(flq), "+{v[204:207]}"(ones), "+{v[208:211]}"(fpl), [bud] "+s"(budget), \
               [km] "+s"(km), [ks] "+s"(ks), [gm] "+s"(gm), [gs] "+s"(gs) \
             : [ka] "v"(ka), [va] "v"(va), [kn] "v"(kn), [vn] "v"(vn), [msk] "s"(msk), [l16] "v"(l16), [rs] "s"(rs), [rg] "s"(rg), [gv0] "v"(gv0), [gv1] "v"(gv1), [gv2] "v"(gv2), \
               [pvd] "v"(pvd), [pvo] "v"(pvo), [wv] "v"(wv), [xv] "v"(xv), [xs] "s"(xs), [psd] "s"(psd), [pso] "s"(pso), [rwd] "s"(rwd), [rwo] "s"(rwo), [rx] "s"(rx), \
               [fo] "s"(fo), [fh] "s"(fh), [fg] "s"(fg), [fsv] "v"(fsv), [fs] "s"(fs), [fg2] "s"(fg2), [fpv] "v"(fpv), [fps] "s"(fps), [fp] "s"(WS_FP), [pvf] "v"(pvf), [fq] "s"(fq) : "m0", "scc", "vcc", "memory"
#define XBAR(n) asm volatile("s_waitcnt vmcnt(" #n ") lgkmcnt(0)\n\ts_barrier" AOPS)
#define XROUND(A1, A2, B1, B2, C1, C2, D1, D2, E1, E2, F1, F2) asm volatile( \
            XTILE(136, XRK("%[ka]", 1024), XRV(144, "%[va]", 512), 2, A1, 144, A2, XRV(140, "%[va]", 1024)) \
            XTILE(144, XRK("%[ka]", 2048), XRV(144, "%[va]", 1536), 2, B1, 144, B2, XRV(140, "%[va]", 2048)) \
            XTILE(144, XRK("%[ka]", 3072), XRV(144, "%[va]", 2560), 2, C1, 144, C2, XRV(140, "%[va]", 3072)) \
            XTILE(144, XRK("%[ka]", 4096), XRV(144, "%[va]", 3584), 2, D1, 144, D2, XRV(140, "%[va]", 4096)) \
            XTILE(144, XRK("%[ka]", 5120), XRV(144, "%[va]", 4608), 2, E1, 144, E2, XRV(140, "%[va]", 5120)) \
            XTILE(144, XRK("%[ka]", 6144), XRV(144, "%[va]", 5632), 2, F1, 144, F2, XRV(140, "%[va]", 6144)) \
            XTILE(144, XRK("%[ka]", 7168), XRV(144, "%[va]", 6656), 2, XRV(136, "%[va]", 7680), 144, "", XRV(140, "%[va]", 7168)) AOPS)
#define XLAST(X1, X2) asm volatile(XTILE(144, XRK("%[kn]", 0), "", 1, X1, 136, X2, XRV(140, "%[vn]", 0)) AOPS)
        unsigned ka = kb, va = vb, kn = kb + 16384, vn = vb + 16384;
        asm volatile("s_nop 15" AOPS);
        STAMP(1, 7);
        XROUND(XQL, XGU(0), "", XQC(1) XKV, XKV, XKV, XKV, XST("%[l16]", "%[fo]", 0), XGU(1), XST("%[fsv]", "%[fs]", 0), XGU2, XST("%[l16]", "%[fh]", 0));
        STAMP(1, 8);
        XBAR(5);
        STAMP(1, 9);
        km -= 16384 + 4096; ks += KV_CHUNK - 4096;
        XLAST(XKV, XKV);
        ka = kb + 16384; va = vb + 16384; kn = kb; vn = vb;
        XROUND(XKV, XKV, XGU(0), XST("%[l16]", "%[fg]", 0), XGU(1), XGU2, XST("%[l16]", "%[fg]", 1024), XGU(0), "", XGU(1), XST("%[l16]", "%[fg2]", 2048), "");
        STAMP(1, 10);
        XBAR(8);
        STAMP(1, 11);
        km += 16384 - 4096; ks += KV_CHUNK - 4096;
        XLAST(XKV, XKV);
        ka = kb; va = vb; kn = kb + 16384; vn = vb + 16384;
        XROUND(XKV, XKV, XGU2, "", XGU(0), "", XGU(1), "", XGU2, "", XPF, "");
        STAMP(1, 12);
        XBAR(6);
        STAMP(1, 13);
        km = lds0 + g * 32768 + wq * 4096; ks = (32 * g + 4 * wq) * 8192 + ct * 128;
        XLAST(XFP, XWO);
        ka = kb + 16384; va = vb + 16384; kn = kb; vn = vb;
        XROUND(XWO, XWO, XWO, "", XXR(192, 0), "", XXR(194, 16), "", XXR(196, 32), "", XXR(198, 48), "");
        XLAST(XFL, "");
        STAMP(1, 14);
        asm volatile(XM(160, 136, 152, 160) "s_waitcnt vmcnt(0) lgkmcnt(0)\n\ts_barrier\n\ts_nop 15" AOPS);
        STAMP(1, 4);
        SKEW(2);
        f32x16 o = o0;
        const unsigned xa = lds0 + g * 32768 + 16384 + wq * 3072 + l16, xb = lds0 + (g ^ 1) * 32768 + 16384 + wq * 3072 + l16;
        *(LDSAS f32x4*)(xa) = f32x4{o1[0], o1[1], o1[2], o1[3]};
        *(LDSAS f32x4*)(xa + 1024) = f32x4{o1[4], o1[5], o1[6], o1[7]};
        *(LDSAS float*)(xa + 2048) = o1[8];
        {
            float big = __builtin_fmaxf(o0[8], o1[8]) * 1e19f;
            float chk = o0[8] + o1[8];
#pragma unroll
            for (int i = 0; i < 8; ++i) { big = __builtin_fmaxf(__builtin_fmaxf(big, __builtin_fabsf(o0[i])), __builtin_fabsf(o1[i])); chk += o0[i] + o1[i]; }
            *(LDSAS unsigned*)(lds0 + L_FLAG + w * 4) = __any(!(big < 1e38f) || chk != chk);
        }
        BAR_L();
        const f32x4 p0 = lds_f4(xb), p1 = lds_f4(xb + 1024); const float pl = *(const LDSAS float*)(xb + 2048);
        const u32x4 f0 = *(const LDSAS u32x4*)(lds0 + L_FLAG), f1 = *(const LDSAS u32x4*)(lds0 + L_FLAG + 16);
#pragma unroll
        for (int i = 0; i < 4; ++i) { o[i] += p0[i]; o[4 + i] += p1[i]; }
        o[8] += pl;
        if (__builtin_expect(RFL(f0[0] | f0[1] | f0[2] | f0[3] | f1[0] | f1[1] | f1[2] | f1[3]), 0)) {
            BAR_L();
            const s16x8 qf = q0;
            const u32x4 cc = {cst, cst, cst, cst};
            const unsigned vf = lds0 + 16384 + 8192 + (16 * h + (r32 & 15)) * 16;
            float m = -__builtin_inff();
            o = f32x16{};
            const f32x16 z = {};
#pragma unroll 1
            for (int c = 0; c < 8; ++c) {
                dma16<true>(rs, l16, kvs + c * KV_CHUNK + w * 2048, lds0 + 16384 + w * 2048);
                dma16<true>(rs, l16, kvs + c * KV_CHUNK + w * 2048 + 1024, lds0 + 16384 + w * 2048 + 1024);
                BAR_VL(0);
#pragma unroll 1
                for (int t = 0; t < 8; ++t) {
                    f32x16 s = MFMA(lds_b128(lds0 + 16384 + l16 + t * 1024), qf, z);
                    float mx = s[0];
#pragma unroll
                    for (int i = 1; i < 16; ++i) mx = __builtin_fmaxf(mx, s[i]);
                    mx = __builtin_fmaxf(swap_max(mx), m);
                    const float al = __builtin_amdgcn_exp2f(m - mx);
                    m = mx;
#pragma unroll
                    for (int i = 0; i < 16; ++i) { o[i] *= al; s[i] = __builtin_amdgcn_exp2f(s[i] - m); }
#pragma unroll
                    for (int i = 0; i < 4; ++i) { pA[i] = pk2(s[2 * i], s[2 * i + 1]); pB[i] = pk2(s[8 + 2 * i], s[9 + 2 * i]); }
                    const u32x4 a = __builtin_bit_cast(u32x4, lds_b128(vf + t * 1024)), c2 = __builtin_bit_cast(u32x4, lds_b128(vf + t * 1024 + 512));
                    o = MFMA(__builtin_bit_cast(s16x8, r32 < 16 ? a : cc), __builtin_bit_cast(s16x8, pA), o);
                    o = MFMA(__builtin_bit_cast(s16x8, r32 < 16 ? c2 : cc), __builtin_bit_cast(s16x8, pB), o);
                }
                BAR_L();
            }
        }
        {
            const float inv = __builtin_amdgcn_rcpf(o[8]);
            u32x4 ov;
#pragma unroll
            for (int i = 0; i < 4; ++i) ov[i] = pk2(o[2 * i] * inv, o[2 * i + 1] * inv);
            ST128(ov, rs, l16, fo, AUX_SC1);
        }
        STAMP(1, 5);
        BAR_L();
        if (w >= 4) issue_wo(16 + ((w - 4) >> 1) * 32 + (w & 1) * 8, 8);
        gu_colsum();
#pragma unroll 1
        for (unsigned spins = 0; spins < SPIN_CAP && !__all(fpl[0] == ep && fpl[1] == ep && fpl[2] == ep && fpl[3] == ep); ++spins) {
            __builtin_amdgcn_s_sleep(2);
            fpl = __builtin_amdgcn_raw_buffer_load_b128(rs, l16, WS_FP, AUX_SC1);
        }
        STAMP(1, 6);
        FLUSH(1);
    }

    {
        SKEW(3);
        STAMP(2, 0);
        f32x16 a0, a1;
        if (w < 4) {
            const unsigned t0 = lds0 + L_WO + kh * 32768 + w64_tr(lane, kh), t1 = lds0 + L_WO + kh * 32768 + w64_tr(lane, kh ^ 1);
            G2 g2;
            g2.tb[0] = g2.tb[2] = g2.tb[4] = t0; g2.tb[1] = g2.tb[3] = g2.tb[5] = t1;
            g2.sa = g2.s0 = RFL(WS_O + ((rt * 2 + wt) * 32 + kh * 16) * 1024); g2.l16 = l16; g2.budget = budget; g2.rs = rs; g2.a1 = f32x16{};
#pragma unroll
            for (int i = 0; i < 8; ++i) { g2.a0[2 * i] = bflo(xres[i]); g2.a0[2 * i + 1] = bfhi(xres[i]); }
            __builtin_amdgcn_s_sleep(DELAY_O);
            STAMP(2, 1);
            g2_steps<0, 16, 0, 6, -1>(g2);
            a0 = g2.a0; a1 = g2.a1; STAMPV(15, budget - g2.budget); budget = g2.budget;
        } else BAR_VL(0);
        STAMP(2, 2);
        SKEW(4);
        BAR_L();
        if (w < 4) {
#pragma unroll
            for (int g = 0; g < 4; ++g) *(LDSAS f32x4*)(lds0 + w * 4096 + g * 1024 + lane * 16) = f32x4{a1[4 * g], a1[4 * g + 1], a1[4 * g + 2], a1[4 * g + 3]};
        }
#pragma unroll
        for (int n = 0; n < 3; ++n) *(LDSAS float*)(lds0 + L_TAB + (w * 96 + n * 32 + r32) * 4) = cs[n];
        BAR_L();
        if (w >= 4) {
            if (tid < 256 + 96) {
                float t[8];
#pragma unroll
                for (int i = 0; i < 8; ++i) t[i] = *(const LDSAS float*)(lds0 + L_TAB + (i * 96 + tid - 256) * 4);
                *(LDSAS float*)(lds0 + L_TAB + 3072 + (tid - 256) * 4) = ((t[0] + t[1]) + (t[2] + t[3])) + ((t[4] + t[5]) + (t[6] + t[7]));
            }
        } else {
            f32x4 p[4];
#pragma unroll
            for (int g = 0; g < 4; ++g) p[g] = lds_f4(lds0 + (w ^ 2) * 4096 + g * 1024 + lane * 16);
            SBAR();
#pragma unroll
            for (int g = 0; g < 4; ++g) {
                hreg[g >> 1][(g & 1) * 2] = pk2(a0[4 * g] + p[g][0], a0[4 * g + 1] + p[g][1]);
                hreg[g >> 1][(g & 1) * 2 + 1] = pk2(a0[4 * g + 2] + p[g][2], a0[4 * g + 3] + p[g][3]);
            }
            const unsigned so = WS_H + ((rt * 2 + wt) * 32 + (ct * 2 + kh) * 2) * 1024;
            ST128(hreg[0], rs, lane * 16, so, AUX_SC1);
            ST128(hreg[1], rs, lane * 16 + 1024, so, AUX_SC1);
            SBAR();
            float sm = 0.f, sq = 0.f;
#pragma unroll
            for (int i = 0; i < 8; ++i) { sm = dot2(hreg[i >> 2][i & 3], ONES2, sm); sq = dot2(hreg[i >> 2][i & 3], hreg[i >> 2][i & 3], sq); }
            sm = swap_add(sm); sq = swap_add(sq);
            if (h == 0) __builtin_amdgcn_raw_buffer_store_b64(u32x2{__float_as_uint(sm), __float_as_uint(sq)}, rs, r32 * 8,
                                                              WS_ST + ((rt * 2 + wt) * 16 + ct * 2 + kh) * 256, AUX_SC1);
        }
        STAMP(2, 3);
        BAR_L();
        if (w >= 4) { issue_dn(0, 15, ((w - 4) >> 1) * 15 * 2048, w - 4); if (bi >= 29) issue_dn(15, 24, L_GU + ((w - 4) >> 1) * 49152, w - 4); }
        STAMP(2, 5);
        FLUSH(2);
    }

    {
        SKEW(5);
        STAMP(3, 0);
        if (bi < 29) {
            const int tt = xcd * 8 + w;
            GU gu;
            gu.a0 = gu.a1 = gu.a2 = f32x16{};
            gu.sa = gu.s0 = RFL(WS_H + tt * 32768); gu.sv = 8 * h * 256 + r32 * 8; gu.ss = RFL(WS_ST + tt * 4096); gu.l16 = l16; gu.budget = budget; gu.rs = rs;
            gu.dm = RFL(lds0 + L_GU + (w >> 2) * 49152 + (w & 3) * 6144); gu.ds = RFL(((43 * (w >> 2) + 15) * 16 + (w & 3) * 48) * 1024 + ct * 128);
            gu.dm2 = RFL(lds0 + L_GU + (w >> 2) * 49152 + 24576 + (w & 3) * 3072); gu.ds2 = RFL(((43 * (w >> 2) + 27) * 16 + (w & 3) * 24) * 1024 + ct * 128);
            gu.dm3 = RFL(lds0 + L_TAB + (w >> 2) * 2048 + (w & 1) * 1024); gu.ds3 = RFL(((43 * (w >> 2) + 39) * 16 + (w & 1) * 8) * 1024 + ct * 128);
            gu.dv = w64_voff(lane); gu.rd = mk_rsrc(wd, F * D * 2);
            gu.wa[0] = gu_wa(0); gu.wa[1] = gu_wa(1); gu.wa[2] = gu_wa(2);
            float csl[3];
#pragma unroll
            for (int n = 0; n < 3; ++n) csl[n] = *(const LDSAS float*)(lds0 + L_TAB + 3072 + (n * 32 + r32) * 4);
            if (w < 4) __builtin_amdgcn_s_sleep(DELAY_H);
            STAMP(3, 2);
            gu_steps<0, 18>(gu);
#pragma unroll 1
            for (;;) {
                unsigned mx = gu.st[0];
#pragma unroll
                for (int i = 1; i < 16; ++i) mx = gu.st[i] > mx ? gu.st[i] : mx;
                if (__builtin_expect(!__any(mx == ~0u) || gu.budget == 0, 1)) break;
                --gu.budget;
                asm volatile(".set tagj, 0\n\t.rept 8\n\tbuffer_load_dwordx2 v[172+2*tagj:173+2*tagj], %[sv], %[rs], %[ss] offen offset:tagj*256 sc1\n\t.set tagj, tagj+1\n\t.endr\n\ts_waitcnt vmcnt(0)"
                             : "+{v[172:187]}"(gu.st), "+{v[140:155]}"(gu.r0), "+{v[156:171]}"(gu.r1), "+{v[112:119]}"(gu.w0), "+{v[124:131]}"(gu.w1), "+{v[132:139]}"(gu.w2),
                               "+{v[64:79]}"(gu.a0), "+{v[80:95]}"(gu.a1), "+{v[96:111]}"(gu.a2) : [sv] "v"(gu.sv), [rs] "s"(rs), [ss] "s"(gu.ss) : "memory");
            }
            float sm = 0.f, sq = 0.f;
#pragma unroll
            for (int i = 0; i < 8; ++i) { sm += __uint_as_float(gu.st[2 * i]); sq += __uint_as_float(gu.st[2 * i + 1]); }
            sm = swap_add(sm); sq = swap_add(sq);
            const float mean = sm * (1.f / D);
            const float var = __builtin_fmaxf(sq * (1.f / D) - mean * mean, 0.f) + LN_EPS;
            float rstd = __builtin_amdgcn_rsqf(var), sd = var * rstd, ga = -LOG2E * rstd;
            u32x4 mf, cf[3];
            {
                const unsigned mh = pk2(-mean, -mean);
                const unsigned ml = pk2(-mean - bflo(mh), 0.f);
                mf = h ? u32x4{0u, 0u, 0u, 0u} : u32x4{(mh & 0xffffu) | (ml << 16), mh & 0xffffu, 0u, 0u};
#pragma unroll
                for (int n = 0; n < 3; ++n) { const unsigned ch = pk2(csl[n], csl[n]); cf[n] = u32x4{ch, pk2(csl[n] - bflo(ch), 0.f), 0u, 0u}; PIN(cf[n]); }
                PIN(mf); PIN(sd); PIN(ga);
            }
            gu_steps<18, 32>(gu);
            asm volatile("v_mfma_f32_32x32x16_bf16 v[64:79], %[c0], %[m], v[64:79]\n\tv_mfma_f32_32x32x16_bf16 v[80:95], %[c1], %[m], v[80:95]\n\t"
                         "v_mfma_f32_32x32x16_bf16 v[96:111], %[c2], %[m], v[96:111]\n\ts_nop 10"
                         : "+{v[64:79]}"(gu.a0), "+{v[80:95]}"(gu.a1), "+{v[96:111]}"(gu.a2) : [c0] "v"(cf[0]), [c1] "v"(cf[1]), [c2] "v"(cf[2]), [m] "v"(mf));
            STAMP(3, 3);
            SKEW(6);
            STAMPV(15, budget - gu.budget); budget = gu.budget;
            auto epi = [&](int n, const f32x16& acc) {
                f32x2 e[4], y[4];
#pragma unroll
                for (int i = 0; i < 4; ++i) e[i] = f32x2{acc[2 * i], acc[2 * i + 1]} * ga;
#pragma unroll
                for (int i = 0; i < 4; ++i) e[i] = f32x2{__builtin_amdgcn_exp2f(e[i][0]), __builtin_amdgcn_exp2f(e[i][1])} * sd + sd;
#pragma unroll
                for (int i = 0; i < 4; ++i) y[i] = f32x2{acc[8 + 2 * i], acc[9 + 2 * i]} * f32x2{__builtin_amdgcn_rcpf(e[i][0]), __builtin_amdgcn_rcpf(e[i][1])};
                const u32x4 ov = {pk2(y[0][0], y[0][1]), pk2(y[1][0], y[1][1]), pk2(y[2][0], y[2][1]), pk2(y[3][0], y[3][1])};
                ST128(ov, rs, (n < 2 || bi < 28) ? l16 : 0x80000000u, WS_HH + (tt * NU + bi * 3 + n) * 1024, AUX_SC1);
            };
            epi(0, gu.a0);
            asm volatile("" : "+{v[80:95]}"(gu.a1));
            epi(1, gu.a1);
            asm volatile("" : "+{v[96:111]}"(gu.a2));
            epi(2, gu.a2);
        }
        STAMP(3, 4);
        if (bi >= 29) WAITV(0);
        BAR_VL(3);
        STAMP(3, 5);
        if (w >= 4) {
            if (bi < 29) issue_dn(33, 6, L_GU + ((w - 4) >> 1) * 49152 + 36864, w - 4); else issue_dn(39, 1, L_TAB + ((w - 4) >> 1) * 2048, w - 4);
            STAMP(3, 6);
        }
        FLUSH(3);
    }

    {
        SKEW(7);
        STAMP(4, 0);
        if (b == 0 && tid == 0) __builtin_amdgcn_raw_buffer_store_b32(ep, rs, 0, WS_EP, AUX_SC1);
        f32x16 a0, a1;
        if (w < 4) {
            const unsigned t0 = lds0 + w64_tr(lane, kh), t1 = lds0 + w64_tr(lane, kh ^ 1);
            G2 g2;
            g2.tb[0] = t0 + kh * 15 * 2048; g2.tb[1] = t1 + kh * 15 * 2048; g2.tb[2] = t0 + L_GU + kh * 24 * 2048; g2.tb[3] = t1 + L_GU + kh * 24 * 2048;
            g2.tb[4] = t0 + L_TAB + kh * 2048; g2.tb[5] = t1 + L_TAB + kh * 2048;
            g2.sa = g2.s0 = RFL(WS_HH + ((rt * 2 + wt) * NU + kh * 43) * 1024); g2.l16 = l16; g2.budget = budget; g2.rs = rs; g2.a1 = f32x16{};
#pragma unroll
            for (int i = 0; i < 8; ++i) { g2.a0[2 * i] = bflo(hreg[i >> 2][i & 3]); g2.a0[2 * i + 1] = bfhi(hreg[i >> 2][i & 3]); }
            __builtin_amdgcn_s_sleep(DELAY_G);
            STAMP(4, 1);
            g2_steps<0, 43, 1, DN_B0, 38>(g2);
            a0 = g2.a0; a1 = g2.a1; STAMPV(15, budget - g2.budget);
        } else {
            BAR_VL(0);
            issue_dn(40, 3, ((w - 4) >> 1) * 15 * 2048, w - 4);
            BAR_VL(0);
        }
        STAMP(4, 2);
        if (w < 4) {
#pragma unroll
            for (int g = 0; g < 4; ++g) *(LDSAS f32x4*)(lds0 + 8192 + w * 4096 + g * 1024 + lane * 16) = f32x4{a1[4 * g], a1[4 * g + 1], a1[4 * g + 2], a1[4 * g + 3]};
        }
        BAR_L();
        if (w < 4) {
            f32x4 p[4];
#pragma unroll
            for (int g = 0; g < 4; ++g) p[g] = lds_f4(lds0 + 8192 + (w ^ 2) * 4096 + g * 1024 + lane * 16);
            SBAR();
            unsigned y[8];
#pragma unroll
            for (int g = 0; g < 4; ++g) {
                y[2 * g] = pk2(a0[4 * g] + p[g][0], a0[4 * g + 1] + p[g][1]);
                y[2 * g + 1] = pk2(a0[4 * g + 2] + p[g][2], a0[4 * g + 3] + p[g][3]);
            }
            const rsrc_t ro = mk_rsrc(out, S * D * 2);
            const unsigned vo = (rt * 64 + wt * 32 + r32) * 1024 + (ct * 64 + kh * 32 + 8 * h) * 2;
#pragma unroll
            for (int g = 0; g < 2; ++g) {
                auto e0 = __builtin_amdgcn_permlane32_swap(y[4 * g], y[4 * g + 2], false, false);
                auto e1 = __builtin_amdgcn_permlane32_swap(y[4 * g + 1], y[4 * g + 3], false, false);
                ST128((u32x4{e0[0], e1[0], e0[1], e1[1]}), ro, vo + g * 32, 0, OUT_AUX);
            }
        }
        STAMP(4, 3);
        STAMP(4, 4);
        FLUSH(4);
        asm volatile(".subsection 2\n.Lmain_end:\n\t.fill 64, 4, 0xBF800000\n\t.subsection 0");
    }
}

extern "C" void kernel_launch(void* const* d_in, const int* in_sizes, int n_in,
                              void* d_out, int out_size, void* d_ws, size_t ws_size,
                              hipStream_t stream) {
    const u16* x = (const u16*)d_in[0];
    const u16* wqkv = (const u16*)d_in[1];
    const u16* wo = (const u16*)d_in[2];
    const u16* wg = (const u16*)d_in[4];
    const u16* wu = (const u16*)d_in[5];
    const u16* wd = (const u16*)d_in[6];
    u16* out = (u16*)d_out;
    char* ws = (char*)d_ws;
    (void)hipFuncSetAttribute((const void*)k_block, hipFuncAttributeMaxDynamicSharedMemorySize, K2_LDS);
    k_block<<<256, 512, K2_LDS, stream>>>(x, wqkv, wo, wg, wu, wd, out, ws);
}
```

```cpp
#include <hip/hip_runtime.h>
#include <hip/hip_bf16.h>
#include <stdint.h>
#pragma clang diagnostic ignored "-Winline-asm"

typedef short s16x8 __attribute__((ext_vector_type(8)));
typedef short s16x4 __attribute__((ext_vector_type(4)));
typedef float f32x16 __attribute__((ext_vector_type(16)));
typedef float f32x4 __attribute__((ext_vector_type(4)));
typedef float f32x2 __attribute__((ext_vector_type(2)));
typedef unsigned u32x4 __attribute__((ext_vector_type(4)));
typedef unsigned u32x2 __attribute__((ext_vector_type(2)));
typedef unsigned u32x8 __attribute__((ext_vector_type(8)));
typedef __bf16 bf16x2 __attribute__((ext_vector_type(2)));
typedef unsigned short u16;
#define LDSAS __attribute__((address_space(3)))
#define GAS __attribute__((address_space(1)))
typedef __amdgpu_buffer_rsrc_t rsrc_t;

constexpr int S = 2048, D = 512, NH = 32, F = 1376, NU = F / 16;
constexpr float LN_EPS = 1e-5f;
constexpr float LOG2E = 1.4426950408889634f;

constexpr unsigned KV_CHUNK = 16384;
constexpr unsigned KV_HEAD = 8 * KV_CHUNK;
constexpr unsigned WS_EP = 0;
constexpr unsigned WS_FQ = 4096;
constexpr unsigned WS_FP = WS_FQ + 16 * 64 * 128;
constexpr unsigned WS_Q = WS_FP + 4096;
constexpr unsigned WS_KV = WS_Q + NH * 64 * 1024;
constexpr unsigned WS_O = WS_KV + NH * KV_HEAD;
constexpr unsigned WS_H = WS_O + 64 * 32 * 1024;
constexpr unsigned WS_ST = WS_H + 64 * 32 * 1024;
constexpr unsigned WS_HH = WS_ST + 64 * 16 * 256;
constexpr unsigned WS_END = WS_HH + 64 * NU * 1024;
constexpr int AUX_SC1 = 16;
#define STAMP_DECL
#define FLUSH(k) do {} while (0)
#define STAMP(k, i) do {} while (0)

#define PROBEV(c, v) do {} while (0)
#define STAMPV(i, v) do {} while (0)
#define SKEW(k) do {} while (0)
#define SBAR() __builtin_amdgcn_sched_barrier(0)
#define RFL(x) ((unsigned)__builtin_amdgcn_readfirstlane(x))
#define PIN(x) asm volatile("" : "+v"(x))
#define MFMA(a, b, c) __builtin_amdgcn_mfma_f32_32x32x16_bf16((a), (b), (c), 0, 0, 0)
#define WAITV(n) asm volatile("s_waitcnt vmcnt(" #n ")" ::: "memory")
#define BAR_L() asm volatile("s_waitcnt lgkmcnt(0)\n\ts_barrier" ::: "memory")
#define BAR_0() asm volatile("s_barrier" ::: "memory")
#define BAR_VL(n) asm volatile("s_waitcnt vmcnt(" #n ") lgkmcnt(0)\n\ts_barrier" ::: "memory")

__device__ __forceinline__ rsrc_t mk_rsrc(const void* p, unsigned bytes) {
    return __builtin_amdgcn_make_buffer_rsrc((void*)p, (short)0, (int)bytes, 0x00020000);
}
template <bool SC1 = false> __device__ __forceinline__ void dma16(rsrc_t r, unsigned voff, unsigned soff, unsigned lds) {
    if constexpr (SC1) asm volatile("s_nop 4\n\ts_mov_b32 m0, %0\n\ts_nop 0\n\tbuffer_load_dwordx4 %1, %2, %3 offen sc1 lds"
                 :: "s"(__builtin_amdgcn_readfirstlane(lds)), "v"(voff), "s"(r), "s"(__builtin_amdgcn_readfirstlane(soff)) : "m0", "memory");
    else asm volatile("s_nop 4\n\ts_mov_b32 m0, %0\n\ts_nop 0\n\tbuffer_load_dwordx4 %1, %2, %3 offen lds"
                 :: "s"(__builtin_amdgcn_readfirstlane(lds)), "v"(voff), "s"(r), "s"(__builtin_amdgcn_readfirstlane(soff)) : "m0", "memory");
}
#define ST128(v, r, voff, soff, aux) do { const u32x4 d_ = (v); __builtin_amdgcn_raw_buffer_store_b128(d_, r, voff, soff, aux); asm volatile("s_nop 1" :: "v"(d_)); } while (0)
__device__ __forceinline__ unsigned pk2(float lo, float hi) {
    f32x2 v = {lo, hi};
    return __builtin_bit_cast(unsigned, __builtin_convertvector(v, bf16x2));
}
__device__ __forceinline__ float dot2(unsigned a, unsigned b, float c) {
    return __builtin_amdgcn_fdot2_f32_bf16(__builtin_bit_cast(bf16x2, a), __builtin_bit_cast(bf16x2, b), c, false);
}
__device__ __forceinline__ float swap_add(float v) {
    auto r = __builtin_amdgcn_permlane32_swap(__float_as_uint(v), __float_as_uint(v), false, false);
    return __uint_as_float(r[0]) + __uint_as_float(r[1]);
}
__device__ __forceinline__ float swap_max(float v) {
    auto r = __builtin_amdgcn_permlane32_swap(__float_as_uint(v), __float_as_uint(v), false, false);
    return __builtin_fmaxf(__uint_as_float(r[0]), __uint_as_float(r[1]));
}
__device__ __forceinline__ s16x8 lds_b128(unsigned a) { return *(const LDSAS s16x8*)(a); }
__device__ __forceinline__ f32x4 lds_f4(unsigned a) { return *(const LDSAS f32x4*)(a); }
__device__ __forceinline__ s16x8 lds_tr2(unsigned a0, unsigned a1) {
    s16x4 lo = __builtin_amdgcn_ds_read_tr16_b64_v4i16((LDSAS s16x4*)(a0));
    s16x4 hi = __builtin_amdgcn_ds_read_tr16_b64_v4i16((LDSAS s16x4*)(a1));
    return __builtin_shufflevector(lo, hi, 0, 1, 2, 3, 4, 5, 6, 7);
}
__device__ __forceinline__ float bflo(unsigned v) { return __uint_as_float(v << 16); }
__device__ __forceinline__ float bfhi(unsigned v) { return __uint_as_float(v & 0xffff0000u); }
constexpr unsigned ONES2 = 0x3f803f80u;
template <int I> struct ic { static constexpr int value = I; constexpr operator int() const { return I; } };
template <int B, int E, class Fn> __device__ __forceinline__ void sfor(Fn&& fn) { if constexpr (B < E) { fn(ic<B>{}); sfor<B + 1, E>(fn); } }

#define CODE_OPEN 1
#define CODE_RT 1
#define K1_ABL 0
#define K1_KV_AT 84
#define DELAY_Q 6
constexpr unsigned K1_W = 65536, K1_FLG = K1_W + 5 * 16384;
__host__ __device__ constexpr unsigned k1_slot(int s) { return s < 5 ? K1_W + s * 16384 : s == 5 ? K1_W : s < 10 ? (s - 6) * 16384 : K1_W + (s - 9) * 16384; }
__device__ __forceinline__ void lds_spin_ge(unsigned addr, unsigned want) {
#pragma unroll 1
    for (unsigned n = 0; n < (1u << 22); ++n) {
        unsigned v; asm volatile("ds_read_b32 %0, %1\n\ts_waitcnt lgkmcnt(0)" : "=v"(v) : "v"(addr) : "memory");
        if ((unsigned)__builtin_amdgcn_readfirstlane(v) >= want) break;
        __builtin_amdgcn_s_sleep(1);
    }
}
__device__ __forceinline__ void lds_post(unsigned addr, unsigned v) {
    asm volatile("s_waitcnt lgkmcnt(0)\n\tds_write_b32 %0, %1" :: "v"(addr), "v"(v) : "memory");
}
__device__ __forceinline__ void lds_inc(unsigned addr, int lane) {
    asm volatile("s_waitcnt lgkmcnt(0)" ::: "memory");
    if (lane == 0) asm volatile("ds_add_u32 %0, %1" :: "v"(addr), "v"(1u) : "memory");
}
typedef unsigned u32x16 __attribute__((ext_vector_type(16)));
template <int CTRL> __device__ __forceinline__ float dpp_add(float v) {
    return v + __builtin_bit_cast(float, __builtin_amdgcn_update_dpp(0, __builtin_bit_cast(int, v), CTRL, 0xf, 0xf, true));
}

constexpr unsigned K2_LDS = 163840;
constexpr unsigned L_WO = 0, L_GU = 65536;
constexpr unsigned L_TAB = 61440;
constexpr unsigned L_FLAG = 16384 + 12288;
constexpr unsigned SPIN_CAP = 1u << 20;
#define POLL_DELAY_Q 2
#define OUT_AUX 2
#define DN_B0 20
#define ATT_PRIO 1
#define DELAY_O 0
#define DELAY_H 8
#define DELAY_G 8
constexpr int GU_AW = 8, G2_AW = 12;
#define TAG_T1 "v_max3_u32 v120, v[%c[cr]], v[%c[cr]+1], v[%c[cr]+2]\n\t"
#define TAG_T2 "v_max_u32 v120, v120, v[%c[cr]+3]\n\tv_cmp_eq_u32 vcc, -1, v120\n\t"
#define TAG_RELOAD(RB, AW) "s_add_u32 %[t], %[s0], (tagj>>2)*4096\n\tbuffer_load_dwordx4 v[" #RB "+4*(tagj%%" #AW "):" #RB "+3+4*(tagj%%" #AW ")], %[l16], %[rs], %[t] offen offset:(tagj&3)*1024 sc1\n\t"
#define TAG_T3(RB, AW) \
    "s_cbranch_vccnz 5f\n6:\n\t" \
    ".subsection 1\n5:\n\ts_cmp_eq_u32 %[bud], 0\n\ts_cbranch_scc1 6b\n\ts_sub_u32 %[bud], %[bud], 1\n\t.set tagj, %c[ci]\n\t" TAG_RELOAD(RB, AW) \
    "s_waitcnt vmcnt(0)\n\t" TAG_T1 TAG_T2 "s_cbranch_vccnz 5b\n\t.rept %c[cnt]-1\n\t.set tagj, tagj+1\n\t" TAG_RELOAD(RB, AW) ".endr\n\ts_branch 6b\n\t.subsection 0\n\t"
#define TAG_LOAD "buffer_load_dwordx4 v[%c[lr]:%c[lr]+3], %[l16], %[rs], %[sa] offen offset:%c[lo] sc1\n\t.if %c[lo] == 3072\n\ts_add_u32 %[sa], %[sa], 0x1000\n\t.endif\n\t"

__device__ __forceinline__ unsigned w64_voff(int lane) { return (lane >> 3) * 1024 + (((lane & 7) ^ (((lane >> 4) & 1) << 2)) << 4); }
__device__ __forceinline__ unsigned w64_tr(int lane, int n) {
    const int q = (lane & 15) >> 2, p = lane & 3, blk = (lane >> 4) & 1, h = lane >> 5;
    return (4 * h + q) * 128 + (((4 * n + 2 * blk + (p >> 1)) ^ (((q >> 1) & 1) << 2)) << 4) + 8 * (p & 1);
}

struct G2 { u32x16 r0, r1, r2; u32x8 w0, w1, w2; f32x16 a0, a1; unsigned tb[6], sa, s0, l16, budget; rsrc_t rs;
};
__host__ __device__ constexpr int g2_w(int DN, int n) {
    return !DN ? n * 2048 : n < 15 ? n * 2048 : n < 39 ? 65536 + (n - 15) * 2048 : n < 40 ? 131072 : (n - 40) * 2048;
}
template <int i, int N, int DN, int B0, int B1> __device__ __forceinline__ void g2_steps(G2& g) {
    constexpr int AW = G2_AW, j = i + AW - 1, n = i + 2, sel = n < N ? g2_w(DN, n) >> 16 : 3;
    unsigned tq;
#define G2_RD(P, R) "ds_read_b64_tr_b16 v[%c[nr]+" #R ":%c[nr]+" #R "+1], %[" P "] offset:%c[no]\n\tds_read_b64_tr_b16 v[%c[nr]+" #R "+2:%c[nr]+" #R "+3], %[" P "] offset:%c[no]+1024\n\t"
#define G2_RDS(P0, P1, P2, R) ".if %c[sel] == 0\n\t" G2_RD(P0, R) ".endif\n\t.if %c[sel] == 1\n\t" G2_RD(P1, R) ".endif\n\t.if %c[sel] == 2\n\t" G2_RD(P2, R) ".endif\n\t"
    if constexpr (i == 0)
        asm volatile(".set tagj, 0\n\t.rept 11\n\tbuffer_load_dwordx4 v[128+4*tagj:131+4*tagj], %[l16], %[rs], %[sa] offen offset:(tagj&3)*1024 sc1\n\t"
                     ".if (tagj&3) == 3\n\ts_add_u32 %[sa], %[sa], 0x1000\n\t.endif\n\t.set tagj, tagj+1\n\t.endr\n\t"
                     ".set tagj, 0\n\t.rept 2\n\tds_read_b64_tr_b16 v[96+8*tagj:97+8*tagj], %[b0] offset:tagj*2048\n\tds_read_b64_tr_b16 v[98+8*tagj:99+8*tagj], %[b0] offset:tagj*2048+1024\n\t"
                     "ds_read_b64_tr_b16 v[100+8*tagj:101+8*tagj], %[b1] offset:tagj*2048\n\tds_read_b64_tr_b16 v[102+8*tagj:103+8*tagj], %[b1] offset:tagj*2048+1024\n\t.set tagj, tagj+1\n\t.endr\n\t"
                     "s_waitcnt vmcnt(10) lgkmcnt(4)\n\t" TAG_T1 TAG_T2 TAG_T3(128, 12)
                     : "={v[128:143]}"(g.r0), "={v[144:159]}"(g.r1), "={v[160:175]}"(g.r2), "={v[96:103]}"(g.w0), "={v[104:111]}"(g.w1), "={v[112:119]}"(g.w2), [sa] "+s"(g.sa),
                       "+{v[64:79]}"(g.a0), "+{v[80:95]}"(g.a1), [bud] "+s"(g.budget), [t] "=&s"(tq)
                     : [l16] "v"(g.l16), [rs] "s"(g.rs), [s0] "s"(g.s0), [b0] "v"(g.tb[0]), [b1] "v"(g.tb[1]), [cr] "n"(128), [ci] "n"(0), [cnt] "n"(AW - 1) : "vcc", "scc", "v120", "memory");
    asm volatile("v_mfma_f32_32x32x16_bf16 v[64:79], v[%c[wr]:%c[wr]+3], v[%c[ar]:%c[ar]+3], v[64:79]\n\t"
                 ".if %c[ld]\n\t" TAG_LOAD ".endif\n\t"
                 ".if %c[bar]\n\ts_waitcnt lgkmcnt(0)\n\ts_barrier\n\t.endif\n\t"
                 G2_RDS("b0", "b2", "b4", 0)
                 ".if %c[cnt]\n\ts_waitcnt vmcnt(%c[vn])\n\t" TAG_T1 ".endif\n\t"
                 "v_mfma_f32_32x32x16_bf16 v[80:95], v[%c[wr]+4:%c[wr]+7], v[%c[ar]:%c[ar]+3], v[80:95]\n\t"
                 G2_RDS("b1", "b3", "b5", 4)
                 ".if %c[cnt]\n\t" TAG_T2 "s_waitcnt lgkmcnt(%c[ln])\n\t" TAG_T3(128, 12) ".else\n\ts_nop 15\n\t.endif"
                 : "+{v[64:79]}"(g.a0), "+{v[80:95]}"(g.a1), "+{v[128:143]}"(g.r0), "+{v[144:159]}"(g.r1), "+{v[160:175]}"(g.r2), "+{v[96:103]}"(g.w0), "+{v[104:111]}"(g.w1), "+{v[112:119]}"(g.w2),
                   [sa] "+s"(g.sa), [bud] "+s"(g.budget), [t] "=&s"(tq)
                 : [l16] "v"(g.l16), [rs] "s"(g.rs), [s0] "s"(g.s0), [b0] "v"(g.tb[0]), [b1] "v"(g.tb[1]), [b2] "v"(g.tb[2]), [b3] "v"(g.tb[3]), [b4] "v"(g.tb[4]), [b5] "v"(g.tb[5]),
                   [ld] "n"(j < N ? 1 : 0), [lr] "n"(128 + 4 * (j % AW)), [lo] "n"((j & 3) * 1024), [bar] "n"(i == B0 || i == B1 ? 1 : 0), [sel] "n"(sel),
                   [nr] "n"(96 + 8 * (n % 3)), [no] "n"(n < N ? g2_w(DN, n) & 65535 : 0), [vn] "n"(j < N ? AW - 2 : i + 1 < N ? N - 2 - i : 0), [ln] "n"(n < N ? 4 : 0),
                   [ar] "n"(128 + 4 * (i % AW)), [wr] "n"(96 + 8 * (i % 3)), [cr] "n"(128 + 4 * ((i + 1) % AW)), [ci] "n"(i + 1), [cnt] "n"(j < N ? AW - 1 : N - 1 - i) : "vcc", "scc", "v120", "memory");
    if constexpr (i + 1 < N) g2_steps<i + 1, N, DN, B0, B1>(g);
}
__host__ __device__ constexpr int gu_dma(int s) { return (s >= 16 && s < 24 && !(s & 1)) || (s >= 24 && s < 30); }
__host__ __device__ constexpr int gu_ops(int s) { return (s + GU_AW - 1 < 32 ? 1 : 0) + (s >= 2 && s < 10 ? 1 : 0) + gu_dma(s); }
__host__ __device__ constexpr int gu_younger(int k) {
    int c = 0, s = k - GU_AW + 1;
    if (s < 0) { c = GU_AW - 2 - k; s = 0; } else { c = gu_ops(s) - 1; ++s; }
    for (; s < k; ++s) c += gu_ops(s);
    return c;
}

struct GU { u32x16 r0, r1, st; u32x8 w0, w1, w2; f32x16 a0, a1, a2; unsigned wa[3], sa, s0, sv, ss, l16, budget, dm, ds, dv, dm2, ds2, dm3, ds3; rsrc_t rs, rd; };
template <int i, int E> __device__ __forceinline__ void gu_steps(GU& g) {
    constexpr int AW = GU_AW, j = i + AW - 1, n = i + 1;
    unsigned tq;
#define GU_RD(R, B) "ds_read_b64_tr_b16 v[" R ":" R "+1], %[" B "] offset:%c[no]\n\tds_read_b64_tr_b16 v[" R "+2:" R "+3], %[" B "] offset:%c[no]+768\n\t"
    if constexpr (i == 0)
        asm volatile(".set tagj, 0\n\t.rept 7\n\tbuffer_load_dwordx4 v[140+4*tagj:143+4*tagj], %[l16], %[rs], %[sa] offen offset:(tagj&3)*1024 sc1\n\t"
                     ".if (tagj&3) == 3\n\ts_add_u32 %[sa], %[sa], 0x1000\n\t.endif\n\t.set tagj, tagj+1\n\t.endr\n\t" GU_RD("112", "b0") GU_RD("116", "b1") GU_RD("124", "b2")
                     "s_waitcnt vmcnt(6)\n\t" TAG_T1 TAG_T2 TAG_T3(140, 8)
                     : "={v[140:155]}"(g.r0), "={v[156:171]}"(g.r1), "={v[172:187]}"(g.st), "={v[112:119]}"(g.w0), "={v[124:131]}"(g.w1), "={v[132:139]}"(g.w2), [sa] "+s"(g.sa),
                       "+{v[64:79]}"(g.a0), "+{v[80:95]}"(g.a1), "+{v[96:111]}"(g.a2), [bud] "+s"(g.budget), [t] "=&s"(tq)
                     : [l16] "v"(g.l16), [rs] "s"(g.rs), [s0] "s"(g.s0), [b0] "v"(g.wa[0]), [b1] "v"(g.wa[1]), [b2] "v"(g.wa[2]), [no] "n"(0), [cr] "n"(140), [ci] "n"(0), [cnt] "n"(AW - 1)
                     : "vcc", "scc", "v120", "memory");
    asm volatile(".if %c[i] == 15 || %c[i] == 23\n\ts_waitcnt lgkmcnt(0)\n\ts_barrier\n\t.else\n\ts_waitcnt lgkmcnt(4)\n\t.endif\n\t"
                 "v_mfma_f32_32x32x16_bf16 v[64:79], v[%c[c0]:%c[c0]+3], v[%c[ar]:%c[ar]+3], v[64:79]\n\t"
                 ".if %c[rd]\n\t" GU_RD("%c[n0]", "b0") "s_waitcnt lgkmcnt(4)\n\t.else\n\ts_waitcnt lgkmcnt(2)\n\t.endif\n\t"
                 "v_mfma_f32_32x32x16_bf16 v[80:95], v[%c[c1]:%c[c1]+3], v[%c[ar]:%c[ar]+3], v[80:95]\n\t"
                 ".if %c[rd]\n\t" GU_RD("%c[n1]", "b1") ".endif\n\t"
                 ".if %c[ld]\n\t" TAG_LOAD ".endif\n\t"
                 ".if %c[sk] >= 0\n\tbuffer_load_dwordx2 v[172+2*%c[sk]:173+2*%c[sk]], %[sv], %[rs], %[ss] offen offset:%c[sk]*256 sc1\n\t.endif\n\t"
                 ".if %c[i] == 26\n\ts_mov_b32 %[dm], %[dm2]\n\ts_mov_b32 %[ds], %[ds2]\n\t.endif\n\t.if %c[i] == 29\n\ts_mov_b32 %[dm], %[dm3]\n\ts_mov_b32 %[ds], %[ds3]\n\t.endif\n\t"
                 ".if %c[dma]\n\ts_mov_b32 m0, %[dm]\n\ts_add_u32 %[dm], %[dm], 0x400\n\tbuffer_load_dwordx4 %[dv], %[rwd], %[ds] offen lds\n\ts_add_u32 %[ds], %[ds], 0x2000\n\t.endif\n\t"
                 ".if %c[rd]\n\ts_waitcnt lgkmcnt(4)\n\t.else\n\ts_waitcnt lgkmcnt(0)\n\t.endif\n\t"
                 "v_mfma_f32_32x32x16_bf16 v[96:111], v[%c[c2]:%c[c2]+3], v[%c[ar]:%c[ar]+3], v[96:111]\n\t"
                 ".if %c[rd]\n\t" GU_RD("%c[n2]", "b2") "s_waitcnt vmcnt(%c[vn])\n\t" TAG_T1 TAG_T2 TAG_T3(140, 8) ".endif"
                 : "+{v[64:79]}"(g.a0), "+{v[80:95]}"(g.a1), "+{v[96:111]}"(g.a2), "+{v[140:155]}"(g.r0), "+{v[156:171]}"(g.r1), "+{v[172:187]}"(g.st),
                   "+{v[112:119]}"(g.w0), "+{v[124:131]}"(g.w1), "+{v[132:139]}"(g.w2), [sa] "+s"(g.sa), [bud] "+s"(g.budget), [t] "=&s"(tq), [dm] "+s"(g.dm), [ds] "+s"(g.ds)
                 : [l16] "v"(g.l16), [rs] "s"(g.rs), [s0] "s"(g.s0), [dv] "v"(g.dv), [rwd] "s"(g.rd), [dm2] "s"(g.dm2), [ds2] "s"(g.ds2), [dm3] "s"(g.dm3), [ds3] "s"(g.ds3),
                   [dma] "n"(gu_dma(i)), [sv] "v"(g.sv), [ss] "s"(g.ss), [b0] "v"(g.wa[0]), [b1] "v"(g.wa[1]), [b2] "v"(g.wa[2]),
                   [ld] "n"(j < 32 ? 1 : 0), [lr] "n"(140 + 4 * (j % AW)), [lo] "n"((j & 3) * 1024), [sk] "n"(i >= 2 && i < 10 ? i - 2 : -1), [rd] "n"(n < 32 ? 1 : 0), [no] "n"(n * 1536),
                   [n0] "n"(n & 1 ? 128 : 112), [n1] "n"(n & 1 ? 132 : 116), [n2] "n"(n & 1 ? 136 : 124), [c0] "n"(i & 1 ? 128 : 112), [c1] "n"(i & 1 ? 132 : 116), [c2] "n"(i & 1 ? 136 : 124),
                   [vn] "n"(n < 32 ? gu_younger(n) : 0), [ar] "n"(140 + 4 * (i % AW)), [i] "n"(i), [cr] "n"(140 + 4 * (n % AW)), [ci] "n"(n), [cnt] "n"(j < 32 ? AW - 1 : 31 - i)
                 : "vcc", "scc", "m0", "v120", "memory");
    if constexpr (i + 1 < E) gu_steps<i + 1, E>(g);
}

__global__ __launch_bounds__(512, 2) void k_block(const u16* __restrict__ x, const u16* __restrict__ wqkv, const u16* wo, const u16* wg,
                                                  const u16* wu, const u16* wd, u16* out, char* __restrict__ ws) {
    extern __shared__ __attribute__((aligned(16))) char smem[];
    const unsigned lds0 = (unsigned)(uintptr_t)(LDSAS char*)smem;
    const int tid = threadIdx.x, lane = tid & 63;
    const int w = __builtin_amdgcn_readfirstlane(tid >> 6);
    const int b = blockIdx.x, xcd = b & 7, bi = b >> 3;
    const int r32 = lane & 31, h = lane >> 5;
    const rsrc_t rs = mk_rsrc(ws, WS_END);
    const int wt = w & 1, kh = (w >> 1) & 1;
    STAMP_DECL;
    unsigned ep;
    asm volatile("" : "+s"(wo), "+s"(wg), "+s"(wu), "+s"(wd), "+s"(out));
    const int head = xcd * 4 + (bi >> 3);
    const unsigned l16 = lane * 16;
    const int rt2 = xcd * 4 + (bi >> 3), ct = bi & 7;
    const unsigned flv = (lane * 4) | ((unsigned)(31 - lane) & 0x80000000u);
    const unsigned fls = RFL(WS_FQ + ((((xcd & 1) * 4 + (bi >> 3)) * 2 + kh) * 64 + ((xcd >> 1) * 8 + (bi & 7)) * 2 + wt) * 128);
    {
    const int rt = (xcd >> 1) * 8 + (bi & 7), cb = (xcd & 1) * 4 + (bi >> 3), rot = bi & 3;
    const int wc = (w >> 1) & 1, hw = w - 4;
    STAMP(0, 0);
    SKEW(0);
    const rsrc_t rw = mk_rsrc(wqkv, D * 3 * D * 2);
    const unsigned wv = (lane >> 3) * 3072 + (((lane & 7) ^ (((lane >> 4) & 1) << 2)) << 4);
    const unsigned fa = lds0 + K1_FLG;
    if (tid < 12) *(LDSAS unsigned*)(fa + tid * 4) = 0u;
    u32x16 r0, r1, r2, r3;
    {
        const rsrc_t rx = mk_rsrc(x, S * D * 2);
        const unsigned xv0 = (lane >> 3) * 1024 + (((lane & 7) ^ (lane >> 4)) << 4);
        unsigned tm = RFL(lds0 + K1_W + hw * 4096), so0 = RFL((rot * 128 + hw * 32) * 3072 + 1024 + cb * 128), so1 = RFL((((1 + rot) & 3) * 128 + hw * 32) * 3072 + 1024 + cb * 128);
        unsigned long long pc; unsigned cv = (((lane & 3) * 4 + (w & 3)) * 32 + bi) * 128;
#define KWL(SO) "s_mov_b32 m0, %[tm]\n\ts_add_u32 %[tm], %[tm], 0x400\n\tbuffer_load_dwordx4 %[wv], %[rw], " SO " offen lds\n\ts_add_u32 " SO ", " SO ", 0x6000\n\t"
        asm volatile("s_cmp_lt_u32 %[w], 4\n\ts_cbranch_scc0 1f\n\ts_nop 2\n\t"
                     ".set tagj, 0\n\t.rept 8\n\tbuffer_load_dwordx4 v[128+4*tagj:131+4*tagj], %[v0], %[rx], %[xs] offen offset:tagj*128 nt\n\t.set tagj, tagj+1\n\t.endr\n\t"
                     ".set tagj, 0\n\t.rept 8\n\tbuffer_load_dwordx4 v[160+4*tagj:163+4*tagj], %[v1], %[rx], %[xs] offen offset:tagj*128 nt\n\t.set tagj, tagj+1\n\t.endr\n"
                     "1:\n\ts_waitcnt lgkmcnt(0)\n\ts_barrier\n\ts_cbranch_scc1 2f\n\t"
                     "s_getpc_b64 %[pc]\n.Lopen_pc:\n\tv_min_u32 %[cv], .Lmain_end-.Lopen_pc-4, %[cv]\n\ts_mov_b32 m0, %[sc]\n\ts_nop 0\n\tglobal_load_lds_dword %[cv], %[pc]\n\t"
                     "s_mov_b32 m0, %[se]\n\ts_nop 0\n\tbuffer_load_dword %[ev], %[rs], 0 offen sc1 lds\n\t"
                     KWL("%[s0]") KWL("%[s0]") KWL("%[s0]") KWL("%[s0]") "s_add_u32 %[tm], %[tm], 0x3000\n\t" KWL("%[s1]") KWL("%[s1]") KWL("%[s1]") KWL("%[s1]") "\n2:"
                     : "={v[128:143]}"(r0), "={v[144:159]}"(r1), "={v[160:175]}"(r2), "={v[176:191]}"(r3), [tm] "+s"(tm), [s0] "+s"(so0), [s1] "+s"(so1), [pc] "=&s"(pc), [cv] "+v"(cv)
                     : [sc] "s"(RFL(fa + 256)), [se] "s"(RFL(fa + 512 + hw * 256)), [ev] "v"((unsigned)(0 - lane) & 0x80000000u), [rs] "s"(rs), [v0] "v"(xv0), [v1] "v"((xv0 ^ 64) + 8192), [rx] "s"(rx), [xs] "s"(RFL((rt * 64 + (w & 3) * 16) * 1024)), [w] "s"(w), [wv] "v"(wv), [rw] "s"(rw) : "m0", "scc", "memory");
    }
    if (w >= 4) {
        const unsigned pa = fa + hw * 4;
        auto issue = [&](int s) {
            const unsigned so = RFL(((((s & 3) + rot) & 3) * 128 + hw * 32) * 3072 + (((s >> 2) + 1) % 3) * 1024 + cb * 128);
            asm volatile("s_nop 4" :: "s"(so));
#pragma unroll
            for (int i = 0; i < 4; ++i)
                asm volatile("s_mov_b32 m0, %0\n\ts_nop 0\n\tbuffer_load_dwordx4 %1, %2, %3 offen lds"
                             :: "s"(lds0 + k1_slot(s) + (4 * hw + i) * 1024), "v"(wv), "s"(rw), "s"(so + i * 8 * 3072) : "m0", "memory");
        };
#define K1_LAND(N, s) asm volatile("s_waitcnt vmcnt(" #N ")\n\tds_write_b32 %0, %1" :: "v"(pa), "v"((s) + 1u) : "memory")
        issue(2); K1_LAND(8, 0);
        issue(3); K1_LAND(8, 1);
        issue(4); K1_LAND(8, 2);
        {
            const int tt = rt * 2 + wt, hd = cb * 4 + wc * 2;
            const unsigned kvf = RFL((tt & 31) < 8 ? WS_KV + hd * KV_HEAD + (tt >> 3) * KV_CHUNK + (tt & 7) * 1024 : 0x40000000u);
            const u32x4 ones = {~0u, ~0u, ~0u, ~0u};
#pragma unroll
            for (int j = 0; j < 2; ++j) ST128(ones, rs, l16, WS_Q + ((hd + j) * 64 + tt) * 1024, AUX_SC1);
#pragma unroll
            for (int j = 0; j < 4; ++j) ST128(ones, rs, l16 + (j >> 1) * KV_HEAD + (j & 1) * 8192, kvf, AUX_SC1);
        }
        K1_LAND(10, 3); K1_LAND(6, 4);
        ep = RFL(*(const LDSAS unsigned*)(fa + 512 + hw * 256)) + 1u;
        SKEW(9);
        lds_spin_ge(fa + 28, 4);
        issue(5);
        lds_spin_ge(fa + 20, 4);
        issue(6); issue(7); K1_LAND(8, 5);
        __builtin_amdgcn_raw_buffer_store_b32(ep ^ 0x80000000u, rs, flv, fls, AUX_SC1);
        issue(8); K1_LAND(9, 6);
        issue(9); K1_LAND(9, 7);
        lds_spin_ge(fa + 24, 4);
        issue(10); K1_LAND(8, 8);
        issue(11); K1_LAND(8, 9);
        {
            unsigned f, cap = 1u << 20;
            asm volatile("buffer_load_dword %[f], %[pv], %[rs], %[fq] offen sc1\n\ts_waitcnt vmcnt(5)\n\tds_write_b32 %[pa], %[c11]\n\ts_waitcnt vmcnt(1)\n\tds_write_b32 %[pa], %[c12]\n"
                         "1:\n\ts_waitcnt vmcnt(0)\n\tv_xor_b32 %[f], %[ep], %[f]\n\tv_lshlrev_b32 %[f], 1, %[f]\n\tv_cmp_ne_u32 vcc, 0, %[f]\n\ts_cbranch_vccz 2f\n\t"
                         "s_sub_u32 %[cap], %[cap], 1\n\ts_cbranch_scc1 2f\n\ts_sleep 2\n\tbuffer_load_dword %[f], %[pv], %[rs], %[fq] offen sc1\n\ts_branch 1b\n2:"
                         : [f] "=&v"(f), [cap] "+s"(cap) : [pv] "v"(lane * 128), [rs] "s"(rs), [fq] "s"(RFL(WS_FQ + (head >> 1) * 8192)), [pa] "v"(pa), [c11] "v"(11u), [c12] "v"(12u), [ep] "s"(ep)
                         : "vcc", "scc", "memory");
        }
        if (w == 4) lds_post(fa + 36, 1u);
        lds_spin_ge(fa + 32, 4);
        __builtin_amdgcn_s_sleep(POLL_DELAY_Q);
#pragma unroll
        for (int j = 0; j < 8; ++j)
            dma16<true>(rs, l16, WS_KV + head * KV_HEAD + (hw >> 1) * 4 * KV_CHUNK + ((hw & 1) * 8 + j) * 1024, lds0 + (hw >> 1) * 32768 + ((hw & 1) * 8 + j) * 1024);
        STAMP(0, 1);
    } else {
    {
        unsigned xw = lds0 + 2 * w * 1024 + lane * 16;
#pragma unroll 1
        for (int p = 1; p < 3; ++p) {
            asm volatile("s_cmp_eq_u32 %[p], 1\n\ts_cbranch_scc0 1f\n\ts_waitcnt vmcnt(8)\n1:\n\ts_cmp_eq_u32 %[p], 2\n\ts_cbranch_scc0 2f\n\ts_waitcnt vmcnt(0)\n\t"
                         ".set tagj, 0\n\t.rept 16\n\tv_mov_b64 v[128+2*tagj:129+2*tagj], v[160+2*tagj:161+2*tagj]\n\t.set tagj, tagj+1\n\t.endr\n\tv_add_u32 %[xw], 0x400, %[xw]\n2:"
                         : "+{v[128:143]}"(r0), "+{v[144:159]}"(r1), "+{v[160:175]}"(r2), "+{v[176:191]}"(r3), [xw] "+v"(xw) : [p] "s"(p) : "scc", "memory");
            float s0 = 0.f, s1 = 0.f, q0 = 0.f, q1 = 0.f;
#pragma unroll
            for (int i = 0; i < 16; ++i) { s0 = dot2(r0[i], ONES2, s0); q0 = dot2(r0[i], r0[i], q0); s1 = dot2(r1[i], ONES2, s1); q1 = dot2(r1[i], r1[i], q1); }
            float sum = s0 + s1, sq = q0 + q1;
            sum = dpp_add<0xB1>(sum); sq = dpp_add<0xB1>(sq); sum = dpp_add<0x4E>(sum); sq = dpp_add<0x4E>(sq); sum = dpp_add<0x141>(sum); sq = dpp_add<0x141>(sq);
            const float mean = sum * (1.f / D);
            const float rstd = __builtin_amdgcn_rsqf(__builtin_fmaxf(sq * (1.f / D) - mean * mean, 0.f) + LN_EPS), nb = -mean * rstd;
#pragma unroll
            for (int g = 0; g < 8; g += 2) {
                f32x2 t[8];
#pragma unroll
                for (int i = 0; i < 8; ++i) { const unsigned v = g < 4 ? r0[4 * g + i] : r1[4 * g - 16 + i]; t[i] = f32x2{bflo(v), bfhi(v)}; }
                asm volatile("" : "+v"(t[0]), "+v"(t[1]), "+v"(t[2]), "+v"(t[3]), "+v"(t[4]), "+v"(t[5]), "+v"(t[6]), "+v"(t[7]));
#pragma unroll
                for (int i = 0; i < 8; ++i) t[i] = t[i] * rstd + nb;
                asm volatile("" : "+v"(t[0]), "+v"(t[1]), "+v"(t[2]), "+v"(t[3]), "+v"(t[4]), "+v"(t[5]), "+v"(t[6]), "+v"(t[7]));
                u32x4 y0, y1;
#pragma unroll
                for (int i = 0; i < 4; ++i) { y0[i] = pk2(t[i][0], t[i][1]); y1[i] = pk2(t[4 + i][0], t[4 + i][1]); }
                *(LDSAS u32x4*)(xw + g * 8192) = y0;
                *(LDSAS u32x4*)(xw + g * 8192 + 8192) = y1;
            }
        }
    }
    SKEW(8);
    lds_inc(fa + 16, lane);
    STAMP(0, 3);
    u32x16 xa0, xa1, xa2, xa3, xa4, xa5, xa6, xa7;
    u32x4 w0, w1, w2, w3, w4, fl;
    f32x16 acA, acB;
    unsigned xb[4], tq;
#pragma unroll
    for (int m = 0; m < 4; ++m) xb[m] = lds0 + (32 * wt + r32) * 128 + (((2 * m + h) ^ ((r32 >> 1) & 7)) << 4) + rot * 16384;
    const int q4 = (lane & 15) >> 2, p4 = lane & 3, blk = (lane >> 4) & 1;
    const unsigned tA = lds0 + (8 * h + q4) * 128 + (((4 * wc + 2 * blk + (p4 >> 1)) ^ (((q4 >> 1) & 1) << 2)) << 4) + 8 * (p4 & 1);
    const unsigned tB = tA + 65536, tC = tA + 131072;
    const int tt = rt * 2 + wt, hd = cb * 4 + wc * 2;
#define KXR(R, m, O) "ds_read_b128 v[" #R ":" #R "+3], %[x" #m "] offset:" #O "\n\t"
#define KXN(m) "v_add_u32 %[x" #m "], 0x4000, %[x" #m "]\n\tv_and_b32 %[x" #m "], 0xffff, %[x" #m "]\n\t"
#define KCHK(ST) "v_min3_u32 v116, v116, v117, v118\n\tv_min_u32 v116, v116, v119\n\ts_nop 0\n\tv_readfirstlane_b32 %[t], v116\n\ts_nop 0\n\ts_cmp_gt_u32 %[t], " ST "\n\t"
#define KSPIN(ST) "1:\n\tds_read_b128 v[116:119], %[fa]\n\ts_waitcnt lgkmcnt(0)\n\t" KCHK(ST) "s_cbranch_scc0 1b\n\t"
#define KWR(R, O) "ds_read_b64_tr_b16 v[" #R ":" #R "+1], %[tb] offset:" #O "\n\tds_read_b64_tr_b16 v[" #R "+2:" #R "+3], %[tb] offset:" #O "+512\n\t"
    asm volatile("3:\n\tds_read_b32 v116, %[fa] offset:16\n\ts_waitcnt lgkmcnt(0)\n\tv_readfirstlane_b32 %[t], v116\n\ts_nop 0\n\ts_cmp_gt_u32 %[t], 3\n\ts_cbranch_scc0 3b\n\t"
                 KXR(128, 0, 0) KXR(132, 1, 0) KXR(136, 2, 0) KXR(140, 3, 0) KXR(144, 0, 8192) KXR(148, 1, 8192) KXR(152, 2, 8192) KXR(156, 3, 8192) KXN(0) KXN(1) KXN(2) KXN(3)
                 KSPIN("0") KWR(96, 0) KWR(100, 2048) KWR(104, 4096) KWR(108, 6144)
                 : "={v[116:119]}"(fl), [t] "=&s"(tq), "={v[96:99]}"(w0), "={v[100:103]}"(w1), "={v[104:107]}"(w2), "={v[108:111]}"(w3), "={v[112:115]}"(w4), "={v[64:79]}"(acA), "={v[80:95]}"(acB),
                   "={v[128:143]}"(xa0), "={v[144:159]}"(xa1), "={v[160:175]}"(xa2), "={v[176:191]}"(xa3), "={v[192:207]}"(xa4), "={v[208:223]}"(xa5), "={v[224:239]}"(xa6), "={v[240:255]}"(xa7),
                   [x0] "+v"(xb[0]), [x1] "+v"(xb[1]), [x2] "+v"(xb[2]), [x3] "+v"(xb[3])
                 : [fa] "v"(fa), [tb] "v"(tB) : "scc", "memory");
    STAMP(0, 4);
    auto epi = [&](int m, const f32x16& acc, int hh) {
        const float sc = 0.25f * LOG2E;
        u32x4 o;
#pragma unroll
        for (int i = 0; i < 4; ++i) o[i] = m == 2 ? pk2(acc[8 * hh + 2 * i] * sc, acc[8 * hh + 2 * i + 1] * sc) : pk2(acc[8 * hh + 2 * i], acc[8 * hh + 2 * i + 1]);
        if (m == 2) ST128(o, rs, l16, WS_Q + ((hd + hh) * 64 + tt) * 1024, AUX_SC1);
        else if (m == 0) ST128(o, rs, l16, WS_KV + (hd + hh) * KV_HEAD + (tt >> 3) * KV_CHUNK + (tt & 7) * 1024, AUX_SC1);
        else ST128(o, rs, (r32 >> 4) * KV_HEAD + (16 * h + (lane & 15)) * 16 + hh * 512,
                   WS_KV + hd * KV_HEAD + (tt >> 3) * KV_CHUNK + 8192 + (tt & 7) * 1024, AUX_SC1);
    };
    sfor<0, 96>([&](auto G) {
        constexpr int g = G.value, n = g + 4, m = g >> 5;
        constexpr unsigned o = k1_slot(n >> 3) + (n & 7) * 2048, oi = o & 65535;
        constexpr int aR = m == 1 ? 80 : 64, wR = 96 + 4 * (g % 5), nR = 96 + 4 * (n % 5), xR = 128 + 4 * (g & 31);
        constexpr int nx = (g - 4 >= 0 && g - 4 < 12) + (g - 3 >= 0 && g - 3 < 12) + (g - 2 >= 0 && g - 2 < 12) + (g - 1 >= 0 && g - 1 < 12), wc_ = n >= 96 ? 0 : (8 + 2 * nx > 14 ? 14 : 8 + 2 * nx);
#define KRD(B) "ds_read_b64_tr_b16 v[%c[nr]:%c[nr]+1], %[" B "] offset:%c[oi]\n\tds_read_b64_tr_b16 v[%c[nr]+2:%c[nr]+3], %[" B "] offset:%c[oi]+512\n\t"
        asm volatile(".if %c[bar] == 1\n\t" KCHK("%c[st]") "s_cbranch_scc0 1f\n2:\n\t.subsection 1\n" KSPIN("%c[st]") "s_branch 2b\n\t.subsection 0\n\t.endif\n\t"
                     ".if %c[bar] == 2\n\tds_read_b128 v[116:119], %[fa]\n\t.endif\n\t"
                     ".if %c[rd] == 1\n\t" KRD("tb") ".endif\n\t.if %c[rd] == 2\n\t" KRD("tc") ".endif\n\t.if %c[rd] == 3\n\t" KRD("ta") ".endif\n\t"
                     "s_waitcnt lgkmcnt(%c[wn])\n\t"
                     ".if %c[md] == 0\n\tv_mfma_f32_32x32x16_bf16 v[%c[ar]:%c[ar]+15], v[%c[wr]:%c[wr]+3], v[%c[xr]:%c[xr]+3], v[%c[ar]:%c[ar]+15]\n\t.endif\n\t"
                     ".if %c[md] == 1\n\tv_mfma_f32_32x32x16_bf16 v[%c[ar]:%c[ar]+15], v[%c[xr]:%c[xr]+3], v[%c[wr]:%c[wr]+3], v[%c[ar]:%c[ar]+15]\n\t.endif\n\t"
                     ".if %c[md] == 2\n\tv_mfma_f32_32x32x16_bf16 v[%c[ar]:%c[ar]+15], v[%c[xr]:%c[xr]+3], v[%c[wr]:%c[wr]+3], 0\n\t.endif\n\t"
                     ".if %c[md] == 3\n\tv_mfma_f32_32x32x16_bf16 v[%c[ar]:%c[ar]+15], v[%c[wr]:%c[wr]+3], v[%c[xr]:%c[xr]+3], 0\n\t.endif\n\t"
                     ".if %c[xm] == 1\n\tds_read_b128 v[%c[xd]:%c[xd]+3], %[x0] offset:%c[xo]\n\tds_read_b128 v[%c[xd]+4:%c[xd]+7], %[x1] offset:%c[xo]\n\t.endif\n\t"
                     ".if %c[xm] == 2\n\tds_read_b128 v[%c[xd]:%c[xd]+3], %[x2] offset:%c[xo]\n\tds_read_b128 v[%c[xd]+4:%c[xd]+7], %[x3] offset:%c[xo]\n\t.endif\n\t"
                     ".if %c[xn]\n\t" KXN(0) KXN(1) KXN(2) KXN(3) ".endif"
                     : "+{v[64:79]}"(acA), "+{v[80:95]}"(acB), "+{v[96:99]}"(w0), "+{v[100:103]}"(w1), "+{v[104:107]}"(w2), "+{v[108:111]}"(w3), "+{v[112:115]}"(w4),
                       "+{v[128:143]}"(xa0), "+{v[144:159]}"(xa1), "+{v[160:175]}"(xa2), "+{v[176:191]}"(xa3), "+{v[192:207]}"(xa4), "+{v[208:223]}"(xa5), "+{v[224:239]}"(xa6), "+{v[240:255]}"(xa7),
                       "+{v[116:119]}"(fl), [t] "=&s"(tq), [x0] "+v"(xb[0]), [x1] "+v"(xb[1]), [x2] "+v"(xb[2]), [x3] "+v"(xb[3])
                     : [ta] "v"(tA), [tb] "v"(tB), [tc] "v"(tC), [fa] "v"(fa), [st] "n"(n >> 3), [wn] "n"(wc_),
                       [bar] "n"(K1_ABL ? 0 : n < 96 && (n & 7) == 0 ? 1 : n < 92 && (n & 7) == 4 ? 2 : 0), [rd] "n"(n >= 96 ? 0 : o < 65536 ? 3 : o < 131072 ? 1 : 2), [oi] "n"(oi), [nr] "n"(nR),
                       [md] "n"(m != 1 ? ((g & 31) == 0 ? 3 : 0) : g == 32 ? 2 : 1), [ar] "n"(aR), [wr] "n"(wR), [xr] "n"(xR),
                       [xm] "n"(g < 12 ? 1 + (g & 1) : 0), [xd] "n"(160 + 8 * g), [xo] "n"(((g >> 1) & 1) * 8192), [xn] "n"(g < 11 && (g & 3) == 3 ? 1 : 0) : "scc", "memory");
        if (g == 7 && lane == 0) asm volatile("ds_add_u32 %0, %1" :: "v"(fa + 28), "v"(1u) : "memory");
        if (g == 16 && lane == 0) asm volatile("ds_add_u32 %0, %1" :: "v"(fa + 20), "v"(1u) : "memory");
        if (g == 23 && lane == 0) asm volatile("ds_add_u32 %0, %1" :: "v"(fa + 24), "v"(1u) : "memory");
        if (g == 36 || g == 38) epi(0, acA, (g - 36) >> 1);
        if (g == 67 || g == 68) epi(1, acB, g - 67);
        if (g == K1_KV_AT && lane == 0) asm volatile("ds_add_u32 %0, %1" :: "v"(fa + 32), "v"(1u) : "memory");
    });
    STAMP(0, 2);
    ep = RFL(*(const LDSAS unsigned*)(fa + 512)) + 1u;
    asm volatile("s_nop 11" : "+{v[64:79]}"(acA));
    epi(2, acA, 0); epi(2, acA, 1);
    if (lane == 0) asm volatile("ds_add_u32 %0, %1" :: "v"(fa + 40), "v"(1u) : "memory");
    STAMP(0, 5);
    }
    }
    SBAR();
    const int rt = rt2;
    u32x8 xres;
    u32x4 hreg[2];

    auto issue_wo = [&](int first, int n) {
        const rsrc_t rwo = mk_rsrc(wo, D * D * 2);
        const unsigned v = w64_voff(lane);
        for (int p = first; p < first + n; ++p) dma16(rwo, v, p * 8192 + ct * 128, lds0 + L_WO + p * 1024);
    };
    auto issue_dn = [&](int i0, int n, unsigned dst, int hw) {
        const rsrc_t rwd = mk_rsrc(wd, F * D * 2);
        const unsigned v = w64_voff(lane);
        const unsigned so = ((43 * (hw >> 1) + i0) * 16 + (hw & 1) * 8) * 1024 + ct * 128;
        for (int i = 0; i < n; ++i) dma16(rwd, v, so + i * 16384, lds0 + dst + (hw & 1) * 1024 + i * 2048);
    };
    auto gu_wa = [&](int n) -> unsigned { const unsigned blk = (lane >> 4) & 1;
        return lds0 + L_GU + blk * 49152 + (((4 * h + ((lane & 15) >> 2)) * 96 + n * 32 + (lane & 3) * 8) ^ (blk * 128)); };
    float cs[3] = {0.f, 0.f, 0.f};
    auto gu_colsum = [&]() {
        u32x4 f[3][4];
#pragma unroll
        for (int n = 0; n < 3; ++n) {
            const unsigned a0 = gu_wa(n) + w * 4 * 1536;
#pragma unroll
            for (int i = 0; i < 4; ++i) f[n][i] = __builtin_bit_cast(u32x4, lds_tr2(a0 + i * 1536, a0 + i * 1536 + 768));
        }
        SBAR();
#pragma unroll
        for (int i = 0; i < 4; ++i)
#pragma unroll
            for (int e = 0; e < 4; ++e)
#pragma unroll
                for (int n = 0; n < 3; ++n) cs[n] = dot2(f[n][i][e], ONES2, cs[n]);
#pragma unroll
        for (int n = 0; n < 3; ++n) cs[n] = swap_add(cs[n]);
    };

    unsigned budget = 1u << 16;

    {
        const int qb = bi & 7, wq = w & 3, g = w >> 2, qt = qb * 8 + wq * 2;
        s16x8 q0, q1;
        const unsigned qs0 = RFL(WS_Q + (head * 64 + qt + g) * 1024), qs1 = qs0 ^ 1024;
        unsigned tq, cap = 1u << 22;
#define XSPIN(L, O, N) #L ":\n\tds_read_b32 v132, %[fa] offset:" #O "\n\ts_sub_u32 %[cap], %[cap], 1\n\ts_cbranch_scc1 9f\n\ts_waitcnt lgkmcnt(0)\n\tv_readfirstlane_b32 %[t], v132\n\ts_sleep 1\n\ts_cmp_lt_u32 %[t], " #N "\n\ts_cbranch_scc1 " #L "b\n\t"
        SKEW(1);
        asm volatile(XSPIN(3, 40, 4) XSPIN(4, 36, 1) "s_sleep %c[dl]\n9:\n\tbuffer_load_dwordx4 v[128:131], %[l16], %[rs], %[qs] offen sc1\n\tbuffer_load_dwordx4 v[156:159], %[l16], %[rs], %[qs1] offen sc1"
                     : "={v[128:131]}"(q0), "={v[156:159]}"(q1), [cap] "+s"(cap), [t] "=&s"(tq)
                     : [l16] "v"(l16), [rs] "s"(rs), [qs] "s"(qs0), [qs1] "s"(qs1), [fa] "v"(lds0 + K1_FLG), [dl] "n"(DELAY_Q) : "scc", "v132", "memory");
        SBAR();
        const unsigned kvs = WS_KV + head * KV_HEAD;
        unsigned ks = kvs + (g * 4 + 1) * KV_CHUNK + wq * 4096, km = lds0 + g * 32768 + 16384 + wq * 4096;
        const rsrc_t rg = mk_rsrc(g ? wu : wg, D * F * 2), rwd = mk_rsrc(wd, F * D * 2), rwo = mk_rsrc(wo, D * D * 2), rx = mk_rsrc(x, S * D * 2);
        unsigned gs = RFL(bi < 29 ? wq * 4 * 32 * (F * 2) + bi * 96 : 0x40000000), gm = lds0 + L_GU + g * 49152 + wq * 12288;
        const unsigned long long msk = 0x0000ffff0000ffffull;
        constexpr unsigned OOB = 0x80000000u, SOOB = 0x40000000u;
        auto gu_voff = [&](unsigned c) { const unsigned r = __umul24(c, 171u) >> 10; return __umul24(r, F * 2 - 96u) + c * 16; };
        const unsigned gv0 = gu_voff(lane ^ (g * 8)), gv1 = gu_voff((64 + lane) ^ (g * 8)), gv2 = gu_voff((128 + lane) ^ (g * 8));
        const unsigned pvd = (lane * 128) | ((unsigned)(42 - lane) & OOB), pvo = (lane * 128) | ((unsigned)(15 - lane) & OOB), psd = RFL((bi * 344 + w * 43) * 128), pso = RFL((bi * 128 + w * 16) * 128);
        const unsigned tv = (lane * 128) | ((unsigned)(10 - lane) & OOB), tsg = RFL((b * 43 + wq * 11) * 128), tso = RFL((b * 16 + wq * 4) * 128);
        unsigned long long pc; unsigned clen;
        asm volatile("s_getpc_b64 %0\n.Latt_pc:\n\ts_mov_b32 %1, .Lmain_end-.Latt_pc-4" : "=s"(pc), "=s"(clen));
        const unsigned cv = __builtin_elementwise_min((unsigned)(((lane & 1) * 8 + w) * 32 + bi) * 128u, clen & ~3u);
        const unsigned wv = w64_voff(lane), xv = r32 * 1024 + 8 * h, xs = RFL((rt * 64 + wt * 32) * 1024 + (ct * 64 + kh * 32) * 2);
        const unsigned kb = lds0 + g * 32768 + l16, vb = lds0 + g * 32768 + 8192 + (16 * h + (r32 & 15)) * 16;
        const unsigned cst = (r32 & 27) == 16 ? ONES2 : 0u;
        const unsigned fsv = h * 4096 + r32 * 16, fs = RFL(w == 0 ? WS_ST + (rt * 32 + ct * 2) * 256 : SOOB), fpv = (unsigned)(0 - lane) & OOB, fps = RFL(w == 0 ? WS_FP + b * 4 : SOOB);
        u32x4 ones, fpl, pA, pB; unsigned pfd, epv, flq; f32x16 sB, o0, o1; s16x8 v1, v1b;
        const unsigned fo = WS_O + ((qt + g) * 32 + head) * 1024, fh = WS_H + ((rt * 2 + wt) * 32 + ct * 4 + (w >> 1)) * 1024, fg = RFL(bi < 29 ? WS_HH + ((xcd * 8 + w) * NU + bi * 3) * 1024 : SOOB), fg2 = RFL(bi < 28 ? fg : SOOB);
        STAMP(0, 6);
        SBAR();
#define XKV "s_mov_b32 m0, %[km]\n\ts_add_u32 %[km], %[km], 0x400\n\tbuffer_load_dwordx4 %[l16], %[rs], %[ks] offen sc1 lds\n\ts_add_u32 %[ks], %[ks], 0x400\n\t"
        const unsigned lm0 = RFL(lds0 + ((w >> 1) & 1) * 32768 + (w & 1) * 8192), ls0 = RFL(kvs + ((w >> 1) & 1) * 4 * KV_CHUNK + (w & 1) * 8192), fq = RFL(WS_FQ + (head >> 1) * 8192);
        const unsigned pvf = lane * 128;
        unsigned lm, ls;
#define XLD "s_mov_b32 m0, %[lm]\n\ts_add_u32 %[lm], %[lm], 0x400\n\tbuffer_load_dwordx4 %[l16], %[rs], %[ls] offen sc1 lds\n\ts_add_u32 %[ls], %[ls], 0x400\n\t"
#define XRB(R, O) "ds_read_b128 v[" #R ":" #R "+3], v133 offset:" #O "\n\t"
#define XMX(R) "v_max3_u32 v132, v132, v[" #R "], v[" #R "+1]\n\tv_max3_u32 v132, v132, v[" #R "+2], v[" #R "+3]\n\t"
        asm volatile(".set tagj, 0\n\t.rept 8\n\tv_mov_b64 v[96+tagj:97+tagj], 0\n\tv_mov_b64 v[160+tagj:161+tagj], 0\n\t.set tagj, tagj+2\n\t.endr\n\t"
                     "v_mov_b64 v[152:153], 0\n\tv_mov_b64 v[154:155], 0\n\tv_mov_b64 v[204:205], -1\n\tv_mov_b64 v[206:207], -1\n\tv_mov_b32 v201, %[ep]\n\t"
                     ".set tagj, 0\n\t.rept 4\n\tv_mov_b32 v[136+tagj], %[cst]\n\tv_mov_b32 v[144+tagj], %[cst]\n\t.set tagj, tagj+1\n\t.endr\n\t"
                     "s_cmp_lt_u32 %[w], 4\n\ts_cbranch_scc1 1f\n\tv_add_u32 v133, %[lm0], %[l16]\n\ts_waitcnt vmcnt(2)\n"
                     "5:\n\t" XRB(64, 0) XRB(68, 1024) XRB(72, 2048) XRB(76, 3072) XRB(112, 4096) XRB(116, 5120) XRB(120, 6144) XRB(124, 7168) "v_mov_b32 v132, 0\n\ts_waitcnt lgkmcnt(0)\n\t"
                     XMX(64) XMX(68) XMX(72) XMX(76) XMX(112) XMX(116) XMX(120) XMX(124) "v_cmp_eq_u32 vcc, -1, v132\n\ts_cbranch_vccnz 6f\n\t"
                     ".subsection 1\n6:\n\ts_cmp_eq_u32 %[bud], 0\n\ts_cbranch_scc1 1f\n\ts_sub_u32 %[bud], %[bud], 1\n\ts_mov_b32 %[lm], %[lm0]\n\ts_mov_b32 %[ls], %[ls0]\n\t"
                     XLD XLD XLD XLD XLD XLD XLD XLD "s_waitcnt vmcnt(0)\n\ts_branch 5b\n\t.subsection 0\n"
                     "1:\n\ts_waitcnt vmcnt(0)\n\tv_max3_u32 v132, v128, v129, v130\n\tv_max3_u32 v132, v132, v131, v156\n\tv_max3_u32 v132, v132, v157, v158\n\tv_max_u32 v132, v132, v159\n\t"
                     "v_cmp_eq_u32 vcc, -1, v132\n\ts_cbranch_vccnz 3f\n2:\n\t.subsection 1\n3:\n\ts_cmp_eq_u32 %[bud], 0\n\ts_cbranch_scc1 2b\n\ts_sub_u32 %[bud], %[bud], 1\n\t"
                     "buffer_load_dwordx4 v[128:131], %[l16], %[rs], %[qs] offen sc1\n\tbuffer_load_dwordx4 v[156:159], %[l16], %[rs], %[qs1] offen sc1\n\ts_branch 1b\n\t.subsection 0\n\t"
                     "buffer_store_dword v201, %[flv], %[rs], %[fls] offen sc1\n\ts_barrier"
                     : "+{v[128:131]}"(q0), "+{v[156:159]}"(q1), [bud] "+s"(budget), [lm] "=&s"(lm), [ls] "=&s"(ls),
                       "={v[80:95]}"(sB), "={v[96:111]}"(o0), "={v[160:175]}"(o1), "={v[144:147]}"(v1), "={v[136:139]}"(v1b), "={v[148:151]}"(pA), "={v[152:155]}"(pB),
                       "={v[192:199]}"(xres), "={v200}"(pfd), "={v201}"(epv), "={v202}"(flq), "={v[204:207]}"(ones), "={v[208:211]}"(fpl)
                     : [l16] "v"(l16), [rs] "s"(rs), [qs] "s"(qs0), [qs1] "s"(qs1), [w] "s"(w), [ep] "s"(ep), [cst] "v"(cst), [lm0] "s"(lm0), [ls0] "s"(ls0), [flv] "v"(flv), [fls] "s"(RFL(w < 4 ? fls : SOOB)),
                       "v"(gv0), "v"(gv1), "v"(gv2), "v"(pvd), "v"(pvo), "v"(wv), "v"(xv), "v"(kb), "v"(vb), "v"(fsv), "v"(fpv), "v"(pvf), "v"(tv), "s"(tsg), "s"(tso), "v"(cv), "s"(pc),
                       "s"(rg), "s"(rwd), "s"(rwo), "s"(rx), "s"(gs), "s"(gm), "s"(psd), "s"(pso), "s"(xs), "s"(fs), "s"(fps), "s"(fo), "s"(fh), "s"(fg), "s"(fg2), "s"(fq), "s"(km), "s"(ks)
                     : "m0", "scc", "vcc", "v132", "v133", "v64", "v65", "v66", "v67", "v68", "v69", "v70", "v71", "v72", "v73", "v74", "v75", "v76", "v77", "v78", "v79",
                       "v112", "v113", "v114", "v115", "v116", "v117", "v118", "v119", "v120", "v121", "v122", "v123", "v124", "v125", "v126", "v127", "memory");
        STAMP(0, 7);
        FLUSH(0);
        km = RFL(km); ks = RFL(ks); budget = RFL(budget);
        PROBEV(99, 65536u - budget);
        STAMP(1, 0);
        STAMP(1, 1);
        f32x16 n0, n1, sA;
        s16x8 kf = lds_b128(kb), v0;
        {
            const f32x16 z = {};
            const s16x8 kz = lds_b128(lds0 + l16);
            const u32x4 vv = __builtin_bit_cast(u32x4, lds_b128(vb)), cc = {cst, cst, cst, cst};
            const f32x16 s0 = MFMA(kz, q0, z), s1 = MFMA(kz, q1, z);
            float m0 = __builtin_fmaxf(s0[0], s0[1]), m1 = __builtin_fmaxf(s1[0], s1[1]);
#pragma unroll
            for (int i = 2; i < 16; i += 2) { m0 = __builtin_fmaxf(__builtin_fmaxf(m0, s0[i]), s0[i + 1]); m1 = __builtin_fmaxf(__builtin_fmaxf(m1, s1[i]), s1[i + 1]); }
            m0 = swap_max(m0); m1 = swap_max(m1);
#pragma unroll
            for (int i = 0; i < 16; ++i) { n0[i] = -m0; n1[i] = -m1; }
            sA = MFMA(kf, q0, n0);
            v0 = __builtin_bit_cast(s16x8, r32 < 16 ? vv : cc);
        }
#define XE(C, i) "v_exp_f32 v[" #C "+" #i "], v[" #C "+" #i "]\n\t"
#define XC(P, C, i) "v_cvt_pk_bf16_f32 v" #P ", v[" #C "+" #i "], v[" #C "+" #i "+1]\n\t"
#define XG1(C) XE(C, 0) XE(C, 1) XE(C, 2) XE(C, 3) XC(148, C, 0) XE(C, 4) XE(C, 5) XC(149, C, 2)
#define XG2(C) XE(C, 6) XE(C, 7) XC(150, C, 4) XE(C, 8) XC(151, C, 6) XE(C, 9) XE(C, 10)
#define XG3(C) XE(C, 11) XC(152, C, 8) XE(C, 12) XE(C, 13) XC(153, C, 10) XE(C, 14) XE(C, 15) XC(154, C, 12) XC(155, C, 14)
#define XM(D, A, B, C) "v_mfma_f32_32x32x16_bf16 v[" #D ":" #D "+15], v[" #A ":" #A "+3], v[" #B ":" #B "+3], v[" #C ":" #C "+15]\n\t"
#define XRK(BASE, O) "ds_read_b128 v[132:135], " BASE " offset:" #O "\n\t"
#define XRV(R, BASE, O) "s_mov_b64 exec, %[msk]\n\tds_read_b128 v[" #R ":" #R "+3], " BASE " offset:" #O "\n\ts_mov_b64 exec, -1\n\t"
#define XW(n) "s_waitcnt lgkmcnt(" #n ")\n\t"
#define XTILE(VA, RK, RV1, WA, X1, VB, X2, RV0) \
        XM(80, 132, 156, 176) RK XG1(64) XM(160, VA, 152, 160) RV1 XG2(64) XW(WA) XM(96, 140, 148, 96) XG3(64) \
        XW(0) XM(64, 132, 128, 112) X1 XG1(80) XM(96, VB, 152, 96) X2 XG2(80) XM(160, 140, 148, 160) RV0 XG3(80)
#define XGU(j) "s_mov_b32 m0, %[gm]\n\ts_add_u32 %[gm], %[gm], 0x400\n\tbuffer_load_dwordx4 %[gv" #j "], %[rg], %[gs] offen lds\n\t"
#define XGU2 XGU(2) "s_add_u32 %[gs], %[gs], 88064\n\t"
#define XPF "buffer_load_dword v200, %[pvd], %[rwd], %[psd] offen\n\tbuffer_load_dword v200, %[pvo], %[rwo], %[pso] offen\n\t"
#define XWO "s_mov_b32 m0, %[km]\n\ts_add_u32 %[km], %[km], 0x400\n\tbuffer_load_dwordx4 %[wv], %[rwo], %[ks] offen lds\n\ts_add_u32 %[ks], %[ks], 0x2000\n\t"
#define XST(V, SO, O) "buffer_store_dwordx4 v[204:207], " V ", %[rs], " SO " offen offset:" #O " sc1\n\t"
#define XFP "buffer_store_dword v201, %[fpv], %[rs], %[fps] offen sc1\n\t"
#define XFL "buffer_load_dwordx4 v[208:211], %[l16], %[rs], %[fp] offen sc1\n\t"
#define XCP "global_load_dword v200, %[cv], %[pc]\n\t"
#define XTG "buffer_load_dword v200, %[tv], %[rg], %[tsg] offen\n\t"
#define XTD "s_cmp_eq_u32 %[g], 0\n\ts_cbranch_scc0 1f\n\tbuffer_load_dword v200, %[tv], %[rwd], %[tsg] offen\n\ts_branch 2f\n1:\n\tbuffer_load_dword v200, %[tv], %[rwo], %[tso] offen\n2:\n\t"
#define XQL "buffer_load_dword v202, %[pvf], %[rs], %[fq] offen sc1\n\t"
#define XQC(n) "s_waitcnt vmcnt(" #n ")\n\tv_cmp_ne_u32 vcc, v201, v202\n\ts_cbranch_vccnz 3f\n2:\n\t.subsection 1\n3:\n\ts_cmp_eq_u32 %[bud], 0\n\ts_cbranch_scc1 2b\n\ts_sub_u32 %[bud], %[bud], 1\n\t" \
               XQL "s_waitcnt vmcnt(0)\n\tv_cmp_ne_u32 vcc, v201, v202\n\ts_cbranch_vccnz 3b\n\ts_branch 2b\n\t.subsection 0\n\t"
#define XXR(R, O) "buffer_load_dwordx2 v[" #R ":" #R "+1], %[xv], %[rx], %[xs] offen offset:" #O "\n\t"
#define AOPS : "+{v[64:79]}"(sA), "+{v[80:95]}"(sB), "+{v[96:111]}"(o0), "+{v[160:175]}"(o1), "+{v[112:127]}"(n0), "+{v[176:191]}"(n1), "+{v[128:131]}"(q0), "+{v[156:159]}"(q1), \
               "+{v[132:135]}"(kf), "+{v[140:143]}"(v0), "+{v[144:147]}"(v1), "+{v[136:139]}"(v1b), "+{v[148:151]}"(pA), "+{v[152:155]}"(pB), "+{v[192:199]}"(xres), "+{v200}"(pfd), \
               "+{v201}"(epv), "+{v202}"(flq), "+{v[204:207]}"(ones), "+{v[208:211]}"(fpl), [bud] "+s"(budget), \
               [km] "+s"(km), [ks] "+s"(ks), [gm] "+s"(gm), [gs] "+s"(gs) \
             : [ka] "v"(ka), [va] "v"(va), [kn] "v"(kn), [vn] "v"(vn), [msk] "s"(msk), [l16] "v"(l16), [rs] "s"(rs), [rg] "s"(rg), [gv0] "v"(gv0), [gv1] "v"(gv1), [gv2] "v"(gv2), \
               [pvd] "v"(pvd), [pvo] "v"(pvo), [wv] "v"(wv), [xv] "v"(xv), [xs] "s"(xs), [psd] "s"(psd), [pso] "s"(pso), [rwd] "s"(rwd), [rwo] "s"(rwo), [rx] "s"(rx), \
               [fo] "s"(fo), [fh] "s"(fh), [fg] "s"(fg), [fsv] "v"(fsv), [fs] "s"(fs), [fg2] "s"(fg2), [fpv] "v"(fpv), [fps] "s"(fps), [fp] "s"(WS_FP), [pvf] "v"(pvf), [fq] "s"(fq), [tv] "v"(tv), [tsg] "s"(tsg), [tso] "s"(tso), [g] "s"(g), [cv] "v"(cv), [pc] "s"(pc) : "m0", "scc", "vcc", "memory"
#define XBAR(n) asm volatile("s_waitcnt vmcnt(" #n ") lgkmcnt(0)\n\ts_barrier" AOPS)
#define XP(n) "s_setprio " #n "\n\t"
#define XROUND(...) XROUND_(__VA_ARGS__)
#define XROUND_(P3, P4, P5, P6, A1, A2, B1, B2, C1, C2, D1, D2, E1, E2, F1, F2) asm volatile( \
            XTILE(136, XRK("%[ka]", 1024), XRV(144, "%[va]", 512), 2, A1, 144, A2, XRV(140, "%[va]", 1024)) \
            XTILE(144, XRK("%[ka]", 2048), XRV(144, "%[va]", 1536), 2, B1, 144, B2, XRV(140, "%[va]", 2048)) \
            XTILE(144, XRK("%[ka]", 3072), XRV(144, "%[va]", 2560), 2, C1, 144, C2, XRV(140, "%[va]", 3072)) \
         P3 XTILE(144, XRK("%[ka]", 4096), XRV(144, "%[va]", 3584), 2, D1, 144, D2, XRV(140, "%[va]", 4096)) \
         P4 XTILE(144, XRK("%[ka]", 5120), XRV(144, "%[va]", 4608), 2, E1, 144, E2, XRV(140, "%[va]", 5120)) \
         P5 XTILE(144, XRK("%[ka]", 6144), XRV(144, "%[va]", 5632), 2, F1, 144, F2, XRV(140, "%[va]", 6144)) \
         P6 XTILE(144, XRK("%[ka]", 7168), XRV(144, "%[va]", 6656), 2, XRV(136, "%[va]", 7680), 144, "", XRV(140, "%[va]", 7168)) AOPS)
#define XLAST(P, X1, X2) asm volatile(P XTILE(144, XRK("%[kn]", 0), "", 1, X1, 136, X2, XRV(140, "%[vn]", 0)) AOPS)
#define XPMID XP(2), "", XP(1), XP(0)
        unsigned ka = kb, va = vb, kn = kb + 16384, vn = vb + 16384;
        asm volatile(XP(3) "s_nop 15" AOPS);
        STAMP(1, 7);
        XROUND(XPMID, XCP, "", "", XQL, "", "", XQC(0) XKV, XKV, XKV, XKV, XTG, XTD);
        STAMP(1, 8);
        PROBEV(98, 65536u - budget);
        XBAR(2);
        STAMP(1, 9);
        km -= 16384 + 4096; ks += KV_CHUNK - 4096;
        XLAST(XP(3), XKV, XKV);
        ka = kb + 16384; va = vb + 16384; kn = kb; vn = vb;
        XROUND(XPMID, XKV, XKV, XST("%[l16]", "%[fo]", 0), XST("%[fsv]", "%[fs]", 0), XGU(0), XST("%[l16]", "%[fh]", 0), XGU(1), "", XGU2, XST("%[l16]", "%[fg]", 0), XGU(0), "");
        STAMP(1, 10);
        XBAR(8);
        STAMP(1, 11);
        km += 16384 - 4096; ks += KV_CHUNK - 4096;
        XLAST(XP(3), XKV, XKV);
        ka = kb; va = vb; kn = kb + 16384; vn = vb + 16384;
        XROUND(XPMID, XKV, XKV, XGU(1), XST("%[l16]", "%[fg]", 1024), XGU2, XST("%[l16]", "%[fg2]", 2048), XGU(0), XCP, XGU(1), "", XGU2, XPF);
        STAMP(1, 12);
        XBAR(5);
        STAMP(1, 13);
        km = lds0 + g * 32768 + wq * 4096; ks = (32 * g + 4 * wq) * 8192 + ct * 128;
        XLAST(XP(3), XFP, XWO);
        ka = kb + 16384; va = vb + 16384; kn = kb; vn = vb;
        XROUND("", XP(2), "", XP(1), XWO, XWO, XWO, XGU(0), XXR(192, 0), XGU(1), XXR(194, 16), XGU2, XXR(196, 32), "", XXR(198, 48), "");
        XLAST(XP(0), XFL, "");
        STAMP(1, 14);
        asm volatile(XM(160, 136, 152, 160) "s_waitcnt vmcnt(0) lgkmcnt(0)\n\ts_barrier\n\ts_nop 15" AOPS);
        STAMP(1, 4);
        SKEW(2);
        f32x16 o = o0;
        const unsigned xa = lds0 + g * 32768 + 16384 + wq * 3072 + l16, xb = lds0 + (g ^ 1) * 32768 + 16384 + wq * 3072 + l16;
        *(LDSAS f32x4*)(xa) = f32x4{o1[0], o1[1], o1[2], o1[3]};
        *(LDSAS f32x4*)(xa + 1024) = f32x4{o1[4], o1[5], o1[6], o1[7]};
        *(LDSAS float*)(xa + 2048) = o1[8];
        {
            float big = __builtin_fmaxf(o0[8], o1[8]) * 1e19f;
            float chk = o0[8] + o1[8];
#pragma unroll
            for (int i = 0; i < 8; ++i) { big = __builtin_fmaxf(__builtin_fmaxf(big, __builtin_fabsf(o0[i])), __builtin_fabsf(o1[i])); chk += o0[i] + o1[i]; }
            *(LDSAS unsigned*)(lds0 + L_FLAG + w * 4) = __any(!(big < 1e38f) || chk != chk);
        }
        BAR_L();
        const f32x4 p0 = lds_f4(xb), p1 = lds_f4(xb + 1024); const float pl = *(const LDSAS float*)(xb + 2048);
        const u32x4 f0 = *(const LDSAS u32x4*)(lds0 + L_FLAG), f1 = *(const LDSAS u32x4*)(lds0 + L_FLAG + 16);
#pragma unroll
        for (int i = 0; i < 4; ++i) { o[i] += p0[i]; o[4 + i] += p1[i]; }
        o[8] += pl;
        if (__builtin_expect(RFL(f0[0] | f0[1] | f0[2] | f0[3] | f1[0] | f1[1] | f1[2] | f1[3]), 0)) {
            BAR_L();
            const s16x8 qf = q0;
            const u32x4 cc = {cst, cst, cst, cst};
            const unsigned vf = lds0 + 16384 + 8192 + (16 * h + (r32 & 15)) * 16;
            float m = -__builtin_inff();
            o = f32x16{};
            const f32x16 z = {};
#pragma unroll 1
            for (int c = 0; c < 8; ++c) {
                dma16<true>(rs, l16, kvs + c * KV_CHUNK + w * 2048, lds0 + 16384 + w * 2048);
                dma16<true>(rs, l16, kvs + c * KV_CHUNK + w * 2048 + 1024, lds0 + 16384 + w * 2048 + 1024);
                BAR_VL(0);
#pragma unroll 1
                for (int t = 0; t < 8; ++t) {
                    f32x16 s = MFMA(lds_b128(lds0 + 16384 + l16 + t * 1024), qf, z);
                    float mx = s[0];
#pragma unroll
                    for (int i = 1; i < 16; ++i) mx = __builtin_fmaxf(mx, s[i]);
                    mx = __builtin_fmaxf(swap_max(mx), m);
                    const float al = __builtin_amdgcn_exp2f(m - mx);
                    m = mx;
#pragma unroll
                    for (int i = 0; i < 16; ++i) { o[i] *= al; s[i] = __builtin_amdgcn_exp2f(s[i] - m); }
#pragma unroll
                    for (int i = 0; i < 4; ++i) { pA[i] = pk2(s[2 * i], s[2 * i + 1]); pB[i] = pk2(s[8 + 2 * i], s[9 + 2 * i]); }
                    const u32x4 a = __builtin_bit_cast(u32x4, lds_b128(vf + t * 1024)), c2 = __builtin_bit_cast(u32x4, lds_b128(vf + t * 1024 + 512));
                    o = MFMA(__builtin_bit_cast(s16x8, r32 < 16 ? a : cc), __builtin_bit_cast(s16x8, pA), o);
                    o = MFMA(__builtin_bit_cast(s16x8, r32 < 16 ? c2 : cc), __builtin_bit_cast(s16x8, pB), o);
                }
                BAR_L();
            }
        }
        {
            const float inv = __builtin_amdgcn_rcpf(o[8]);
            u32x4 ov;
#pragma unroll
            for (int i = 0; i < 4; ++i) ov[i] = pk2(o[2 * i] * inv, o[2 * i + 1] * inv);
            ST128(ov, rs, l16, fo, AUX_SC1);
        }
        STAMP(1, 5);
        BAR_L();
        if (w >= 4) issue_wo(16 + ((w - 4) >> 1) * 32 + (w & 1) * 8, 8);
        gu_colsum();
#pragma unroll 1
        for (unsigned spins = 0; spins < SPIN_CAP && !__all(fpl[0] == ep && fpl[1] == ep && fpl[2] == ep && fpl[3] == ep); ++spins) {
            __builtin_amdgcn_s_sleep(2);
            fpl = __builtin_amdgcn_raw_buffer_load_b128(rs, l16, WS_FP, AUX_SC1);
        }
        STAMP(1, 6);
        FLUSH(1);
    }

    {
        SKEW(3);
        STAMP(2, 0);
        f32x16 a0, a1;
        if (w < 4) {
            const unsigned t0 = lds0 + L_WO + kh * 32768 + w64_tr(lane, kh), t1 = lds0 + L_WO + kh * 32768 + w64_tr(lane, kh ^ 1);
            G2 g2;
            g2.tb[0] = g2.tb[2] = g2.tb[4] = t0; g2.tb[1] = g2.tb[3] = g2.tb[5] = t1;
            g2.sa = g2.s0 = RFL(WS_O + ((rt * 2 + wt) * 32 + kh * 16) * 1024); g2.l16 = l16; g2.budget = budget; g2.rs = rs; g2.a1 = f32x16{};
#pragma unroll
            for (int i = 0; i < 8; ++i) { g2.a0[2 * i] = bflo(xres[i]); g2.a0[2 * i + 1] = bfhi(xres[i]); }
            __builtin_amdgcn_s_sleep(DELAY_O);
            STAMP(2, 1);
            g2_steps<0, 16, 0, 6, -1>(g2);
            a0 = g2.a0; a1 = g2.a1; STAMPV(15, budget - g2.budget); budget = g2.budget;
        } else BAR_VL(0);
        STAMP(2, 2);
        SKEW(4);
        if (w < 4) {
#pragma unroll
            for (int g = 0; g < 4; ++g) *(LDSAS f32x4*)(lds0 + w * 4096 + g * 1024 + lane * 16) = f32x4{a1[4 * g], a1[4 * g + 1], a1[4 * g + 2], a1[4 * g + 3]};
        }
#pragma unroll
        for (int n = 0; n < 3; ++n) *(LDSAS float*)(lds0 + 32768 + (w * 96 + n * 32 + r32) * 4) = cs[n];
        BAR_L();
        if (w >= 4) {
            if (tid < 256 + 96) {
                float t[8];
#pragma unroll
                for (int i = 0; i < 8; ++i) t[i] = *(const LDSAS float*)(lds0 + 32768 + (i * 96 + tid - 256) * 4);
                *(LDSAS float*)(lds0 + L_TAB + 3072 + (tid - 256) * 4) = ((t[0] + t[1]) + (t[2] + t[3])) + ((t[4] + t[5]) + (t[6] + t[7]));
            }
        } else {
            f32x4 p[4];
#pragma unroll
            for (int g = 0; g < 4; ++g) p[g] = lds_f4(lds0 + (w ^ 2) * 4096 + g * 1024 + lane * 16);
            SBAR();
#pragma unroll
            for (int g = 0; g < 4; ++g) {
                hreg[g >> 1][(g & 1) * 2] = pk2(a0[4 * g] + p[g][0], a0[4 * g + 1] + p[g][1]);
                hreg[g >> 1][(g & 1) * 2 + 1] = pk2(a0[4 * g + 2] + p[g][2], a0[4 * g + 3] + p[g][3]);
            }
            const unsigned so = WS_H + ((rt * 2 + wt) * 32 + (ct * 2 + kh) * 2) * 1024;
            ST128(hreg[0], rs, lane * 16, so, AUX_SC1);
            ST128(hreg[1], rs, lane * 16 + 1024, so, AUX_SC1);
            SBAR();
            float sm = 0.f, sq = 0.f;
#pragma unroll
            for (int i = 0; i < 8; ++i) { sm = dot2(hreg[i >> 2][i & 3], ONES2, sm); sq = dot2(hreg[i >> 2][i & 3], hreg[i >> 2][i & 3], sq); }
            sm = swap_add(sm); sq = swap_add(sq);
            if (h == 0) __builtin_amdgcn_raw_buffer_store_b64(u32x2{__float_as_uint(sm), __float_as_uint(sq)}, rs, r32 * 8,
                                                              WS_ST + ((rt * 2 + wt) * 16 + ct * 2 + kh) * 256, AUX_SC1);
        }
        STAMP(2, 3);
        BAR_L();
        if (w >= 4) { issue_dn(0, 15, ((w - 4) >> 1) * 15 * 2048, w - 4); if (bi >= 29) issue_dn(15, 24, L_GU + ((w - 4) >> 1) * 49152, w - 4); }
        STAMP(2, 5);
        FLUSH(2);
    }

    {
        SKEW(5);
        STAMP(3, 0);
        if (bi < 29) {
            const int tt = xcd * 8 + w;
            GU gu;
            gu.a0 = gu.a1 = gu.a2 = f32x16{};
            gu.sa = gu.s0 = RFL(WS_H + tt * 32768); gu.sv = 8 * h * 256 + r32 * 8; gu.ss = RFL(WS_ST + tt * 4096); gu.l16 = l16; gu.budget = budget; gu.rs = rs;
            gu.dm = RFL(lds0 + L_GU + (w >> 2) * 49152 + (w & 3) * 6144); gu.ds = RFL(((43 * (w >> 2) + 15) * 16 + (w & 3) * 48) * 1024 + ct * 128);
            gu.dm2 = RFL(lds0 + L_GU + (w >> 2) * 49152 + 24576 + (w & 3) * 3072); gu.ds2 = RFL(((43 * (w >> 2) + 27) * 16 + (w & 3) * 24) * 1024 + ct * 128);
            gu.dm3 = RFL(lds0 + L_TAB + (w >> 2) * 2048 + (w & 1) * 1024); gu.ds3 = RFL(((43 * (w >> 2) + 39) * 16 + (w & 1) * 8) * 1024 + ct * 128);
            gu.dv = w64_voff(lane); gu.rd = mk_rsrc(wd, F * D * 2);
            gu.wa[0] = gu_wa(0); gu.wa[1] = gu_wa(1); gu.wa[2] = gu_wa(2);
            float csl[3];
#pragma unroll
            for (int n = 0; n < 3; ++n) csl[n] = *(const LDSAS float*)(lds0 + L_TAB + 3072 + (n * 32 + r32) * 4);
            if (w < 4) __builtin_amdgcn_s_sleep(DELAY_H);
            STAMP(3, 2);
            gu_steps<0, 18>(gu);
#pragma unroll 1
            for (;;) {
                unsigned mx = gu.st[0];
#pragma unroll
                for (int i = 1; i < 16; ++i) mx = gu.st[i] > mx ? gu.st[i] : mx;
                if (__builtin_expect(!__any(mx == ~0u) || gu.budget == 0, 1)) break;
                --gu.budget;
                asm volatile(".set tagj, 0\n\t.rept 8\n\tbuffer_load_dwordx2 v[172+2*tagj:173+2*tagj], %[sv], %[rs], %[ss] offen offset:tagj*256 sc1\n\t.set tagj, tagj+1\n\t.endr\n\ts_waitcnt vmcnt(0)"
                             : "+{v[172:187]}"(gu.st), "+{v[140:155]}"(gu.r0), "+{v[156:171]}"(gu.r1), "+{v[112:119]}"(gu.w0), "+{v[124:131]}"(gu.w1), "+{v[132:139]}"(gu.w2),
                               "+{v[64:79]}"(gu.a0), "+{v[80:95]}"(gu.a1), "+{v[96:111]}"(gu.a2) : [sv] "v"(gu.sv), [rs] "s"(rs), [ss] "s"(gu.ss) : "memory");
            }
            float sm = 0.f, sq = 0.f;
#pragma unroll
            for (int i = 0; i < 8; ++i) { sm += __uint_as_float(gu.st[2 * i]); sq += __uint_as_float(gu.st[2 * i + 1]); }
            sm = swap_add(sm); sq = swap_add(sq);
            const float mean = sm * (1.f / D);
            const float var = __builtin_fmaxf(sq * (1.f / D) - mean * mean, 0.f) + LN_EPS;
            float rstd = __builtin_amdgcn_rsqf(var), sd = var * rstd, ga = -LOG2E * rstd;
            u32x4 mf, cf[3];
            {
                const unsigned mh = pk2(-mean, -mean);
                const unsigned ml = pk2(-mean - bflo(mh), 0.f);
                mf = h ? u32x4{0u, 0u, 0u, 0u} : u32x4{(mh & 0xffffu) | (ml << 16), mh & 0xffffu, 0u, 0u};
#pragma unroll
                for (int n = 0; n < 3; ++n) { const unsigned ch = pk2(csl[n], csl[n]); cf[n] = u32x4{ch, pk2(csl[n] - bflo(ch), 0.f), 0u, 0u}; PIN(cf[n]); }
                PIN(mf); PIN(sd); PIN(ga);
            }
            gu_steps<18, 32>(gu);
            asm volatile("v_mfma_f32_32x32x16_bf16 v[64:79], %[c0], %[m], v[64:79]\n\tv_mfma_f32_32x32x16_bf16 v[80:95], %[c1], %[m], v[80:95]\n\t"
                         "v_mfma_f32_32x32x16_bf16 v[96:111], %[c2], %[m], v[96:111]\n\ts_nop 10"
                         : "+{v[64:79]}"(gu.a0), "+{v[80:95]}"(gu.a1), "+{v[96:111]}"(gu.a2) : [c0] "v"(cf[0]), [c1] "v"(cf[1]), [c2] "v"(cf[2]), [m] "v"(mf));
            STAMP(3, 3);
            SKEW(6);
            STAMPV(15, budget - gu.budget); budget = gu.budget;
            auto epi = [&](int n, const f32x16& acc) {
                f32x2 e[4], y[4];
#pragma unroll
                for (int i = 0; i < 4; ++i) e[i] = f32x2{acc[2 * i], acc[2 * i + 1]} * ga;
#pragma unroll
                for (int i = 0; i < 4; ++i) e[i] = f32x2{__builtin_amdgcn_exp2f(e[i][0]), __builtin_amdgcn_exp2f(e[i][1])} * sd + sd;
#pragma unroll
                for (int i = 0; i < 4; ++i) y[i] = f32x2{acc[8 + 2 * i], acc[9 + 2 * i]} * f32x2{__builtin_amdgcn_rcpf(e[i][0]), __builtin_amdgcn_rcpf(e[i][1])};
                const u32x4 ov = {pk2(y[0][0], y[0][1]), pk2(y[1][0], y[1][1]), pk2(y[2][0], y[2][1]), pk2(y[3][0], y[3][1])};
                ST128(ov, rs, (n < 2 || bi < 28) ? l16 : 0x80000000u, WS_HH + (tt * NU + bi * 3 + n) * 1024, AUX_SC1);
            };
            epi(0, gu.a0);
            asm volatile("" : "+{v[80:95]}"(gu.a1));
            epi(1, gu.a1);
            asm volatile("" : "+{v[96:111]}"(gu.a2));
            epi(2, gu.a2);
        }
        STAMP(3, 4);
        if (bi >= 29) WAITV(0);
        BAR_VL(3);
        STAMP(3, 5);
        if (w >= 4) {
            if (bi < 29) issue_dn(33, 6, L_GU + ((w - 4) >> 1) * 49152 + 36864, w - 4); else issue_dn(39, 1, L_TAB + ((w - 4) >> 1) * 2048, w - 4);
            STAMP(3, 6);
        }
        FLUSH(3);
    }

    {
        SKEW(7);
        STAMP(4, 0);
        if (b == 0 && tid == 0) __builtin_amdgcn_raw_buffer_store_b32(ep, rs, 0, WS_EP, AUX_SC1);
        f32x16 a0, a1;
        if (w < 4) {
            const unsigned t0 = lds0 + w64_tr(lane, kh), t1 = lds0 + w64_tr(lane, kh ^ 1);
            G2 g2;
            g2.tb[0] = t0 + kh * 15 * 2048; g2.tb[1] = t1 + kh * 15 * 2048; g2.tb[2] = t0 + L_GU + kh * 24 * 2048; g2.tb[3] = t1 + L_GU + kh * 24 * 2048;
            g2.tb[4] = t0 + L_TAB + kh * 2048; g2.tb[5] = t1 + L_TAB + kh * 2048;
            g2.sa = g2.s0 = RFL(WS_HH + ((rt * 2 + wt) * NU + kh * 43) * 1024); g2.l16 = l16; g2.budget = budget; g2.rs = rs; g2.a1 = f32x16{};
#pragma unroll
            for (int i = 0; i < 8; ++i) { g2.a0[2 * i] = bflo(hreg[i >> 2][i & 3]); g2.a0[2 * i + 1] = bfhi(hreg[i >> 2][i & 3]); }
            __builtin_amdgcn_s_sleep(DELAY_G);
            STAMP(4, 1);
            g2_steps<0, 43, 1, DN_B0, 38>(g2);
            a0 = g2.a0; a1 = g2.a1; STAMPV(15, budget - g2.budget);
        } else {
            BAR_VL(0);
            issue_dn(40, 3, ((w - 4) >> 1) * 15 * 2048, w - 4);
            BAR_VL(0);
        }
        STAMP(4, 2);
        if (w < 4) {
#pragma unroll
            for (int g = 0; g < 4; ++g) *(LDSAS f32x4*)(lds0 + 8192 + w * 4096 + g * 1024 + lane * 16) = f32x4{a1[4 * g], a1[4 * g + 1], a1[4 * g + 2], a1[4 * g + 3]};
        }
        BAR_L();
        if (w < 4) {
            f32x4 p[4];
#pragma unroll
            for (int g = 0; g < 4; ++g) p[g] = lds_f4(lds0 + 8192 + (w ^ 2) * 4096 + g * 1024 + lane * 16);
            SBAR();
            unsigned y[8];
#pragma unroll
            for (int g = 0; g < 4; ++g) {
                y[2 * g] = pk2(a0[4 * g] + p[g][0], a0[4 * g + 1] + p[g][1]);
                y[2 * g + 1] = pk2(a0[4 * g + 2] + p[g][2], a0[4 * g + 3] + p[g][3]);
            }
            const rsrc_t ro = mk_rsrc(out, S * D * 2);
            const unsigned vo = (rt * 64 + wt * 32 + r32) * 1024 + (ct * 64 + kh * 32 + 8 * h) * 2;
#pragma unroll
            for (int g = 0; g < 2; ++g) {
                auto e0 = __builtin_amdgcn_permlane32_swap(y[4 * g], y[4 * g + 2], false, false);
                auto e1 = __builtin_amdgcn_permlane32_swap(y[4 * g + 1], y[4 * g + 3], false, false);
                ST128((u32x4{e0[0], e1[0], e0[1], e1[1]}), ro, vo + g * 32, 0, OUT_AUX);
            }
        }
        STAMP(4, 3);
        STAMP(4, 4);
        FLUSH(4);
        asm volatile(".subsection 2\n.Lmain_end:\n\t.fill 64, 4, 0xBF800000\n\t.subsection 0");
    }
}

extern "C" void kernel_launch(void* const* d_in, const int* in_sizes, int n_in,
                              void* d_out, int out_size, void* d_ws, size_t ws_size,
                              hipStream_t stream) {
    const u16* x = (const u16*)d_in[0];
    const u16* wqkv = (const u16*)d_in[1];
    const u16* wo = (const u16*)d_in[2];
    const u16* wg = (const u16*)d_in[4];
    const u16* wu = (const u16*)d_in[5];
    const u16* wd = (const u16*)d_in[6];
    u16* out = (u16*)d_out;
    char* ws = (char*)d_ws;
    (void)hipFuncSetAttribute((const void*)k_block, hipFuncAttributeMaxDynamicSharedMemorySize, K2_LDS);
    k_block<<<256, 512, K2_LDS, stream>>>(x, wqkv, wo, wg, wu, wd, out, ws);
}
```
